```python
import torch

def _softmax(x, axis=-1):
    return torch.softmax(x, dim=axis)
def _gelu(x):
    return torch.nn.functional.gelu(x, approximate="tanh")
def _silu(x):
    return torch.nn.functional.silu(x)
def _sigmoid(x):
    return torch.sigmoid(x)
def _layernorm(x, axis=-1, eps=1e-5):
    m = x.mean(dim=axis, keepdim=True)
    v = x.var(dim=axis, keepdim=True, unbiased=False)
    return (x - m) / torch.sqrt(v + eps)
def _scatter_add(tgt, idx, src, axis=0):
    return tgt.clone().index_add_(axis, idx, src)
def _segment_sum(src, seg, n):
    out = torch.zeros((n,) + src.shape[1:], dtype=src.dtype)
    return out.index_add_(0, seg, src)
def _pad(x, pw):
    flat = tuple(v for pair in reversed(pw) for v in pair)
    return torch.nn.functional.pad(x, flat)

kernel_name = 'gated_mix__sliding_local_dilated__linear_attn_block__prompt'

def setup_inputs():

    _w = (lambda a: (lambda t: t.to(torch.bfloat16) if t.is_floating_point() else t)(torch.as_tensor(a)))
    def _A_setup():
        B, S, D = 1, 2048, 448
        return {
            "x": _w((lambda *s: torch.randn(*s, dtype=torch.bfloat16) * 0.05)(B, S, D)),
            "wqkv": _w((lambda *s: torch.randn(*s, dtype=torch.bfloat16) * 0.8)(D, 3 * D)),
            "wo": _w((lambda *s: torch.randn(*s, dtype=torch.bfloat16) * 0.05)(D, D)),
        }
    _A_in = _A_setup()

    def _B_setup():
        B, S, D = 1, 2048, 448
        return {
            "x": _w((lambda *s: torch.randn(*s, dtype=torch.bfloat16) * 0.05)(B, S, D)),
            "wqkv": _w((lambda *s: torch.randn(*s, dtype=torch.bfloat16) * 1.6)(D, 3 * D)),
            "wo": _w((lambda *s: torch.randn(*s, dtype=torch.bfloat16) * 0.4)(D, D)),
        }
    _B_in = _B_setup()

    return {
        "A_x": _A_in["x"],
        "A_wqkv": _A_in["wqkv"],
        "A_wo": _A_in["wo"],
        "B_x": _B_in["x"],
        "B_wqkv": _B_in["wqkv"],
        "B_wo": _B_in["wo"],
        "gate_w": _w((lambda *s: torch.randn(*s, dtype=torch.bfloat16) * 0.05)(448) * 16.0),
    }

def reference(A_x, A_wqkv, A_wo, B_x, B_wqkv, B_wo, gate_w):
    def _A_ref(x, wqkv, wo):
        B, S, D = x.shape
        H = 16; Dh = D // H
        W, dil = 8, 2
        qkv = (lambda x, *d: x.permute(*d))((x @ wqkv).reshape(B, S, 3, H, Dh), 2, 0, 3, 1, 4)
        q, k, v = qkv[0], qkv[1], qkv[2]
        scale = Dh ** -0.5
        pos = torch.arange(S)
        off = torch.arange(W) * dil
        idx = pos[:, None] - off[None, :]
        mask = idx >= 0
        idx_c = torch.where(mask, idx, 0).reshape(-1)
        kg = k[:, :, idx_c, :].reshape(B, H, S, W, Dh)
        vg = v[:, :, idx_c, :].reshape(B, H, S, W, Dh)
        logits = torch.einsum("bhsd,bhswd->bhsw", q, kg) * scale
        logits = torch.where(mask[None, None, :, :], logits, -1e9)
        attn = _softmax(logits, axis=-1)
        o = torch.einsum("bhsw,bhswd->bhsd", attn, vg)
        return (lambda x, *d: x.permute(*d))(o, 0, 2, 1, 3).reshape(B, S, D) @ wo

    def _B_ref(x, wqkv, wo):
        B, S, D = x.shape
        H = 16; Dh = D // H
        qkv = (lambda x, *d: x.permute(*d))((x @ wqkv).reshape(B, S, 3, H, Dh), 2, 0, 3, 1, 4)
        q, k, v = qkv[0], qkv[1], qkv[2]
        phi = lambda t: torch.where(t > 0, t + 1, torch.exp(t)) + 1e-6
        pq, pk = phi(q), phi(k)
        kv = torch.einsum("bhsd,bhse->bhde", pk, v)
        z = torch.einsum("bhsd->bhd", pk)
        num = torch.einsum("bhqd,bhde->bhqe", pq, kv)
        den = torch.einsum("bhqd,bhd->bhq", pq, z)[..., None] + 1e-6
        o = num / den
        return (lambda x, *d: x.permute(*d))(o, 0, 2, 1, 3).reshape(B, S, D) @ wo

    x = A_x
    g = _sigmoid((x * gate_w).sum(-1, keepdims=True))
    a = _A_ref(x, A_wqkv, A_wo)
    b = _B_ref(x, B_wqkv, B_wo)
    return g * a + (1 - g) * b
```

```cpp
#include <hip/hip_runtime.h>
#include <stdint.h>

typedef __bf16 bf16x2 __attribute__((ext_vector_type(2)));
typedef __bf16 bf16x4 __attribute__((ext_vector_type(4)));
typedef __bf16 bf16x8 __attribute__((ext_vector_type(8)));
typedef float f32x2 __attribute__((ext_vector_type(2)));
typedef float f32x4 __attribute__((ext_vector_type(4)));
typedef unsigned u32x2 __attribute__((ext_vector_type(2)));
typedef unsigned u32x4 __attribute__((ext_vector_type(4)));
typedef unsigned long long u64;
#define LAS __attribute__((address_space(3)))
#define GAS __attribute__((address_space(1)))
typedef GAS u64 gu64;
#define RLX_AGENT __ATOMIC_RELAXED, __HIP_MEMORY_SCOPE_AGENT

namespace {

constexpr int kS = 2048, kD = 448;
constexpr int XROW = kD * 2;
constexpr int WROW = 3 * kD * 2;

constexpr int XS_B = 144 * 128;
constexpr int WM_B = 64 * 112;
constexpr int SLOT = XS_B + 3 * WM_B;
constexpr int G_OFF = 3 * SLOT;
constexpr int EX_OFF = G_OFF + 448 * 8;
constexpr int KVL_OFF = EX_OFF + 32768;
constexpr int LDS_B = 163840;
static_assert(KVL_OFF + 4096 <= LDS_B, "lds");

constexpr int WS_EO = 0;
constexpr int WS_EKV = 2048;
constexpr int WS_OFLAG = 4096;
constexpr int WS_KVFLAG = WS_OFLAG + 16 * 4 * 32 * 8;
constexpr int WS_KVP = 65536;
constexpr int WS_O = WS_KVP + 8 * 8 * 16 * 1024;
constexpr int WS_END = WS_O + 16 * 8 * 32 * 1024;

constexpr unsigned SPIN_CAP = 1u << 20;

__device__ __forceinline__ unsigned pk2(float a, float b) {
  f32x2 v = {a, b};
  bf16x2 r = __builtin_convertvector(v, bf16x2);
  return __builtin_bit_cast(unsigned, r);
}
__device__ __forceinline__ bf16x8 as_frag(u32x4 v) { return __builtin_bit_cast(bf16x8, v); }
__device__ __forceinline__ f32x4 mfma(bf16x8 a, bf16x8 b, f32x4 c) {
  return __builtin_amdgcn_mfma_f32_16x16x32_bf16(a, b, c, 0, 0, 0);
}
__device__ __forceinline__ bf16x8 lds_tr2(unsigned a0, unsigned a1) {
  bf16x4 lo = __builtin_amdgcn_ds_read_tr16_b64_v4bf16((LAS bf16x4*)(uintptr_t)a0);
  bf16x4 hi = __builtin_amdgcn_ds_read_tr16_b64_v4bf16((LAS bf16x4*)(uintptr_t)a1);
  return __builtin_shufflevector(lo, hi, 0, 1, 2, 3, 4, 5, 6, 7);
}
__device__ __forceinline__ u32x4 lds_ld128(unsigned a) { return *(LAS u32x4*)(uintptr_t)a; }
__device__ __forceinline__ void lds_st128(unsigned a, u32x4 v) { *(LAS u32x4*)(uintptr_t)a = v; }
__device__ __forceinline__ void lds_st64(unsigned a, u32x2 v) { *(LAS u32x2*)(uintptr_t)a = v; }

__device__ __forceinline__ u32x4 make_srd(const void* p, unsigned bytes) {
  uint64_t a = (uint64_t)p;
  u32x4 r;
  r[0] = __builtin_amdgcn_readfirstlane((unsigned)a);
  r[1] = __builtin_amdgcn_readfirstlane((unsigned)(a >> 32) & 0xffffu);
  r[2] = bytes;
  r[3] = 0x00020000u;
  return r;
}
__device__ __forceinline__ void dma16(u32x4 srd, unsigned voff, unsigned soff, unsigned ldsaddr) {
  asm volatile(
      "s_mov_b32 m0, %3\n\t"
      "s_nop 0\n\t"
      "buffer_load_dwordx4 %0, %1, %2 offen lds"
      :
      : "v"(voff), "s"(srd), "s"(soff), "s"(ldsaddr)
      : "memory");
}
#define WAIT_BAR(N) asm volatile("s_waitcnt vmcnt(" #N ") lgkmcnt(0)\n\ts_barrier" ::: "memory")
#define LDS_BAR() asm volatile("s_waitcnt lgkmcnt(0)\n\ts_barrier" ::: "memory")
#define DRAIN() asm volatile("s_waitcnt vmcnt(0)" ::: "memory")

__device__ __forceinline__ float rows_max(float v) {
  unsigned b = __float_as_uint(v);
  auto r = __builtin_amdgcn_permlane16_swap(b, b, false, false);
  unsigned r0 = r[0], r1 = r[1];
  v = fmaxf(__uint_as_float(r0), __uint_as_float(r1));
  b = __float_as_uint(v);
  auto s = __builtin_amdgcn_permlane32_swap(b, b, false, false);
  unsigned s0 = s[0], s1 = s[1];
  return fmaxf(__uint_as_float(s0), __uint_as_float(s1));
}
__device__ __forceinline__ float rows_sum(float v) {
  unsigned b = __float_as_uint(v);
  auto r = __builtin_amdgcn_permlane16_swap(b, b, false, false);
  unsigned r0 = r[0], r1 = r[1];
  v = __uint_as_float(r0) + __uint_as_float(r1);
  b = __float_as_uint(v);
  auto s = __builtin_amdgcn_permlane32_swap(b, b, false, false);
  unsigned s0 = s[0], s1 = s[1];
  return __uint_as_float(s0) + __uint_as_float(s1);
}
__device__ __forceinline__ float row3_bcast(float v) {
  unsigned b = __float_as_uint(v);
  auto s = __builtin_amdgcn_permlane32_swap(b, b, false, false);
  unsigned hi = s[1];
  auto r = __builtin_amdgcn_permlane16_swap(hi, hi, false, false);
  unsigned r1 = r[1];
  return __uint_as_float(r1);
}
__device__ __forceinline__ float phi(float t) {
  float e = __builtin_amdgcn_exp2f(t * 1.44269504088896f);
  return (t > 0.f ? t + 1.f : e) + 1e-6f;
}
__device__ __forceinline__ u64 next_tag(u64 e) { return e * 6364136223846793005ull + 1442695040888963407ull; }

__device__ __forceinline__ void poll_flags(const gu64* f, int lane, int n, u64 tag) {
  const gu64* p = f + (lane < n ? lane : 0);
#pragma nounroll
  for (unsigned spins = 0; spins < SPIN_CAP; ++spins) {
    u64 v = __hip_atomic_load(p, RLX_AGENT);
    if (__all(v == tag)) break;
    __builtin_amdgcn_s_sleep(1);
  }
  asm volatile("" ::: "memory");
}

template <bool ISB>
__device__ __forceinline__ void body(const char* __restrict__ x, const char* __restrict__ wqkv, const char* __restrict__ woA,
                                     const char* __restrict__ woB, const unsigned short* __restrict__ gate_w,
                                     char* __restrict__ out, char* __restrict__ ws, unsigned lds0, int rt, int hp, int rot,
                                     int rgh, int cg) {
  const int tid = threadIdx.x;
  const int lane = tid & 63;
  const int w = __builtin_amdgcn_readfirstlane(tid >> 6) & 7;
  const int pg = w & 3, hh = w >> 2;
  const int li = lane & 15, g = lane >> 4, q = (lane >> 2) & 3, p = lane & 3;

  const u32x4 srdX = make_srd(x, kS * XROW);
  const u32x4 srdW = make_srd(wqkv, kD * WROW);
  const bool w7 = w == 7;
  const unsigned xsw = ((lane & 7) ^ (lane >> 4)) << 4;
  const unsigned vx0 = (rt * 128 + w * 16 + (lane >> 3)) * XROW + xsw;
  const unsigned vx1 = (vx0 ^ 64u) + 8 * XROW;
  unsigned va0, va1;
  {
    const unsigned s = w * 64 + lane;
    const unsigned pos = (s * 9363u) >> 16, ch = s - pos * 7;
    const unsigned kl = (pos & 56) | ((pos >> 1) & 3) | ((pos & 1) << 2);
    const unsigned wv = kl * WROW + hp * 112 + ch * 16;
    const unsigned h0 = (rt == 0 || ISB) ? 0u : (rt * 128 - 16 + (lane >> 3)) * XROW + xsw;
    const unsigned h1 = (rt == 0 || ISB) ? 0u : (h0 ^ 64u) + 8 * XROW;
    va0 = w7 ? h0 : wv;
    va1 = w7 ? h1 : wv + XROW;
  }
  const u32x4 srdA = w7 ? srdX : srdW;
  const unsigned stepA = w7 ? 128u : 64u * WROW;
  const unsigned la0 = lds0 + (w7 ? 0u : XS_B + w * 1024u);
  const unsigned la1 = la0 + (w7 ? 1024u : (unsigned)WM_B);
  const unsigned la2 = la1 + (w7 ? 0u : (unsigned)WM_B);
  const unsigned sa2 = w7 ? 0u : (unsigned)XROW;
  const unsigned lx0 = lds0 + 2048 + w * 2048;
  auto issue_chunk = [&](int c) {
    int cc = c + rot;
    cc = cc >= 7 ? cc - 7 : cc;
    const unsigned so = (c % 3) * SLOT, sA = cc * stepA, sX = cc * 128;
    dma16(srdX, vx0, sX, lx0 + so);
    dma16(srdX, vx1, sX, lx0 + so + 1024);
    dma16(srdA, va0, sA, la0 + so);
    dma16(srdA, va1, sA, la1 + so);
    dma16(srdA, va1, sA + sa2, la2 + so);
  };
  issue_chunk(0);

  u64 eo, ekv = 0;
  unsigned gwv;
  {
    const char* pe = ws + WS_EO + rt * 128;
    asm volatile("global_load_dwordx2 %0, %1, off sc1" : "=v"(eo) : "v"(pe) : "memory");
    if (ISB) {
      const char* pk = ws + WS_EKV + hp * 128;
      asm volatile("global_load_dwordx2 %0, %1, off sc1" : "=v"(ekv) : "v"(pk) : "memory");
    }
    const unsigned short* pgw = gate_w + (tid < kD ? tid : 0);
    asm volatile("global_load_ushort %0, %1, off" : "=v"(gwv) : "v"(pgw) : "memory");
  }
  issue_chunk(1);
  issue_chunk(2);

  const u32x4 srdOA = make_srd(woA, kD * XROW);
  const u32x4 srdOB = make_srd(woB, kD * XROW);
  const unsigned wo_voff = (lane >> 3) * XROW + cg * 128 + (((lane & 7) ^ (((lane >> 4) & 3) << 1)) << 4);
  auto issue_wo = [&](int s) {
#pragma unroll
    for (int j = 0; j < 5; ++j) {
      int n = w + 8 * j;
      const int lim = s == 2 ? 33 : 38;
      n = n > lim ? lim : n;
      const int blk = s * 39 + n;
      const bool isB = blk >= 56;
      dma16(isB ? srdOB : srdOA, wo_voff, (unsigned)((isB ? blk - 56 : blk) * 8 * XROW), lds0 + blk * 1024);
    }
  };

  asm volatile("s_waitcnt vmcnt(10)" : "+v"(gwv), "+v"(eo), "+v"(ekv)::"memory");
  if (tid < kD) {
    const unsigned d = gwv | (gwv << 16);
    lds_st64(lds0 + G_OFF + tid * 8, (u32x2){d, d});
  }
  const u64 tagO = next_tag(((u64)__builtin_amdgcn_readfirstlane((unsigned)(eo >> 32)) << 32) |
                            (unsigned)__builtin_amdgcn_readfirstlane((unsigned)eo));
  const u64 tagKV = next_tag(((u64)__builtin_amdgcn_readfirstlane((unsigned)(ekv >> 32)) << 32) |
                             (unsigned)__builtin_amdgcn_readfirstlane((unsigned)ekv));

  f32x4 aq[2][2], ak[2][2], av[2][2], ah = {0.f, 0.f, 0.f, 0.f};
#pragma unroll
  for (int t = 0; t < 2; ++t)
#pragma unroll
    for (int f = 0; f < 2; ++f) aq[t][f] = ak[t][f] = av[t][f] = (f32x4){0.f, 0.f, 0.f, 0.f};

  const unsigned xl0 = lds0 + li * 128 + ((g ^ (li >> 1)) << 4);
  const unsigned xl = xl0 + (1 + 2 * pg) * 2048;
  const unsigned wl = lds0 + XS_B + (8 * g + 2 * q) * 112 + 8 * p + 56 * hh;
  const unsigned gl = lds0 + G_OFF + (8 * g + q) * 8;
  const bool p3 = p == 3;
  const unsigned d1 = p3 ? 32u : 112u, d2 = p3 ? 256u : 3584u;
  const unsigned hoff = (1 + (pg >> 1)) * WM_B + (pg & 1) * 32;

#pragma unroll
  for (int c = 0; c < 7; ++c) {
    if (c == 0) { WAIT_BAR(10); } else { WAIT_BAR(5); }
    if (c >= 1 && c <= 4) issue_chunk(c + 2);
    if (c == 5) issue_wo(1);
    if (c == 6) issue_wo(2);
    int cc = c + rot;
    cc = cc >= 7 ? cc - 7 : cc;
    const unsigned so = (c % 3) * SLOT;
    const unsigned wb = wl + so;
    const unsigned q1 = p3 ? gl + cc * 512 : wb + 32;
#pragma unroll
    for (int ks = 0; ks < 2; ++ks) {
      const unsigned xa = (xl ^ (ks * 64)) + so;
      const bf16x8 x0 = as_frag(lds_ld128(xa)), x1 = as_frag(lds_ld128(xa + 2048));
      const unsigned wk = wb + ks * 3584;
      {
        const bf16x8 f0 = lds_tr2(wk, wk + 112);
        const unsigned a = q1 + ks * d2;
        const bf16x8 f1 = lds_tr2(a, a + d1);
        aq[0][0] = mfma(f0, x0, aq[0][0]);
        aq[1][0] = mfma(f0, x1, aq[1][0]);
        aq[0][1] = mfma(f1, x0, aq[0][1]);
        aq[1][1] = mfma(f1, x1, aq[1][1]);
      }
#pragma unroll
      for (int f = 0; f < 2; ++f) {
        const bf16x8 fk = lds_tr2(wk + WM_B + f * 32, wk + WM_B + f * 32 + 112);
        if (ISB) {
          ak[0][f] = mfma(x0, fk, ak[0][f]);
          ak[1][f] = mfma(x1, fk, ak[1][f]);
        } else {
          ak[0][f] = mfma(fk, x0, ak[0][f]);
          ak[1][f] = mfma(fk, x1, ak[1][f]);
        }
        const bf16x8 fv = lds_tr2(wk + 2 * WM_B + f * 32, wk + 2 * WM_B + f * 32 + 112);
        av[0][f] = mfma(x0, fv, av[0][f]);
        av[1][f] = mfma(x1, fv, av[1][f]);
      }
      if (!ISB) {
        const bf16x8 xh = as_frag(lds_ld128((xl0 ^ (ks * 64)) + so));
        const bf16x8 fh = lds_tr2(wk + hoff, wk + hoff + 112);
        if (pg < 2) ah = mfma(fh, xh, ah); else ah = mfma(xh, fh, ah);
      }
    }
  }

  const __amdgpu_buffer_rsrc_t wsr = __builtin_amdgcn_make_buffer_rsrc(ws, 0, WS_END, 0x00020000);
  const bool g3 = g == 3;
  float gate[2];
#pragma unroll
  for (int t = 0; t < 2; ++t) {
    const float z = row3_bcast(aq[t][1][0]);
    gate[t] = __builtin_amdgcn_rcpf(1.f + __builtin_amdgcn_exp2f(-1.44269504088896f * z));
  }
  u32x4 ofr[2];

  if (!ISB) {
    u32x4 qf[2], kf[2];
    u32x2 vf[2][2];
#pragma unroll
    for (int t = 0; t < 2; ++t) {
      qf[t] = (u32x4){pk2(aq[t][0][0], aq[t][0][1]), pk2(aq[t][0][2], aq[t][0][3]),
                      g3 ? 0u : pk2(aq[t][1][0], aq[t][1][1]), g3 ? 0u : pk2(aq[t][1][2], aq[t][1][3])};
      kf[t] = (u32x4){pk2(ak[t][0][0], ak[t][0][1]), pk2(ak[t][0][2], ak[t][0][3]),
                      g3 ? 0u : pk2(ak[t][1][0], ak[t][1][1]), g3 ? 0u : pk2(ak[t][1][2], ak[t][1][3])};
#pragma unroll
      for (int f = 0; f < 2; ++f) vf[t][f] = (u32x2){pk2(av[t][f][0], av[t][f][1]), pk2(av[t][f][2], av[t][f][3])};
    }
    const unsigned exb = lds0 + EX_OFF + hh * 5 * 2048 + lane * 16;
    lds_st128(exb + (pg + 1) * 2048, kf[1]);
    lds_st128(exb + (pg + 1) * 2048 + 1024, (u32x4){vf[1][0][0], vf[1][0][1], vf[1][1][0], vf[1][1][1]});
    {
      u32x2 hv = {pk2(ah[0], ah[1]), pk2(ah[2], ah[3])};
      if (pg == 1 && g3) hv = (u32x2){0u, 0u};
      lds_st64(exb + (pg >> 1) * 1024 + (pg & 1) * 8, hv);
    }
    LDS_BAR();
    issue_wo(0);
    const u32x4 kprev = lds_ld128(exb + pg * 2048);
    const u32x4 vprev = lds_ld128(exb + pg * 2048 + 1024);
    const bool odd = li & 1;
    const int kk0 = 4 * g + (li & 1);
    const bool own0 = kk0 <= li, own1 = kk0 + 2 <= li;
    const float sc = 0.18898223650461362f * 1.44269504088896f;
#pragma unroll
    for (int t = 0; t < 2; ++t) {
      const f32x4 z4 = {0.f, 0.f, 0.f, 0.f};
      const f32x4 sp = mfma(as_frag(t == 0 ? kprev : kf[0]), as_frag(qf[t]), z4);
      const f32x4 so = mfma(as_frag(kf[t]), as_frag(qf[t]), z4);
      const float o0 = odd ? so[1] : so[0], o1 = odd ? so[3] : so[2];
      float p0 = odd ? sp[1] : sp[0], p1 = odd ? sp[3] : sp[2];
      float l0 = (own0 ? o0 : p0) * sc, l1 = (own1 ? o1 : p1) * sc;
      if (t == 0 && rt == 0 && pg == 0) {
        l0 = own0 ? l0 : -__builtin_inff();
        l1 = own1 ? l1 : -__builtin_inff();
      }
      const float m = rows_max(fmaxf(l0, l1));
      float e0 = __builtin_amdgcn_exp2f(l0 - m), e1 = __builtin_amdgcn_exp2f(l1 - m);
      const float inv = __builtin_amdgcn_rcpf(rows_sum(e0 + e1));
      e0 *= inv;
      e1 *= inv;
      const unsigned w0 = odd ? pk2(0.f, e0) : pk2(e0, 0.f);
      const unsigned w1 = odd ? pk2(0.f, e1) : pk2(e1, 0.f);
      const u32x4 pf = {own0 ? 0u : w0, own1 ? 0u : w1, own0 ? w0 : 0u, own1 ? w1 : 0u};
      f32x4 o[2];
#pragma unroll
      for (int f = 0; f < 2; ++f) {
        const u32x4 va = t == 0 ? (u32x4){vprev[2 * f], vprev[2 * f + 1], vf[0][f][0], vf[0][f][1]}
                                : (u32x4){vf[0][f][0], vf[0][f][1], vf[1][f][0], vf[1][f][1]};
        o[f] = mfma(as_frag(va), as_frag(pf), z4);
      }
      const float s = gate[t];
      ofr[t] = (u32x4){pk2(o[0][0] * s, o[0][1] * s), pk2(o[0][2] * s, o[0][3] * s),
                       g3 ? 0u : pk2(o[1][0] * s, o[1][1] * s), g3 ? 0u : pk2(o[1][2] * s, o[1][3] * s)};
    }
  } else {
    u32x4 pkf[2], vfr[2];
#pragma unroll
    for (int f = 0; f < 2; ++f) {
      pkf[f] = (u32x4){pk2(phi(ak[0][f][0]), phi(ak[0][f][1])), pk2(phi(ak[0][f][2]), phi(ak[0][f][3])),
                       pk2(phi(ak[1][f][0]), phi(ak[1][f][1])), pk2(phi(ak[1][f][2]), phi(ak[1][f][3]))};
      vfr[f] = (u32x4){pk2(av[0][f][0], av[0][f][1]), pk2(av[0][f][2], av[0][f][3]),
                       pk2(av[1][f][0], av[1][f][1]), pk2(av[1][f][2], av[1][f][3])};
    }
    if (li >= 12) {
      pkf[1] = (u32x4){0u, 0u, 0u, 0u};
      const unsigned one = li == 12 ? 0x3f803f80u : 0u;
      vfr[1] = (u32x4){one, one, one, one};
    }
    const f32x4 z4 = {0.f, 0.f, 0.f, 0.f};
    const unsigned exb = lds0 + EX_OFF + hh * 16384 + lane * 16;
#pragma unroll
    for (int et = 0; et < 2; ++et)
#pragma unroll
      for (int dt = 0; dt < 2; ++dt) {
        const f32x4 kv = mfma(as_frag(pkf[dt]), as_frag(vfr[et]), z4);
        lds_st128(exb + (et * 2 + dt) * 4096 + pg * 1024, __builtin_bit_cast(u32x4, kv));
      }
    LDS_BAR();
    f32x4 part;
    {
      const unsigned a = exb + pg * 4096;
      const f32x4 s0 = __builtin_bit_cast(f32x4, lds_ld128(a)), s1 = __builtin_bit_cast(f32x4, lds_ld128(a + 1024));
      const f32x4 s2 = __builtin_bit_cast(f32x4, lds_ld128(a + 2048)), s3 = __builtin_bit_cast(f32x4, lds_ld128(a + 3072));
      part = (s0 + s1) + (s2 + s3);
    }
    const unsigned kvbase = WS_KVP + (hp * 8 + w) * 16 * 1024 + lane * 16;
    __builtin_amdgcn_raw_buffer_store_b128(__builtin_bit_cast(u32x4, part), wsr, kvbase + rt * 1024, 0, 16);
    DRAIN();
    gu64* kvf = (gu64*)(ws + WS_KVFLAG) + (hp * 8 + w) * 16;
    if (lane == 0) __hip_atomic_store(kvf + rt, tagKV, RLX_AGENT);
    issue_wo(0);
    u32x4 pqf[2];
#pragma unroll
    for (int t = 0; t < 2; ++t)
      pqf[t] = (u32x4){pk2(phi(aq[t][0][0]), phi(aq[t][0][1])), pk2(phi(aq[t][0][2]), phi(aq[t][0][3])),
                       g3 ? 0u : pk2(phi(aq[t][1][0]), phi(aq[t][1][1])), g3 ? 0u : pk2(phi(aq[t][1][2]), phi(aq[t][1][3]))};
    poll_flags(kvf, lane, 16, tagKV);
    if (rt == 0 && w == 0 && lane == 0) __hip_atomic_store((gu64*)(ws + WS_EKV + hp * 128), tagKV, RLX_AGENT);
    f32x4 pr[16];
#pragma unroll
    for (int r = 0; r < 16; ++r)
      pr[r] = __builtin_bit_cast(f32x4, __builtin_amdgcn_raw_buffer_load_b128(wsr, kvbase + r * 1024, 0, 16));
#pragma unroll
    for (int s = 1; s < 16; s <<= 1)
#pragma unroll
      for (int r = 0; r < 16; r += 2 * s) pr[r] += pr[r + s];
    lds_st64(lds0 + KVL_OFF + hh * 2048 + (pg >> 1) * 1024 + lane * 16 + (pg & 1) * 8,
             (u32x2){pk2(pr[0][0], pr[0][1]), pk2(pr[0][2], pr[0][3])});
    LDS_BAR();
    const unsigned kb = lds0 + KVL_OFF + hh * 2048;
    const u32x4 kv0 = lds_ld128(kb + lane * 16), kv1 = lds_ld128(kb + 1024 + lane * 16);
    const u32x4 zf = lds_ld128(kb + 1024 + (g * 16 + 12) * 16);
#pragma unroll
    for (int t = 0; t < 2; ++t) {
      const f32x4 n0 = mfma(as_frag(kv0), as_frag(pqf[t]), z4);
      const f32x4 n1 = mfma(as_frag(kv1), as_frag(pqf[t]), z4);
      const f32x4 dn = mfma(as_frag(zf), as_frag(pqf[t]), z4);
      const float s = (1.f - gate[t]) / (dn[0] + 1e-6f);
      ofr[t] = (u32x4){pk2(n0[0] * s, n0[1] * s), pk2(n0[2] * s, n0[3] * s),
                       g3 ? 0u : pk2(n1[0] * s, n1[1] * s), g3 ? 0u : pk2(n1[2] * s, n1[3] * s)};
    }
  }

  const int slot = (ISB ? 16 : 0) + hp * 2 + hh;
  {
    const unsigned ob = WS_O + ((rt * 8 + pg * 2) * 32 + slot) * 1024 + lane * 16;
    __builtin_amdgcn_raw_buffer_store_b128(ofr[0], wsr, ob, 0, 16);
    __builtin_amdgcn_raw_buffer_store_b128(ofr[1], wsr, ob + 32 * 1024, 0, 16);
    DRAIN();
    if (lane == 0) __hip_atomic_store((gu64*)(ws + WS_OFLAG) + (rt * 4 + pg) * 32 + slot, tagO, RLX_AGENT);
  }
  if (cg >= 7) return;

  LDS_BAR();
  const int kq = w & 3, rh = w >> 2;
  bf16x8 wf[8][4];
  {
    const unsigned rb = lds0 + (kq * 224 + 4 * g + q) * 128 + 8 * p;
    const unsigned sw = (2 * g + (q >> 1)) & 3;
    const unsigned hi = g3 ? 0u : 2048u;
#pragma unroll
    for (int h = 0; h < 8; ++h)
#pragma unroll
      for (int ct = 0; ct < 4; ++ct) {
        const unsigned a = rb + h * 28 * 128 + (((ct ^ (2 * (h & 1))) ^ sw) << 5);
        wf[h][ct] = lds_tr2(a, a + hi);
      }
  }
  LDS_BAR();
  const int ppg = rgh * 2 + rh;
  poll_flags((gu64*)(ws + WS_OFLAG) + (rt * 4 + ppg) * 32 + kq * 8, lane, 8, tagO);
  f32x4 acc[2][4];
#pragma unroll
  for (int t = 0; t < 2; ++t)
#pragma unroll
    for (int ct = 0; ct < 4; ++ct) acc[t][ct] = (f32x4){0.f, 0.f, 0.f, 0.f};
  {
    const unsigned ob = WS_O + ((rt * 8 + ppg * 2) * 32 + kq * 8) * 1024 + lane * 16;
    u32x4 of[8][2];
#pragma unroll
    for (int h = 0; h < 8; ++h)
#pragma unroll
      for (int t = 0; t < 2; ++t) of[h][t] = __builtin_amdgcn_raw_buffer_load_b128(wsr, ob + (t * 32 + h) * 1024, 0, 16);
    __builtin_amdgcn_sched_barrier(0);
#pragma unroll
    for (int h = 0; h < 8; ++h)
#pragma unroll
      for (int t = 0; t < 2; ++t)
#pragma unroll
        for (int ct = 0; ct < 4; ++ct) acc[t][ct] = mfma(wf[h][ct], as_frag(of[h][t]), acc[t][ct]);
  }
  {
    const unsigned rbase = lds0 + rh * 32768 + lane * 16;
#pragma unroll
    for (int ct = 0; ct < 4; ++ct)
#pragma unroll
      for (int t = 0; t < 2; ++t) lds_st128(rbase + (ct * 2 + t) * 4096 + kq * 1024, __builtin_bit_cast(u32x4, acc[t][ct]));
    LDS_BAR();
    if (rgh == 0 && cg == 0 && tid == 0) __hip_atomic_store((gu64*)(ws + WS_EO + rt * 128), tagO, RLX_AGENT);
#pragma unroll
    for (int t = 0; t < 2; ++t) {
      const unsigned a = rbase + (kq * 2 + t) * 4096;
      const f32x4 s0 = __builtin_bit_cast(f32x4, lds_ld128(a)), s1 = __builtin_bit_cast(f32x4, lds_ld128(a + 1024));
      const f32x4 s2 = __builtin_bit_cast(f32x4, lds_ld128(a + 2048)), s3 = __builtin_bit_cast(f32x4, lds_ld128(a + 3072));
      const f32x4 r = (s0 + s1) + (s2 + s3);
      const int row = rt * 128 + ppg * 32 + t * 16 + li;
      *(u32x2*)(out + row * XROW + cg * 128 + kq * 32 + g * 8) = (u32x2){pk2(r[0], r[1]), pk2(r[2], r[3])};
    }
  }
}

__global__ __launch_bounds__(512, 2) void gated_mix_kernel(const char* __restrict__ x, const char* __restrict__ wqkvA,
                                                           const char* __restrict__ woA, const char* __restrict__ wqkvB,
                                                           const char* __restrict__ woB,
                                                           const unsigned short* __restrict__ gate_w, char* __restrict__ out,
                                                           char* __restrict__ ws) {
  __shared__ __attribute__((aligned(1024))) char lds[LDS_B];
  const unsigned lds0 = (unsigned)(uintptr_t)(LAS char*)lds;
  const int bid = blockIdx.x;
  const int xcd = bid & 7, idx = bid >> 3;
  const int j = xcd >> 1, ci = idx >> 3;
  const int br = ci >> 1;
  const int hp = 2 * (br ? (j ^ 1) : j) + (ci & 1);
  const int rt = (xcd & 1) * 8 + (idx & 7);
  const int rot = ((idx & 7) + 2 * ci) % 7;
  const int rgh = j >> 1, cg = (j & 1) * 4 + ci;
  if (br) body<true>(x, wqkvB, woA, woB, gate_w, out, ws, lds0, rt, hp, rot, rgh, cg);
  else body<false>(x, wqkvA, woA, woB, gate_w, out, ws, lds0, rt, hp, rot, rgh, cg);
}

}

extern "C" void kernel_launch(void* const* d_in, const int* in_sizes, int n_in, void* d_out, int out_size, void* d_ws,
                              size_t ws_size, hipStream_t stream) {
  (void)in_sizes; (void)n_in; (void)out_size; (void)ws_size;
  gated_mix_kernel<<<256, 512, 0, stream>>>((const char*)d_in[0], (const char*)d_in[1], (const char*)d_in[2],
                                            (const char*)d_in[4], (const char*)d_in[5],
                                            (const unsigned short*)d_in[6], (char*)d_out, (char*)d_ws);
}
```

```cpp
#include <hip/hip_runtime.h>
#include <stdint.h>

typedef __bf16 bf16x2 __attribute__((ext_vector_type(2)));
typedef __bf16 bf16x4 __attribute__((ext_vector_type(4)));
typedef __bf16 bf16x8 __attribute__((ext_vector_type(8)));
typedef float f32x2 __attribute__((ext_vector_type(2)));
typedef float f32x4 __attribute__((ext_vector_type(4)));
typedef unsigned u32x2 __attribute__((ext_vector_type(2)));
typedef unsigned u32x4 __attribute__((ext_vector_type(4)));
typedef unsigned long long u64;
#define LAS __attribute__((address_space(3)))
#define GAS __attribute__((address_space(1)))
typedef GAS u64 gu64;
#define RLX_AGENT __ATOMIC_RELAXED, __HIP_MEMORY_SCOPE_AGENT

namespace {

constexpr int kS = 2048, kD = 448;
constexpr int XROW = kD * 2;
constexpr int WROW = 3 * kD * 2;
constexpr int WM_B = 64 * 112;

constexpr int A_FRONT = 34816, A_SLOT = 144 * 128 + 3 * WM_B, A_W = 144 * 128;
constexpr int A_G = A_FRONT + 3 * A_SLOT;
constexpr int B_SLOT = 128 * 128 + 2 * WM_B, B_W = 128 * 128;
constexpr int B_WQ = 3 * B_SLOT, B_G = B_WQ + 7 * WM_B, B_EX = B_G + 3584, B_KVL = B_EX + 8192;
constexpr int SYNC = 163584;
constexpr int LDS_B = 163840;
static_assert(A_G + 3584 <= SYNC && B_KVL + 4096 <= SYNC && A_FRONT + 2 * A_SLOT == 114688, "lds map");
__host__ __device__ constexpr int a_slot(int c) { return A_FRONT + ((c + 2) % 3) * A_SLOT; }
__host__ __device__ constexpr int b_slot(int c) { return ((c + 2) % 3) * B_SLOT; }
enum { SY_WOA = 0, SY_WOB = 16, SY_WQ1 = 48, SY_WQ2 = 64, SY_EX = 96, SY_KV = 112, SY_ZERO = 192, SY_TAG = 240 };
#define LOOKA 10
#define LOOKB 8

constexpr int WS_E = 0;
#define FS 16
#define PSLEEP 1
constexpr int WS_FA = 4096;
constexpr int WS_FB = WS_FA + 16 * 2 * 16 * 8 * FS;
constexpr int WS_FKV = WS_FB + 16 * 4 * 16 * 8 * FS;
constexpr int WS_KVP = 524288;
constexpr int WS_O = WS_KVP + 8 * 4 * 16 * 1024;
constexpr int WS_END = WS_O + 16 * 8 * 32 * 1024;

constexpr unsigned SPIN_CAP = 1u << 14;

#define STAMP(i) do {} while (0)
#define STAMPC(i) do {} while (0)
#define STAMP_FLUSH() do {} while (0)

__device__ __forceinline__ unsigned pk2(float a, float b) {
  f32x2 v = {a, b};
  bf16x2 r = __builtin_convertvector(v, bf16x2);
  return __builtin_bit_cast(unsigned, r);
}
__device__ __forceinline__ u32x2 pk4(f32x4 v) { return (u32x2){pk2(v[0], v[1]), pk2(v[2], v[3])}; }
__device__ __forceinline__ bf16x8 as_frag(u32x4 v) { return __builtin_bit_cast(bf16x8, v); }
__device__ __forceinline__ bf16x8 frag2(u32x2 a, u32x2 b) { return as_frag((u32x4){a[0], a[1], b[0], b[1]}); }
__device__ __forceinline__ f32x4 mfma(bf16x8 a, bf16x8 b, f32x4 c) {
  return __builtin_amdgcn_mfma_f32_16x16x32_bf16(a, b, c, 0, 0, 0);
}
__device__ __forceinline__ bf16x8 lds_tr2(unsigned a0, unsigned a1) {
  bf16x4 lo = __builtin_amdgcn_ds_read_tr16_b64_v4bf16((LAS bf16x4*)(uintptr_t)a0);
  bf16x4 hi = __builtin_amdgcn_ds_read_tr16_b64_v4bf16((LAS bf16x4*)(uintptr_t)a1);
  return __builtin_shufflevector(lo, hi, 0, 1, 2, 3, 4, 5, 6, 7);
}
__device__ __forceinline__ u32x4 lds_ld128(unsigned a) { return *(LAS u32x4*)(uintptr_t)a; }
__device__ __forceinline__ void lds_st128(unsigned a, u32x4 v) { *(LAS u32x4*)(uintptr_t)a = v; }
__device__ __forceinline__ void lds_st64(unsigned a, u32x2 v) { *(LAS u32x2*)(uintptr_t)a = v; }
__device__ __forceinline__ unsigned opaque(unsigned v) { asm volatile("" : "+v"(v)); return v; }

__device__ __forceinline__ void sig(unsigned a) {
  asm volatile("" ::: "memory");
  *(volatile LAS unsigned*)(uintptr_t)a = 1u;
  asm volatile("" ::: "memory");
  __builtin_amdgcn_sched_barrier(0);
}
__device__ __forceinline__ void wait1(unsigned a) {
  while (__builtin_amdgcn_readfirstlane(*(volatile LAS unsigned*)(uintptr_t)a) == 0u) __builtin_amdgcn_s_sleep(1);
  asm volatile("" ::: "memory");
}
__device__ __forceinline__ void wait4(unsigned a) {
  for (;;) {
    const u32x4 v = *(volatile LAS u32x4*)(uintptr_t)a;
    if (__builtin_amdgcn_readfirstlane(v[0] & v[1] & v[2] & v[3])) break;
    __builtin_amdgcn_s_sleep(1);
  }
  asm volatile("" ::: "memory");
}

__device__ __forceinline__ u32x4 make_srd(const void* p, unsigned bytes) {
  uint64_t a = (uint64_t)p;
  u32x4 r;
  r[0] = __builtin_amdgcn_readfirstlane((unsigned)a);
  r[1] = __builtin_amdgcn_readfirstlane((unsigned)(a >> 32) & 0xffffu);
  r[2] = bytes;
  r[3] = 0x00020000u;
  return r;
}
__device__ __forceinline__ void dma16(u32x4 srd, unsigned voff, unsigned soff, unsigned ldsaddr) {
  asm volatile(
      "s_mov_b32 m0, %3\n\t"
      "s_nop 3\n\t"
      "buffer_load_dwordx4 %0, %1, %2 offen lds"
      :
      : "v"(voff), "s"(srd), "s"(soff), "s"(ldsaddr));
}
#define VM_BAR(N) asm volatile("s_waitcnt vmcnt(" #N ") lgkmcnt(0)\n\ts_barrier" ::: "memory")
#define LDS_BAR() asm volatile("s_waitcnt lgkmcnt(0)\n\ts_barrier" ::: "memory")
#define VM_WAIT(N) asm volatile("s_waitcnt vmcnt(" #N ")" ::: "memory")
template <int N> __device__ __forceinline__ void vm_bar() { asm volatile("s_waitcnt vmcnt(%0) lgkmcnt(0)\n\ts_barrier" ::"n"(N) : "memory"); }
#define SB() __builtin_amdgcn_sched_barrier(0)

__device__ __forceinline__ float fmax1(float a, float b) { return __builtin_amdgcn_fmed3f(a, b, 3.0e38f); }
__device__ __forceinline__ void swap16(float& a, float& b) {
  auto r = __builtin_amdgcn_permlane16_swap(__float_as_uint(a), __float_as_uint(b), false, false);
  const unsigned r0 = r[0], r1 = r[1];
  a = __uint_as_float(r0);
  b = __uint_as_float(r1);
}
__device__ __forceinline__ void swap32(float& a, float& b) {
  auto r = __builtin_amdgcn_permlane32_swap(__float_as_uint(a), __float_as_uint(b), false, false);
  const unsigned r0 = r[0], r1 = r[1];
  a = __uint_as_float(r0);
  b = __uint_as_float(r1);
}
template <bool MAX>
__device__ __forceinline__ float rows_reduce4(float v0, float v1, float v2, float v3) {
  swap32(v0, v1);
  swap32(v2, v3);
  float a = MAX ? fmax1(v0, v1) : v0 + v1, b = MAX ? fmax1(v2, v3) : v2 + v3;
  swap16(a, b);
  return MAX ? fmax1(a, b) : a + b;
}
__device__ __forceinline__ void rows_bcast4(float x, float (&o)[4]) {
  float y = x;
  swap16(x, y);
  o[0] = x; o[1] = x; o[2] = y; o[3] = y;
  swap32(o[0], o[1]);
  swap32(o[2], o[3]);
}
__device__ __forceinline__ float row3_bcast(float v) {
  float a = v;
  swap32(a, v);
  a = v;
  swap16(a, v);
  return v;
}
template <int NM, int ND>
__device__ __forceinline__ void interleave() {
#pragma unroll
  for (int i = 0; i < NM; ++i) {
    __builtin_amdgcn_sched_group_barrier(0x8, 1, 0);
    if (i < ND) {
      __builtin_amdgcn_sched_group_barrier(0x2, 1, 0);
      __builtin_amdgcn_sched_group_barrier(0x100, 1, 0);
    }
  }
}
constexpr float kEps = 1e-6f, kLog2e = 1.44269504088896f;
__device__ __forceinline__ float phi(float u) {
  const float e = __builtin_amdgcn_fmed3f(__builtin_amdgcn_exp2f(__builtin_fmaf(u, kLog2e, -kEps * kLog2e)), 0.f, 1.f);
  return fmax1(u, kEps) + e;
}
__device__ __forceinline__ u32x2 phi4(f32x4 v) { return (u32x2){pk2(phi(v[0]), phi(v[1])), pk2(phi(v[2]), phi(v[3]))}; }
__device__ __forceinline__ float sigmoidf(float z) {
  return __builtin_amdgcn_rcpf(1.f + __builtin_amdgcn_exp2f(-1.44269504088896f * z));
}
__device__ __forceinline__ u64 next_tag(u64 e) {
  const u64 s = ((u64)(unsigned)__builtin_amdgcn_readfirstlane((unsigned)(e >> 32)) << 32) |
                (unsigned)__builtin_amdgcn_readfirstlane((unsigned)e);
  return s * 6364136223846793005ull + 1442695040888963407ull;
}
__device__ __forceinline__ u64 uniform64(u64 e) {
  return ((u64)(unsigned)__builtin_amdgcn_readfirstlane((unsigned)(e >> 32)) << 32) |
         (unsigned)__builtin_amdgcn_readfirstlane((unsigned)e);
}
__device__ __forceinline__ void poll_flags(const gu64* f, int lane, int n, u64 tag) {
  const gu64* p = f + (lane < n ? lane : 0) * FS;
#pragma nounroll
  for (unsigned spins = 0; spins < SPIN_CAP; ++spins) {
    u64 v = __hip_atomic_load(p, RLX_AGENT);
    if (__all(v == tag)) break;
    __builtin_amdgcn_s_sleep(PSLEEP);
  }
  asm volatile("" ::: "memory");
}
__device__ __forceinline__ int wrap7(int c) { return c >= 7 ? c - 7 : c; }

__global__ __launch_bounds__(512, 2) void gated_mix_kernel(const char* __restrict__ x, const char* __restrict__ wqkvA,
                                                           const char* __restrict__ woA, const char* __restrict__ wqkvB,
                                                           const char* __restrict__ woB,
                                                           const unsigned short* __restrict__ gate_w, char* __restrict__ out,
                                                           char* __restrict__ ws) {
  __shared__ __attribute__((aligned(1024))) char lds[LDS_B];
  const unsigned L0 = (unsigned)(uintptr_t)(LAS char*)lds;
  const int bid = blockIdx.x;
  const int xcd = bid & 7, idx = bid >> 3;
  const int xj = xcd >> 1, ci = idx >> 3;
  const int br = ci >> 1;
  const int hp = 2 * (br ? (xj ^ 1) : xj) + (ci & 1);
  const int rt = (xcd & 1) * 8 + (idx & 7);
  const int rot = wrap7(wrap7((idx & 7) + 2 * ci));
  const int cg = idx & 7, rt2 = 2 * xcd + ci;

  const int tid = threadIdx.x;
  const int lane = tid & 63;
  const int w = __builtin_amdgcn_readfirstlane(tid >> 6) & 7;
  const int wl = w & 3;
  const int li = lane & 15, g = lane >> 4, q = (lane >> 2) & 3, p = lane & 3;
  const bool g3 = g == 3;
  const f32x4 z4 = {0.f, 0.f, 0.f, 0.f};
  STAMP(0); STAMPC(18);
  const __amdgpu_buffer_rsrc_t wsr = __builtin_amdgcn_make_buffer_rsrc(ws, 0, WS_END, 0x00020000);
  u64 tagO;
  auto wo_blk = [&](int blk) {
    const unsigned vwo = (lane >> 3) * XROW + cg * 128 + (((lane & 7) ^ (((lane >> 4) & 3) << 1)) << 4);
    const bool isB = blk >= 56;
    int sb = (isB ? blk - 56 : blk) + 7 * cg;
    sb = sb >= 56 ? sb - 56 : sb;
    dma16(make_srd(isB ? woB : woA, kD * XROW), vwo, (unsigned)(sb * 8 * XROW), L0 + blk * 1024);
  };

  if (w >= 4) {
    const u32x4 srdX = make_srd(x, kS * XROW);
    const u32x4 srdW = make_srd(br ? wqkvB : wqkvA, kD * WROW);
    const bool l3 = wl == 3;
    const unsigned vx0 = (lane >> 3) * XROW + (((lane & 7) ^ (lane >> 4)) << 4);
    const unsigned vx1 = vx0 ^ 64u;
    unsigned vw0, vw1;
    {
      const unsigned s0 = wl * 64 + lane, s1 = s0 + 256;
      const unsigned p0 = (s0 * 9363u) >> 16, p1 = (s1 * 9363u) >> 16;
      const unsigned k0 = (p0 & 56) | ((p0 >> 1) & 3) | ((p0 & 1) << 2);
      const unsigned k1 = (p1 & 56) | ((p1 >> 1) & 3) | ((p1 & 1) << 2);
      vw0 = k0 * WROW + hp * 112 + (s0 - p0 * 7) * 16;
      vw1 = k1 * WROW + hp * 112 + (s1 - p1 * 7) * 16;
    }
    if (!br) {
      const u32x4 srd2 = l3 ? srdX : srdW;
      const unsigned v2a = l3 ? vx0 : vw1, v2b = l3 ? vx1 : vw1;
      const int xr = rt * 128 - 16 + wl * 32;
      const unsigned sx01 = (xr < 0 ? 0 : xr) * XROW, sx23 = (xr + 16) * XROW;
      auto chunk1 = [&](int c, int j) {
        const int cc = wrap7(c + rot);
        const unsigned sl = L0 + a_slot(c), sw = cc * (64 * WROW), sx = cc * 128;
        if (j < 4) dma16(srdX, (j & 1) ? vx1 : vx0, (j < 2 ? sx01 : sx23) + (j & 1) * 8 * XROW + sx, sl + (wl * 4 + j) * 1024);
        else if (j < 7) dma16(srdW, vw0, sw + (j - 4) * XROW, sl + A_W + (j - 4) * WM_B + wl * 1024);
        else {
          const int m = j - 7, xb = m == 0 ? 16 : 17;
          dma16(srd2, m == 0 ? v2a : v2b, l3 ? (rt * 128 - 16 + xb * 8) * XROW + sx : sw + m * XROW,
                sl + (l3 ? xb * 1024 : A_W + m * WM_B + (wl + 4) * 1024));
        }
      };
      auto instr = [&](int n) {
        if (n < 70) chunk1(n / 10, n % 10);
        else wo_blk(wl + 4 * (n - 70));
      };
#pragma unroll
      for (int n = 0; n < 10 + LOOKA; ++n) instr(n);
      STAMP(1);
#pragma unroll
      for (int c = 0; c < 7; ++c) {
        vm_bar<LOOKA>();
        if (c < 6) {
#pragma unroll
          for (int n = 0; n < 10; ++n) instr(10 * c + 10 + LOOKA + n);
        }
      }
      if (cg >= 7) { STAMP_FLUSH(); return; }
#pragma unroll
      for (int n = 70 + LOOKA; n < 98; ++n) instr(n);
      tagO = uniform64(*(LAS u64*)(uintptr_t)(L0 + SYNC + SY_TAG));
      VM_WAIT(14);
      sig(L0 + SYNC + SY_WOA + wl * 4);
      VM_WAIT(0);
      sig(L0 + SYNC + SY_WOB + wl * 4);
    } else {
      const unsigned sx0 = (rt * 128 + wl * 32) * XROW;
      const unsigned v2 = l3 ? vw0 : vw1;
      const unsigned sub2 = (l3 ? wl : wl + 4) * 1024;
      auto instr = [&](int n) {
        if (n < 56) {
          const int c = n >> 3, j = n & 7, cc = wrap7(c + rot);
          const unsigned sl = L0 + b_slot(c), sw = cc * (64 * WROW), sx = cc * 128;
          if (j < 4) dma16(srdX, (j & 1) ? vx1 : vx0, sx0 + j * 8 * XROW + sx, sl + (wl * 4 + j) * 1024);
          else dma16(srdW, j < 6 ? vw0 : v2, sw + ((j & 1) + 1) * XROW, sl + B_W + (j & 1) * WM_B + (j < 6 ? wl * 1024 : sub2));
        } else {
          const int c = (n - 56) >> 1;
          dma16(srdW, (n & 1) ? v2 : vw0, wrap7(c + rot) * (64 * WROW), L0 + B_WQ + c * WM_B + ((n & 1) ? sub2 : wl * 1024));
        }
      };
#pragma unroll
      for (int n = 0; n < 8 + LOOKB; ++n) instr(n);
      STAMP(1);
      bf16x8 xq[14][2];
      const unsigned xl = L0 + (wl * 32 + li) * 128 + ((g ^ (li >> 1)) << 4);
#pragma unroll
      for (int c = 0; c < 7; ++c) {
        vm_bar<LOOKB>();
#pragma unroll
        for (int ks = 0; ks < 2; ++ks) {
          const unsigned xa = (xl ^ (ks * 64)) + b_slot(c);
          xq[c * 2 + ks][0] = as_frag(lds_ld128(xa));
          xq[c * 2 + ks][1] = as_frag(lds_ld128(xa + 2048));
        }
#pragma unroll
        for (int n = 8 * c + 8 + LOOKB; n < (c < 6 ? 8 * c + 16 + LOOKB : 70); ++n) instr(n);
      }
      tagO = uniform64(*(LAS u64*)(uintptr_t)(L0 + SYNC + SY_TAG));
      STAMP(4);
      VM_WAIT(0);
      sig(L0 + SYNC + SY_WQ1 + wl * 4);
      wait4(L0 + SYNC + SY_WQ1);
      wait4(L0 + SYNC + SY_EX);
      STAMP(3);
      f32x4 aq[2][2][2];
#pragma unroll
      for (int i = 0; i < 8; ++i) aq[i >> 2][(i >> 1) & 1][i & 1] = (f32x4){kEps, kEps, kEps, kEps};
      const unsigned wb = L0 + B_WQ + (8 * g + 2 * q) * 112 + 8 * p;
      const unsigned gl = L0 + B_G + (8 * g + q) * 8;
      const bool p3 = p == 3;
      const unsigned d1 = p3 ? 32u : 112u;
      bf16x8 fq[2][4];
      auto rdq = [&](int s) {
        const unsigned wk = wb + s * 3584;
        const unsigned a = p3 ? gl + wrap7((s >> 1) + rot) * 512 + (s & 1) * 256 : wk + 32;
        fq[s & 1][0] = lds_tr2(wk, wk + 112);
        fq[s & 1][1] = lds_tr2(a, a + d1);
        fq[s & 1][2] = lds_tr2(wk + 56, wk + 56 + 112);
        fq[s & 1][3] = lds_tr2(wk + 88, wk + 88 + 112);
      };
      rdq(0);
#pragma unroll
      for (int s = 0; s < 14; ++s) {
        SB();
        if (s + 1 < 14) rdq(s + 1);
#pragma unroll
        for (int i = 0; i < 4; ++i) {
          aq[0][i >> 1][i & 1] = mfma(fq[s & 1][i], xq[s][0], aq[0][i >> 1][i & 1]);
          aq[1][i >> 1][i & 1] = mfma(fq[s & 1][i], xq[s][1], aq[1][i >> 1][i & 1]);
        }
        interleave<8, 8>();
      }
      SB();
      STAMP(5);
      u32x4 pqf[2][2];
      float omg[2];
#pragma unroll
      for (int t = 0; t < 2; ++t) {
        omg[t] = 1.f - sigmoidf(row3_bcast(aq[t][0][1][0]));
#pragma unroll
        for (int h = 0; h < 2; ++h) {
          const u32x2 a = phi4(aq[t][h][0]), b = phi4(aq[t][h][1]);
          pqf[t][h] = (u32x4){a[0], a[1], g3 ? 0u : b[0], g3 ? 0u : b[1]};
        }
      }
      STAMP(6);
      wait4(L0 + SYNC + SY_KV);
      STAMP(8);
      const unsigned ob = WS_O + ((rt * 8 + wl * 2) * 32 + 16 + hp * 2) * 1024 + lane * 16;
#pragma unroll
      for (int h = 0; h < 2; ++h) {
        const unsigned kb = L0 + B_KVL + h * 2048;
        const bf16x8 kv0 = as_frag(lds_ld128(kb + lane * 16)), kv1 = as_frag(lds_ld128(kb + 1024 + lane * 16));
        const bf16x8 zf = as_frag(lds_ld128(kb + 1024 + (g * 16 + 12) * 16));
#pragma unroll
        for (int t = 0; t < 2; ++t) {
          const f32x4 n0 = mfma(kv0, as_frag(pqf[t][h]), z4);
          const f32x4 n1 = mfma(kv1, as_frag(pqf[t][h]), z4);
          const f32x4 dn = mfma(zf, as_frag(pqf[t][h]), z4);
          const float s = omg[t] * __builtin_amdgcn_rcpf(dn[0] + 1e-6f);
          const u32x2 a = pk4(n0 * s), b = pk4(n1 * s);
          __builtin_amdgcn_raw_buffer_store_b128((u32x4){a[0], a[1], g3 ? 0u : b[0], g3 ? 0u : b[1]}, wsr,
                                                 ob + (t * 32 + h) * 1024, 0, 16);
        }
      }
      STAMP(9);
      VM_WAIT(0);
      if (lane == 0) __hip_atomic_store((gu64*)(ws + WS_FB) + ((rt * 4 + wl) * 16 + hp) * FS, tagO, RLX_AGENT);
      STAMP(10); STAMPC(19);
      STAMP_FLUSH();
      return;
    }
  } else {
    const int rh = w & 1, hh = w >> 1;
    {
      const unsigned gv = ((const unsigned*)gate_w)[tid < 224 ? tid : 0];
      if (w == 3) {
        const u64 e = __hip_atomic_load((gu64*)(ws + WS_E), RLX_AGENT);
        if (lane < 60) *(LAS unsigned*)(uintptr_t)(L0 + SYNC + lane * 4) = 0u;
        *(LAS u64*)(uintptr_t)(L0 + SYNC + SY_TAG) = next_tag(e);
      }
      const unsigned lo = (gv & 0xffffu) * 0x10001u, hi = (gv >> 16) * 0x10001u;
      if (tid < 224) lds_st128(L0 + (br ? B_G : A_G) + tid * 16, (u32x4){lo, lo, hi, hi});
    }
    const unsigned xsw = li * 128 + ((g ^ (li >> 1)) << 4);
    const unsigned wln = (8 * g + 2 * q) * 112 + 8 * p + 56 * hh;

    if (!br) {
      f32x4 aq[4][2], ak[5][2], av[5][2];
#pragma unroll
      for (int t = 0; t < 5; ++t)
#pragma unroll
        for (int f = 0; f < 2; ++f) { ak[t][f] = av[t][f] = z4; if (t < 4) aq[t][f] = z4; }
      const unsigned gl = L0 + A_G + (8 * g + q) * 8;
      const bool p3 = p == 3;
      const unsigned d1 = p3 ? 32u : 112u;
      bf16x8 xf[2][5], fq[2][2], fk[2][2], fv[2][2];
      unsigned xb, wb;
      auto rd = [&](int c, int ks, int part) {
        const unsigned xa = xb ^ (ks * 64), wk = wb + ks * 3584;
        if (part == 0) {
          fk[ks][0] = lds_tr2(wk + WM_B, wk + WM_B + 112);
          xf[ks][0] = as_frag(lds_ld128(xa));
          xf[ks][1] = as_frag(lds_ld128(xa + 2048));
        } else if (part == 1) {
          fv[ks][0] = lds_tr2(wk + 2 * WM_B, wk + 2 * WM_B + 112);
          xf[ks][2] = as_frag(lds_ld128(xa + 4096));
        } else if (part == 2) {
          fq[ks][0] = lds_tr2(wk, wk + 112);
          xf[ks][3] = as_frag(lds_ld128(xa + 6144));
        } else if (part == 3) {
          fk[ks][1] = lds_tr2(wk + WM_B + 32, wk + WM_B + 32 + 112);
          xf[ks][4] = as_frag(lds_ld128(xa + 8192));
        } else if (part == 4) {
          fv[ks][1] = lds_tr2(wk + 2 * WM_B + 32, wk + 2 * WM_B + 32 + 112);
        } else {
          const unsigned a = p3 ? gl + wrap7(c + rot) * 512 + ks * 256 : wk + 32;
          fq[ks][1] = lds_tr2(a, a + d1);
        }
      };
      auto mm = [&](int ks, int part) {
        const int f = part / 3, m = part % 3;
#pragma unroll
        for (int t = 0; t < 5; ++t) {
          if (m == 0) ak[t][f] = mfma(fk[ks][f], xf[ks][t], ak[t][f]);
          if (m == 1) av[t][f] = mfma(xf[ks][t], fv[ks][f], av[t][f]);
          if (m == 2 && t > 0) aq[t - 1][f] = mfma(fq[ks][f], xf[ks][t], aq[t - 1][f]);
        }
      };
#pragma unroll
      for (int c = 0; c < 7; ++c) {
        xb = opaque(L0 + a_slot(c) + rh * 8192 + xsw);
        wb = opaque(L0 + a_slot(c) + A_W + wln);
        LDS_BAR();
        if (c == 0) { STAMP(2); STAMPC(16); }
        if (c == 3) STAMP(3);
        if (c == 6) { STAMP(4); STAMPC(17); }
        SB();
#pragma unroll
        for (int ks = 0; ks < 2; ++ks) {
#pragma unroll
          for (int part = 0; part < 6; ++part) rd(c, ks, part);
          if (ks == 1 || c > 0) {
#pragma unroll
            for (int part = 0; part < 6; ++part) mm(ks ^ 1, part);
            interleave<28, 17>();
          }
          SB();
        }
      }
      tagO = uniform64(*(LAS u64*)(uintptr_t)(L0 + SYNC + SY_TAG));
      mm(1, 0); mm(1, 3); mm(1, 2); mm(1, 5);
      SB();
      mm(1, 1); mm(1, 4);
      STAMP(5);
      const bool odd = li & 1;
      const int kk0 = 4 * g + (li & 1);
      const bool own0 = kk0 <= li, own1 = kk0 + 2 <= li;
      const float sc = 0.18898223650461362f * kLog2e;
      const float ninf = (rt == 0 && rh == 0) ? -__builtin_inff() : 0.f;
      const unsigned ob = WS_O + ((rt * 8 + rh * 4) * 32 + hp * 2 + hh) * 1024 + lane * 16;
      bf16x8 kf[5];
#pragma unroll
      for (int t = 0; t < 5; ++t) kf[t] = frag2(pk4(ak[t][0]), pk4(ak[t][1]));
      f32x4 sp[4], so[4];
#pragma unroll
      for (int t = 0; t < 4; ++t) {
        const u32x2 b = pk4(aq[t][1]);
        const bf16x8 qf = frag2(pk4(aq[t][0]), (u32x2){g3 ? 0u : b[0], g3 ? 0u : b[1]});
        sp[t] = mfma(kf[t], qf, t == 0 ? (f32x4){ninf, ninf, ninf, ninf} : z4);
        so[t] = mfma(kf[t + 1], qf, z4);
      }
      float ug;
      {
        float z0 = aq[0][1][0], z1 = aq[1][1][0], z2 = aq[2][1][0], z3 = aq[3][1][0];
        swap32(z0, z1);
        swap32(z2, z3);
        swap16(z1, z3);
        ug = 1.f + __builtin_amdgcn_exp2f(-kLog2e * z3);
      }
      u32x2 vf[5][2];
#pragma unroll
      for (int t = 0; t < 5; ++t) {
        vf[t][0] = pk4(av[t][0]);
        vf[t][1] = pk4(av[t][1]);
      }
      float l0[4], l1[4], bc[4];
#pragma unroll
      for (int t = 0; t < 4; ++t) {
        float a0 = so[t][0], a2 = so[t][2], b0 = sp[t][0], b2 = sp[t][2];
        const float c0 = own0 ? a0 : b0, c1 = own0 ? so[t][1] : sp[t][1], c2 = own1 ? a2 : b2, c3 = own1 ? so[t][3] : sp[t][3];
        l0[t] = odd ? c1 : c0;
        l1[t] = odd ? c3 : c2;
      }
      rows_bcast4(rows_reduce4<true>(fmax1(l0[0], l1[0]), fmax1(l0[1], l1[1]), fmax1(l0[2], l1[2]), fmax1(l0[3], l1[3])) * -sc, bc);
#pragma unroll
      for (int t = 0; t < 4; ++t) {
        l0[t] = __builtin_amdgcn_exp2f(__builtin_fmaf(l0[t], sc, bc[t]));
        l1[t] = __builtin_amdgcn_exp2f(__builtin_fmaf(l1[t], sc, bc[t]));
      }
      rows_bcast4(__builtin_amdgcn_rcpf(rows_reduce4<false>(l0[0] + l1[0], l0[1] + l1[1], l0[2] + l1[2], l0[3] + l1[3]) * ug), bc);
      const unsigned sh = odd ? 16u : 0u;
      const unsigned mk0 = own0 ? 0u : 0xffffu, mk1 = own1 ? 0u : 0xffffu;
#pragma unroll
      for (int t = 0; t < 4; ++t) {
        const unsigned pe = pk2(l0[t] * bc[t], l1[t] * bc[t]), ph = pe >> 16;
        const bf16x8 pf = as_frag((u32x4){(pe & mk0) << sh, (ph & mk1) << sh, (pe & (mk0 ^ 0xffffu)) << sh, (ph & (mk1 ^ 0xffffu)) << sh});
        const f32x4 r0 = mfma(frag2(vf[t][0], vf[t + 1][0]), pf, z4);
        const f32x4 r1 = mfma(frag2(vf[t][1], vf[t + 1][1]), pf, z4);
        const u32x2 c0 = pk4(r0), c1 = pk4(r1);
        __builtin_amdgcn_raw_buffer_store_b128((u32x4){c0[0], c0[1], g3 ? 0u : c1[0], g3 ? 0u : c1[1]}, wsr, ob + t * 32 * 1024, 0, 16);
      }
      STAMP(9);
      VM_WAIT(0);
      if (lane == 0) __hip_atomic_store((gu64*)(ws + WS_FA) + ((rt * 2 + rh) * 16 + hp * 2 + hh) * FS, tagO, RLX_AGENT);
      STAMP(10); STAMPC(19);
      if (cg >= 7) { STAMP_FLUSH(); return; }
    } else {
      __builtin_amdgcn_s_setprio(2);
      f32x4 ak[4][2], av[4][2];
      {
        const float one = li == 12 ? 1.f : 0.f, i0 = rh ? one : 0.f, i1 = rh ? 0.f : one;
#pragma unroll
        for (int t = 0; t < 4; ++t) {
          ak[t][0] = ak[t][1] = (f32x4){kEps, kEps, kEps, kEps};
          av[t][0] = (f32x4){i0, i0, i0, i0};
          av[t][1] = (f32x4){i1, i1, i1, i1};
        }
      }
      bf16x8 xf[2][4], fw[2][4];
      unsigned xb, wb;
      const bool z0 = p == 3 && rh, z1 = p == 3 && !rh;
      const unsigned zaddr = L0 + SYNC + SY_ZERO;
      auto rd = [&](int ks, int part) {
        const unsigned xa = xb ^ (ks * 64), wk = wb + ks * 3584;
        if (part & 1) {
          const unsigned a = wk + WM_B + (((part >> 1) ^ rh) << 5);
          const bool z = (part >> 1) ? z1 : z0;
          fw[ks][part] = lds_tr2(z ? zaddr : a, z ? zaddr : a + 112);
        } else {
          fw[ks][part] = lds_tr2(wk + (part >> 1) * 32, wk + (part >> 1) * 32 + 112);
        }
        if (part < 2) {
          xf[ks][part * 2] = as_frag(lds_ld128(xa + part * 4096));
          xf[ks][part * 2 + 1] = as_frag(lds_ld128(xa + part * 4096 + 2048));
        }
      };
      auto mm = [&](int ks, int part) {
#pragma unroll
        for (int t = 0; t < 4; ++t) {
          if (part & 1) av[t][part >> 1] = mfma(xf[ks][t], fw[ks][part], av[t][part >> 1]);
          else ak[t][part >> 1] = mfma(xf[ks][t], fw[ks][part], ak[t][part >> 1]);
        }
      };
#pragma unroll
      for (int c = 0; c < 7; ++c) {
        xb = opaque(L0 + b_slot(c) + rh * 8192 + xsw);
        wb = opaque(L0 + b_slot(c) + B_W + wln);
        LDS_BAR();
        if (c == 0) { STAMP(2); STAMPC(16); }
        if (c == 3) STAMP(3);
        if (c == 6) { STAMP(4); STAMPC(17); }
        SB();
#pragma unroll
        for (int ks = 0; ks < 2; ++ks) {
#pragma unroll
          for (int part = 0; part < 4; ++part) rd(ks, part);
          if (ks == 1 || c > 0) {
#pragma unroll
            for (int part = 0; part < 4; ++part) mm(ks ^ 1, part);
            interleave<16, 12>();
          }
          SB();
        }
      }
      tagO = uniform64(*(LAS u64*)(uintptr_t)(L0 + SYNC + SY_TAG));
      mm(1, 0); mm(1, 2);
      SB();
      mm(1, 3); mm(1, 1);
      STAMP(5);
      f32x4 kv[2][2];
      {
        u32x2 pk[4][2], vf[4][2];
#pragma unroll
        for (int t = 0; t < 4; ++t) {
          pk[t][0] = phi4(ak[t][0]);
          pk[t][1] = phi4(ak[t][1]);
          vf[t][0] = pk4(av[t][0]);
          vf[t][1] = pk4(av[t][1]);
        }
#pragma unroll
        for (int e = 1; e >= 0; --e)
#pragma unroll
          for (int d = 0; d < 2; ++d) {
            kv[e][d] = mfma(frag2(pk[0][d], pk[1][d]), frag2(vf[0][e], vf[1][e]), z4);
            kv[e][d] = mfma(frag2(pk[2][d], pk[3][d]), frag2(vf[2][e], vf[3][e]), kv[e][d]);
          }
      }
      lds_st128(L0 + B_EX + w * 2048 + lane * 16, __builtin_bit_cast(u32x4, kv[1][0]));
      lds_st128(L0 + B_EX + w * 2048 + 1024 + lane * 16, __builtin_bit_cast(u32x4, kv[1][1]));
      sig(L0 + SYNC + SY_EX + w * 4);
      wait1(L0 + SYNC + SY_EX + (w ^ 1) * 4);
      const f32x4 s0 = kv[0][0] + __builtin_bit_cast(f32x4, lds_ld128(L0 + B_EX + (w ^ 1) * 2048 + lane * 16));
      const f32x4 s1 = kv[0][1] + __builtin_bit_cast(f32x4, lds_ld128(L0 + B_EX + (w ^ 1) * 2048 + 1024 + lane * 16));
      const unsigned kvb = WS_KVP + (hp * 4 + w) * 16 * 1024 + lane * 16;
      {
        const u32x2 a = pk4(s0), b = pk4(s1);
        __builtin_amdgcn_raw_buffer_store_b128((u32x4){a[0], a[1], b[0], b[1]}, wsr, kvb + rt * 1024, 0, 16);
      }
      STAMP(6);
      VM_WAIT(0);
      gu64* kvf = (gu64*)(ws + WS_FKV) + (hp * 4 + w) * 16 * FS;
      if (lane == 0) __hip_atomic_store(kvf + rt * FS, tagO, RLX_AGENT);
      STAMP(7);
      poll_flags(kvf, lane, 16, tagO);
      STAMP(8);
      u32x4 pr[16];
#pragma unroll
      for (int r = 0; r < 16; ++r) pr[r] = __builtin_amdgcn_raw_buffer_load_b128(wsr, kvb + r * 1024, 0, 16);
      SB();
      float sum[8];
#pragma unroll
      for (int i = 0; i < 8; ++i) sum[i] = 0.f;
#pragma unroll
      for (int r = 0; r < 16; ++r)
#pragma unroll
        for (int i = 0; i < 4; ++i) {
          const unsigned u = pr[r][i];
          sum[2 * i] += __uint_as_float(u << 16);
          sum[2 * i + 1] += __uint_as_float(u & 0xffff0000u);
        }
      lds_st128(L0 + B_KVL + w * 1024 + lane * 16,
                (u32x4){pk2(sum[0], sum[1]), pk2(sum[2], sum[3]), pk2(sum[4], sum[5]), pk2(sum[6], sum[7])});
      sig(L0 + SYNC + SY_KV + w * 4);
      STAMP(10); STAMPC(19);
      STAMP_FLUSH();
      if (bid == 128 && w == 0) {
        __builtin_amdgcn_s_setprio(0);
#pragma nounroll
        for (int k = 0; k < 4; ++k) {
          const int i = (k & 1) * 64 + lane, r = i >> 3, h = i & 7;
          const gu64* f = (k < 2) ? (gu64*)(ws + WS_FA) + (r * 32 + h * 2) * FS : (gu64*)(ws + WS_FB) + (r * 64 + h) * FS;
          poll_flags(f, 0, 1, tagO);
        }
        if (lane == 0) __hip_atomic_store((gu64*)(ws + WS_E), tagO, RLX_AGENT);
      }
      return;
    }
  }

  const unsigned sw = (2 * g + (q >> 1)) & 3;
  f32x4 acc[4] = {z4, z4, z4, z4};
#pragma unroll
  for (int pb = 0; pb < 2; ++pb) {
    if (pb) STAMP(7);
    wait4(L0 + SYNC + (pb ? SY_WOB : SY_WOA));
    const unsigned rb = L0 + (pb * 448 + 4 * g + q) * 128 + 8 * p;
    unsigned wa[4], wa2[4];
#pragma unroll
    for (int v = 0; v < 4; ++v) {
      wa[v] = opaque(rb + ((v ^ sw) << 5));
      wa2[v] = opaque(wa[v] + (g3 ? 0u : 2048u));
    }
    auto wfrag = [&](int h, int ct) {
      const int v = ct ^ (2 * (h & 1));
      return lds_tr2(wa[v] + h * 3584, wa2[v] + h * 3584);
    };
    constexpr int NPRE = 8;
    bf16x8 wf[16][4];
#pragma unroll
    for (int h = 0; h < NPRE; ++h)
#pragma unroll
      for (int ct = 0; ct < 4; ++ct) wf[h][ct] = wfrag(h, ct);
    if (pb) STAMP(12); else STAMP(11);
    if (pb) poll_flags((gu64*)(ws + WS_FB) + (rt2 * 4 + (w >> 1)) * 16 * FS, lane, 8, tagO);
    else poll_flags((gu64*)(ws + WS_FA) + (rt2 * 2 + (w >> 2)) * 16 * FS, lane, 16, tagO);
    if (pb) STAMP(13); else STAMP(6);
    const unsigned ob = WS_O + ((rt2 * 8 + w) * 32 + pb * 16) * 1024 + lane * 16;
    u32x4 of[16];
#pragma unroll
    for (int h = 0; h < 16; ++h) of[h] = __builtin_amdgcn_raw_buffer_load_b128(wsr, ob, ((h + 2 * cg) & 15) * 1024, 16);
    SB();
#pragma unroll
    for (int h = 0; h < 16; ++h) {
#pragma unroll
      for (int ct = 0; ct < 4; ++ct) acc[ct] = mfma(wf[h][ct], as_frag(of[h]), acc[ct]);
      SB();
      if (h + NPRE < 16) {
#pragma unroll
        for (int ct = 0; ct < 4; ++ct) wf[h + NPRE][ct] = wfrag(h + NPRE, ct);
      }
      SB();
    }
  }
  {
    char* orow = out + (rt2 * 128 + w * 16 + li) * XROW + cg * 128 + g * 8;
#pragma unroll
    for (int ct = 0; ct < 4; ++ct) *(u32x2*)(orow + ct * 32) = pk4(acc[ct]);
  }
  STAMP(14);
  STAMP(15);
  STAMP_FLUSH();
}

}

extern "C" void kernel_launch(void* const* d_in, const int* in_sizes, int n_in, void* d_out, int out_size, void* d_ws,
                              size_t ws_size, hipStream_t stream) {
  (void)in_sizes; (void)n_in; (void)out_size; (void)ws_size;
  gated_mix_kernel<<<256, 512, 0, stream>>>((const char*)d_in[0], (const char*)d_in[1], (const char*)d_in[2],
                                            (const char*)d_in[4], (const char*)d_in[5],
                                            (const unsigned short*)d_in[6], (char*)d_out, (char*)d_ws);
}
```

```cpp
#define PRETOUCH 4096
#include <hip/hip_runtime.h>
#include <stdint.h>

typedef __bf16 bf16x2 __attribute__((ext_vector_type(2)));
typedef __bf16 bf16x4 __attribute__((ext_vector_type(4)));
typedef __bf16 bf16x8 __attribute__((ext_vector_type(8)));
typedef float f32x2 __attribute__((ext_vector_type(2)));
typedef float f32x4 __attribute__((ext_vector_type(4)));
typedef unsigned u32x2 __attribute__((ext_vector_type(2)));
typedef unsigned u32x4 __attribute__((ext_vector_type(4)));
typedef unsigned long long u64;
#define LAS __attribute__((address_space(3)))
#define GAS __attribute__((address_space(1)))
typedef GAS u64 gu64;
#define RLX_AGENT __ATOMIC_RELAXED, __HIP_MEMORY_SCOPE_AGENT

namespace {

constexpr int kS = 2048, kD = 448;
constexpr int XROW = kD * 2;
constexpr int WROW = 3 * kD * 2;
constexpr int WM_B = 64 * 112;

constexpr int A_FRONT = 34816, A_SLOT = 144 * 128 + 3 * WM_B, A_W = 144 * 128;
constexpr int A_G = A_FRONT + 3 * A_SLOT;
constexpr int B_SLOT = 128 * 128 + 2 * WM_B, B_W = 128 * 128;
constexpr int B_WQ = 3 * B_SLOT, B_G = B_WQ + 7 * WM_B, B_EX = B_G + 3584, B_KVL = B_EX + 8192;
constexpr int SYNC = 163584;
constexpr int LDS_B = 163840;
static_assert(A_G + 3584 <= SYNC && B_KVL + 4096 <= SYNC && A_FRONT + 2 * A_SLOT == 114688, "lds map");
__host__ __device__ constexpr int a_slot(int c) { return A_FRONT + ((c + 2) % 3) * A_SLOT; }
__host__ __device__ constexpr int b_slot(int c) { return ((c + 2) % 3) * B_SLOT; }
enum { SY_WOA = 0, SY_WOB = 16, SY_WQ1 = 48, SY_WQ2 = 64, SY_EX = 96, SY_KV = 112, SY_ZERO = 192, SY_TAG = 240 };
#define LOOKA 10
#define LOOKB 8

constexpr int WS_E = 0;
#define FS 16
#define PSLEEP 1
constexpr int WS_FA = 4096;
constexpr int WS_FB = WS_FA + 16 * 2 * 16 * 8 * FS;
constexpr int WS_FKV = WS_FB + 16 * 4 * 16 * 8 * FS;
constexpr int WS_KVP = 524288;
constexpr int WS_O = WS_KVP + 8 * 4 * 16 * 1024;
constexpr int WS_END = WS_O + 16 * 8 * 32 * 1024;

constexpr unsigned SPIN_CAP = 1u << 14;

#define STAMP(i) do {} while (0)
#define STAMPC(i) do {} while (0)
#define STAMP_FLUSH() do {} while (0)

__device__ __forceinline__ unsigned pk2(float a, float b) {
  f32x2 v = {a, b};
  bf16x2 r = __builtin_convertvector(v, bf16x2);
  return __builtin_bit_cast(unsigned, r);
}
__device__ __forceinline__ u32x2 pk4(f32x4 v) { return (u32x2){pk2(v[0], v[1]), pk2(v[2], v[3])}; }
__device__ __forceinline__ bf16x8 as_frag(u32x4 v) { return __builtin_bit_cast(bf16x8, v); }
__device__ __forceinline__ bf16x8 frag2(u32x2 a, u32x2 b) { return as_frag((u32x4){a[0], a[1], b[0], b[1]}); }
__device__ __forceinline__ f32x4 mfma(bf16x8 a, bf16x8 b, f32x4 c) {
  return __builtin_amdgcn_mfma_f32_16x16x32_bf16(a, b, c, 0, 0, 0);
}
__device__ __forceinline__ bf16x8 lds_tr2(unsigned a0, unsigned a1) {
  bf16x4 lo = __builtin_amdgcn_ds_read_tr16_b64_v4bf16((LAS bf16x4*)(uintptr_t)a0);
  bf16x4 hi = __builtin_amdgcn_ds_read_tr16_b64_v4bf16((LAS bf16x4*)(uintptr_t)a1);
  return __builtin_shufflevector(lo, hi, 0, 1, 2, 3, 4, 5, 6, 7);
}
__device__ __forceinline__ u32x4 lds_ld128(unsigned a) { return *(LAS u32x4*)(uintptr_t)a; }
__device__ __forceinline__ void lds_st128(unsigned a, u32x4 v) { *(LAS u32x4*)(uintptr_t)a = v; }
__device__ __forceinline__ void lds_st64(unsigned a, u32x2 v) { *(LAS u32x2*)(uintptr_t)a = v; }
__device__ __forceinline__ unsigned opaque(unsigned v) { asm volatile("" : "+v"(v)); return v; }

__device__ __forceinline__ void sig(unsigned a) {
  asm volatile("" ::: "memory");
  *(volatile LAS unsigned*)(uintptr_t)a = 1u;
  asm volatile("" ::: "memory");
  __builtin_amdgcn_sched_barrier(0);
}
__device__ __forceinline__ void wait1(unsigned a) {
  while (__builtin_amdgcn_readfirstlane(*(volatile LAS unsigned*)(uintptr_t)a) == 0u) __builtin_amdgcn_s_sleep(1);
  asm volatile("" ::: "memory");
}
__device__ __forceinline__ void wait4(unsigned a) {
  for (;;) {
    const u32x4 v = *(volatile LAS u32x4*)(uintptr_t)a;
    if (__builtin_amdgcn_readfirstlane(v[0] & v[1] & v[2] & v[3])) break;
    __builtin_amdgcn_s_sleep(1);
  }
  asm volatile("" ::: "memory");
}

__device__ __forceinline__ u32x4 make_srd(const void* p, unsigned bytes) {
  uint64_t a = (uint64_t)p;
  u32x4 r;
  r[0] = __builtin_amdgcn_readfirstlane((unsigned)a);
  r[1] = __builtin_amdgcn_readfirstlane((unsigned)(a >> 32) & 0xffffu);
  r[2] = bytes;
  r[3] = 0x00020000u;
  return r;
}
__device__ __forceinline__ void dma16(u32x4 srd, unsigned voff, unsigned soff, unsigned ldsaddr) {
  asm volatile(
      "s_mov_b32 m0, %3\n\t"
      "s_nop 3\n\t"
      "buffer_load_dwordx4 %0, %1, %2 offen lds"
      :
      : "v"(voff), "s"(srd), "s"(soff), "s"(ldsaddr));
}
#define VM_BAR(N) asm volatile("s_waitcnt vmcnt(" #N ") lgkmcnt(0)\n\ts_barrier" ::: "memory")
#define LDS_BAR() asm volatile("s_waitcnt lgkmcnt(0)\n\ts_barrier" ::: "memory")
#define VM_WAIT(N) asm volatile("s_waitcnt vmcnt(" #N ")" ::: "memory")
template <int N> __device__ __forceinline__ void vm_bar() { asm volatile("s_waitcnt vmcnt(%0) lgkmcnt(0)\n\ts_barrier" ::"n"(N) : "memory"); }
#define SB() __builtin_amdgcn_sched_barrier(0)

__device__ __forceinline__ float fmax1(float a, float b) { return __builtin_amdgcn_fmed3f(a, b, 3.0e38f); }
__device__ __forceinline__ void swap16(float& a, float& b) {
  auto r = __builtin_amdgcn_permlane16_swap(__float_as_uint(a), __float_as_uint(b), false, false);
  const unsigned r0 = r[0], r1 = r[1];
  a = __uint_as_float(r0);
  b = __uint_as_float(r1);
}
__device__ __forceinline__ void swap32(float& a, float& b) {
  auto r = __builtin_amdgcn_permlane32_swap(__float_as_uint(a), __float_as_uint(b), false, false);
  const unsigned r0 = r[0], r1 = r[1];
  a = __uint_as_float(r0);
  b = __uint_as_float(r1);
}
template <bool MAX>
__device__ __forceinline__ float rows_reduce4(float v0, float v1, float v2, float v3) {
  swap32(v0, v1);
  swap32(v2, v3);
  float a = MAX ? fmax1(v0, v1) : v0 + v1, b = MAX ? fmax1(v2, v3) : v2 + v3;
  swap16(a, b);
  return MAX ? fmax1(a, b) : a + b;
}
__device__ __forceinline__ void rows_bcast4(float x, float (&o)[4]) {
  float y = x;
  swap16(x, y);
  o[0] = x; o[1] = x; o[2] = y; o[3] = y;
  swap32(o[0], o[1]);
  swap32(o[2], o[3]);
}
__device__ __forceinline__ float row3_bcast(float v) {
  float a = v;
  swap32(a, v);
  a = v;
  swap16(a, v);
  return v;
}
template <int NM, int ND>
__device__ __forceinline__ void interleave() {
#pragma unroll
  for (int i = 0; i < NM; ++i) {
    __builtin_amdgcn_sched_group_barrier(0x8, 1, 0);
    if (i < ND) {
      __builtin_amdgcn_sched_group_barrier(0x2, 1, 0);
      __builtin_amdgcn_sched_group_barrier(0x100, 1, 0);
    }
  }
}
constexpr float kEps = 1e-6f, kLog2e = 1.44269504088896f;
__device__ __forceinline__ float phi(float u) {
  const float e = __builtin_amdgcn_fmed3f(__builtin_amdgcn_exp2f(__builtin_fmaf(u, kLog2e, -kEps * kLog2e)), 0.f, 1.f);
  return fmax1(u, kEps) + e;
}
__device__ __forceinline__ u32x2 phi4(f32x4 v) { return (u32x2){pk2(phi(v[0]), phi(v[1])), pk2(phi(v[2]), phi(v[3]))}; }
__device__ __forceinline__ float sigmoidf(float z) {
  return __builtin_amdgcn_rcpf(1.f + __builtin_amdgcn_exp2f(-1.44269504088896f * z));
}
__device__ __forceinline__ u64 next_tag(u64 e) {
  const u64 s = ((u64)(unsigned)__builtin_amdgcn_readfirstlane((unsigned)(e >> 32)) << 32) |
                (unsigned)__builtin_amdgcn_readfirstlane((unsigned)e);
  return s * 6364136223846793005ull + 1442695040888963407ull;
}
__device__ __forceinline__ u64 uniform64(u64 e) {
  return ((u64)(unsigned)__builtin_amdgcn_readfirstlane((unsigned)(e >> 32)) << 32) |
         (unsigned)__builtin_amdgcn_readfirstlane((unsigned)e);
}
__device__ __forceinline__ void poll_flags(const gu64* f, int lane, int n, u64 tag) {
  const gu64* p = f + (lane < n ? lane : 0) * FS;
#pragma nounroll
  for (unsigned spins = 0; spins < SPIN_CAP; ++spins) {
    u64 v = __hip_atomic_load(p, RLX_AGENT);
    if (__all(v == tag)) break;
    __builtin_amdgcn_s_sleep(PSLEEP);
  }
  asm volatile("" ::: "memory");
}
__device__ __forceinline__ int wrap7(int c) { return c >= 7 ? c - 7 : c; }

__global__ __launch_bounds__(512, 2) void gated_mix_kernel(const char* __restrict__ x, const char* __restrict__ wqkvA,
                                                           const char* __restrict__ woA, const char* __restrict__ wqkvB,
                                                           const char* __restrict__ woB,
                                                           const unsigned short* __restrict__ gate_w, char* __restrict__ out,
                                                           char* __restrict__ ws) {
  __shared__ __attribute__((aligned(1024))) char lds[LDS_B];
  const unsigned L0 = (unsigned)(uintptr_t)(LAS char*)lds;
  const int bid = blockIdx.x;
  const int xcd = bid & 7, idx = bid >> 3;
  const int xj = xcd >> 1, ci = idx >> 3;
  const int br = ci >> 1;
  const int hp = 2 * (br ? (xj ^ 1) : xj) + (ci & 1);
  const int rt = (xcd & 1) * 8 + (idx & 7);
  const int rot = wrap7(wrap7((idx & 7) + 2 * ci));
  const int cg = idx & 7, rt2 = 2 * xcd + ci;

  const int tid = threadIdx.x;
  const int lane = tid & 63;
  const int w = __builtin_amdgcn_readfirstlane(tid >> 6) & 7;
  const int wl = w & 3;
  const int li = lane & 15, g = lane >> 4, q = (lane >> 2) & 3, p = lane & 3;
  const bool g3 = g == 3;
  const f32x4 z4 = {0.f, 0.f, 0.f, 0.f};
  STAMP(0); STAMPC(18);
  const __amdgpu_buffer_rsrc_t wsr = __builtin_amdgcn_make_buffer_rsrc(ws, 0, WS_END, 0x00020000);
  u64 tagO;
  auto wo_blk = [&](int blk) {
    const unsigned vwo = (lane >> 3) * XROW + cg * 128 + (((lane & 7) ^ (((lane >> 4) & 3) << 1)) << 4);
    const bool isB = blk >= 56;
    int sb = (isB ? blk - 56 : blk) + 7 * cg;
    sb = sb >= 56 ? sb - 56 : sb;
    dma16(make_srd(isB ? woB : woA, kD * XROW), vwo, (unsigned)(sb * 8 * XROW), L0 + blk * 1024);
  };

  if (w >= 4) {
    const u32x4 srdX = make_srd(x, kS * XROW);
    const u32x4 srdW = make_srd(br ? wqkvB : wqkvA, kD * WROW);
    const bool l3 = wl == 3;
    const unsigned vx0 = (lane >> 3) * XROW + (((lane & 7) ^ (lane >> 4)) << 4);
    const unsigned vx1 = vx0 ^ 64u;
    unsigned vw0, vw1;
    {
      const unsigned s0 = wl * 64 + lane, s1 = s0 + 256;
      const unsigned p0 = (s0 * 9363u) >> 16, p1 = (s1 * 9363u) >> 16;
      const unsigned k0 = (p0 & 56) | ((p0 >> 1) & 3) | ((p0 & 1) << 2);
      const unsigned k1 = (p1 & 56) | ((p1 >> 1) & 3) | ((p1 & 1) << 2);
      vw0 = k0 * WROW + hp * 112 + (s0 - p0 * 7) * 16;
      vw1 = k1 * WROW + hp * 112 + (s1 - p1 * 7) * 16;
    }
    if (!br) {
      const u32x4 srd2 = l3 ? srdX : srdW;
      const unsigned v2a = l3 ? vx0 : vw1, v2b = l3 ? vx1 : vw1;
      const int xr = rt * 128 - 16 + wl * 32;
      const unsigned sx01 = (xr < 0 ? 0 : xr) * XROW, sx23 = (xr + 16) * XROW;
      auto chunk1 = [&](int c, int j) {
        const int cc = wrap7(c + rot);
        const unsigned sl = L0 + a_slot(c), sw = cc * (64 * WROW), sx = cc * 128;
        if (j < 4) dma16(srdX, (j & 1) ? vx1 : vx0, (j < 2 ? sx01 : sx23) + (j & 1) * 8 * XROW + sx, sl + (wl * 4 + j) * 1024);
        else if (j < 7) dma16(srdW, vw0, sw + (j - 4) * XROW, sl + A_W + (j - 4) * WM_B + wl * 1024);
        else {
          const int m = j - 7, xb = m == 0 ? 16 : 17;
          dma16(srd2, m == 0 ? v2a : v2b, l3 ? (rt * 128 - 16 + xb * 8) * XROW + sx : sw + m * XROW,
                sl + (l3 ? xb * 1024 : A_W + m * WM_B + (wl + 4) * 1024));
        }
      };
      auto instr = [&](int n) {
        if (n < 70) chunk1(n / 10, n % 10);
        else wo_blk(wl + 4 * (n - 70));
      };
#pragma unroll
      for (int n = 0; n < 10 + LOOKA; ++n) instr(n);
      STAMP(1);
#pragma unroll
      for (int c = 0; c < 7; ++c) {
        vm_bar<LOOKA>();
        if (c < 6) {
#pragma unroll
          for (int n = 0; n < 10; ++n) instr(10 * c + 10 + LOOKA + n);
        }
      }
      if (cg >= 7) { STAMP_FLUSH(); return; }
#pragma unroll
      for (int n = 70 + LOOKA; n < 98; ++n) instr(n);
      tagO = uniform64(*(LAS u64*)(uintptr_t)(L0 + SYNC + SY_TAG));
      VM_WAIT(14);
      sig(L0 + SYNC + SY_WOA + wl * 4);
      VM_WAIT(0);
      sig(L0 + SYNC + SY_WOB + wl * 4);
    } else {
      const unsigned sx0 = (rt * 128 + wl * 32) * XROW;
      const unsigned v2 = l3 ? vw0 : vw1;
      const unsigned sub2 = (l3 ? wl : wl + 4) * 1024;
      auto instr = [&](int n) {
        if (n < 56) {
          const int c = n >> 3, j = n & 7, cc = wrap7(c + rot);
          const unsigned sl = L0 + b_slot(c), sw = cc * (64 * WROW), sx = cc * 128;
          if (j < 4) dma16(srdX, (j & 1) ? vx1 : vx0, sx0 + j * 8 * XROW + sx, sl + (wl * 4 + j) * 1024);
          else dma16(srdW, j < 6 ? vw0 : v2, sw + ((j & 1) + 1) * XROW, sl + B_W + (j & 1) * WM_B + (j < 6 ? wl * 1024 : sub2));
        } else {
          const int c = (n - 56) >> 1;
          dma16(srdW, (n & 1) ? v2 : vw0, wrap7(c + rot) * (64 * WROW), L0 + B_WQ + c * WM_B + ((n & 1) ? sub2 : wl * 1024));
        }
      };
#pragma unroll
      for (int n = 0; n < 8 + LOOKB; ++n) instr(n);
      STAMP(1);
      bf16x8 xq[14][2];
      const unsigned xl = L0 + (wl * 32 + li) * 128 + ((g ^ (li >> 1)) << 4);
#pragma unroll
      for (int c = 0; c < 7; ++c) {
        vm_bar<LOOKB>();
#pragma unroll
        for (int ks = 0; ks < 2; ++ks) {
          const unsigned xa = (xl ^ (ks * 64)) + b_slot(c);
          xq[c * 2 + ks][0] = as_frag(lds_ld128(xa));
          xq[c * 2 + ks][1] = as_frag(lds_ld128(xa + 2048));
        }
#pragma unroll
        for (int n = 8 * c + 8 + LOOKB; n < (c < 6 ? 8 * c + 16 + LOOKB : 70); ++n) instr(n);
      }
      tagO = uniform64(*(LAS u64*)(uintptr_t)(L0 + SYNC + SY_TAG));
      STAMP(4);
      VM_WAIT(0);
      sig(L0 + SYNC + SY_WQ1 + wl * 4);
      wait4(L0 + SYNC + SY_WQ1);
      wait4(L0 + SYNC + SY_EX);
      STAMP(3);
      f32x4 aq[2][2][2];
#pragma unroll
      for (int i = 0; i < 8; ++i) aq[i >> 2][(i >> 1) & 1][i & 1] = (f32x4){kEps, kEps, kEps, kEps};
      const unsigned wb = L0 + B_WQ + (8 * g + 2 * q) * 112 + 8 * p;
      const unsigned gl = L0 + B_G + (8 * g + q) * 8;
      const bool p3 = p == 3;
      const unsigned d1 = p3 ? 32u : 112u;
      bf16x8 fq[2][4];
      auto rdq = [&](int s) {
        const unsigned wk = wb + s * 3584;
        const unsigned a = p3 ? gl + wrap7((s >> 1) + rot) * 512 + (s & 1) * 256 : wk + 32;
        fq[s & 1][0] = lds_tr2(wk, wk + 112);
        fq[s & 1][1] = lds_tr2(a, a + d1);
        fq[s & 1][2] = lds_tr2(wk + 56, wk + 56 + 112);
        fq[s & 1][3] = lds_tr2(wk + 88, wk + 88 + 112);
      };
      rdq(0);
#pragma unroll
      for (int s = 0; s < 14; ++s) {
        SB();
        if (s + 1 < 14) rdq(s + 1);
#pragma unroll
        for (int i = 0; i < 4; ++i) {
          aq[0][i >> 1][i & 1] = mfma(fq[s & 1][i], xq[s][0], aq[0][i >> 1][i & 1]);
          aq[1][i >> 1][i & 1] = mfma(fq[s & 1][i], xq[s][1], aq[1][i >> 1][i & 1]);
        }
        interleave<8, 8>();
      }
      SB();
      STAMP(5);
      u32x4 pqf[2][2];
      float omg[2];
#pragma unroll
      for (int t = 0; t < 2; ++t) {
        omg[t] = 1.f - sigmoidf(row3_bcast(aq[t][0][1][0]));
#pragma unroll
        for (int h = 0; h < 2; ++h) {
          const u32x2 a = phi4(aq[t][h][0]), b = phi4(aq[t][h][1]);
          pqf[t][h] = (u32x4){a[0], a[1], g3 ? 0u : b[0], g3 ? 0u : b[1]};
        }
      }
      STAMP(6);
      wait4(L0 + SYNC + SY_KV);
      STAMP(8);
      const unsigned ob = WS_O + ((rt * 8 + wl * 2) * 32 + 16 + hp * 2) * 1024 + lane * 16;
#pragma unroll
      for (int h = 0; h < 2; ++h) {
        const unsigned kb = L0 + B_KVL + h * 2048;
        const bf16x8 kv0 = as_frag(lds_ld128(kb + lane * 16)), kv1 = as_frag(lds_ld128(kb + 1024 + lane * 16));
        const bf16x8 zf = as_frag(lds_ld128(kb + 1024 + (g * 16 + 12) * 16));
#pragma unroll
        for (int t = 0; t < 2; ++t) {
          const f32x4 n0 = mfma(kv0, as_frag(pqf[t][h]), z4);
          const f32x4 n1 = mfma(kv1, as_frag(pqf[t][h]), z4);
          const f32x4 dn = mfma(zf, as_frag(pqf[t][h]), z4);
          const float s = omg[t] * __builtin_amdgcn_rcpf(dn[0] + 1e-6f);
          const u32x2 a = pk4(n0 * s), b = pk4(n1 * s);
          __builtin_amdgcn_raw_buffer_store_b128((u32x4){a[0], a[1], g3 ? 0u : b[0], g3 ? 0u : b[1]}, wsr,
                                                 ob + (t * 32 + h) * 1024, 0, 16);
        }
      }
      STAMP(9);
      VM_WAIT(0);
      if (lane == 0) __hip_atomic_store((gu64*)(ws + WS_FB) + ((rt * 4 + wl) * 16 + hp) * FS, tagO, RLX_AGENT);
      STAMP(10); STAMPC(19);
      STAMP_FLUSH();
      return;
    }
  } else {
    const int rh = w & 1, hh = w >> 1;
    {
      const unsigned gv = ((const unsigned*)gate_w)[tid < 224 ? tid : 0];
      if (w == 3) {
        const u64 e = __hip_atomic_load((gu64*)(ws + WS_E), RLX_AGENT);
        if (lane < 60) *(LAS unsigned*)(uintptr_t)(L0 + SYNC + lane * 4) = 0u;
        *(LAS u64*)(uintptr_t)(L0 + SYNC + SY_TAG) = next_tag(e);
      }
      const unsigned lo = (gv & 0xffffu) * 0x10001u, hi = (gv >> 16) * 0x10001u;
      if (tid < 224) lds_st128(L0 + (br ? B_G : A_G) + tid * 16, (u32x4){lo, lo, hi, hi});
    }
    {
      unsigned t0 = 0, t1 = 0;
      typedef GAS unsigned gu32;
      const int st = PRETOUCH;
      if (!br) {
        if (w == 0) { const int o = lane * st; t0 = __hip_atomic_load((gu32*)(ws + WS_O + rt2 * 262144 + (o < 262144 ? o : 0)), RLX_AGENT); }
        if (w == 1) { const int o = lane * st; t0 = __hip_atomic_load((gu32*)(out + rt2 * 128 * XROW + (o < 128 * XROW ? o : 0)), RLX_AGENT);
                      t1 = __hip_atomic_load((gu32*)(ws + WS_O + (rt * 8 + (lane & 7)) * 32768 + hp * 2048), RLX_AGENT); }
        if (w == 2) { const int o = lane * st; t0 = __hip_atomic_load((gu32*)(ws + WS_FA + (o < WS_FKV - WS_FA ? o : 0)), RLX_AGENT);
                      const int o2 = lane * st * 2; t1 = *(const unsigned*)((lane & 1 ? woA : woB) + (o2 < kD * XROW ? o2 : 0)); }
      } else {
        if (w == 0) { const int o = lane * st; t0 = __hip_atomic_load((gu32*)(ws + WS_KVP + hp * 65536 + (o < 65536 ? o : 0)), RLX_AGENT); }
        if (w == 1) { t0 = __hip_atomic_load((gu32*)(ws + WS_O + (rt * 8 + (lane & 7)) * 32768 + 16384 + hp * 2048), RLX_AGENT); }
        if (w == 2) { t0 = __hip_atomic_load((gu32*)(ws + (lane & 1 ? WS_FB + (rt * 64 + (lane & 48)) * 8 * FS : WS_FKV + (hp * 64 + (lane & 48)) * 8 * FS)), RLX_AGENT); }
      }
      asm volatile("" ::"v"(t0), "v"(t1));
    }
    const unsigned xsw = li * 128 + ((g ^ (li >> 1)) << 4);
    const unsigned wln = (8 * g + 2 * q) * 112 + 8 * p + 56 * hh;

    if (!br) {
      f32x4 aq[4][2], ak[5][2], av[5][2];
#pragma unroll
      for (int t = 0; t < 5; ++t)
#pragma unroll
        for (int f = 0; f < 2; ++f) { ak[t][f] = av[t][f] = z4; if (t < 4) aq[t][f] = z4; }
      const unsigned gl = L0 + A_G + (8 * g + q) * 8;
      const bool p3 = p == 3;
      const unsigned d1 = p3 ? 32u : 112u;
      bf16x8 xf[2][5], fq[2][2], fk[2][2], fv[2][2];
      unsigned xb, wb;
      auto rd = [&](int c, int ks, int part) {
        const unsigned xa = xb ^ (ks * 64), wk = wb + ks * 3584;
        if (part == 0) {
          fk[ks][0] = lds_tr2(wk + WM_B, wk + WM_B + 112);
          xf[ks][0] = as_frag(lds_ld128(xa));
          xf[ks][1] = as_frag(lds_ld128(xa + 2048));
        } else if (part == 1) {
          fv[ks][0] = lds_tr2(wk + 2 * WM_B, wk + 2 * WM_B + 112);
          xf[ks][2] = as_frag(lds_ld128(xa + 4096));
        } else if (part == 2) {
          fq[ks][0] = lds_tr2(wk, wk + 112);
          xf[ks][3] = as_frag(lds_ld128(xa + 6144));
        } else if (part == 3) {
          fk[ks][1] = lds_tr2(wk + WM_B + 32, wk + WM_B + 32 + 112);
          xf[ks][4] = as_frag(lds_ld128(xa + 8192));
        } else if (part == 4) {
          fv[ks][1] = lds_tr2(wk + 2 * WM_B + 32, wk + 2 * WM_B + 32 + 112);
        } else {
          const unsigned a = p3 ? gl + wrap7(c + rot) * 512 + ks * 256 : wk + 32;
          fq[ks][1] = lds_tr2(a, a + d1);
        }
      };
      auto mm = [&](int ks, int part) {
        const int f = part / 3, m = part % 3;
#pragma unroll
        for (int t = 0; t < 5; ++t) {
          if (m == 0) ak[t][f] = mfma(fk[ks][f], xf[ks][t], ak[t][f]);
          if (m == 1) av[t][f] = mfma(xf[ks][t], fv[ks][f], av[t][f]);
          if (m == 2 && t > 0) aq[t - 1][f] = mfma(fq[ks][f], xf[ks][t], aq[t - 1][f]);
        }
      };
#pragma unroll
      for (int c = 0; c < 7; ++c) {
        xb = opaque(L0 + a_slot(c) + rh * 8192 + xsw);
        wb = opaque(L0 + a_slot(c) + A_W + wln);
        LDS_BAR();
        if (c == 0) { STAMP(2); STAMPC(16); }
        if (c == 3) STAMP(3);
        if (c == 6) { STAMP(4); STAMPC(17); }
        SB();
#pragma unroll
        for (int ks = 0; ks < 2; ++ks) {
#pragma unroll
          for (int part = 0; part < 6; ++part) rd(c, ks, part);
          if (ks == 1 || c > 0) {
#pragma unroll
            for (int part = 0; part < 6; ++part) mm(ks ^ 1, part);
            interleave<28, 17>();
          }
          SB();
        }
      }
      tagO = uniform64(*(LAS u64*)(uintptr_t)(L0 + SYNC + SY_TAG));
      mm(1, 0); mm(1, 3); mm(1, 2); mm(1, 5);
      SB();
      mm(1, 1); mm(1, 4);
      STAMP(5);
      const bool odd = li & 1;
      const int kk0 = 4 * g + (li & 1);
      const bool own0 = kk0 <= li, own1 = kk0 + 2 <= li;
      const float sc = 0.18898223650461362f * kLog2e;
      const float ninf = (rt == 0 && rh == 0) ? -__builtin_inff() : 0.f;
      const unsigned ob = WS_O + ((rt * 8 + rh * 4) * 32 + hp * 2 + hh) * 1024 + lane * 16;
      bf16x8 kf[5];
#pragma unroll
      for (int t = 0; t < 5; ++t) kf[t] = frag2(pk4(ak[t][0]), pk4(ak[t][1]));
      f32x4 sp[4], so[4];
#pragma unroll
      for (int t = 0; t < 4; ++t) {
        const u32x2 b = pk4(aq[t][1]);
        const bf16x8 qf = frag2(pk4(aq[t][0]), (u32x2){g3 ? 0u : b[0], g3 ? 0u : b[1]});
        sp[t] = mfma(kf[t], qf, t == 0 ? (f32x4){ninf, ninf, ninf, ninf} : z4);
        so[t] = mfma(kf[t + 1], qf, z4);
      }
      float ug;
      {
        float z0 = aq[0][1][0], z1 = aq[1][1][0], z2 = aq[2][1][0], z3 = aq[3][1][0];
        swap32(z0, z1);
        swap32(z2, z3);
        swap16(z1, z3);
        ug = 1.f + __builtin_amdgcn_exp2f(-kLog2e * z3);
      }
      u32x2 vf[5][2];
#pragma unroll
      for (int t = 0; t < 5; ++t) {
        vf[t][0] = pk4(av[t][0]);
        vf[t][1] = pk4(av[t][1]);
      }
      float l0[4], l1[4], bc[4];
#pragma unroll
      for (int t = 0; t < 4; ++t) {
        float a0 = so[t][0], a2 = so[t][2], b0 = sp[t][0], b2 = sp[t][2];
        const float c0 = own0 ? a0 : b0, c1 = own0 ? so[t][1] : sp[t][1], c2 = own1 ? a2 : b2, c3 = own1 ? so[t][3] : sp[t][3];
        l0[t] = odd ? c1 : c0;
        l1[t] = odd ? c3 : c2;
      }
      rows_bcast4(rows_reduce4<true>(fmax1(l0[0], l1[0]), fmax1(l0[1], l1[1]), fmax1(l0[2], l1[2]), fmax1(l0[3], l1[3])) * -sc, bc);
#pragma unroll
      for (int t = 0; t < 4; ++t) {
        l0[t] = __builtin_amdgcn_exp2f(__builtin_fmaf(l0[t], sc, bc[t]));
        l1[t] = __builtin_amdgcn_exp2f(__builtin_fmaf(l1[t], sc, bc[t]));
      }
      rows_bcast4(__builtin_amdgcn_rcpf(rows_reduce4<false>(l0[0] + l1[0], l0[1] + l1[1], l0[2] + l1[2], l0[3] + l1[3]) * ug), bc);
      const unsigned sh = odd ? 16u : 0u;
      const unsigned mk0 = own0 ? 0u : 0xffffu, mk1 = own1 ? 0u : 0xffffu;
#pragma unroll
      for (int t = 0; t < 4; ++t) {
        const unsigned pe = pk2(l0[t] * bc[t], l1[t] * bc[t]), ph = pe >> 16;
        const bf16x8 pf = as_frag((u32x4){(pe & mk0) << sh, (ph & mk1) << sh, (pe & (mk0 ^ 0xffffu)) << sh, (ph & (mk1 ^ 0xffffu)) << sh});
        const f32x4 r0 = mfma(frag2(vf[t][0], vf[t + 1][0]), pf, z4);
        const f32x4 r1 = mfma(frag2(vf[t][1], vf[t + 1][1]), pf, z4);
        const u32x2 c0 = pk4(r0), c1 = pk4(r1);
        __builtin_amdgcn_raw_buffer_store_b128((u32x4){c0[0], c0[1], g3 ? 0u : c1[0], g3 ? 0u : c1[1]}, wsr, ob + t * 32 * 1024, 0, 16);
      }
      STAMP(9);
      VM_WAIT(0);
      if (lane == 0) __hip_atomic_store((gu64*)(ws + WS_FA) + ((rt * 2 + rh) * 16 + hp * 2 + hh) * FS, tagO, RLX_AGENT);
      STAMP(10); STAMPC(19);
      if (cg >= 7) { STAMP_FLUSH(); return; }
    } else {
      __builtin_amdgcn_s_setprio(2);
      f32x4 ak[4][2], av[4][2];
      {
        const float one = li == 12 ? 1.f : 0.f, i0 = rh ? one : 0.f, i1 = rh ? 0.f : one;
#pragma unroll
        for (int t = 0; t < 4; ++t) {
          ak[t][0] = ak[t][1] = (f32x4){kEps, kEps, kEps, kEps};
          av[t][0] = (f32x4){i0, i0, i0, i0};
          av[t][1] = (f32x4){i1, i1, i1, i1};
        }
      }
      bf16x8 xf[2][4], fw[2][4];
      unsigned xb, wb;
      const bool z0 = p == 3 && rh, z1 = p == 3 && !rh;
      const unsigned zaddr = L0 + SYNC + SY_ZERO;
      auto rd = [&](int ks, int part) {
        const unsigned xa = xb ^ (ks * 64), wk = wb + ks * 3584;
        if (part & 1) {
          const unsigned a = wk + WM_B + (((part >> 1) ^ rh) << 5);
          const bool z = (part >> 1) ? z1 : z0;
          fw[ks][part] = lds_tr2(z ? zaddr : a, z ? zaddr : a + 112);
        } else {
          fw[ks][part] = lds_tr2(wk + (part >> 1) * 32, wk + (part >> 1) * 32 + 112);
        }
        if (part < 2) {
          xf[ks][part * 2] = as_frag(lds_ld128(xa + part * 4096));
          xf[ks][part * 2 + 1] = as_frag(lds_ld128(xa + part * 4096 + 2048));
        }
      };
      auto mm = [&](int ks, int part) {
#pragma unroll
        for (int t = 0; t < 4; ++t) {
          if (part & 1) av[t][part >> 1] = mfma(xf[ks][t], fw[ks][part], av[t][part >> 1]);
          else ak[t][part >> 1] = mfma(xf[ks][t], fw[ks][part], ak[t][part >> 1]);
        }
      };
#pragma unroll
      for (int c = 0; c < 7; ++c) {
        xb = opaque(L0 + b_slot(c) + rh * 8192 + xsw);
        wb = opaque(L0 + b_slot(c) + B_W + wln);
        LDS_BAR();
        if (c == 0) { STAMP(2); STAMPC(16); }
        if (c == 3) STAMP(3);
        if (c == 6) { STAMP(4); STAMPC(17); }
        SB();
#pragma unroll
        for (int ks = 0; ks < 2; ++ks) {
#pragma unroll
          for (int part = 0; part < 4; ++part) rd(ks, part);
          if (ks == 1 || c > 0) {
#pragma unroll
            for (int part = 0; part < 4; ++part) mm(ks ^ 1, part);
            interleave<16, 12>();
          }
          SB();
        }
      }
      tagO = uniform64(*(LAS u64*)(uintptr_t)(L0 + SYNC + SY_TAG));
      mm(1, 0); mm(1, 2);
      SB();
      mm(1, 3); mm(1, 1);
      STAMP(5);
      f32x4 kv[2][2];
      {
        u32x2 pk[4][2], vf[4][2];
#pragma unroll
        for (int t = 0; t < 4; ++t) {
          pk[t][0] = phi4(ak[t][0]);
          pk[t][1] = phi4(ak[t][1]);
          vf[t][0] = pk4(av[t][0]);
          vf[t][1] = pk4(av[t][1]);
        }
#pragma unroll
        for (int e = 1; e >= 0; --e)
#pragma unroll
          for (int d = 0; d < 2; ++d) {
            kv[e][d] = mfma(frag2(pk[0][d], pk[1][d]), frag2(vf[0][e], vf[1][e]), z4);
            kv[e][d] = mfma(frag2(pk[2][d], pk[3][d]), frag2(vf[2][e], vf[3][e]), kv[e][d]);
          }
      }
      lds_st128(L0 + B_EX + w * 2048 + lane * 16, __builtin_bit_cast(u32x4, kv[1][0]));
      lds_st128(L0 + B_EX + w * 2048 + 1024 + lane * 16, __builtin_bit_cast(u32x4, kv[1][1]));
      sig(L0 + SYNC + SY_EX + w * 4);
      wait1(L0 + SYNC + SY_EX + (w ^ 1) * 4);
      const f32x4 s0 = kv[0][0] + __builtin_bit_cast(f32x4, lds_ld128(L0 + B_EX + (w ^ 1) * 2048 + lane * 16));
      const f32x4 s1 = kv[0][1] + __builtin_bit_cast(f32x4, lds_ld128(L0 + B_EX + (w ^ 1) * 2048 + 1024 + lane * 16));
      const unsigned kvb = WS_KVP + (hp * 4 + w) * 16 * 1024 + lane * 16;
      {
        const u32x2 a = pk4(s0), b = pk4(s1);
        __builtin_amdgcn_raw_buffer_store_b128((u32x4){a[0], a[1], b[0], b[1]}, wsr, kvb + rt * 1024, 0, 16);
      }
      STAMP(6);
      VM_WAIT(0);
      gu64* kvf = (gu64*)(ws + WS_FKV) + (hp * 4 + w) * 16 * FS;
      if (lane == 0) __hip_atomic_store(kvf + rt * FS, tagO, RLX_AGENT);
      STAMP(7);
      poll_flags(kvf, lane, 16, tagO);
      STAMP(8);
      u32x4 pr[16];
#pragma unroll
      for (int r = 0; r < 16; ++r) pr[r] = __builtin_amdgcn_raw_buffer_load_b128(wsr, kvb + r * 1024, 0, 16);
      SB();
      float sum[8];
#pragma unroll
      for (int i = 0; i < 8; ++i) sum[i] = 0.f;
#pragma unroll
      for (int r = 0; r < 16; ++r)
#pragma unroll
        for (int i = 0; i < 4; ++i) {
          const unsigned u = pr[r][i];
          sum[2 * i] += __uint_as_float(u << 16);
          sum[2 * i + 1] += __uint_as_float(u & 0xffff0000u);
        }
      lds_st128(L0 + B_KVL + w * 1024 + lane * 16,
                (u32x4){pk2(sum[0], sum[1]), pk2(sum[2], sum[3]), pk2(sum[4], sum[5]), pk2(sum[6], sum[7])});
      sig(L0 + SYNC + SY_KV + w * 4);
      STAMP(10); STAMPC(19);
      STAMP_FLUSH();
      if (bid == 128 && w == 0) {
        __builtin_amdgcn_s_setprio(0);
#pragma nounroll
        for (int k = 0; k < 4; ++k) {
          const int i = (k & 1) * 64 + lane, r = i >> 3, h = i & 7;
          const gu64* f = (k < 2) ? (gu64*)(ws + WS_FA) + (r * 32 + h * 2) * FS : (gu64*)(ws + WS_FB) + (r * 64 + h) * FS;
          poll_flags(f, 0, 1, tagO);
        }
        if (lane == 0) __hip_atomic_store((gu64*)(ws + WS_E), tagO, RLX_AGENT);
      }
      return;
    }
  }

  const unsigned sw = (2 * g + (q >> 1)) & 3;
  f32x4 acc[4] = {z4, z4, z4, z4};
#pragma unroll
  for (int pb = 0; pb < 2; ++pb) {
    if (pb) STAMP(7);
    wait4(L0 + SYNC + (pb ? SY_WOB : SY_WOA));
    const unsigned rb = L0 + (pb * 448 + 4 * g + q) * 128 + 8 * p;
    unsigned wa[4], wa2[4];
#pragma unroll
    for (int v = 0; v < 4; ++v) {
      wa[v] = opaque(rb + ((v ^ sw) << 5));
      wa2[v] = opaque(wa[v] + (g3 ? 0u : 2048u));
    }
    auto wfrag = [&](int h, int ct) {
      const int v = ct ^ (2 * (h & 1));
      return lds_tr2(wa[v] + h * 3584, wa2[v] + h * 3584);
    };
    constexpr int NPRE = 8;
    bf16x8 wf[16][4];
#pragma unroll
    for (int h = 0; h < NPRE; ++h)
#pragma unroll
      for (int ct = 0; ct < 4; ++ct) wf[h][ct] = wfrag(h, ct);
    if (pb) STAMP(12); else STAMP(11);
    if (pb) poll_flags((gu64*)(ws + WS_FB) + (rt2 * 4 + (w >> 1)) * 16 * FS, lane, 8, tagO);
    else poll_flags((gu64*)(ws + WS_FA) + (rt2 * 2 + (w >> 2)) * 16 * FS, lane, 16, tagO);
    if (pb) STAMP(13); else STAMP(6);
    const unsigned ob = WS_O + ((rt2 * 8 + w) * 32 + pb * 16) * 1024 + lane * 16;
    u32x4 of[16];
#pragma unroll
    for (int h = 0; h < 16; ++h) of[h] = __builtin_amdgcn_raw_buffer_load_b128(wsr, ob, ((h + 2 * cg) & 15) * 1024, 16);
    SB();
#pragma unroll
    for (int h = 0; h < 16; ++h) {
#pragma unroll
      for (int ct = 0; ct < 4; ++ct) acc[ct] = mfma(wf[h][ct], as_frag(of[h]), acc[ct]);
      SB();
      if (h + NPRE < 16) {
#pragma unroll
        for (int ct = 0; ct < 4; ++ct) wf[h + NPRE][ct] = wfrag(h + NPRE, ct);
      }
      SB();
    }
  }
  {
    char* orow = out + (rt2 * 128 + w * 16 + li) * XROW + cg * 128 + g * 8;
#pragma unroll
    for (int ct = 0; ct < 4; ++ct) *(u32x2*)(orow + ct * 32) = pk4(acc[ct]);
  }
  STAMP(14);
  STAMP(15);
  STAMP_FLUSH();
}

}

extern "C" void kernel_launch(void* const* d_in, const int* in_sizes, int n_in, void* d_out, int out_size, void* d_ws,
                              size_t ws_size, hipStream_t stream) {
  (void)in_sizes; (void)n_in; (void)out_size; (void)ws_size;
  gated_mix_kernel<<<256, 512, 0, stream>>>((const char*)d_in[0], (const char*)d_in[1], (const char*)d_in[2],
                                            (const char*)d_in[4], (const char*)d_in[5],
                                            (const unsigned short*)d_in[6], (char*)d_out, (char*)d_ws);
}
```

```cpp
#define DIAG_A 0
#define DIAG_B 0
#define DIAG_ROLE 0
#include <hip/hip_runtime.h>
#include <stdint.h>

typedef __bf16 bf16x2 __attribute__((ext_vector_type(2)));
typedef __bf16 bf16x4 __attribute__((ext_vector_type(4)));
typedef __bf16 bf16x8 __attribute__((ext_vector_type(8)));
typedef float f32x2 __attribute__((ext_vector_type(2)));
typedef float f32x4 __attribute__((ext_vector_type(4)));
typedef unsigned u32x2 __attribute__((ext_vector_type(2)));
typedef unsigned u32x4 __attribute__((ext_vector_type(4)));
typedef unsigned long long u64;
#define LAS __attribute__((address_space(3)))
#define GAS __attribute__((address_space(1)))
typedef GAS u64 gu64;
#define RLX_AGENT __ATOMIC_RELAXED, __HIP_MEMORY_SCOPE_AGENT

namespace {

constexpr int kS = 2048, kD = 448;
constexpr int XROW = kD * 2;
constexpr int WROW = 3 * kD * 2;
constexpr int WM_B = 64 * 112;

constexpr int A_FRONT = 34816, A_SLOT = 144 * 128 + 3 * WM_B, A_W = 144 * 128;
constexpr int A_G = A_FRONT + 3 * A_SLOT;
constexpr int B_SLOT = 128 * 128 + 2 * WM_B, B_W = 128 * 128;
constexpr int B_WQ = 3 * B_SLOT, B_G = B_WQ + 7 * WM_B, B_EX = B_G + 3584, B_KVL = B_EX + 8192;
constexpr int SYNC = 163584;
constexpr int LDS_B = 163840;
static_assert(A_G + 3584 <= SYNC && B_KVL + 4096 <= SYNC && A_FRONT + 2 * A_SLOT == 114688, "lds map");
__host__ __device__ constexpr int a_slot(int c) { return A_FRONT + ((c + 2) % 3) * A_SLOT; }
__host__ __device__ constexpr int b_slot(int c) { return ((c + 2) % 3) * B_SLOT; }
enum { SY_WOA = 0, SY_WOB = 16, SY_WQ1 = 48, SY_WQ2 = 64, SY_EX = 96, SY_KV = 112, SY_ZERO = 192, SY_TAG = 240 };
#define LOOKA 10
#define LOOKB 8

constexpr int WS_E = 0;
#define FS 16
#define PSLEEP 1
constexpr int WS_FA = 4096;
constexpr int WS_FB = WS_FA + 16 * 2 * 16 * 8 * FS;
constexpr int WS_FKV = WS_FB + 16 * 4 * 16 * 8 * FS;
constexpr int WS_KVP = 524288;
constexpr int WS_O = WS_KVP + 8 * 4 * 16 * 1024;
constexpr int WS_END = WS_O + 16 * 8 * 32 * 1024;

constexpr unsigned SPIN_CAP = 1u << 14;

#define STAMP(i) do { if (i == DIAG_A) dta = (unsigned)__builtin_amdgcn_s_memrealtime(); if (i == DIAG_B) dtb = (unsigned)__builtin_amdgcn_s_memrealtime(); } while (0)
#define STAMPC(i) do {} while (0)
#define STAMP_FLUSH() do { if (br * 2 + (w >> 2) == DIAG_ROLE) { unsigned d = (dtb && dta) ? (dtb - dta) * 8u : 0u; d = d > 100000u ? 100000u : d; const unsigned t0 = dt0; while ((unsigned)__builtin_amdgcn_s_memrealtime() - t0 < 4000u + d) __builtin_amdgcn_s_sleep(2); } } while (0)

__device__ __forceinline__ unsigned pk2(float a, float b) {
  f32x2 v = {a, b};
  bf16x2 r = __builtin_convertvector(v, bf16x2);
  return __builtin_bit_cast(unsigned, r);
}
__device__ __forceinline__ u32x2 pk4(f32x4 v) { return (u32x2){pk2(v[0], v[1]), pk2(v[2], v[3])}; }
__device__ __forceinline__ bf16x8 as_frag(u32x4 v) { return __builtin_bit_cast(bf16x8, v); }
__device__ __forceinline__ bf16x8 frag2(u32x2 a, u32x2 b) { return as_frag((u32x4){a[0], a[1], b[0], b[1]}); }
__device__ __forceinline__ f32x4 mfma(bf16x8 a, bf16x8 b, f32x4 c) {
  return __builtin_amdgcn_mfma_f32_16x16x32_bf16(a, b, c, 0, 0, 0);
}
__device__ __forceinline__ bf16x8 lds_tr2(unsigned a0, unsigned a1) {
  bf16x4 lo = __builtin_amdgcn_ds_read_tr16_b64_v4bf16((LAS bf16x4*)(uintptr_t)a0);
  bf16x4 hi = __builtin_amdgcn_ds_read_tr16_b64_v4bf16((LAS bf16x4*)(uintptr_t)a1);
  return __builtin_shufflevector(lo, hi, 0, 1, 2, 3, 4, 5, 6, 7);
}
__device__ __forceinline__ u32x4 lds_ld128(unsigned a) { return *(LAS u32x4*)(uintptr_t)a; }
__device__ __forceinline__ void lds_st128(unsigned a, u32x4 v) { *(LAS u32x4*)(uintptr_t)a = v; }
__device__ __forceinline__ void lds_st64(unsigned a, u32x2 v) { *(LAS u32x2*)(uintptr_t)a = v; }
__device__ __forceinline__ unsigned opaque(unsigned v) { asm volatile("" : "+v"(v)); return v; }

__device__ __forceinline__ void sig(unsigned a) {
  asm volatile("" ::: "memory");
  *(volatile LAS unsigned*)(uintptr_t)a = 1u;
  asm volatile("" ::: "memory");
  __builtin_amdgcn_sched_barrier(0);
}
__device__ __forceinline__ void wait1(unsigned a) {
  while (__builtin_amdgcn_readfirstlane(*(volatile LAS unsigned*)(uintptr_t)a) == 0u) __builtin_amdgcn_s_sleep(1);
  asm volatile("" ::: "memory");
}
__device__ __forceinline__ void wait4(unsigned a) {
  for (;;) {
    const u32x4 v = *(volatile LAS u32x4*)(uintptr_t)a;
    if (__builtin_amdgcn_readfirstlane(v[0] & v[1] & v[2] & v[3])) break;
    __builtin_amdgcn_s_sleep(1);
  }
  asm volatile("" ::: "memory");
}

__device__ __forceinline__ u32x4 make_srd(const void* p, unsigned bytes) {
  uint64_t a = (uint64_t)p;
  u32x4 r;
  r[0] = __builtin_amdgcn_readfirstlane((unsigned)a);
  r[1] = __builtin_amdgcn_readfirstlane((unsigned)(a >> 32) & 0xffffu);
  r[2] = bytes;
  r[3] = 0x00020000u;
  return r;
}
__device__ __forceinline__ void dma16(u32x4 srd, unsigned voff, unsigned soff, unsigned ldsaddr) {
  asm volatile(
      "s_mov_b32 m0, %3\n\t"
      "s_nop 3\n\t"
      "buffer_load_dwordx4 %0, %1, %2 offen lds"
      :
      : "v"(voff), "s"(srd), "s"(soff), "s"(ldsaddr));
}
#define VM_BAR(N) asm volatile("s_waitcnt vmcnt(" #N ") lgkmcnt(0)\n\ts_barrier" ::: "memory")
#define LDS_BAR() asm volatile("s_waitcnt lgkmcnt(0)\n\ts_barrier" ::: "memory")
#define VM_WAIT(N) asm volatile("s_waitcnt vmcnt(" #N ")" ::: "memory")
template <int N> __device__ __forceinline__ void vm_bar() { asm volatile("s_waitcnt vmcnt(%0) lgkmcnt(0)\n\ts_barrier" ::"n"(N) : "memory"); }
#define SB() __builtin_amdgcn_sched_barrier(0)

__device__ __forceinline__ float fmax1(float a, float b) { return __builtin_amdgcn_fmed3f(a, b, 3.0e38f); }
__device__ __forceinline__ void swap16(float& a, float& b) {
  auto r = __builtin_amdgcn_permlane16_swap(__float_as_uint(a), __float_as_uint(b), false, false);
  const unsigned r0 = r[0], r1 = r[1];
  a = __uint_as_float(r0);
  b = __uint_as_float(r1);
}
__device__ __forceinline__ void swap32(float& a, float& b) {
  auto r = __builtin_amdgcn_permlane32_swap(__float_as_uint(a), __float_as_uint(b), false, false);
  const unsigned r0 = r[0], r1 = r[1];
  a = __uint_as_float(r0);
  b = __uint_as_float(r1);
}
template <bool MAX>
__device__ __forceinline__ float rows_reduce4(float v0, float v1, float v2, float v3) {
  swap32(v0, v1);
  swap32(v2, v3);
  float a = MAX ? fmax1(v0, v1) : v0 + v1, b = MAX ? fmax1(v2, v3) : v2 + v3;
  swap16(a, b);
  return MAX ? fmax1(a, b) : a + b;
}
__device__ __forceinline__ void rows_bcast4(float x, float (&o)[4]) {
  float y = x;
  swap16(x, y);
  o[0] = x; o[1] = x; o[2] = y; o[3] = y;
  swap32(o[0], o[1]);
  swap32(o[2], o[3]);
}
__device__ __forceinline__ float row3_bcast(float v) {
  float a = v;
  swap32(a, v);
  a = v;
  swap16(a, v);
  return v;
}
template <int NM, int ND>
__device__ __forceinline__ void interleave() {
#pragma unroll
  for (int i = 0; i < NM; ++i) {
    __builtin_amdgcn_sched_group_barrier(0x8, 1, 0);
    if (i < ND) {
      __builtin_amdgcn_sched_group_barrier(0x2, 1, 0);
      __builtin_amdgcn_sched_group_barrier(0x100, 1, 0);
    }
  }
}
constexpr float kEps = 1e-6f, kLog2e = 1.44269504088896f;
__device__ __forceinline__ float phi(float u) {
  const float e = __builtin_amdgcn_fmed3f(__builtin_amdgcn_exp2f(__builtin_fmaf(u, kLog2e, -kEps * kLog2e)), 0.f, 1.f);
  return fmax1(u, kEps) + e;
}
__device__ __forceinline__ u32x2 phi4(f32x4 v) { return (u32x2){pk2(phi(v[0]), phi(v[1])), pk2(phi(v[2]), phi(v[3]))}; }
__device__ __forceinline__ float sigmoidf(float z) {
  return __builtin_amdgcn_rcpf(1.f + __builtin_amdgcn_exp2f(-1.44269504088896f * z));
}
__device__ __forceinline__ u64 next_tag(u64 e) {
  const u64 s = ((u64)(unsigned)__builtin_amdgcn_readfirstlane((unsigned)(e >> 32)) << 32) |
                (unsigned)__builtin_amdgcn_readfirstlane((unsigned)e);
  return s * 6364136223846793005ull + 1442695040888963407ull;
}
__device__ __forceinline__ u64 uniform64(u64 e) {
  return ((u64)(unsigned)__builtin_amdgcn_readfirstlane((unsigned)(e >> 32)) << 32) |
         (unsigned)__builtin_amdgcn_readfirstlane((unsigned)e);
}
__device__ __forceinline__ void poll_flags(const gu64* f, int lane, int n, u64 tag) {
  const gu64* p = f + (lane < n ? lane : 0) * FS;
#pragma nounroll
  for (unsigned spins = 0; spins < SPIN_CAP; ++spins) {
    u64 v = __hip_atomic_load(p, RLX_AGENT);
    if (__all(v == tag)) break;
    __builtin_amdgcn_s_sleep(PSLEEP);
  }
  asm volatile("" ::: "memory");
}
__device__ __forceinline__ int wrap7(int c) { return c >= 7 ? c - 7 : c; }

__global__ __launch_bounds__(512, 2) void gated_mix_kernel(const char* __restrict__ x, const char* __restrict__ wqkvA,
                                                           const char* __restrict__ woA, const char* __restrict__ wqkvB,
                                                           const char* __restrict__ woB,
                                                           const unsigned short* __restrict__ gate_w, char* __restrict__ out,
                                                           char* __restrict__ ws) {
  __shared__ __attribute__((aligned(1024))) char lds[LDS_B];
  const unsigned L0 = (unsigned)(uintptr_t)(LAS char*)lds;
  const int bid = blockIdx.x;
  const int xcd = bid & 7, idx = bid >> 3;
  const int xj = xcd >> 1, ci = idx >> 3;
  const int br = ci >> 1;
  const int hp = 2 * (br ? (xj ^ 1) : xj) + (ci & 1);
  const int rt = (xcd & 1) * 8 + (idx & 7);
  const int rot = wrap7(wrap7((idx & 7) + 2 * ci));
  const int cg = idx & 7, rt2 = 2 * xcd + ci;

  const int tid = threadIdx.x;
  const int lane = tid & 63;
  const int w = __builtin_amdgcn_readfirstlane(tid >> 6) & 7;
  const int wl = w & 3;
  const int li = lane & 15, g = lane >> 4, q = (lane >> 2) & 3, p = lane & 3;
  const bool g3 = g == 3;
  const f32x4 z4 = {0.f, 0.f, 0.f, 0.f};
  unsigned dta = 0, dtb = 0;
  const unsigned dt0 = (unsigned)__builtin_amdgcn_s_memrealtime();
  STAMP(0); STAMPC(18);
  const __amdgpu_buffer_rsrc_t wsr = __builtin_amdgcn_make_buffer_rsrc(ws, 0, WS_END, 0x00020000);
  u64 tagO;
  auto wo_blk = [&](int blk) {
    const unsigned vwo = (lane >> 3) * XROW + cg * 128 + (((lane & 7) ^ (((lane >> 4) & 3) << 1)) << 4);
    const bool isB = blk >= 56;
    int sb = (isB ? blk - 56 : blk) + 7 * cg;
    sb = sb >= 56 ? sb - 56 : sb;
    dma16(make_srd(isB ? woB : woA, kD * XROW), vwo, (unsigned)(sb * 8 * XROW), L0 + blk * 1024);
  };

  if (w >= 4) {
    const u32x4 srdX = make_srd(x, kS * XROW);
    const u32x4 srdW = make_srd(br ? wqkvB : wqkvA, kD * WROW);
    const bool l3 = wl == 3;
    const unsigned vx0 = (lane >> 3) * XROW + (((lane & 7) ^ (lane >> 4)) << 4);
    const unsigned vx1 = vx0 ^ 64u;
    unsigned vw0, vw1;
    {
      const unsigned s0 = wl * 64 + lane, s1 = s0 + 256;
      const unsigned p0 = (s0 * 9363u) >> 16, p1 = (s1 * 9363u) >> 16;
      const unsigned k0 = (p0 & 56) | ((p0 >> 1) & 3) | ((p0 & 1) << 2);
      const unsigned k1 = (p1 & 56) | ((p1 >> 1) & 3) | ((p1 & 1) << 2);
      vw0 = k0 * WROW + hp * 112 + (s0 - p0 * 7) * 16;
      vw1 = k1 * WROW + hp * 112 + (s1 - p1 * 7) * 16;
    }
    if (!br) {
      const u32x4 srd2 = l3 ? srdX : srdW;
      const unsigned v2a = l3 ? vx0 : vw1, v2b = l3 ? vx1 : vw1;
      const int xr = rt * 128 - 16 + wl * 32;
      const unsigned sx01 = (xr < 0 ? 0 : xr) * XROW, sx23 = (xr + 16) * XROW;
      auto chunk1 = [&](int c, int j) {
        const int cc = wrap7(c + rot);
        const unsigned sl = L0 + a_slot(c), sw = cc * (64 * WROW), sx = cc * 128;
        if (j < 4) dma16(srdX, (j & 1) ? vx1 : vx0, (j < 2 ? sx01 : sx23) + (j & 1) * 8 * XROW + sx, sl + (wl * 4 + j) * 1024);
        else if (j < 7) dma16(srdW, vw0, sw + (j - 4) * XROW, sl + A_W + (j - 4) * WM_B + wl * 1024);
        else {
          const int m = j - 7, xb = m == 0 ? 16 : 17;
          dma16(srd2, m == 0 ? v2a : v2b, l3 ? (rt * 128 - 16 + xb * 8) * XROW + sx : sw + m * XROW,
                sl + (l3 ? xb * 1024 : A_W + m * WM_B + (wl + 4) * 1024));
        }
      };
      auto instr = [&](int n) {
        if (n < 70) chunk1(n / 10, n % 10);
        else wo_blk(wl + 4 * (n - 70));
      };
#pragma unroll
      for (int n = 0; n < 10 + LOOKA; ++n) instr(n);
      STAMP(1);
#pragma unroll
      for (int c = 0; c < 7; ++c) {
        vm_bar<LOOKA>();
        if (c < 6) {
#pragma unroll
          for (int n = 0; n < 10; ++n) instr(10 * c + 10 + LOOKA + n);
        }
      }
      if (cg >= 7) { STAMP_FLUSH(); return; }
#pragma unroll
      for (int n = 70 + LOOKA; n < 98; ++n) instr(n);
      tagO = uniform64(*(LAS u64*)(uintptr_t)(L0 + SYNC + SY_TAG));
      VM_WAIT(14);
      sig(L0 + SYNC + SY_WOA + wl * 4);
      VM_WAIT(0);
      sig(L0 + SYNC + SY_WOB + wl * 4);
    } else {
      const unsigned sx0 = (rt * 128 + wl * 32) * XROW;
      const unsigned v2 = l3 ? vw0 : vw1;
      const unsigned sub2 = (l3 ? wl : wl + 4) * 1024;
      auto instr = [&](int n) {
        if (n < 56) {
          const int c = n >> 3, j = n & 7, cc = wrap7(c + rot);
          const unsigned sl = L0 + b_slot(c), sw = cc * (64 * WROW), sx = cc * 128;
          if (j < 4) dma16(srdX, (j & 1) ? vx1 : vx0, sx0 + j * 8 * XROW + sx, sl + (wl * 4 + j) * 1024);
          else dma16(srdW, j < 6 ? vw0 : v2, sw + ((j & 1) + 1) * XROW, sl + B_W + (j & 1) * WM_B + (j < 6 ? wl * 1024 : sub2));
        } else {
          const int c = (n - 56) >> 1;
          dma16(srdW, (n & 1) ? v2 : vw0, wrap7(c + rot) * (64 * WROW), L0 + B_WQ + c * WM_B + ((n & 1) ? sub2 : wl * 1024));
        }
      };
#pragma unroll
      for (int n = 0; n < 8 + LOOKB; ++n) instr(n);
      STAMP(1);
      bf16x8 xq[14][2];
      const unsigned xl = L0 + (wl * 32 + li) * 128 + ((g ^ (li >> 1)) << 4);
#pragma unroll
      for (int c = 0; c < 7; ++c) {
        vm_bar<LOOKB>();
#pragma unroll
        for (int ks = 0; ks < 2; ++ks) {
          const unsigned xa = (xl ^ (ks * 64)) + b_slot(c);
          xq[c * 2 + ks][0] = as_frag(lds_ld128(xa));
          xq[c * 2 + ks][1] = as_frag(lds_ld128(xa + 2048));
        }
#pragma unroll
        for (int n = 8 * c + 8 + LOOKB; n < (c < 6 ? 8 * c + 16 + LOOKB : 70); ++n) instr(n);
      }
      tagO = uniform64(*(LAS u64*)(uintptr_t)(L0 + SYNC + SY_TAG));
      STAMP(4);
      VM_WAIT(0);
      sig(L0 + SYNC + SY_WQ1 + wl * 4);
      wait4(L0 + SYNC + SY_WQ1);
      wait4(L0 + SYNC + SY_EX);
      STAMP(3);
      f32x4 aq[2][2][2];
#pragma unroll
      for (int i = 0; i < 8; ++i) aq[i >> 2][(i >> 1) & 1][i & 1] = (f32x4){kEps, kEps, kEps, kEps};
      const unsigned wb = L0 + B_WQ + (8 * g + 2 * q) * 112 + 8 * p;
      const unsigned gl = L0 + B_G + (8 * g + q) * 8;
      const bool p3 = p == 3;
      const unsigned d1 = p3 ? 32u : 112u;
      bf16x8 fq[2][4];
      auto rdq = [&](int s) {
        const unsigned wk = wb + s * 3584;
        const unsigned a = p3 ? gl + wrap7((s >> 1) + rot) * 512 + (s & 1) * 256 : wk + 32;
        fq[s & 1][0] = lds_tr2(wk, wk + 112);
        fq[s & 1][1] = lds_tr2(a, a + d1);
        fq[s & 1][2] = lds_tr2(wk + 56, wk + 56 + 112);
        fq[s & 1][3] = lds_tr2(wk + 88, wk + 88 + 112);
      };
      rdq(0);
#pragma unroll
      for (int s = 0; s < 14; ++s) {
        SB();
        if (s + 1 < 14) rdq(s + 1);
#pragma unroll
        for (int i = 0; i < 4; ++i) {
          aq[0][i >> 1][i & 1] = mfma(fq[s & 1][i], xq[s][0], aq[0][i >> 1][i & 1]);
          aq[1][i >> 1][i & 1] = mfma(fq[s & 1][i], xq[s][1], aq[1][i >> 1][i & 1]);
        }
        interleave<8, 8>();
      }
      SB();
      STAMP(5);
      u32x4 pqf[2][2];
      float omg[2];
#pragma unroll
      for (int t = 0; t < 2; ++t) {
        omg[t] = 1.f - sigmoidf(row3_bcast(aq[t][0][1][0]));
#pragma unroll
        for (int h = 0; h < 2; ++h) {
          const u32x2 a = phi4(aq[t][h][0]), b = phi4(aq[t][h][1]);
          pqf[t][h] = (u32x4){a[0], a[1], g3 ? 0u : b[0], g3 ? 0u : b[1]};
        }
      }
      STAMP(6);
      wait4(L0 + SYNC + SY_KV);
      STAMP(8);
      const unsigned ob = WS_O + ((rt * 8 + wl * 2) * 32 + 16 + hp * 2) * 1024 + lane * 16;
#pragma unroll
      for (int h = 0; h < 2; ++h) {
        const unsigned kb = L0 + B_KVL + h * 2048;
        const bf16x8 kv0 = as_frag(lds_ld128(kb + lane * 16)), kv1 = as_frag(lds_ld128(kb + 1024 + lane * 16));
        const bf16x8 zf = as_frag(lds_ld128(kb + 1024 + (g * 16 + 12) * 16));
#pragma unroll
        for (int t = 0; t < 2; ++t) {
          const f32x4 n0 = mfma(kv0, as_frag(pqf[t][h]), z4);
          const f32x4 n1 = mfma(kv1, as_frag(pqf[t][h]), z4);
          const f32x4 dn = mfma(zf, as_frag(pqf[t][h]), z4);
          const float s = omg[t] * __builtin_amdgcn_rcpf(dn[0] + 1e-6f);
          const u32x2 a = pk4(n0 * s), b = pk4(n1 * s);
          __builtin_amdgcn_raw_buffer_store_b128((u32x4){a[0], a[1], g3 ? 0u : b[0], g3 ? 0u : b[1]}, wsr,
                                                 ob + (t * 32 + h) * 1024, 0, 16);
        }
      }
      STAMP(9);
      VM_WAIT(0);
      if (lane == 0) __hip_atomic_store((gu64*)(ws + WS_FB) + ((rt * 4 + wl) * 16 + hp) * FS, tagO, RLX_AGENT);
      STAMP(10); STAMPC(19);
      STAMP_FLUSH();
      return;
    }
  } else {
    const int rh = w & 1, hh = w >> 1;
    {
      const unsigned gv = ((const unsigned*)gate_w)[tid < 224 ? tid : 0];
      if (w == 3) {
        const u64 e = __hip_atomic_load((gu64*)(ws + WS_E), RLX_AGENT);
        if (lane < 60) *(LAS unsigned*)(uintptr_t)(L0 + SYNC + lane * 4) = 0u;
        *(LAS u64*)(uintptr_t)(L0 + SYNC + SY_TAG) = next_tag(e);
      }
      const unsigned lo = (gv & 0xffffu) * 0x10001u, hi = (gv >> 16) * 0x10001u;
      if (tid < 224) lds_st128(L0 + (br ? B_G : A_G) + tid * 16, (u32x4){lo, lo, hi, hi});
    }
    const unsigned xsw = li * 128 + ((g ^ (li >> 1)) << 4);
    const unsigned wln = (8 * g + 2 * q) * 112 + 8 * p + 56 * hh;

    if (!br) {
      f32x4 aq[4][2], ak[5][2], av[5][2];
#pragma unroll
      for (int t = 0; t < 5; ++t)
#pragma unroll
        for (int f = 0; f < 2; ++f) { ak[t][f] = av[t][f] = z4; if (t < 4) aq[t][f] = z4; }
      const unsigned gl = L0 + A_G + (8 * g + q) * 8;
      const bool p3 = p == 3;
      const unsigned d1 = p3 ? 32u : 112u;
      bf16x8 xf[2][5], fq[2][2], fk[2][2], fv[2][2];
      unsigned xb, wb;
      auto rd = [&](int c, int ks, int part) {
        const unsigned xa = xb ^ (ks * 64), wk = wb + ks * 3584;
        if (part == 0) {
          fk[ks][0] = lds_tr2(wk + WM_B, wk + WM_B + 112);
          xf[ks][0] = as_frag(lds_ld128(xa));
          xf[ks][1] = as_frag(lds_ld128(xa + 2048));
        } else if (part == 1) {
          fv[ks][0] = lds_tr2(wk + 2 * WM_B, wk + 2 * WM_B + 112);
          xf[ks][2] = as_frag(lds_ld128(xa + 4096));
        } else if (part == 2) {
          fq[ks][0] = lds_tr2(wk, wk + 112);
          xf[ks][3] = as_frag(lds_ld128(xa + 6144));
        } else if (part == 3) {
          fk[ks][1] = lds_tr2(wk + WM_B + 32, wk + WM_B + 32 + 112);
          xf[ks][4] = as_frag(lds_ld128(xa + 8192));
        } else if (part == 4) {
          fv[ks][1] = lds_tr2(wk + 2 * WM_B + 32, wk + 2 * WM_B + 32 + 112);
        } else {
          const unsigned a = p3 ? gl + wrap7(c + rot) * 512 + ks * 256 : wk + 32;
          fq[ks][1] = lds_tr2(a, a + d1);
        }
      };
      auto mm = [&](int ks, int part) {
        const int f = part / 3, m = part % 3;
#pragma unroll
        for (int t = 0; t < 5; ++t) {
          if (m == 0) ak[t][f] = mfma(fk[ks][f], xf[ks][t], ak[t][f]);
          if (m == 1) av[t][f] = mfma(xf[ks][t], fv[ks][f], av[t][f]);
          if (m == 2 && t > 0) aq[t - 1][f] = mfma(fq[ks][f], xf[ks][t], aq[t - 1][f]);
        }
      };
#pragma unroll
      for (int c = 0; c < 7; ++c) {
        xb = opaque(L0 + a_slot(c) + rh * 8192 + xsw);
        wb = opaque(L0 + a_slot(c) + A_W + wln);
        LDS_BAR();
        if (c == 0) { STAMP(2); STAMPC(16); }
        if (c == 3) STAMP(3);
        if (c == 6) { STAMP(4); STAMPC(17); }
        SB();
#pragma unroll
        for (int ks = 0; ks < 2; ++ks) {
#pragma unroll
          for (int part = 0; part < 6; ++part) rd(c, ks, part);
          if (ks == 1 || c > 0) {
#pragma unroll
            for (int part = 0; part < 6; ++part) mm(ks ^ 1, part);
            interleave<28, 17>();
          }
          SB();
        }
      }
      tagO = uniform64(*(LAS u64*)(uintptr_t)(L0 + SYNC + SY_TAG));
      mm(1, 0); mm(1, 3); mm(1, 2); mm(1, 5);
      SB();
      mm(1, 1); mm(1, 4);
      STAMP(5);
      const bool odd = li & 1;
      const int kk0 = 4 * g + (li & 1);
      const bool own0 = kk0 <= li, own1 = kk0 + 2 <= li;
      const float sc = 0.18898223650461362f * kLog2e;
      const float ninf = (rt == 0 && rh == 0) ? -__builtin_inff() : 0.f;
      const unsigned ob = WS_O + ((rt * 8 + rh * 4) * 32 + hp * 2 + hh) * 1024 + lane * 16;
      bf16x8 kf[5];
#pragma unroll
      for (int t = 0; t < 5; ++t) kf[t] = frag2(pk4(ak[t][0]), pk4(ak[t][1]));
      f32x4 sp[4], so[4];
#pragma unroll
      for (int t = 0; t < 4; ++t) {
        const u32x2 b = pk4(aq[t][1]);
        const bf16x8 qf = frag2(pk4(aq[t][0]), (u32x2){g3 ? 0u : b[0], g3 ? 0u : b[1]});
        sp[t] = mfma(kf[t], qf, t == 0 ? (f32x4){ninf, ninf, ninf, ninf} : z4);
        so[t] = mfma(kf[t + 1], qf, z4);
      }
      float ug;
      {
        float z0 = aq[0][1][0], z1 = aq[1][1][0], z2 = aq[2][1][0], z3 = aq[3][1][0];
        swap32(z0, z1);
        swap32(z2, z3);
        swap16(z1, z3);
        ug = 1.f + __builtin_amdgcn_exp2f(-kLog2e * z3);
      }
      u32x2 vf[5][2];
#pragma unroll
      for (int t = 0; t < 5; ++t) {
        vf[t][0] = pk4(av[t][0]);
        vf[t][1] = pk4(av[t][1]);
      }
      float l0[4], l1[4], bc[4];
#pragma unroll
      for (int t = 0; t < 4; ++t) {
        float a0 = so[t][0], a2 = so[t][2], b0 = sp[t][0], b2 = sp[t][2];
        const float c0 = own0 ? a0 : b0, c1 = own0 ? so[t][1] : sp[t][1], c2 = own1 ? a2 : b2, c3 = own1 ? so[t][3] : sp[t][3];
        l0[t] = odd ? c1 : c0;
        l1[t] = odd ? c3 : c2;
      }
      rows_bcast4(rows_reduce4<true>(fmax1(l0[0], l1[0]), fmax1(l0[1], l1[1]), fmax1(l0[2], l1[2]), fmax1(l0[3], l1[3])) * -sc, bc);
#pragma unroll
      for (int t = 0; t < 4; ++t) {
        l0[t] = __builtin_amdgcn_exp2f(__builtin_fmaf(l0[t], sc, bc[t]));
        l1[t] = __builtin_amdgcn_exp2f(__builtin_fmaf(l1[t], sc, bc[t]));
      }
      rows_bcast4(__builtin_amdgcn_rcpf(rows_reduce4<false>(l0[0] + l1[0], l0[1] + l1[1], l0[2] + l1[2], l0[3] + l1[3]) * ug), bc);
      const unsigned sh = odd ? 16u : 0u;
      const unsigned mk0 = own0 ? 0u : 0xffffu, mk1 = own1 ? 0u : 0xffffu;
#pragma unroll
      for (int t = 0; t < 4; ++t) {
        const unsigned pe = pk2(l0[t] * bc[t], l1[t] * bc[t]), ph = pe >> 16;
        const bf16x8 pf = as_frag((u32x4){(pe & mk0) << sh, (ph & mk1) << sh, (pe & (mk0 ^ 0xffffu)) << sh, (ph & (mk1 ^ 0xffffu)) << sh});
        const f32x4 r0 = mfma(frag2(vf[t][0], vf[t + 1][0]), pf, z4);
        const f32x4 r1 = mfma(frag2(vf[t][1], vf[t + 1][1]), pf, z4);
        const u32x2 c0 = pk4(r0), c1 = pk4(r1);
        __builtin_amdgcn_raw_buffer_store_b128((u32x4){c0[0], c0[1], g3 ? 0u : c1[0], g3 ? 0u : c1[1]}, wsr, ob + t * 32 * 1024, 0, 16);
      }
      STAMP(9);
      VM_WAIT(0);
      if (lane == 0) __hip_atomic_store((gu64*)(ws + WS_FA) + ((rt * 2 + rh) * 16 + hp * 2 + hh) * FS, tagO, RLX_AGENT);
      STAMP(10); STAMPC(19);
      if (cg >= 7) { STAMP_FLUSH(); return; }
    } else {
      __builtin_amdgcn_s_setprio(2);
      f32x4 ak[4][2], av[4][2];
      {
        const float one = li == 12 ? 1.f : 0.f, i0 = rh ? one : 0.f, i1 = rh ? 0.f : one;
#pragma unroll
        for (int t = 0; t < 4; ++t) {
          ak[t][0] = ak[t][1] = (f32x4){kEps, kEps, kEps, kEps};
          av[t][0] = (f32x4){i0, i0, i0, i0};
          av[t][1] = (f32x4){i1, i1, i1, i1};
        }
      }
      bf16x8 xf[2][4], fw[2][4];
      unsigned xb, wb;
      const bool z0 = p == 3 && rh, z1 = p == 3 && !rh;
      const unsigned zaddr = L0 + SYNC + SY_ZERO;
      auto rd = [&](int ks, int part) {
        const unsigned xa = xb ^ (ks * 64), wk = wb + ks * 3584;
        if (part & 1) {
          const unsigned a = wk + WM_B + (((part >> 1) ^ rh) << 5);
          const bool z = (part >> 1) ? z1 : z0;
          fw[ks][part] = lds_tr2(z ? zaddr : a, z ? zaddr : a + 112);
        } else {
          fw[ks][part] = lds_tr2(wk + (part >> 1) * 32, wk + (part >> 1) * 32 + 112);
        }
        if (part < 2) {
          xf[ks][part * 2] = as_frag(lds_ld128(xa + part * 4096));
          xf[ks][part * 2 + 1] = as_frag(lds_ld128(xa + part * 4096 + 2048));
        }
      };
      auto mm = [&](int ks, int part) {
#pragma unroll
        for (int t = 0; t < 4; ++t) {
          if (part & 1) av[t][part >> 1] = mfma(xf[ks][t], fw[ks][part], av[t][part >> 1]);
          else ak[t][part >> 1] = mfma(xf[ks][t], fw[ks][part], ak[t][part >> 1]);
        }
      };
#pragma unroll
      for (int c = 0; c < 7; ++c) {
        xb = opaque(L0 + b_slot(c) + rh * 8192 + xsw);
        wb = opaque(L0 + b_slot(c) + B_W + wln);
        LDS_BAR();
        if (c == 0) { STAMP(2); STAMPC(16); }
        if (c == 3) STAMP(3);
        if (c == 6) { STAMP(4); STAMPC(17); }
        SB();
#pragma unroll
        for (int ks = 0; ks < 2; ++ks) {
#pragma unroll
          for (int part = 0; part < 4; ++part) rd(ks, part);
          if (ks == 1 || c > 0) {
#pragma unroll
            for (int part = 0; part < 4; ++part) mm(ks ^ 1, part);
            interleave<16, 12>();
          }
          SB();
        }
      }
      tagO = uniform64(*(LAS u64*)(uintptr_t)(L0 + SYNC + SY_TAG));
      mm(1, 0); mm(1, 2);
      SB();
      mm(1, 3); mm(1, 1);
      STAMP(5);
      f32x4 kv[2][2];
      {
        u32x2 pk[4][2], vf[4][2];
#pragma unroll
        for (int t = 0; t < 4; ++t) {
          pk[t][0] = phi4(ak[t][0]);
          pk[t][1] = phi4(ak[t][1]);
          vf[t][0] = pk4(av[t][0]);
          vf[t][1] = pk4(av[t][1]);
        }
#pragma unroll
        for (int e = 1; e >= 0; --e)
#pragma unroll
          for (int d = 0; d < 2; ++d) {
            kv[e][d] = mfma(frag2(pk[0][d], pk[1][d]), frag2(vf[0][e], vf[1][e]), z4);
            kv[e][d] = mfma(frag2(pk[2][d], pk[3][d]), frag2(vf[2][e], vf[3][e]), kv[e][d]);
          }
      }
      lds_st128(L0 + B_EX + w * 2048 + lane * 16, __builtin_bit_cast(u32x4, kv[1][0]));
      lds_st128(L0 + B_EX + w * 2048 + 1024 + lane * 16, __builtin_bit_cast(u32x4, kv[1][1]));
      sig(L0 + SYNC + SY_EX + w * 4);
      wait1(L0 + SYNC + SY_EX + (w ^ 1) * 4);
      const f32x4 s0 = kv[0][0] + __builtin_bit_cast(f32x4, lds_ld128(L0 + B_EX + (w ^ 1) * 2048 + lane * 16));
      const f32x4 s1 = kv[0][1] + __builtin_bit_cast(f32x4, lds_ld128(L0 + B_EX + (w ^ 1) * 2048 + 1024 + lane * 16));
      const unsigned kvb = WS_KVP + (hp * 4 + w) * 16 * 1024 + lane * 16;
      {
        const u32x2 a = pk4(s0), b = pk4(s1);
        __builtin_amdgcn_raw_buffer_store_b128((u32x4){a[0], a[1], b[0], b[1]}, wsr, kvb + rt * 1024, 0, 16);
      }
      STAMP(6);
      VM_WAIT(0);
      gu64* kvf = (gu64*)(ws + WS_FKV) + (hp * 4 + w) * 16 * FS;
      if (lane == 0) __hip_atomic_store(kvf + rt * FS, tagO, RLX_AGENT);
      STAMP(7);
      poll_flags(kvf, lane, 16, tagO);
      STAMP(8);
      u32x4 pr[16];
#pragma unroll
      for (int r = 0; r < 16; ++r) pr[r] = __builtin_amdgcn_raw_buffer_load_b128(wsr, kvb + r * 1024, 0, 16);
      SB();
      float sum[8];
#pragma unroll
      for (int i = 0; i < 8; ++i) sum[i] = 0.f;
#pragma unroll
      for (int r = 0; r < 16; ++r)
#pragma unroll
        for (int i = 0; i < 4; ++i) {
          const unsigned u = pr[r][i];
          sum[2 * i] += __uint_as_float(u << 16);
          sum[2 * i + 1] += __uint_as_float(u & 0xffff0000u);
        }
      lds_st128(L0 + B_KVL + w * 1024 + lane * 16,
                (u32x4){pk2(sum[0], sum[1]), pk2(sum[2], sum[3]), pk2(sum[4], sum[5]), pk2(sum[6], sum[7])});
      sig(L0 + SYNC + SY_KV + w * 4);
      STAMP(10); STAMPC(19);
      STAMP_FLUSH();
      if (bid == 128 && w == 0) {
        __builtin_amdgcn_s_setprio(0);
#pragma nounroll
        for (int k = 0; k < 4; ++k) {
          const int i = (k & 1) * 64 + lane, r = i >> 3, h = i & 7;
          const gu64* f = (k < 2) ? (gu64*)(ws + WS_FA) + (r * 32 + h * 2) * FS : (gu64*)(ws + WS_FB) + (r * 64 + h) * FS;
          poll_flags(f, 0, 1, tagO);
        }
        if (lane == 0) __hip_atomic_store((gu64*)(ws + WS_E), tagO, RLX_AGENT);
      }
      return;
    }
  }

  const unsigned sw = (2 * g + (q >> 1)) & 3;
  f32x4 acc[4] = {z4, z4, z4, z4};
#pragma unroll
  for (int pb = 0; pb < 2; ++pb) {
    if (pb) STAMP(7);
    wait4(L0 + SYNC + (pb ? SY_WOB : SY_WOA));
    const unsigned rb = L0 + (pb * 448 + 4 * g + q) * 128 + 8 * p;
    unsigned wa[4], wa2[4];
#pragma unroll
    for (int v = 0; v < 4; ++v) {
      wa[v] = opaque(rb + ((v ^ sw) << 5));
      wa2[v] = opaque(wa[v] + (g3 ? 0u : 2048u));
    }
    auto wfrag = [&](int h, int ct) {
      const int v = ct ^ (2 * (h & 1));
      return lds_tr2(wa[v] + h * 3584, wa2[v] + h * 3584);
    };
    constexpr int NPRE = 8;
    bf16x8 wf[16][4];
#pragma unroll
    for (int h = 0; h < NPRE; ++h)
#pragma unroll
      for (int ct = 0; ct < 4; ++ct) wf[h][ct] = wfrag(h, ct);
    if (pb) STAMP(12); else STAMP(11);
    if (pb) poll_flags((gu64*)(ws + WS_FB) + (rt2 * 4 + (w >> 1)) * 16 * FS, lane, 8, tagO);
    else poll_flags((gu64*)(ws + WS_FA) + (rt2 * 2 + (w >> 2)) * 16 * FS, lane, 16, tagO);
    if (pb) STAMP(13); else STAMP(6);
    const unsigned ob = WS_O + ((rt2 * 8 + w) * 32 + pb * 16) * 1024 + lane * 16;
    u32x4 of[16];
#pragma unroll
    for (int h = 0; h < 16; ++h) of[h] = __builtin_amdgcn_raw_buffer_load_b128(wsr, ob, ((h + 2 * cg) & 15) * 1024, 16);
    SB();
#pragma unroll
    for (int h = 0; h < 16; ++h) {
#pragma unroll
      for (int ct = 0; ct < 4; ++ct) acc[ct] = mfma(wf[h][ct], as_frag(of[h]), acc[ct]);
      SB();
      if (h + NPRE < 16) {
#pragma unroll
        for (int ct = 0; ct < 4; ++ct) wf[h + NPRE][ct] = wfrag(h + NPRE, ct);
      }
      SB();
    }
  }
  {
    char* orow = out + (rt2 * 128 + w * 16 + li) * XROW + cg * 128 + g * 8;
#pragma unroll
    for (int ct = 0; ct < 4; ++ct) *(u32x2*)(orow + ct * 32) = pk4(acc[ct]);
  }
  STAMP(14);
  STAMP(15);
  STAMP_FLUSH();
}

}

extern "C" void kernel_launch(void* const* d_in, const int* in_sizes, int n_in, void* d_out, int out_size, void* d_ws,
                              size_t ws_size, hipStream_t stream) {
  (void)in_sizes; (void)n_in; (void)out_size; (void)ws_size;
  gated_mix_kernel<<<256, 512, 0, stream>>>((const char*)d_in[0], (const char*)d_in[1], (const char*)d_in[2],
                                            (const char*)d_in[4], (const char*)d_in[5],
                                            (const unsigned short*)d_in[6], (char*)d_out, (char*)d_ws);
}
```

```cpp
#define CODE_PF 232
#include <hip/hip_runtime.h>
#include <stdint.h>

typedef __bf16 bf16x2 __attribute__((ext_vector_type(2)));
typedef __bf16 bf16x4 __attribute__((ext_vector_type(4)));
typedef __bf16 bf16x8 __attribute__((ext_vector_type(8)));
typedef float f32x2 __attribute__((ext_vector_type(2)));
typedef float f32x4 __attribute__((ext_vector_type(4)));
typedef unsigned u32x2 __attribute__((ext_vector_type(2)));
typedef unsigned u32x4 __attribute__((ext_vector_type(4)));
typedef unsigned long long u64;
#define LAS __attribute__((address_space(3)))
#define GAS __attribute__((address_space(1)))
typedef GAS u64 gu64;
#define RLX_AGENT __ATOMIC_RELAXED, __HIP_MEMORY_SCOPE_AGENT

namespace {

constexpr int kS = 2048, kD = 448;
constexpr int XROW = kD * 2;
constexpr int WROW = 3 * kD * 2;
constexpr int WM_B = 64 * 112;

constexpr int A_FRONT = 34816, A_SLOT = 144 * 128 + 3 * WM_B, A_W = 144 * 128;
constexpr int A_G = A_FRONT + 3 * A_SLOT;
constexpr int B_SLOT = 128 * 128 + 2 * WM_B, B_W = 128 * 128;
constexpr int B_WQ = 97536, B_G = B_WQ + 7 * WM_B, B_EX = B_G + 3584, B_KVL = B_EX + 8192;
constexpr int P2X = 114688;
constexpr int SYNC = 163584;
constexpr int LDS_B = 163840;
static_assert(A_G + 3584 <= SYNC && B_KVL + 4096 <= SYNC && A_FRONT + 2 * A_SLOT == 114688, "lds map");
__host__ __device__ constexpr int a_slot(int c) { return A_FRONT + ((c + 2) % 3) * A_SLOT; }
__host__ __device__ constexpr int b_slot(int c) { return ((c + 2) % 3) * B_SLOT; }
enum { SY_WOA = 0, SY_WOB = 16, SY_D6 = 32, SY_D6Q = 48, SY_WQ = 64, SY_QD = 80, SY_EX = 96, SY_KV = 112, SY_P2 = 128, SY_ZERO = 192, SY_TAG = 240 };

constexpr int WS_E = 0;
constexpr int WS_CLR = 4096;
constexpr int FS = 16;
constexpr int WS_FA = 8192;
constexpr int WS_FB = WS_FA + 16 * 2 * 16 * 8 * FS;
constexpr int WS_KVP = WS_FB + 16 * 4 * 8 * 8 * FS;
constexpr int WS_O = WS_KVP + 8 * 4 * 16 * 1024;
constexpr int WS_END = WS_O + 16 * 8 * 32 * 1024;

constexpr unsigned SPIN_CAP = 1u << 14;
constexpr unsigned SENT = 0xffffffffu;
#define ADELAY 0
#define QWO 14

#define STAMP(i) do {} while (0)
#define STAMPC(i) do {} while (0)
#define STAMP_FLUSH() do {} while (0)

__device__ __forceinline__ unsigned pk2(float a, float b) {
  f32x2 v = {a, b};
  bf16x2 r = __builtin_convertvector(v, bf16x2);
  return __builtin_bit_cast(unsigned, r);
}
__device__ __forceinline__ u32x2 pk4(f32x4 v) { return (u32x2){pk2(v[0], v[1]), pk2(v[2], v[3])}; }
__device__ __forceinline__ bf16x8 as_frag(u32x4 v) { return __builtin_bit_cast(bf16x8, v); }
__device__ __forceinline__ bf16x8 frag2(u32x2 a, u32x2 b) { return as_frag((u32x4){a[0], a[1], b[0], b[1]}); }
__device__ __forceinline__ f32x4 mfma(bf16x8 a, bf16x8 b, f32x4 c) {
  return __builtin_amdgcn_mfma_f32_16x16x32_bf16(a, b, c, 0, 0, 0);
}
__device__ __forceinline__ bf16x8 lds_tr2(unsigned a0, unsigned a1) {
  bf16x4 lo = __builtin_amdgcn_ds_read_tr16_b64_v4bf16((LAS bf16x4*)(uintptr_t)a0);
  bf16x4 hi = __builtin_amdgcn_ds_read_tr16_b64_v4bf16((LAS bf16x4*)(uintptr_t)a1);
  return __builtin_shufflevector(lo, hi, 0, 1, 2, 3, 4, 5, 6, 7);
}
__device__ __forceinline__ u32x4 lds_ld128(unsigned a) { return *(LAS u32x4*)(uintptr_t)a; }
__device__ __forceinline__ void lds_st128(unsigned a, u32x4 v) { *(LAS u32x4*)(uintptr_t)a = v; }
__device__ __forceinline__ void lds_st64(unsigned a, u32x2 v) { *(LAS u32x2*)(uintptr_t)a = v; }
__device__ __forceinline__ unsigned opaque(unsigned v) { asm volatile("" : "+v"(v)); return v; }

__device__ __forceinline__ void sig(unsigned a) {
  asm volatile("" ::: "memory");
  *(volatile LAS unsigned*)(uintptr_t)a = 1u;
  asm volatile("" ::: "memory");
  __builtin_amdgcn_sched_barrier(0);
}
__device__ __forceinline__ void wait1(unsigned a) {
  while (__builtin_amdgcn_readfirstlane(*(volatile LAS unsigned*)(uintptr_t)a) == 0u) __builtin_amdgcn_s_sleep(1);
  asm volatile("" ::: "memory");
}
__device__ __forceinline__ void wait4(unsigned a) {
  for (;;) {
    const u32x4 v = *(volatile LAS u32x4*)(uintptr_t)a;
    if (__builtin_amdgcn_readfirstlane(v[0] & v[1] & v[2] & v[3])) break;
    __builtin_amdgcn_s_sleep(1);
  }
  asm volatile("" ::: "memory");
}

__device__ __forceinline__ u32x4 make_srd(const void* p, unsigned bytes) {
  uint64_t a = (uint64_t)p;
  u32x4 r;
  r[0] = __builtin_amdgcn_readfirstlane((unsigned)a);
  r[1] = __builtin_amdgcn_readfirstlane((unsigned)(a >> 32) & 0xffffu);
  r[2] = bytes;
  r[3] = 0x00020000u;
  return r;
}
__device__ __forceinline__ void dma16(u32x4 srd, unsigned voff, unsigned soff, unsigned ldsaddr) {
  asm volatile(
      "s_mov_b32 m0, %3\n\t"
      "s_nop 3\n\t"
      "buffer_load_dwordx4 %0, %1, %2 offen lds"
      :
      : "v"(voff), "s"(srd), "s"(soff), "s"(ldsaddr));
}
#define VM_BAR(N) asm volatile("s_waitcnt vmcnt(" #N ") lgkmcnt(0)\n\ts_barrier" ::: "memory")
#define LDS_BAR() asm volatile("s_waitcnt lgkmcnt(0)\n\ts_barrier" ::: "memory")
#define VM_WAIT(N) asm volatile("s_waitcnt vmcnt(" #N ")" ::: "memory")
template <int N> __device__ __forceinline__ void vm_bar() { asm volatile("s_waitcnt vmcnt(%0) lgkmcnt(0)\n\ts_barrier" ::"n"(N) : "memory"); }
#define SB() __builtin_amdgcn_sched_barrier(0)

__device__ __forceinline__ float fmax1(float a, float b) { return __builtin_amdgcn_fmed3f(a, b, 3.0e38f); }
__device__ __forceinline__ void swap16(float& a, float& b) {
  auto r = __builtin_amdgcn_permlane16_swap(__float_as_uint(a), __float_as_uint(b), false, false);
  const unsigned r0 = r[0], r1 = r[1];
  a = __uint_as_float(r0);
  b = __uint_as_float(r1);
}
__device__ __forceinline__ void swap32(float& a, float& b) {
  auto r = __builtin_amdgcn_permlane32_swap(__float_as_uint(a), __float_as_uint(b), false, false);
  const unsigned r0 = r[0], r1 = r[1];
  a = __uint_as_float(r0);
  b = __uint_as_float(r1);
}
template <bool MAX>
__device__ __forceinline__ float rows_reduce4(float v0, float v1, float v2, float v3) {
  swap32(v0, v1);
  swap32(v2, v3);
  float a = MAX ? fmax1(v0, v1) : v0 + v1, b = MAX ? fmax1(v2, v3) : v2 + v3;
  swap16(a, b);
  return MAX ? fmax1(a, b) : a + b;
}
__device__ __forceinline__ void rows_bcast4(float x, float (&o)[4]) {
  float y = x;
  swap16(x, y);
  o[0] = x; o[1] = x; o[2] = y; o[3] = y;
  swap32(o[0], o[1]);
  swap32(o[2], o[3]);
}
__device__ __forceinline__ float row3_bcast(float v) {
  float a = v;
  swap32(a, v);
  a = v;
  swap16(a, v);
  return v;
}
template <int NM, int ND>
__device__ __forceinline__ void interleave() {
#pragma unroll
  for (int i = 0; i < NM; ++i) {
    __builtin_amdgcn_sched_group_barrier(0x8, 1, 0);
    if (i < ND) {
      __builtin_amdgcn_sched_group_barrier(0x2, 1, 0);
      __builtin_amdgcn_sched_group_barrier(0x100, 1, 0);
    }
  }
}
constexpr float kEps = 1e-6f, kLog2e = 1.44269504088896f;
__device__ __forceinline__ float phi(float u) {
  const float e = __builtin_amdgcn_fmed3f(__builtin_amdgcn_exp2f(__builtin_fmaf(u, kLog2e, -kEps * kLog2e)), 0.f, 1.f);
  return fmax1(u, kEps) + e;
}
__device__ __forceinline__ u32x2 phi4(f32x4 v) { return (u32x2){pk2(phi(v[0]), phi(v[1])), pk2(phi(v[2]), phi(v[3]))}; }
__device__ __forceinline__ float sigmoidf(float z) {
  return __builtin_amdgcn_rcpf(1.f + __builtin_amdgcn_exp2f(-1.44269504088896f * z));
}
__device__ __forceinline__ u64 next_tag(u64 e) {
  const u64 s = ((u64)(unsigned)__builtin_amdgcn_readfirstlane((unsigned)(e >> 32)) << 32) |
                (unsigned)__builtin_amdgcn_readfirstlane((unsigned)e);
  return s * 6364136223846793005ull + 1442695040888963407ull;
}
__device__ __forceinline__ u64 uniform64(u64 e) {
  return ((u64)(unsigned)__builtin_amdgcn_readfirstlane((unsigned)(e >> 32)) << 32) |
         (unsigned)__builtin_amdgcn_readfirstlane((unsigned)e);
}
__device__ __forceinline__ void poll_flags(const gu64* p, u64 tag) {
#pragma nounroll
  for (unsigned spins = 0; spins < SPIN_CAP; ++spins) {
    u64 v = __hip_atomic_load(p, RLX_AGENT);
    if (__all(v == tag)) break;
    __builtin_amdgcn_s_sleep(2);
  }
  asm volatile("" ::: "memory");
}
__device__ __forceinline__ void publish(u32x4 v, __amdgpu_buffer_rsrc_t r, unsigned voff) {
#pragma unroll
  for (int i = 0; i < 4; ++i) v[i] = v[i] < SENT - 1u ? v[i] : SENT - 1u;
  __builtin_amdgcn_raw_buffer_store_b128(v, r, voff, 0, 16);
}
__device__ __forceinline__ unsigned lane_id() {
  unsigned l = __builtin_amdgcn_mbcnt_hi(~0u, __builtin_amdgcn_mbcnt_lo(~0u, 0u));
  asm volatile("" : "+v"(l));
  return l;
}
__device__ __forceinline__ int wrap7(int c) { return c >= 7 ? c - 7 : c; }

__global__ __launch_bounds__(512, 2) void gated_mix_kernel(const char* __restrict__ x, const char* __restrict__ wqkvA,
                                                           const char* __restrict__ woA, const char* __restrict__ wqkvB,
                                                           const char* __restrict__ woB,
                                                           const unsigned short* __restrict__ gate_w, char* __restrict__ out,
                                                           char* ws) {
  __shared__ __attribute__((aligned(1024))) char lds[LDS_B];
  const unsigned L0 = (unsigned)(uintptr_t)(LAS char*)lds;
  const uint64_t pc0 = __builtin_amdgcn_s_getpc() & ~127ull;
  const int bid = blockIdx.x;
  const int xcd = bid & 7, idx = bid >> 3;
  const int xj = xcd >> 1, ci = idx >> 3;
  const int br = ci >> 1;
  const int hp = 2 * (br ? (xj ^ 1) : xj) + (ci & 1);
  const int rt = (xcd & 1) * 8 + (idx & 7);
  const int rot = wrap7(wrap7((idx & 7) + 2 * ci));
  const int cg = idx & 7, rg = 4 * xcd + ci;

  const int tid = threadIdx.x;
  const int lane = tid & 63;
  const int w = __builtin_amdgcn_readfirstlane(tid >> 6) & 7;
  const int wl = w & 3;
  const int li = lane & 15, g = lane >> 4, q = (lane >> 2) & 3, p = lane & 3;
  const bool g3 = g == 3;
  const f32x4 z4 = {0.f, 0.f, 0.f, 0.f};
  STAMP(0); STAMPC(18);
  unsigned vx0 = (lane >> 3) * XROW + (((lane & 7) ^ (lane >> 4)) << 4);
  unsigned vx1 = vx0 ^ 64u;
  unsigned vw0, vw1;
  {
    const unsigned s0 = wl * 64 + lane, s1 = s0 + 256;
    const unsigned p0 = (s0 * 9363u) >> 16, p1 = (s1 * 9363u) >> 16;
    const unsigned k0 = (p0 & 56) | ((p0 >> 1) & 3) | ((p0 & 1) << 2);
    const unsigned k1 = (p1 & 56) | ((p1 >> 1) & 3) | ((p1 & 1) << 2);
    vw0 = k0 * WROW + hp * 112 + (s0 - p0 * 7) * 16;
    vw1 = k1 * WROW + hp * 112 + (s1 - p1 * 7) * 16;
  }
  asm volatile("" : "+v"(vx0), "+v"(vx1), "+v"(vw0), "+v"(vw1));
  asm volatile("" ::"s"(x), "s"(wqkvA), "s"(woA), "s"(wqkvB), "s"(woB), "s"(gate_w), "s"(out), "s"(ws));
  const __amdgpu_buffer_rsrc_t wsr = __builtin_amdgcn_make_buffer_rsrc(ws, 0, WS_END, 0x00020000);
  u64 tagO;
  auto wo_run = [&](int j0, int j1) {
    const unsigned vwo = (lane >> 3) * XROW + cg * 128 + (((lane & 7) ^ (((lane >> 4) & 3) << 1)) << 4);
    const u32x4 sa = make_srd(woA, kD * XROW), sb = make_srd(woB, kD * XROW);
    unsigned so = (wl + 7 * cg + 4 * (j0 % 14)) * (8 * XROW);
    so = so >= kD * XROW ? so - kD * XROW : so;
#pragma unroll
    for (int j = j0; j < j1; ++j) {
      dma16(j < 14 ? sa : sb, vwo, so, L0 + (wl + 4 * j) * 1024);
      so += 32 * XROW;
      so = so >= kD * XROW ? so - kD * XROW : so;
    }
  };
  unsigned gv = 0, cpf = 0;
  u64 epoch = 0;
  {
    int wx = w;
    asm volatile("" : "+s"(wx));
    if (wx < 4) {
      gv = ((const unsigned*)gate_w)[tid < 224 ? tid : 0];
      epoch = __hip_atomic_load((gu64*)(ws + WS_E), RLX_AGENT);
      cpf = *(const GAS unsigned*)(pc0 + (tid < CODE_PF ? tid : 0) * 128);
    }
  }

    const int rh = w & 1, hh = w >> 1;
    const unsigned obs = WS_O + ((rt * 8 + rh * 4) * 32 + hp * 2 + hh) * 1024;
    const unsigned kvs = WS_KVP + (hp * 4 + w) * 16 * 1024;
    auto setup = [&](int goff) {
      if (tid < 60) *(LAS unsigned*)(uintptr_t)(L0 + SYNC + tid * 4) = 0u;
      const unsigned lo = (gv & 0xffffu) * 0x10001u, hi = (gv >> 16) * 0x10001u;
      const int pos = tid - rot * 32;
      if (tid < 224) lds_st128(L0 + goff + (pos < 0 ? pos + 224 : pos) * 16, (u32x4){lo, lo, hi, hi});
    };
    u64 clr = 0;
    auto clrp = [&]() { return (const gu64*)(ws + WS_CLR) + hp * 16 + (lane_id() & 15); };
    auto hook = [&](int c, bool before, bool isb) {
      if (c == 0 && !before && isb)
        __builtin_amdgcn_raw_buffer_store_b128((u32x4){SENT, SENT, SENT, SENT}, wsr, lane_id() * 16 + kvs + rt * 1024, 0, 16);
      if (c == 3 && before) {
        asm volatile("" : "+v"(epoch) : "v"(cpf));
        tagO = next_tag(epoch);
        *(LAS u64*)(uintptr_t)(L0 + SYNC + SY_TAG) = tagO;
        VM_WAIT(0);
      }
      if (c == 3 && !before && isb)
        __builtin_amdgcn_raw_buffer_store_b64((u32x2){(unsigned)tagO, (unsigned)(tagO >> 32)}, wsr,
                                              (lane_id() | w) == 0 ? WS_CLR + (hp * 16 + rt) * 8 : 0x7ffffff0u, 0, 16);
      if (c == 6 && !before && isb) clr = __hip_atomic_load(clrp(), RLX_AGENT);
    };
    const unsigned xsw = li * 128 + ((g ^ (li >> 1)) << 4);
    const unsigned wln = (8 * g + 2 * q) * 112 + 8 * p + 56 * hh;

  if (__builtin_expect(w >= 4, 1)) {
    __builtin_amdgcn_s_waitcnt(0x0f70);
    const u32x4 srdX = make_srd(x, kS * XROW);
    const u32x4 srdW = make_srd(br ? wqkvB : wqkvA, kD * WROW);
    const bool l3 = wl == 3;
    const int xr = rt * 128 + wl * 32 - (br ? 0 : 16);
    const unsigned sx01 = (xr < 0 ? 0 : xr) * XROW, sx23 = (xr + 16) * XROW;
    const bool l3a = l3 && !br;
    const u32x4 srd2 = l3a ? srdX : srdW;
    const unsigned v2 = l3 ? vw0 : vw1;
    const unsigned v2a = l3a ? vx0 : v2, v2b = l3a ? vx1 : v2;
    const unsigned st2 = l3a ? 128u : 64u * WROW, s2a = l3a ? (rt * 128 + 112) * XROW : 0u;
    const unsigned s2d = l3a ? 8u * XROW : (unsigned)XROW, s2e = l3a ? 8u * XROW : 2u * XROW;
    const unsigned sub2 = (l3 ? wl : wl + 4) * 1024;
    auto chunk = [&](unsigned sl, unsigned lq, unsigned lk, int cc) {
      const unsigned sw = cc * (64 * WROW), sx = cc * 128, s2 = cc * st2 + s2a;
#pragma unroll
      for (int j = 0; j < 4; ++j)
        dma16(srdX, (j & 1) ? vx1 : vx0, (j < 2 ? sx01 : sx23) + (j & 1) * 8 * XROW + sx, sl + (wl * 4 + j) * 1024);
      dma16(srdW, vw0, sw + XROW, lk + wl * 1024);
      dma16(srdW, vw0, sw + 2 * XROW, lk + WM_B + wl * 1024);
      dma16(srd2, v2b, s2 + s2d, l3a ? sl + 17408 : lk + sub2);
      dma16(srd2, v2b, s2 + s2e, l3a ? sl + 17408 : lk + WM_B + sub2);
      if (!br) {
        dma16(srdW, vw0, sw, lq + wl * 1024);
        dma16(srd2, v2a, s2, l3a ? sl + 16384 : lq + sub2);
      }
    };
    if (!br) __builtin_amdgcn_s_sleep(ADELAY);
    int cc = rot;
    unsigned sl = L0 + (br ? b_slot(0) : a_slot(0));
    const int nc = br ? 2 : 7;
#pragma nounroll
    for (int c = 0; c < nc; ++c) {
      chunk(sl, sl + A_W, sl + (br ? B_W : A_W + WM_B), cc);
      cc = wrap7(cc + 1);
      sl = br ? L0 + b_slot(1) : (sl == L0 + a_slot(0) ? L0 + a_slot(1) : sl + A_SLOT);
      if (c > 0 && !br) vm_bar<10>();
    }
    if (!br) {
      if (cg >= 7) { vm_bar<0>(); STAMP_FLUSH(); return; }
      wo_run(0, 10);
      vm_bar<10>();
      wo_run(10, 28);
      VM_WAIT(14);
      sig(L0 + SYNC + SY_WOA + wl * 4);
      VM_WAIT(0);
      sig(L0 + SYNC + SY_WOB + wl * 4);
    } else {
      auto instr = [&](int n) {
        if (n < 56) {
          const int c = n >> 3, j = n & 7, cc = wrap7(c + rot);
          const unsigned sl = L0 + b_slot(c), sw = cc * (64 * WROW), sx = cc * 128;
          if (j < 4) dma16(srdX, (j & 1) ? vx1 : vx0, sx01 + j * 8 * XROW + sx, sl + (wl * 4 + j) * 1024);
          else dma16(srdW, j < 6 ? vw0 : v2, sw + ((j & 1) + 1) * XROW, sl + B_W + (j & 1) * WM_B + (j < 6 ? wl * 1024 : sub2));
        } else {
          const int c = (n - 56) >> 1;
          dma16(srdW, (n & 1) ? v2 : vw0, wrap7(c + rot) * (64 * WROW), L0 + B_WQ + c * WM_B + ((n & 1) ? sub2 : wl * 1024));
        }
      };
      STAMP(1);
      bf16x8 xq[14][2];
      const unsigned xl = L0 + (wl * 32 + li) * 128 + ((g ^ (li >> 1)) << 4);
#pragma unroll
      for (int c = 0; c < 7; ++c) {
        vm_bar<8>();
#pragma unroll
        for (int ks = 0; ks < 2; ++ks) {
          const unsigned xa = (xl ^ (ks * 64)) + b_slot(c);
          xq[c * 2 + ks][0] = as_frag(lds_ld128(xa));
          xq[c * 2 + ks][1] = as_frag(lds_ld128(xa + 2048));
        }
#pragma unroll
        for (int n = 8 * c + 16; n < (c < 6 ? 8 * c + 24 : 70); ++n) instr(n);
      }
      STAMP(4);
      VM_WAIT(0);
      sig(L0 + SYNC + SY_WQ + wl * 4);
      wait4(L0 + SYNC + SY_WQ);
      wait4(L0 + SYNC + SY_EX);
      STAMP(3);
      f32x4 aq[2][2][2];
#pragma unroll
      for (int i = 0; i < 8; ++i) aq[i >> 2][(i >> 1) & 1][i & 1] = (f32x4){kEps, kEps, kEps, kEps};
      const unsigned wb = L0 + B_WQ + (8 * g + 2 * q) * 112 + 8 * p;
      const unsigned gl = L0 + B_G + (8 * g + q) * 8;
      const bool p3 = p == 3;
      const unsigned d1 = p3 ? 32u : 112u;
      const unsigned qa0 = opaque(p3 ? gl : wb + 32), qstep = opaque(p3 ? 256u : 3584u);
      bf16x8 fq[3][4];
      auto rdq = [&](int s) {
        const unsigned wk = wb + s * 3584;
        const unsigned a = qa0 + s * qstep;
        fq[s % 3][0] = lds_tr2(wk, wk + 112);
        fq[s % 3][1] = lds_tr2(a, a + d1);
        fq[s % 3][2] = lds_tr2(wk + 56, wk + 56 + 112);
        fq[s % 3][3] = lds_tr2(wk + 88, wk + 88 + 112);
      };
      rdq(0);
      rdq(1);
#pragma unroll
      for (int s = 0; s < 14; ++s) {
        SB();
        if (s + 2 < 14) rdq(s + 2);
#pragma unroll
        for (int i = 0; i < 4; ++i) {
          aq[0][i >> 1][i & 1] = mfma(fq[s % 3][i], xq[s][0], aq[0][i >> 1][i & 1]);
          aq[1][i >> 1][i & 1] = mfma(fq[s % 3][i], xq[s][1], aq[1][i >> 1][i & 1]);
        }
        interleave<8, 8>();
      }
      sig(L0 + SYNC + SY_QD + wl * 4);
      STAMP(5);
      u32x4 pqf[2][2];
      float omg[2];
#pragma unroll
      for (int t = 0; t < 2; ++t) {
        omg[t] = 1.f - sigmoidf(row3_bcast(aq[t][0][1][0]));
#pragma unroll
        for (int h = 0; h < 2; ++h) {
          const u32x2 a = phi4(aq[t][h][0]), b = phi4(aq[t][h][1]);
          pqf[t][h] = (u32x4){a[0], a[1], g3 ? 0u : b[0], g3 ? 0u : b[1]};
        }
      }
      tagO = uniform64(*(LAS u64*)(uintptr_t)(L0 + SYNC + SY_TAG));
      __builtin_amdgcn_s_setprio(2);
      STAMP(6);
      wait4(L0 + SYNC + SY_KV);
      STAMP(8);
      const unsigned ob = WS_O + ((rt * 8 + wl * 2) * 32 + 16 + hp * 2) * 1024 + lane * 16;
#pragma unroll
      for (int h = 0; h < 2; ++h) {
        const unsigned kb = L0 + B_KVL + h * 2048;
        const bf16x8 kv0 = as_frag(lds_ld128(kb + lane * 16)), kv1 = as_frag(lds_ld128(kb + 1024 + lane * 16));
        const bf16x8 zf = as_frag(lds_ld128(kb + 1024 + (g * 16 + 12) * 16));
#pragma unroll
        for (int t = 0; t < 2; ++t) {
          const f32x4 n0 = mfma(kv0, as_frag(pqf[t][h]), z4);
          const f32x4 n1 = mfma(kv1, as_frag(pqf[t][h]), z4);
          const f32x4 dn = mfma(zf, as_frag(pqf[t][h]), z4);
          const float s = omg[t] * __builtin_amdgcn_rcpf(dn[0] + 1e-6f);
          const u32x2 a = pk4(n0 * s), b = pk4(n1 * s);
          __builtin_amdgcn_raw_buffer_store_b128((u32x4){a[0], a[1], g3 ? 0u : b[0], g3 ? 0u : b[1]}, wsr, ob + (t * 32 + h) * 1024, 0, 16);
        }
      }
      STAMP(9);
      VM_WAIT(0);
      if (lane == 0) __hip_atomic_store((gu64*)(ws + WS_FB) + ((rt * 4 + wl) * 8 + hp) * FS, tagO, RLX_AGENT);
      STAMP(10); STAMPC(19);
      if (cg >= 7) { STAMP_FLUSH(); return; }
    }
  } else if (__builtin_expect(br, 1)) {
      __builtin_amdgcn_s_setprio(2);
      setup(B_G);
      f32x4 ak[4][2], av[4][2];
      {
        const float one = li == 12 ? 1.f : 0.f, i0 = rh ? one : 0.f, i1 = rh ? 0.f : one;
#pragma unroll
        for (int t = 0; t < 4; ++t) {
          ak[t][0] = ak[t][1] = (f32x4){kEps, kEps, kEps, kEps};
          av[t][0] = (f32x4){i0, i0, i0, i0};
          av[t][1] = (f32x4){i1, i1, i1, i1};
        }
      }
      bf16x8 xf[2][4], fw[2][4];
      unsigned xb, wb;
      const bool z0 = p == 3 && rh, z1 = p == 3 && !rh;
      const unsigned zaddr = L0 + SYNC + SY_ZERO;
      auto rd = [&](int ks, int part) {
        const unsigned xa = xb ^ (ks * 64), wk = wb + ks * 3584;
        if (part & 1) {
          const unsigned a = wk + WM_B + (((part >> 1) ^ rh) << 5);
          const bool z = (part >> 1) ? z1 : z0;
          fw[ks][part] = lds_tr2(z ? zaddr : a, z ? zaddr : a + 112);
        } else {
          fw[ks][part] = lds_tr2(wk + (part >> 1) * 32, wk + (part >> 1) * 32 + 112);
        }
        if (part < 2) {
          xf[ks][part * 2] = as_frag(lds_ld128(xa + part * 4096));
          xf[ks][part * 2 + 1] = as_frag(lds_ld128(xa + part * 4096 + 2048));
        }
      };
      auto mm = [&](int ks, int part) {
#pragma unroll
        for (int t = 0; t < 4; ++t) {
          if (part & 1) av[t][part >> 1] = mfma(xf[ks][t], fw[ks][part], av[t][part >> 1]);
          else ak[t][part >> 1] = mfma(xf[ks][t], fw[ks][part], ak[t][part >> 1]);
        }
      };
#pragma unroll
      for (int c = 0; c < 7; ++c) {
        xb = opaque(L0 + b_slot(c) + rh * 8192 + xsw);
        wb = opaque(L0 + b_slot(c) + B_W + wln);
        hook(c, true, true);
        LDS_BAR();
        hook(c, false, true);
        if (c == 0) { STAMP(2); STAMPC(16); }
        if (c == 3) STAMP(3);
        if (c == 6) { STAMP(4); STAMPC(17); }
        SB();
#pragma unroll
        for (int ks = 0; ks < 2; ++ks) {
#pragma unroll
          for (int part = 0; part < 4; ++part) rd(ks, part);
          if (ks == 1 || c > 0) {
#pragma unroll
            for (int part = 0; part < 4; ++part) mm(ks ^ 1, part);
            interleave<16, 12>();
          }
          SB();
        }
      }
      sig(L0 + SYNC + SY_D6 + w * 4);
      mm(1, 0); mm(1, 2);
      SB();
      mm(1, 3); mm(1, 1);
      STAMP(5);
      f32x4 kv[2][2];
      {
        u32x2 pk[4][2], vf[4][2];
#pragma unroll
        for (int t = 0; t < 4; ++t) {
          pk[t][0] = phi4(ak[t][0]);
          pk[t][1] = phi4(ak[t][1]);
          vf[t][0] = pk4(av[t][0]);
          vf[t][1] = pk4(av[t][1]);
        }
#pragma unroll
        for (int e = 1; e >= 0; --e)
#pragma unroll
          for (int d = 0; d < 2; ++d) {
            kv[e][d] = mfma(frag2(pk[0][d], pk[1][d]), frag2(vf[0][e], vf[1][e]), z4);
            kv[e][d] = mfma(frag2(pk[2][d], pk[3][d]), frag2(vf[2][e], vf[3][e]), kv[e][d]);
          }
      }
      lds_st128(L0 + B_EX + w * 2048 + lane * 16, __builtin_bit_cast(u32x4, kv[1][0]));
      lds_st128(L0 + B_EX + w * 2048 + 1024 + lane * 16, __builtin_bit_cast(u32x4, kv[1][1]));
      sig(L0 + SYNC + SY_EX + w * 4);
      wait1(L0 + SYNC + SY_EX + (w ^ 1) * 4);
      const f32x4 s0 = kv[0][0] + __builtin_bit_cast(f32x4, lds_ld128(L0 + B_EX + (w ^ 1) * 2048 + lane * 16));
      const f32x4 s1 = kv[0][1] + __builtin_bit_cast(f32x4, lds_ld128(L0 + B_EX + (w ^ 1) * 2048 + 1024 + lane * 16));
      {
        const u32x2 a = pk4(s0), b = pk4(s1);
        publish((u32x4){a[0], a[1], b[0], b[1]}, wsr, lane * 16 + kvs + rt * 1024);
      }
      STAMP(6);
      if (!__all(clr == tagO)) poll_flags(clrp(), tagO);
      wo_run(0, QWO);
      STAMP(7);
      f32x4 sum0, sum1;
      {
        const unsigned e0 = q == g ? ((p & 1) ? 0x3f800000u : 0x3f80u) : 0u;
        const unsigned ea = p < 2 ? e0 : 0u, eb = p < 2 ? 0u : e0;
        const bf16x8 sel0 = as_frag((u32x4){ea, eb, 0u, 0u}), sel1 = as_frag((u32x4){0u, 0u, ea, eb});
        unsigned vo = kvs + lane * 16;
#pragma nounroll
        for (unsigned spins = 0; spins < SPIN_CAP; ++spins) {
          asm volatile("" : "+v"(vo)::"memory");
          u32x4 pr[16];
#pragma unroll
          for (int r = 0; r < 16; ++r) pr[r] = __builtin_amdgcn_raw_buffer_load_b128(wsr, vo, ((r + 2 * rt) & 15) * 1024, 16);
          SB();
          sum0 = sum1 = z4;
#pragma unroll
          for (int r = 0; r < 16; ++r) {
            sum0 = mfma(sel0, as_frag(pr[r]), sum0);
            sum1 = mfma(sel1, as_frag(pr[r]), sum1);
          }
          if (!__any(sum0[0] != sum0[0])) break;
          unsigned m = 0;
#pragma unroll
          for (int r = 0; r < 16; ++r) m = max(max(max(max(m, pr[r][0]), pr[r][1]), pr[r][2]), pr[r][3]);
          if (!__any(m == SENT)) break;
        }
      }
      STAMP(8);
      lds_st128(L0 + B_KVL + w * 1024 + lane * 16, __builtin_bit_cast(u32x4, frag2(pk4(sum0), pk4(sum1))));
      sig(L0 + SYNC + SY_KV + w * 4);
      STAMP(10); STAMPC(19);
      __builtin_amdgcn_s_setprio(0);
      if (cg >= 7) { STAMP_FLUSH(); return; }
      sig(L0 + SYNC + SY_WOA + w * 4);
      wait4(L0 + SYNC + SY_D6);
      wait4(L0 + SYNC + SY_QD);
      wo_run(QWO, 28);
      VM_WAIT(0);
      sig(L0 + SYNC + SY_WOB + w * 4);
  } else {
      asm volatile("; role A");
      setup(A_G);
      f32x4 aq[4][2], ak[5][2], av[5][2];
#pragma unroll
      for (int t = 0; t < 5; ++t)
#pragma unroll
        for (int f = 0; f < 2; ++f) { ak[t][f] = av[t][f] = z4; if (t < 4) aq[t][f] = z4; }
      const unsigned gl = L0 + A_G + (8 * g + q) * 8;
      const bool p3 = p == 3;
      const unsigned d1 = p3 ? 32u : 112u;
      bf16x8 xf[2][5], fq[2][2], fk[2][2], fv[2][2];
      unsigned xb, wb;
      auto rd = [&](int c, int ks, int part) {
        const unsigned xa = xb ^ (ks * 64), wk = wb + ks * 3584;
        if (part == 0) {
          fk[ks][0] = lds_tr2(wk + WM_B, wk + WM_B + 112);
          xf[ks][0] = as_frag(lds_ld128(xa));
          xf[ks][1] = as_frag(lds_ld128(xa + 2048));
        } else if (part == 1) {
          fv[ks][0] = lds_tr2(wk + 2 * WM_B, wk + 2 * WM_B + 112);
          xf[ks][2] = as_frag(lds_ld128(xa + 4096));
        } else if (part == 2) {
          fq[ks][0] = lds_tr2(wk, wk + 112);
          xf[ks][3] = as_frag(lds_ld128(xa + 6144));
        } else if (part == 3) {
          fk[ks][1] = lds_tr2(wk + WM_B + 32, wk + WM_B + 32 + 112);
          xf[ks][4] = as_frag(lds_ld128(xa + 8192));
        } else if (part == 4) {
          fv[ks][1] = lds_tr2(wk + 2 * WM_B + 32, wk + 2 * WM_B + 32 + 112);
        } else {
          const unsigned a = p3 ? gl + c * 512 + ks * 256 : wk + 32;
          fq[ks][1] = lds_tr2(a, a + d1);
        }
      };
      auto mm = [&](int ks, int part) {
        const int f = part / 3, m = part % 3;
#pragma unroll
        for (int t = 0; t < 5; ++t) {
          if (m == 0) ak[t][f] = mfma(fk[ks][f], xf[ks][t], ak[t][f]);
          if (m == 1) av[t][f] = mfma(xf[ks][t], fv[ks][f], av[t][f]);
          if (m == 2 && t > 0) aq[t - 1][f] = mfma(fq[ks][f], xf[ks][t], aq[t - 1][f]);
        }
      };
#pragma unroll
      for (int c = 0; c < 7; ++c) {
        xb = opaque(L0 + a_slot(c) + rh * 8192 + xsw);
        wb = opaque(L0 + a_slot(c) + A_W + wln);
        hook(c, true, false);
        LDS_BAR();
        hook(c, false, false);
        if (c == 0) { STAMP(2); STAMPC(16); }
        if (c == 3) STAMP(3);
        if (c == 6) { STAMP(4); STAMPC(17); }
        SB();
#pragma unroll
        for (int ks = 0; ks < 2; ++ks) {
#pragma unroll
          for (int part = 0; part < 6; ++part) rd(c, ks, part);
          if (ks == 1 || c > 0) {
#pragma unroll
            for (int part = 0; part < 6; ++part) mm(ks ^ 1, part);
            interleave<28, 17>();
          }
          SB();
        }
      }
      sig(L0 + SYNC + SY_D6 + w * 4);
      mm(1, 0); mm(1, 3); mm(1, 2); mm(1, 5);
      SB();
      mm(1, 1); mm(1, 4);
      STAMP(5);
      const bool odd = li & 1;
      const int kk0 = 4 * g + (li & 1);
      const bool own0 = kk0 <= li, own1 = kk0 + 2 <= li;
      const float sc = 0.18898223650461362f * kLog2e;
      const float ninf = (rt == 0 && rh == 0) ? -__builtin_inff() : 0.f;
      const unsigned l16 = lane_id() * 16;
      bf16x8 kf[5];
#pragma unroll
      for (int t = 0; t < 5; ++t) kf[t] = frag2(pk4(ak[t][0]), pk4(ak[t][1]));
      f32x4 sp[4], so[4];
#pragma unroll
      for (int t = 0; t < 4; ++t) {
        const u32x2 b = pk4(aq[t][1]);
        const bf16x8 qf = frag2(pk4(aq[t][0]), (u32x2){g3 ? 0u : b[0], g3 ? 0u : b[1]});
        sp[t] = mfma(kf[t], qf, t == 0 ? (f32x4){ninf, ninf, ninf, ninf} : z4);
        so[t] = mfma(kf[t + 1], qf, z4);
      }
      float ug;
      {
        float z0 = aq[0][1][0], z1 = aq[1][1][0], z2 = aq[2][1][0], z3 = aq[3][1][0];
        swap32(z0, z1);
        swap32(z2, z3);
        swap16(z1, z3);
        ug = 1.f + __builtin_amdgcn_exp2f(-kLog2e * z3);
      }
      u32x2 vf[5][2];
#pragma unroll
      for (int t = 0; t < 5; ++t) {
        vf[t][0] = pk4(av[t][0]);
        vf[t][1] = pk4(av[t][1]);
      }
      float l0[4], l1[4], bc[4];
#pragma unroll
      for (int t = 0; t < 4; ++t) {
        float a0 = so[t][0], a2 = so[t][2], b0 = sp[t][0], b2 = sp[t][2];
        const float c0 = own0 ? a0 : b0, c1 = own0 ? so[t][1] : sp[t][1], c2 = own1 ? a2 : b2, c3 = own1 ? so[t][3] : sp[t][3];
        l0[t] = odd ? c1 : c0;
        l1[t] = odd ? c3 : c2;
      }
      rows_bcast4(rows_reduce4<true>(fmax1(l0[0], l1[0]), fmax1(l0[1], l1[1]), fmax1(l0[2], l1[2]), fmax1(l0[3], l1[3])) * -sc, bc);
#pragma unroll
      for (int t = 0; t < 4; ++t) {
        l0[t] = __builtin_amdgcn_exp2f(__builtin_fmaf(l0[t], sc, bc[t]));
        l1[t] = __builtin_amdgcn_exp2f(__builtin_fmaf(l1[t], sc, bc[t]));
      }
      rows_bcast4(__builtin_amdgcn_rcpf(rows_reduce4<false>(l0[0] + l1[0], l0[1] + l1[1], l0[2] + l1[2], l0[3] + l1[3]) * ug), bc);
      const unsigned sh = odd ? 16u : 0u;
      const unsigned mk0 = own0 ? 0u : 0xffffu, mk1 = own1 ? 0u : 0xffffu;
#pragma unroll
      for (int t = 0; t < 4; ++t) {
        const unsigned pe = pk2(l0[t] * bc[t], l1[t] * bc[t]), ph = pe >> 16;
        const bf16x8 pf = as_frag((u32x4){(pe & mk0) << sh, (ph & mk1) << sh, (pe & (mk0 ^ 0xffffu)) << sh, (ph & (mk1 ^ 0xffffu)) << sh});
        const f32x4 r0 = mfma(frag2(vf[t][0], vf[t + 1][0]), pf, z4);
        const f32x4 r1 = mfma(frag2(vf[t][1], vf[t + 1][1]), pf, z4);
        const u32x2 c0 = pk4(r0), c1 = pk4(r1);
        __builtin_amdgcn_raw_buffer_store_b128((u32x4){c0[0], c0[1], g3 ? 0u : c1[0], g3 ? 0u : c1[1]}, wsr, l16 + obs + t * 32768, 0, 16);
      }
      STAMP(9);
      VM_WAIT(0);
      if (l16 == 0) __hip_atomic_store((gu64*)(ws + WS_FA) + ((rt * 2 + rh) * 16 + hp * 2 + hh) * FS, tagO, RLX_AGENT);
      STAMP(10); STAMPC(19);
      if (cg >= 7) {
        if (bid == 56 && w == 0) {
#pragma nounroll
          for (int k = 0; k < 4; ++k) {
            const int i = (k & 1) * 64 + lane;
            poll_flags(k < 2 ? (gu64*)(ws + WS_CLR) + i : (gu64*)(ws + WS_FA) + ((i >> 3) * 32 + (i & 7) * 2) * FS, tagO);
          }
          if (lane == 0) __hip_atomic_store((gu64*)(ws + WS_E), tagO, RLX_AGENT);
        }
        STAMP_FLUSH();
        return;
      }
  }

  const int pb = w >> 2;
  wait4(L0 + SYNC + (pb ? SY_WOB : SY_WOA));
  const unsigned sw = (2 * g + (q >> 1)) & 3;
  const unsigned rb = L0 + (pb * 448 + 4 * g + q) * 128 + 8 * p;
  unsigned wa[4], wa2[4];
#pragma unroll
  for (int v = 0; v < 4; ++v) {
    wa[v] = opaque(rb + ((v ^ sw) << 5));
    wa2[v] = opaque(wa[v] + (g3 ? 0u : 2048u));
  }
  auto wfrag = [&](int h, int ct) {
    const int v = ct ^ (2 * (h & 1));
    return lds_tr2(wa[v] + h * 3584, wa2[v] + h * 3584);
  };
  constexpr int NPRE = 8;
  bf16x8 wf[16][4];
#pragma unroll
  for (int h = 0; h < NPRE; ++h)
#pragma unroll
    for (int ct = 0; ct < 4; ++ct) wf[h][ct] = wfrag(h, ct);
  STAMP(11);
  const unsigned ub = WS_O + ((rg * 4 + wl) * 32 + pb * 16) * 1024;
  tagO = uniform64(*(LAS u64*)(uintptr_t)(L0 + SYNC + SY_TAG));
  poll_flags(pb ? (gu64*)(ws + WS_FB) + ((rg * 2 + (wl >> 1)) * 8 + (li & 7)) * FS : (gu64*)(ws + WS_FA) + (rg * 16 + li) * FS, tagO);
  STAMP(12);
  u32x4 of[16];
  {
    unsigned vo = ub + lane * 16;
    asm volatile("" : "+v"(vo)::"memory");
#pragma unroll
    for (int h = 0; h < 16; ++h) of[h] = __builtin_amdgcn_raw_buffer_load_b128(wsr, vo, ((h + 2 * cg) & 15) * 1024, 16);
    SB();
  }
  f32x4 acc[4] = {z4, z4, z4, z4};
#pragma unroll
  for (int h = 0; h < 16; ++h) {
#pragma unroll
    for (int ct = 0; ct < 4; ++ct) acc[ct] = mfma(wf[h][ct], as_frag(of[h]), acc[ct]);
    SB();
    if (h + NPRE < 16) {
#pragma unroll
      for (int ct = 0; ct < 4; ++ct) wf[h + NPRE][ct] = wfrag(h + NPRE, ct);
    }
    SB();
  }
  STAMP(13);
  const unsigned xa = L0 + P2X + wl * 4096 + lane * 16;
  if (!pb) {
    wait4(L0 + SYNC + SY_D6);
#pragma unroll
    for (int ct = 0; ct < 4; ++ct) lds_st128(xa + ct * 1024, __builtin_bit_cast(u32x4, acc[ct]));
    sig(L0 + SYNC + SY_P2 + wl * 4);
  } else {
    wait1(L0 + SYNC + SY_P2 + wl * 4);
    char* orow = out + (rg * 64 + wl * 16 + li) * XROW + cg * 128 + g * 8;
#pragma unroll
    for (int ct = 0; ct < 4; ++ct) {
      const f32x4 r = acc[ct] + __builtin_bit_cast(f32x4, lds_ld128(xa + ct * 1024));
      *(u32x2*)(orow + ct * 32) = pk4(r);
    }
  }
  STAMP(14);
  STAMP(15);
  STAMP_FLUSH();
}

}

extern "C" void kernel_launch(void* const* d_in, const int* in_sizes, int n_in, void* d_out, int out_size, void* d_ws,
                              size_t ws_size, hipStream_t stream) {
  (void)in_sizes; (void)n_in; (void)out_size; (void)ws_size;
  gated_mix_kernel<<<256, 512, 0, stream>>>((const char*)d_in[0], (const char*)d_in[1], (const char*)d_in[2],
                                            (const char*)d_in[4], (const char*)d_in[5],
                                            (const unsigned short*)d_in[6], (char*)d_out, (char*)d_ws);
}
```

```cpp
#define DIAG_A 0
#define DIAG_B 0
#define DIAG_ROLE 0
#include <hip/hip_runtime.h>
#include <stdint.h>

typedef __bf16 bf16x2 __attribute__((ext_vector_type(2)));
typedef __bf16 bf16x4 __attribute__((ext_vector_type(4)));
typedef __bf16 bf16x8 __attribute__((ext_vector_type(8)));
typedef float f32x2 __attribute__((ext_vector_type(2)));
typedef float f32x4 __attribute__((ext_vector_type(4)));
typedef unsigned u32x2 __attribute__((ext_vector_type(2)));
typedef unsigned u32x4 __attribute__((ext_vector_type(4)));
typedef unsigned long long u64;
#define LAS __attribute__((address_space(3)))
#define GAS __attribute__((address_space(1)))
typedef GAS u64 gu64;
#define RLX_AGENT __ATOMIC_RELAXED, __HIP_MEMORY_SCOPE_AGENT

namespace {

constexpr int kS = 2048, kD = 448;
constexpr int XROW = kD * 2;
constexpr int WROW = 3 * kD * 2;
constexpr int WM_B = 64 * 112;

constexpr int A_FRONT = 34816, A_SLOT = 144 * 128 + 3 * WM_B, A_W = 144 * 128;
constexpr int A_G = A_FRONT + 3 * A_SLOT;
constexpr int B_SLOT = 128 * 128 + 2 * WM_B, B_W = 128 * 128;
constexpr int B_WQ = 97536, B_G = B_WQ + 7 * WM_B, B_EX = B_G + 3584, B_KVL = B_EX + 8192;
constexpr int P2X = 114688;
constexpr int SYNC = 163584;
constexpr int LDS_B = 163840;
static_assert(A_G + 3584 <= SYNC && B_KVL + 4096 <= SYNC && A_FRONT + 2 * A_SLOT == 114688, "lds map");
__host__ __device__ constexpr int a_slot(int c) { return A_FRONT + ((c + 2) % 3) * A_SLOT; }
__host__ __device__ constexpr int b_slot(int c) { return ((c + 2) % 3) * B_SLOT; }
enum { SY_WOA = 0, SY_WOB = 16, SY_D6 = 32, SY_D6Q = 48, SY_WQ = 64, SY_QD = 80, SY_EX = 96, SY_KV = 112, SY_P2 = 128, SY_ZERO = 192, SY_TAG = 240 };

constexpr int WS_E = 0;
constexpr int WS_CLR = 4096;
constexpr int FS = 16;
constexpr int WS_FA = 8192;
constexpr int WS_FB = WS_FA + 16 * 2 * 16 * 8 * FS;
constexpr int WS_KVP = WS_FB + 16 * 4 * 8 * 8 * FS;
constexpr int WS_O = WS_KVP + 8 * 4 * 16 * 1024;
constexpr int WS_END = WS_O + 16 * 8 * 32 * 1024;

constexpr unsigned SPIN_CAP = 1u << 14;
constexpr unsigned SENT = 0xffffffffu;
#define ADELAY 0
#define QWO 14

#define STAMP(i) do { if (i == DIAG_A) dta = (unsigned)__builtin_amdgcn_s_memrealtime(); if (i == DIAG_B) dtb = (unsigned)__builtin_amdgcn_s_memrealtime(); } while (0)
#define STAMPC(i) do {} while (0)
#define STAMP_FLUSH() do { if (br * 2 + (w >> 2) == DIAG_ROLE) { unsigned d = (dtb && dta) ? (dtb - dta) * 8u : 0u; d = d > 100000u ? 100000u : d; const unsigned t0 = dt0; while ((unsigned)__builtin_amdgcn_s_memrealtime() - t0 < 4000u + d) __builtin_amdgcn_s_sleep(2); } } while (0)

__device__ __forceinline__ unsigned pk2(float a, float b) {
  f32x2 v = {a, b};
  bf16x2 r = __builtin_convertvector(v, bf16x2);
  return __builtin_bit_cast(unsigned, r);
}
__device__ __forceinline__ u32x2 pk4(f32x4 v) { return (u32x2){pk2(v[0], v[1]), pk2(v[2], v[3])}; }
__device__ __forceinline__ bf16x8 as_frag(u32x4 v) { return __builtin_bit_cast(bf16x8, v); }
__device__ __forceinline__ bf16x8 frag2(u32x2 a, u32x2 b) { return as_frag((u32x4){a[0], a[1], b[0], b[1]}); }
__device__ __forceinline__ f32x4 mfma(bf16x8 a, bf16x8 b, f32x4 c) {
  return __builtin_amdgcn_mfma_f32_16x16x32_bf16(a, b, c, 0, 0, 0);
}
__device__ __forceinline__ bf16x8 lds_tr2(unsigned a0, unsigned a1) {
  bf16x4 lo = __builtin_amdgcn_ds_read_tr16_b64_v4bf16((LAS bf16x4*)(uintptr_t)a0);
  bf16x4 hi = __builtin_amdgcn_ds_read_tr16_b64_v4bf16((LAS bf16x4*)(uintptr_t)a1);
  return __builtin_shufflevector(lo, hi, 0, 1, 2, 3, 4, 5, 6, 7);
}
__device__ __forceinline__ u32x4 lds_ld128(unsigned a) { return *(LAS u32x4*)(uintptr_t)a; }
__device__ __forceinline__ void lds_st128(unsigned a, u32x4 v) { *(LAS u32x4*)(uintptr_t)a = v; }
__device__ __forceinline__ void lds_st64(unsigned a, u32x2 v) { *(LAS u32x2*)(uintptr_t)a = v; }
__device__ __forceinline__ unsigned opaque(unsigned v) { asm volatile("" : "+v"(v)); return v; }

__device__ __forceinline__ void sig(unsigned a) {
  asm volatile("" ::: "memory");
  *(volatile LAS unsigned*)(uintptr_t)a = 1u;
  asm volatile("" ::: "memory");
  __builtin_amdgcn_sched_barrier(0);
}
__device__ __forceinline__ void wait1(unsigned a) {
  while (__builtin_amdgcn_readfirstlane(*(volatile LAS unsigned*)(uintptr_t)a) == 0u) __builtin_amdgcn_s_sleep(1);
  asm volatile("" ::: "memory");
}
__device__ __forceinline__ void wait4(unsigned a) {
  for (;;) {
    const u32x4 v = *(volatile LAS u32x4*)(uintptr_t)a;
    if (__builtin_amdgcn_readfirstlane(v[0] & v[1] & v[2] & v[3])) break;
    __builtin_amdgcn_s_sleep(1);
  }
  asm volatile("" ::: "memory");
}

__device__ __forceinline__ u32x4 make_srd(const void* p, unsigned bytes) {
  uint64_t a = (uint64_t)p;
  u32x4 r;
  r[0] = __builtin_amdgcn_readfirstlane((unsigned)a);
  r[1] = __builtin_amdgcn_readfirstlane((unsigned)(a >> 32) & 0xffffu);
  r[2] = bytes;
  r[3] = 0x00020000u;
  return r;
}
__device__ __forceinline__ void dma16(u32x4 srd, unsigned voff, unsigned soff, unsigned ldsaddr) {
  asm volatile(
      "s_mov_b32 m0, %3\n\t"
      "s_nop 3\n\t"
      "buffer_load_dwordx4 %0, %1, %2 offen lds"
      :
      : "v"(voff), "s"(srd), "s"(soff), "s"(ldsaddr));
}
#define VM_BAR(N) asm volatile("s_waitcnt vmcnt(" #N ") lgkmcnt(0)\n\ts_barrier" ::: "memory")
#define LDS_BAR() asm volatile("s_waitcnt lgkmcnt(0)\n\ts_barrier" ::: "memory")
#define VM_WAIT(N) asm volatile("s_waitcnt vmcnt(" #N ")" ::: "memory")
template <int N> __device__ __forceinline__ void vm_bar() { asm volatile("s_waitcnt vmcnt(%0) lgkmcnt(0)\n\ts_barrier" ::"n"(N) : "memory"); }
#define SB() __builtin_amdgcn_sched_barrier(0)

__device__ __forceinline__ float fmax1(float a, float b) { return __builtin_amdgcn_fmed3f(a, b, 3.0e38f); }
__device__ __forceinline__ void swap16(float& a, float& b) {
  auto r = __builtin_amdgcn_permlane16_swap(__float_as_uint(a), __float_as_uint(b), false, false);
  const unsigned r0 = r[0], r1 = r[1];
  a = __uint_as_float(r0);
  b = __uint_as_float(r1);
}
__device__ __forceinline__ void swap32(float& a, float& b) {
  auto r = __builtin_amdgcn_permlane32_swap(__float_as_uint(a), __float_as_uint(b), false, false);
  const unsigned r0 = r[0], r1 = r[1];
  a = __uint_as_float(r0);
  b = __uint_as_float(r1);
}
template <bool MAX>
__device__ __forceinline__ float rows_reduce4(float v0, float v1, float v2, float v3) {
  swap32(v0, v1);
  swap32(v2, v3);
  float a = MAX ? fmax1(v0, v1) : v0 + v1, b = MAX ? fmax1(v2, v3) : v2 + v3;
  swap16(a, b);
  return MAX ? fmax1(a, b) : a + b;
}
__device__ __forceinline__ void rows_bcast4(float x, float (&o)[4]) {
  float y = x;
  swap16(x, y);
  o[0] = x; o[1] = x; o[2] = y; o[3] = y;
  swap32(o[0], o[1]);
  swap32(o[2], o[3]);
}
__device__ __forceinline__ float row3_bcast(float v) {
  float a = v;
  swap32(a, v);
  a = v;
  swap16(a, v);
  return v;
}
template <int NM, int ND>
__device__ __forceinline__ void interleave() {
#pragma unroll
  for (int i = 0; i < NM; ++i) {
    __builtin_amdgcn_sched_group_barrier(0x8, 1, 0);
    if (i < ND) {
      __builtin_amdgcn_sched_group_barrier(0x2, 1, 0);
      __builtin_amdgcn_sched_group_barrier(0x100, 1, 0);
    }
  }
}
constexpr float kEps = 1e-6f, kLog2e = 1.44269504088896f;
__device__ __forceinline__ float phi(float u) {
  const float e = __builtin_amdgcn_fmed3f(__builtin_amdgcn_exp2f(__builtin_fmaf(u, kLog2e, -kEps * kLog2e)), 0.f, 1.f);
  return fmax1(u, kEps) + e;
}
__device__ __forceinline__ u32x2 phi4(f32x4 v) { return (u32x2){pk2(phi(v[0]), phi(v[1])), pk2(phi(v[2]), phi(v[3]))}; }
__device__ __forceinline__ float sigmoidf(float z) {
  return __builtin_amdgcn_rcpf(1.f + __builtin_amdgcn_exp2f(-1.44269504088896f * z));
}
__device__ __forceinline__ u64 next_tag(u64 e) {
  const u64 s = ((u64)(unsigned)__builtin_amdgcn_readfirstlane((unsigned)(e >> 32)) << 32) |
                (unsigned)__builtin_amdgcn_readfirstlane((unsigned)e);
  return s * 6364136223846793005ull + 1442695040888963407ull;
}
__device__ __forceinline__ u64 uniform64(u64 e) {
  return ((u64)(unsigned)__builtin_amdgcn_readfirstlane((unsigned)(e >> 32)) << 32) |
         (unsigned)__builtin_amdgcn_readfirstlane((unsigned)e);
}
__device__ __forceinline__ void poll_flags(const gu64* p, u64 tag) {
#pragma nounroll
  for (unsigned spins = 0; spins < SPIN_CAP; ++spins) {
    u64 v = __hip_atomic_load(p, RLX_AGENT);
    if (__all(v == tag)) break;
    __builtin_amdgcn_s_sleep(2);
  }
  asm volatile("" ::: "memory");
}
__device__ __forceinline__ void publish(u32x4 v, __amdgpu_buffer_rsrc_t r, unsigned voff) {
#pragma unroll
  for (int i = 0; i < 4; ++i) v[i] = v[i] < SENT - 1u ? v[i] : SENT - 1u;
  __builtin_amdgcn_raw_buffer_store_b128(v, r, voff, 0, 16);
}
__device__ __forceinline__ unsigned lane_id() {
  unsigned l = __builtin_amdgcn_mbcnt_hi(~0u, __builtin_amdgcn_mbcnt_lo(~0u, 0u));
  asm volatile("" : "+v"(l));
  return l;
}
__device__ __forceinline__ int wrap7(int c) { return c >= 7 ? c - 7 : c; }

__global__ __launch_bounds__(512, 2) void gated_mix_kernel(const char* __restrict__ x, const char* __restrict__ wqkvA,
                                                           const char* __restrict__ woA, const char* __restrict__ wqkvB,
                                                           const char* __restrict__ woB,
                                                           const unsigned short* __restrict__ gate_w, char* __restrict__ out,
                                                           char* ws) {
  __shared__ __attribute__((aligned(1024))) char lds[LDS_B];
  const unsigned L0 = (unsigned)(uintptr_t)(LAS char*)lds;
  const int bid = blockIdx.x;
  const int xcd = bid & 7, idx = bid >> 3;
  const int xj = xcd >> 1, ci = idx >> 3;
  const int br = ci >> 1;
  const int hp = 2 * (br ? (xj ^ 1) : xj) + (ci & 1);
  const int rt = (xcd & 1) * 8 + (idx & 7);
  const int rot = wrap7(wrap7((idx & 7) + 2 * ci));
  const int cg = idx & 7, rg = 4 * xcd + ci;

  const int tid = threadIdx.x;
  const int lane = tid & 63;
  const int w = __builtin_amdgcn_readfirstlane(tid >> 6) & 7;
  const int wl = w & 3;
  const int li = lane & 15, g = lane >> 4, q = (lane >> 2) & 3, p = lane & 3;
  const bool g3 = g == 3;
  const f32x4 z4 = {0.f, 0.f, 0.f, 0.f};
  unsigned dta = 0, dtb = 0;
  const unsigned dt0 = (unsigned)__builtin_amdgcn_s_memrealtime();
  STAMP(0); STAMPC(18);
  unsigned vx0 = (lane >> 3) * XROW + (((lane & 7) ^ (lane >> 4)) << 4);
  unsigned vx1 = vx0 ^ 64u;
  unsigned vw0, vw1;
  {
    const unsigned s0 = wl * 64 + lane, s1 = s0 + 256;
    const unsigned p0 = (s0 * 9363u) >> 16, p1 = (s1 * 9363u) >> 16;
    const unsigned k0 = (p0 & 56) | ((p0 >> 1) & 3) | ((p0 & 1) << 2);
    const unsigned k1 = (p1 & 56) | ((p1 >> 1) & 3) | ((p1 & 1) << 2);
    vw0 = k0 * WROW + hp * 112 + (s0 - p0 * 7) * 16;
    vw1 = k1 * WROW + hp * 112 + (s1 - p1 * 7) * 16;
  }
  asm volatile("" : "+v"(vx0), "+v"(vx1), "+v"(vw0), "+v"(vw1));
  asm volatile("" ::"s"(x), "s"(wqkvA), "s"(woA), "s"(wqkvB), "s"(woB), "s"(gate_w), "s"(out), "s"(ws));
  const __amdgpu_buffer_rsrc_t wsr = __builtin_amdgcn_make_buffer_rsrc(ws, 0, WS_END, 0x00020000);
  u64 tagO;
  auto wo_run = [&](int j0, int j1) {
    const unsigned vwo = (lane >> 3) * XROW + cg * 128 + (((lane & 7) ^ (((lane >> 4) & 3) << 1)) << 4);
    const u32x4 sa = make_srd(woA, kD * XROW), sb = make_srd(woB, kD * XROW);
    unsigned so = (wl + 7 * cg + 4 * (j0 % 14)) * (8 * XROW);
    so = so >= kD * XROW ? so - kD * XROW : so;
#pragma unroll
    for (int j = j0; j < j1; ++j) {
      dma16(j < 14 ? sa : sb, vwo, so, L0 + (wl + 4 * j) * 1024);
      so += 32 * XROW;
      so = so >= kD * XROW ? so - kD * XROW : so;
    }
  };
  unsigned gv = 0, cpf = 0;
  u64 epoch = 0;
  {
    int wx = w;
    asm volatile("" : "+s"(wx));
    if (wx < 4) {
      gv = ((const unsigned*)gate_w)[tid < 224 ? tid : 0];
      epoch = __hip_atomic_load((gu64*)(ws + WS_E), RLX_AGENT);
    }
  }

    const int rh = w & 1, hh = w >> 1;
    const unsigned obs = WS_O + ((rt * 8 + rh * 4) * 32 + hp * 2 + hh) * 1024;
    const unsigned kvs = WS_KVP + (hp * 4 + w) * 16 * 1024;
    auto setup = [&](int goff) {
      if (tid < 60) *(LAS unsigned*)(uintptr_t)(L0 + SYNC + tid * 4) = 0u;
      const unsigned lo = (gv & 0xffffu) * 0x10001u, hi = (gv >> 16) * 0x10001u;
      const int pos = tid - rot * 32;
      if (tid < 224) lds_st128(L0 + goff + (pos < 0 ? pos + 224 : pos) * 16, (u32x4){lo, lo, hi, hi});
    };
    u64 clr = 0;
    auto clrp = [&]() { return (const gu64*)(ws + WS_CLR) + hp * 16 + (lane_id() & 15); };
    auto hook = [&](int c, bool before, bool isb) {
      if (c == 0 && !before && isb)
        __builtin_amdgcn_raw_buffer_store_b128((u32x4){SENT, SENT, SENT, SENT}, wsr, lane_id() * 16 + kvs + rt * 1024, 0, 16);
      if (c == 3 && before) {
        asm volatile("" : "+v"(epoch) : "v"(cpf));
        tagO = next_tag(epoch);
        *(LAS u64*)(uintptr_t)(L0 + SYNC + SY_TAG) = tagO;
        VM_WAIT(0);
      }
      if (c == 3 && !before && isb)
        __builtin_amdgcn_raw_buffer_store_b64((u32x2){(unsigned)tagO, (unsigned)(tagO >> 32)}, wsr,
                                              (lane_id() | w) == 0 ? WS_CLR + (hp * 16 + rt) * 8 : 0x7ffffff0u, 0, 16);
      if (c == 6 && !before && isb) clr = __hip_atomic_load(clrp(), RLX_AGENT);
    };
    const unsigned xsw = li * 128 + ((g ^ (li >> 1)) << 4);
    const unsigned wln = (8 * g + 2 * q) * 112 + 8 * p + 56 * hh;

  if (__builtin_expect(w >= 4, 1)) {
    __builtin_amdgcn_s_waitcnt(0x0f70);
    const u32x4 srdX = make_srd(x, kS * XROW);
    const u32x4 srdW = make_srd(br ? wqkvB : wqkvA, kD * WROW);
    const bool l3 = wl == 3;
    const int xr = rt * 128 + wl * 32 - (br ? 0 : 16);
    const unsigned sx01 = (xr < 0 ? 0 : xr) * XROW, sx23 = (xr + 16) * XROW;
    const bool l3a = l3 && !br;
    const u32x4 srd2 = l3a ? srdX : srdW;
    const unsigned v2 = l3 ? vw0 : vw1;
    const unsigned v2a = l3a ? vx0 : v2, v2b = l3a ? vx1 : v2;
    const unsigned st2 = l3a ? 128u : 64u * WROW, s2a = l3a ? (rt * 128 + 112) * XROW : 0u;
    const unsigned s2d = l3a ? 8u * XROW : (unsigned)XROW, s2e = l3a ? 8u * XROW : 2u * XROW;
    const unsigned sub2 = (l3 ? wl : wl + 4) * 1024;
    auto chunk = [&](unsigned sl, unsigned lq, unsigned lk, int cc) {
      const unsigned sw = cc * (64 * WROW), sx = cc * 128, s2 = cc * st2 + s2a;
#pragma unroll
      for (int j = 0; j < 4; ++j)
        dma16(srdX, (j & 1) ? vx1 : vx0, (j < 2 ? sx01 : sx23) + (j & 1) * 8 * XROW + sx, sl + (wl * 4 + j) * 1024);
      dma16(srdW, vw0, sw + XROW, lk + wl * 1024);
      dma16(srdW, vw0, sw + 2 * XROW, lk + WM_B + wl * 1024);
      dma16(srd2, v2b, s2 + s2d, l3a ? sl + 17408 : lk + sub2);
      dma16(srd2, v2b, s2 + s2e, l3a ? sl + 17408 : lk + WM_B + sub2);
      if (!br) {
        dma16(srdW, vw0, sw, lq + wl * 1024);
        dma16(srd2, v2a, s2, l3a ? sl + 16384 : lq + sub2);
      }
    };
    if (!br) __builtin_amdgcn_s_sleep(ADELAY);
    int cc = rot;
    unsigned sl = L0 + (br ? b_slot(0) : a_slot(0));
    const int nc = br ? 2 : 7;
#pragma nounroll
    for (int c = 0; c < nc; ++c) {
      chunk(sl, sl + A_W, sl + (br ? B_W : A_W + WM_B), cc);
      cc = wrap7(cc + 1);
      sl = br ? L0 + b_slot(1) : (sl == L0 + a_slot(0) ? L0 + a_slot(1) : sl + A_SLOT);
      if (c > 0 && !br) vm_bar<10>();
    }
    if (!br) {
      if (cg >= 7) { vm_bar<0>(); STAMP_FLUSH(); return; }
      wo_run(0, 10);
      vm_bar<10>();
      wo_run(10, 28);
      VM_WAIT(14);
      sig(L0 + SYNC + SY_WOA + wl * 4);
      VM_WAIT(0);
      sig(L0 + SYNC + SY_WOB + wl * 4);
    } else {
      auto instr = [&](int n) {
        if (n < 56) {
          const int c = n >> 3, j = n & 7, cc = wrap7(c + rot);
          const unsigned sl = L0 + b_slot(c), sw = cc * (64 * WROW), sx = cc * 128;
          if (j < 4) dma16(srdX, (j & 1) ? vx1 : vx0, sx01 + j * 8 * XROW + sx, sl + (wl * 4 + j) * 1024);
          else dma16(srdW, j < 6 ? vw0 : v2, sw + ((j & 1) + 1) * XROW, sl + B_W + (j & 1) * WM_B + (j < 6 ? wl * 1024 : sub2));
        } else {
          const int c = (n - 56) >> 1;
          dma16(srdW, (n & 1) ? v2 : vw0, wrap7(c + rot) * (64 * WROW), L0 + B_WQ + c * WM_B + ((n & 1) ? sub2 : wl * 1024));
        }
      };
      STAMP(1);
      bf16x8 xq[14][2];
      const unsigned xl = L0 + (wl * 32 + li) * 128 + ((g ^ (li >> 1)) << 4);
#pragma unroll
      for (int c = 0; c < 7; ++c) {
        vm_bar<8>();
#pragma unroll
        for (int ks = 0; ks < 2; ++ks) {
          const unsigned xa = (xl ^ (ks * 64)) + b_slot(c);
          xq[c * 2 + ks][0] = as_frag(lds_ld128(xa));
          xq[c * 2 + ks][1] = as_frag(lds_ld128(xa + 2048));
        }
#pragma unroll
        for (int n = 8 * c + 16; n < (c < 6 ? 8 * c + 24 : 70); ++n) instr(n);
      }
      STAMP(4);
      VM_WAIT(0);
      sig(L0 + SYNC + SY_WQ + wl * 4);
      wait4(L0 + SYNC + SY_WQ);
      wait4(L0 + SYNC + SY_EX);
      STAMP(3);
      f32x4 aq[2][2][2];
#pragma unroll
      for (int i = 0; i < 8; ++i) aq[i >> 2][(i >> 1) & 1][i & 1] = (f32x4){kEps, kEps, kEps, kEps};
      const unsigned wb = L0 + B_WQ + (8 * g + 2 * q) * 112 + 8 * p;
      const unsigned gl = L0 + B_G + (8 * g + q) * 8;
      const bool p3 = p == 3;
      const unsigned d1 = p3 ? 32u : 112u;
      const unsigned qa0 = opaque(p3 ? gl : wb + 32), qstep = opaque(p3 ? 256u : 3584u);
      bf16x8 fq[3][4];
      auto rdq = [&](int s) {
        const unsigned wk = wb + s * 3584;
        const unsigned a = qa0 + s * qstep;
        fq[s % 3][0] = lds_tr2(wk, wk + 112);
        fq[s % 3][1] = lds_tr2(a, a + d1);
        fq[s % 3][2] = lds_tr2(wk + 56, wk + 56 + 112);
        fq[s % 3][3] = lds_tr2(wk + 88, wk + 88 + 112);
      };
      rdq(0);
      rdq(1);
#pragma unroll
      for (int s = 0; s < 14; ++s) {
        SB();
        if (s + 2 < 14) rdq(s + 2);
#pragma unroll
        for (int i = 0; i < 4; ++i) {
          aq[0][i >> 1][i & 1] = mfma(fq[s % 3][i], xq[s][0], aq[0][i >> 1][i & 1]);
          aq[1][i >> 1][i & 1] = mfma(fq[s % 3][i], xq[s][1], aq[1][i >> 1][i & 1]);
        }
        interleave<8, 8>();
      }
      sig(L0 + SYNC + SY_QD + wl * 4);
      STAMP(5);
      u32x4 pqf[2][2];
      float omg[2];
#pragma unroll
      for (int t = 0; t < 2; ++t) {
        omg[t] = 1.f - sigmoidf(row3_bcast(aq[t][0][1][0]));
#pragma unroll
        for (int h = 0; h < 2; ++h) {
          const u32x2 a = phi4(aq[t][h][0]), b = phi4(aq[t][h][1]);
          pqf[t][h] = (u32x4){a[0], a[1], g3 ? 0u : b[0], g3 ? 0u : b[1]};
        }
      }
      tagO = uniform64(*(LAS u64*)(uintptr_t)(L0 + SYNC + SY_TAG));
      __builtin_amdgcn_s_setprio(2);
      STAMP(6);
      wait4(L0 + SYNC + SY_KV);
      STAMP(8);
      const unsigned ob = WS_O + ((rt * 8 + wl * 2) * 32 + 16 + hp * 2) * 1024 + lane * 16;
#pragma unroll
      for (int h = 0; h < 2; ++h) {
        const unsigned kb = L0 + B_KVL + h * 2048;
        const bf16x8 kv0 = as_frag(lds_ld128(kb + lane * 16)), kv1 = as_frag(lds_ld128(kb + 1024 + lane * 16));
        const bf16x8 zf = as_frag(lds_ld128(kb + 1024 + (g * 16 + 12) * 16));
#pragma unroll
        for (int t = 0; t < 2; ++t) {
          const f32x4 n0 = mfma(kv0, as_frag(pqf[t][h]), z4);
          const f32x4 n1 = mfma(kv1, as_frag(pqf[t][h]), z4);
          const f32x4 dn = mfma(zf, as_frag(pqf[t][h]), z4);
          const float s = omg[t] * __builtin_amdgcn_rcpf(dn[0] + 1e-6f);
          const u32x2 a = pk4(n0 * s), b = pk4(n1 * s);
          __builtin_amdgcn_raw_buffer_store_b128((u32x4){a[0], a[1], g3 ? 0u : b[0], g3 ? 0u : b[1]}, wsr, ob + (t * 32 + h) * 1024, 0, 16);
        }
      }
      STAMP(9);
      VM_WAIT(0);
      if (lane == 0) __hip_atomic_store((gu64*)(ws + WS_FB) + ((rt * 4 + wl) * 8 + hp) * FS, tagO, RLX_AGENT);
      STAMP(10); STAMPC(19);
      if (cg >= 7) { STAMP_FLUSH(); return; }
    }
  } else if (__builtin_expect(br, 1)) {
      __builtin_amdgcn_s_setprio(2);
      setup(B_G);
      f32x4 ak[4][2], av[4][2];
      {
        const float one = li == 12 ? 1.f : 0.f, i0 = rh ? one : 0.f, i1 = rh ? 0.f : one;
#pragma unroll
        for (int t = 0; t < 4; ++t) {
          ak[t][0] = ak[t][1] = (f32x4){kEps, kEps, kEps, kEps};
          av[t][0] = (f32x4){i0, i0, i0, i0};
          av[t][1] = (f32x4){i1, i1, i1, i1};
        }
      }
      bf16x8 xf[2][4], fw[2][4];
      unsigned xb, wb;
      const bool z0 = p == 3 && rh, z1 = p == 3 && !rh;
      const unsigned zaddr = L0 + SYNC + SY_ZERO;
      auto rd = [&](int ks, int part) {
        const unsigned xa = xb ^ (ks * 64), wk = wb + ks * 3584;
        if (part & 1) {
          const unsigned a = wk + WM_B + (((part >> 1) ^ rh) << 5);
          const bool z = (part >> 1) ? z1 : z0;
          fw[ks][part] = lds_tr2(z ? zaddr : a, z ? zaddr : a + 112);
        } else {
          fw[ks][part] = lds_tr2(wk + (part >> 1) * 32, wk + (part >> 1) * 32 + 112);
        }
        if (part < 2) {
          xf[ks][part * 2] = as_frag(lds_ld128(xa + part * 4096));
          xf[ks][part * 2 + 1] = as_frag(lds_ld128(xa + part * 4096 + 2048));
        }
      };
      auto mm = [&](int ks, int part) {
#pragma unroll
        for (int t = 0; t < 4; ++t) {
          if (part & 1) av[t][part >> 1] = mfma(xf[ks][t], fw[ks][part], av[t][part >> 1]);
          else ak[t][part >> 1] = mfma(xf[ks][t], fw[ks][part], ak[t][part >> 1]);
        }
      };
#pragma unroll
      for (int c = 0; c < 7; ++c) {
        xb = opaque(L0 + b_slot(c) + rh * 8192 + xsw);
        wb = opaque(L0 + b_slot(c) + B_W + wln);
        hook(c, true, true);
        LDS_BAR();
        hook(c, false, true);
        if (c == 0) { STAMP(2); STAMPC(16); }
        if (c == 3) STAMP(3);
        if (c == 6) { STAMP(4); STAMPC(17); }
        SB();
#pragma unroll
        for (int ks = 0; ks < 2; ++ks) {
#pragma unroll
          for (int part = 0; part < 4; ++part) rd(ks, part);
          if (ks == 1 || c > 0) {
#pragma unroll
            for (int part = 0; part < 4; ++part) mm(ks ^ 1, part);
            interleave<16, 12>();
          }
          SB();
        }
      }
      sig(L0 + SYNC + SY_D6 + w * 4);
      mm(1, 0); mm(1, 2);
      SB();
      mm(1, 3); mm(1, 1);
      STAMP(5);
      f32x4 kv[2][2];
      {
        u32x2 pk[4][2], vf[4][2];
#pragma unroll
        for (int t = 0; t < 4; ++t) {
          pk[t][0] = phi4(ak[t][0]);
          pk[t][1] = phi4(ak[t][1]);
          vf[t][0] = pk4(av[t][0]);
          vf[t][1] = pk4(av[t][1]);
        }
#pragma unroll
        for (int e = 1; e >= 0; --e)
#pragma unroll
          for (int d = 0; d < 2; ++d) {
            kv[e][d] = mfma(frag2(pk[0][d], pk[1][d]), frag2(vf[0][e], vf[1][e]), z4);
            kv[e][d] = mfma(frag2(pk[2][d], pk[3][d]), frag2(vf[2][e], vf[3][e]), kv[e][d]);
          }
      }
      lds_st128(L0 + B_EX + w * 2048 + lane * 16, __builtin_bit_cast(u32x4, kv[1][0]));
      lds_st128(L0 + B_EX + w * 2048 + 1024 + lane * 16, __builtin_bit_cast(u32x4, kv[1][1]));
      sig(L0 + SYNC + SY_EX + w * 4);
      wait1(L0 + SYNC + SY_EX + (w ^ 1) * 4);
      const f32x4 s0 = kv[0][0] + __builtin_bit_cast(f32x4, lds_ld128(L0 + B_EX + (w ^ 1) * 2048 + lane * 16));
      const f32x4 s1 = kv[0][1] + __builtin_bit_cast(f32x4, lds_ld128(L0 + B_EX + (w ^ 1) * 2048 + 1024 + lane * 16));
      {
        const u32x2 a = pk4(s0), b = pk4(s1);
        publish((u32x4){a[0], a[1], b[0], b[1]}, wsr, lane * 16 + kvs + rt * 1024);
      }
      STAMP(6);
      if (!__all(clr == tagO)) poll_flags(clrp(), tagO);
      wo_run(0, QWO);
      STAMP(7);
      f32x4 sum0, sum1;
      {
        const unsigned e0 = q == g ? ((p & 1) ? 0x3f800000u : 0x3f80u) : 0u;
        const unsigned ea = p < 2 ? e0 : 0u, eb = p < 2 ? 0u : e0;
        const bf16x8 sel0 = as_frag((u32x4){ea, eb, 0u, 0u}), sel1 = as_frag((u32x4){0u, 0u, ea, eb});
        unsigned vo = kvs + lane * 16;
#pragma nounroll
        for (unsigned spins = 0; spins < SPIN_CAP; ++spins) {
          asm volatile("" : "+v"(vo)::"memory");
          u32x4 pr[16];
#pragma unroll
          for (int r = 0; r < 16; ++r) pr[r] = __builtin_amdgcn_raw_buffer_load_b128(wsr, vo, ((r + 2 * rt) & 15) * 1024, 16);
          SB();
          sum0 = sum1 = z4;
#pragma unroll
          for (int r = 0; r < 16; ++r) {
            sum0 = mfma(sel0, as_frag(pr[r]), sum0);
            sum1 = mfma(sel1, as_frag(pr[r]), sum1);
          }
          if (!__any(sum0[0] != sum0[0])) break;
          unsigned m = 0;
#pragma unroll
          for (int r = 0; r < 16; ++r) m = max(max(max(max(m, pr[r][0]), pr[r][1]), pr[r][2]), pr[r][3]);
          if (!__any(m == SENT)) break;
        }
      }
      STAMP(8);
      lds_st128(L0 + B_KVL + w * 1024 + lane * 16, __builtin_bit_cast(u32x4, frag2(pk4(sum0), pk4(sum1))));
      sig(L0 + SYNC + SY_KV + w * 4);
      STAMP(10); STAMPC(19);
      __builtin_amdgcn_s_setprio(0);
      if (cg >= 7) { STAMP_FLUSH(); return; }
      sig(L0 + SYNC + SY_WOA + w * 4);
      wait4(L0 + SYNC + SY_D6);
      wait4(L0 + SYNC + SY_QD);
      wo_run(QWO, 28);
      VM_WAIT(0);
      sig(L0 + SYNC + SY_WOB + w * 4);
  } else {
      asm volatile("; role A");
      setup(A_G);
      f32x4 aq[4][2], ak[5][2], av[5][2];
#pragma unroll
      for (int t = 0; t < 5; ++t)
#pragma unroll
        for (int f = 0; f < 2; ++f) { ak[t][f] = av[t][f] = z4; if (t < 4) aq[t][f] = z4; }
      const unsigned gl = L0 + A_G + (8 * g + q) * 8;
      const bool p3 = p == 3;
      const unsigned d1 = p3 ? 32u : 112u;
      bf16x8 xf[2][5], fq[2][2], fk[2][2], fv[2][2];
      unsigned xb, wb;
      auto rd = [&](int c, int ks, int part) {
        const unsigned xa = xb ^ (ks * 64), wk = wb + ks * 3584;
        if (part == 0) {
          fk[ks][0] = lds_tr2(wk + WM_B, wk + WM_B + 112);
          xf[ks][0] = as_frag(lds_ld128(xa));
          xf[ks][1] = as_frag(lds_ld128(xa + 2048));
        } else if (part == 1) {
          fv[ks][0] = lds_tr2(wk + 2 * WM_B, wk + 2 * WM_B + 112);
          xf[ks][2] = as_frag(lds_ld128(xa + 4096));
        } else if (part == 2) {
          fq[ks][0] = lds_tr2(wk, wk + 112);
          xf[ks][3] = as_frag(lds_ld128(xa + 6144));
        } else if (part == 3) {
          fk[ks][1] = lds_tr2(wk + WM_B + 32, wk + WM_B + 32 + 112);
          xf[ks][4] = as_frag(lds_ld128(xa + 8192));
        } else if (part == 4) {
          fv[ks][1] = lds_tr2(wk + 2 * WM_B + 32, wk + 2 * WM_B + 32 + 112);
        } else {
          const unsigned a = p3 ? gl + c * 512 + ks * 256 : wk + 32;
          fq[ks][1] = lds_tr2(a, a + d1);
        }
      };
      auto mm = [&](int ks, int part) {
        const int f = part / 3, m = part % 3;
#pragma unroll
        for (int t = 0; t < 5; ++t) {
          if (m == 0) ak[t][f] = mfma(fk[ks][f], xf[ks][t], ak[t][f]);
          if (m == 1) av[t][f] = mfma(xf[ks][t], fv[ks][f], av[t][f]);
          if (m == 2 && t > 0) aq[t - 1][f] = mfma(fq[ks][f], xf[ks][t], aq[t - 1][f]);
        }
      };
#pragma unroll
      for (int c = 0; c < 7; ++c) {
        xb = opaque(L0 + a_slot(c) + rh * 8192 + xsw);
        wb = opaque(L0 + a_slot(c) + A_W + wln);
        hook(c, true, false);
        LDS_BAR();
        hook(c, false, false);
        if (c == 0) { STAMP(2); STAMPC(16); }
        if (c == 3) STAMP(3);
        if (c == 6) { STAMP(4); STAMPC(17); }
        SB();
#pragma unroll
        for (int ks = 0; ks < 2; ++ks) {
#pragma unroll
          for (int part = 0; part < 6; ++part) rd(c, ks, part);
          if (ks == 1 || c > 0) {
#pragma unroll
            for (int part = 0; part < 6; ++part) mm(ks ^ 1, part);
            interleave<28, 17>();
          }
          SB();
        }
      }
      sig(L0 + SYNC + SY_D6 + w * 4);
      mm(1, 0); mm(1, 3); mm(1, 2); mm(1, 5);
      SB();
      mm(1, 1); mm(1, 4);
      STAMP(5);
      const bool odd = li & 1;
      const int kk0 = 4 * g + (li & 1);
      const bool own0 = kk0 <= li, own1 = kk0 + 2 <= li;
      const float sc = 0.18898223650461362f * kLog2e;
      const float ninf = (rt == 0 && rh == 0) ? -__builtin_inff() : 0.f;
      const unsigned l16 = lane_id() * 16;
      bf16x8 kf[5];
#pragma unroll
      for (int t = 0; t < 5; ++t) kf[t] = frag2(pk4(ak[t][0]), pk4(ak[t][1]));
      f32x4 sp[4], so[4];
#pragma unroll
      for (int t = 0; t < 4; ++t) {
        const u32x2 b = pk4(aq[t][1]);
        const bf16x8 qf = frag2(pk4(aq[t][0]), (u32x2){g3 ? 0u : b[0], g3 ? 0u : b[1]});
        sp[t] = mfma(kf[t], qf, t == 0 ? (f32x4){ninf, ninf, ninf, ninf} : z4);
        so[t] = mfma(kf[t + 1], qf, z4);
      }
      float ug;
      {
        float z0 = aq[0][1][0], z1 = aq[1][1][0], z2 = aq[2][1][0], z3 = aq[3][1][0];
        swap32(z0, z1);
        swap32(z2, z3);
        swap16(z1, z3);
        ug = 1.f + __builtin_amdgcn_exp2f(-kLog2e * z3);
      }
      u32x2 vf[5][2];
#pragma unroll
      for (int t = 0; t < 5; ++t) {
        vf[t][0] = pk4(av[t][0]);
        vf[t][1] = pk4(av[t][1]);
      }
      float l0[4], l1[4], bc[4];
#pragma unroll
      for (int t = 0; t < 4; ++t) {
        float a0 = so[t][0], a2 = so[t][2], b0 = sp[t][0], b2 = sp[t][2];
        const float c0 = own0 ? a0 : b0, c1 = own0 ? so[t][1] : sp[t][1], c2 = own1 ? a2 : b2, c3 = own1 ? so[t][3] : sp[t][3];
        l0[t] = odd ? c1 : c0;
        l1[t] = odd ? c3 : c2;
      }
      rows_bcast4(rows_reduce4<true>(fmax1(l0[0], l1[0]), fmax1(l0[1], l1[1]), fmax1(l0[2], l1[2]), fmax1(l0[3], l1[3])) * -sc, bc);
#pragma unroll
      for (int t = 0; t < 4; ++t) {
        l0[t] = __builtin_amdgcn_exp2f(__builtin_fmaf(l0[t], sc, bc[t]));
        l1[t] = __builtin_amdgcn_exp2f(__builtin_fmaf(l1[t], sc, bc[t]));
      }
      rows_bcast4(__builtin_amdgcn_rcpf(rows_reduce4<false>(l0[0] + l1[0], l0[1] + l1[1], l0[2] + l1[2], l0[3] + l1[3]) * ug), bc);
      const unsigned sh = odd ? 16u : 0u;
      const unsigned mk0 = own0 ? 0u : 0xffffu, mk1 = own1 ? 0u : 0xffffu;
#pragma unroll
      for (int t = 0; t < 4; ++t) {
        const unsigned pe = pk2(l0[t] * bc[t], l1[t] * bc[t]), ph = pe >> 16;
        const bf16x8 pf = as_frag((u32x4){(pe & mk0) << sh, (ph & mk1) << sh, (pe & (mk0 ^ 0xffffu)) << sh, (ph & (mk1 ^ 0xffffu)) << sh});
        const f32x4 r0 = mfma(frag2(vf[t][0], vf[t + 1][0]), pf, z4);
        const f32x4 r1 = mfma(frag2(vf[t][1], vf[t + 1][1]), pf, z4);
        const u32x2 c0 = pk4(r0), c1 = pk4(r1);
        __builtin_amdgcn_raw_buffer_store_b128((u32x4){c0[0], c0[1], g3 ? 0u : c1[0], g3 ? 0u : c1[1]}, wsr, l16 + obs + t * 32768, 0, 16);
      }
      STAMP(9);
      VM_WAIT(0);
      if (l16 == 0) __hip_atomic_store((gu64*)(ws + WS_FA) + ((rt * 2 + rh) * 16 + hp * 2 + hh) * FS, tagO, RLX_AGENT);
      STAMP(10); STAMPC(19);
      if (cg >= 7) {
        if (bid == 56 && w == 0) {
#pragma nounroll
          for (int k = 0; k < 4; ++k) {
            const int i = (k & 1) * 64 + lane;
            poll_flags(k < 2 ? (gu64*)(ws + WS_CLR) + i : (gu64*)(ws + WS_FA) + ((i >> 3) * 32 + (i & 7) * 2) * FS, tagO);
          }
          if (lane == 0) __hip_atomic_store((gu64*)(ws + WS_E), tagO, RLX_AGENT);
        }
        STAMP_FLUSH();
        return;
      }
  }

  const int pb = w >> 2;
  wait4(L0 + SYNC + (pb ? SY_WOB : SY_WOA));
  const unsigned sw = (2 * g + (q >> 1)) & 3;
  const unsigned rb = L0 + (pb * 448 + 4 * g + q) * 128 + 8 * p;
  unsigned wa[4], wa2[4];
#pragma unroll
  for (int v = 0; v < 4; ++v) {
    wa[v] = opaque(rb + ((v ^ sw) << 5));
    wa2[v] = opaque(wa[v] + (g3 ? 0u : 2048u));
  }
  auto wfrag = [&](int h, int ct) {
    const int v = ct ^ (2 * (h & 1));
    return lds_tr2(wa[v] + h * 3584, wa2[v] + h * 3584);
  };
  constexpr int NPRE = 8;
  bf16x8 wf[16][4];
#pragma unroll
  for (int h = 0; h < NPRE; ++h)
#pragma unroll
    for (int ct = 0; ct < 4; ++ct) wf[h][ct] = wfrag(h, ct);
  STAMP(11);
  const unsigned ub = WS_O + ((rg * 4 + wl) * 32 + pb * 16) * 1024;
  tagO = uniform64(*(LAS u64*)(uintptr_t)(L0 + SYNC + SY_TAG));
  poll_flags(pb ? (gu64*)(ws + WS_FB) + ((rg * 2 + (wl >> 1)) * 8 + (li & 7)) * FS : (gu64*)(ws + WS_FA) + (rg * 16 + li) * FS, tagO);
  STAMP(12);
  u32x4 of[16];
  {
    unsigned vo = ub + lane * 16;
    asm volatile("" : "+v"(vo)::"memory");
#pragma unroll
    for (int h = 0; h < 16; ++h) of[h] = __builtin_amdgcn_raw_buffer_load_b128(wsr, vo, ((h + 2 * cg) & 15) * 1024, 16);
    SB();
  }
  f32x4 acc[4] = {z4, z4, z4, z4};
#pragma unroll
  for (int h = 0; h < 16; ++h) {
#pragma unroll
    for (int ct = 0; ct < 4; ++ct) acc[ct] = mfma(wf[h][ct], as_frag(of[h]), acc[ct]);
    SB();
    if (h + NPRE < 16) {
#pragma unroll
      for (int ct = 0; ct < 4; ++ct) wf[h + NPRE][ct] = wfrag(h + NPRE, ct);
    }
    SB();
  }
  STAMP(13);
  const unsigned xa = L0 + P2X + wl * 4096 + lane * 16;
  if (!pb) {
    wait4(L0 + SYNC + SY_D6);
#pragma unroll
    for (int ct = 0; ct < 4; ++ct) lds_st128(xa + ct * 1024, __builtin_bit_cast(u32x4, acc[ct]));
    sig(L0 + SYNC + SY_P2 + wl * 4);
  } else {
    wait1(L0 + SYNC + SY_P2 + wl * 4);
    char* orow = out + (rg * 64 + wl * 16 + li) * XROW + cg * 128 + g * 8;
#pragma unroll
    for (int ct = 0; ct < 4; ++ct) {
      const f32x4 r = acc[ct] + __builtin_bit_cast(f32x4, lds_ld128(xa + ct * 1024));
      *(u32x2*)(orow + ct * 32) = pk4(r);
    }
  }
  STAMP(14);
  STAMP(15);
  STAMP_FLUSH();
}

}

extern "C" void kernel_launch(void* const* d_in, const int* in_sizes, int n_in, void* d_out, int out_size, void* d_ws,
                              size_t ws_size, hipStream_t stream) {
  (void)in_sizes; (void)n_in; (void)out_size; (void)ws_size;
  gated_mix_kernel<<<256, 512, 0, stream>>>((const char*)d_in[0], (const char*)d_in[1], (const char*)d_in[2],
                                            (const char*)d_in[4], (const char*)d_in[5],
                                            (const unsigned short*)d_in[6], (char*)d_out, (char*)d_ws);
}
```

```cpp
#include <hip/hip_runtime.h>
#include <stdint.h>

typedef __bf16 bf16x2 __attribute__((ext_vector_type(2)));
typedef __bf16 bf16x4 __attribute__((ext_vector_type(4)));
typedef __bf16 bf16x8 __attribute__((ext_vector_type(8)));
typedef float f32x2 __attribute__((ext_vector_type(2)));
typedef float f32x4 __attribute__((ext_vector_type(4)));
typedef unsigned u32x2 __attribute__((ext_vector_type(2)));
typedef unsigned u32x4 __attribute__((ext_vector_type(4)));
typedef unsigned long long u64;
#define LAS __attribute__((address_space(3)))
#define GAS __attribute__((address_space(1)))
typedef GAS u64 gu64;
#define RLX_AGENT __ATOMIC_RELAXED, __HIP_MEMORY_SCOPE_AGENT

namespace {

constexpr int kS = 2048, kD = 448;
constexpr int XROW = kD * 2;
constexpr int WROW = 3 * kD * 2;
constexpr int WM_B = 64 * 112;

constexpr int A_FRONT = 34816, A_SLOT = 144 * 128 + 3 * WM_B, A_W = 144 * 128;
constexpr int A_G = A_FRONT + 3 * A_SLOT;
constexpr int B_SLOT = 128 * 128 + 2 * WM_B, B_W = 128 * 128;
constexpr int B_WQ = 97536, B_G = B_WQ + 7 * WM_B, B_EX = B_G + 3584, B_KVL = B_EX + 8192;
constexpr int P2X = 114688;
constexpr int SYNC = 163584;
constexpr int LDS_B = 163840;
static_assert(A_G + 3584 <= SYNC && B_KVL + 4096 <= SYNC && A_FRONT + 2 * A_SLOT == 114688, "lds map");
__host__ __device__ constexpr int a_slot(int c) { return A_FRONT + ((c + 2) % 3) * A_SLOT; }
__host__ __device__ constexpr int b_slot(int c) { return ((c + 2) % 3) * B_SLOT; }
enum { SY_WOA = 0, SY_WOB = 16, SY_D6 = 32, SY_D6Q = 48, SY_WQ = 64, SY_QD = 80, SY_EX = 96, SY_KV = 112, SY_P2 = 128, SY_ZERO = 192, SY_TAG = 240 };

constexpr int WS_E = 0;
constexpr int WS_CLR = 4096;
constexpr int FS = 16;
constexpr int WS_FA = 8192;
constexpr int WS_FB = WS_FA + 16 * 2 * 16 * 8 * FS;
constexpr int WS_KVP = WS_FB + 16 * 4 * 8 * 8 * FS;
constexpr int WS_O = WS_KVP + 8 * 4 * 16 * 1024;
constexpr int WS_END = WS_O + 16 * 8 * 32 * 1024;

constexpr int PT_WS = (WS_END / 8) & ~127;
constexpr unsigned SPIN_CAP = 1u << 14;
constexpr unsigned SENT = 0xffffffffu;
#define QWO 14

#define STAMP(i) do {} while (0)
#define STAMPC(i) do {} while (0)
#define STAMP_FLUSH() do {} while (0)

__device__ __forceinline__ unsigned pk2(float a, float b) {
  f32x2 v = {a, b};
  bf16x2 r = __builtin_convertvector(v, bf16x2);
  return __builtin_bit_cast(unsigned, r);
}
__device__ __forceinline__ u32x2 pk4(f32x4 v) { return (u32x2){pk2(v[0], v[1]), pk2(v[2], v[3])}; }
__device__ __forceinline__ bf16x8 as_frag(u32x4 v) { return __builtin_bit_cast(bf16x8, v); }
__device__ __forceinline__ bf16x8 frag2(u32x2 a, u32x2 b) { return as_frag((u32x4){a[0], a[1], b[0], b[1]}); }
__device__ __forceinline__ f32x4 mfma(bf16x8 a, bf16x8 b, f32x4 c) {
  return __builtin_amdgcn_mfma_f32_16x16x32_bf16(a, b, c, 0, 0, 0);
}
__device__ __forceinline__ bf16x8 lds_tr2(unsigned a0, unsigned a1) {
  bf16x4 lo = __builtin_amdgcn_ds_read_tr16_b64_v4bf16((LAS bf16x4*)(uintptr_t)a0);
  bf16x4 hi = __builtin_amdgcn_ds_read_tr16_b64_v4bf16((LAS bf16x4*)(uintptr_t)a1);
  return __builtin_shufflevector(lo, hi, 0, 1, 2, 3, 4, 5, 6, 7);
}
__device__ __forceinline__ u32x4 lds_ld128(unsigned a) { return *(LAS u32x4*)(uintptr_t)a; }
__device__ __forceinline__ void lds_st128(unsigned a, u32x4 v) { *(LAS u32x4*)(uintptr_t)a = v; }
__device__ __forceinline__ void lds_st64(unsigned a, u32x2 v) { *(LAS u32x2*)(uintptr_t)a = v; }
__device__ __forceinline__ unsigned opaque(unsigned v) { asm volatile("" : "+v"(v)); return v; }

__device__ __forceinline__ void sig(unsigned a) {
  asm volatile("" ::: "memory");
  *(volatile LAS unsigned*)(uintptr_t)a = 1u;
  asm volatile("" ::: "memory");
  __builtin_amdgcn_sched_barrier(0);
}
__device__ __forceinline__ void wait1(unsigned a) {
  while (__builtin_amdgcn_readfirstlane(*(volatile LAS unsigned*)(uintptr_t)a) == 0u) __builtin_amdgcn_s_sleep(1);
  asm volatile("" ::: "memory");
}
__device__ __forceinline__ void wait4(unsigned a) {
  for (;;) {
    const u32x4 v = *(volatile LAS u32x4*)(uintptr_t)a;
    if (__builtin_amdgcn_readfirstlane(v[0] & v[1] & v[2] & v[3])) break;
    __builtin_amdgcn_s_sleep(1);
  }
  asm volatile("" ::: "memory");
}

__device__ __forceinline__ u32x4 make_srd(const void* p, unsigned bytes) {
  uint64_t a = (uint64_t)p;
  u32x4 r;
  r[0] = __builtin_amdgcn_readfirstlane((unsigned)a);
  r[1] = __builtin_amdgcn_readfirstlane((unsigned)(a >> 32) & 0xffffu);
  r[2] = bytes;
  r[3] = 0x00020000u;
  return r;
}
__device__ __forceinline__ void dma16(u32x4 srd, unsigned voff, unsigned soff, unsigned ldsaddr) {
  asm volatile(
      "s_mov_b32 m0, %3\n\t"
      "s_nop 3\n\t"
      "buffer_load_dwordx4 %0, %1, %2 offen lds"
      :
      : "v"(voff), "s"(srd), "s"(soff), "s"(ldsaddr));
}
#define VM_BAR(N) asm volatile("s_waitcnt vmcnt(" #N ") lgkmcnt(0)\n\ts_barrier" ::: "memory")
#define LDS_BAR() asm volatile("s_waitcnt lgkmcnt(0)\n\ts_barrier" ::: "memory")
#define VM_WAIT(N) asm volatile("s_waitcnt vmcnt(" #N ")" ::: "memory")
template <int N> __device__ __forceinline__ void vm_bar() { asm volatile("s_waitcnt vmcnt(%0) lgkmcnt(0)\n\ts_barrier" ::"n"(N) : "memory"); }
#define SB() __builtin_amdgcn_sched_barrier(0)

__device__ __forceinline__ float fmax1(float a, float b) { return __builtin_amdgcn_fmed3f(a, b, 3.0e38f); }
__device__ __forceinline__ void swap16(float& a, float& b) {
  auto r = __builtin_amdgcn_permlane16_swap(__float_as_uint(a), __float_as_uint(b), false, false);
  const unsigned r0 = r[0], r1 = r[1];
  a = __uint_as_float(r0);
  b = __uint_as_float(r1);
}
__device__ __forceinline__ void swap32(float& a, float& b) {
  auto r = __builtin_amdgcn_permlane32_swap(__float_as_uint(a), __float_as_uint(b), false, false);
  const unsigned r0 = r[0], r1 = r[1];
  a = __uint_as_float(r0);
  b = __uint_as_float(r1);
}
template <bool MAX>
__device__ __forceinline__ float rows_reduce4(float v0, float v1, float v2, float v3) {
  swap32(v0, v1);
  swap32(v2, v3);
  float a = MAX ? fmax1(v0, v1) : v0 + v1, b = MAX ? fmax1(v2, v3) : v2 + v3;
  swap16(a, b);
  return MAX ? fmax1(a, b) : a + b;
}
__device__ __forceinline__ void rows_bcast4(float x, float (&o)[4]) {
  float y = x;
  swap16(x, y);
  o[0] = x; o[1] = x; o[2] = y; o[3] = y;
  swap32(o[0], o[1]);
  swap32(o[2], o[3]);
}
__device__ __forceinline__ float row3_bcast(float v) {
  float a = v;
  swap32(a, v);
  a = v;
  swap16(a, v);
  return v;
}
template <int NM, int ND>
__device__ __forceinline__ void interleave() {
#pragma unroll
  for (int i = 0; i < NM; ++i) {
    __builtin_amdgcn_sched_group_barrier(0x8, 1, 0);
    if (i < ND) {
      __builtin_amdgcn_sched_group_barrier(0x2, 1, 0);
      __builtin_amdgcn_sched_group_barrier(0x100, 1, 0);
    }
  }
}
constexpr float kEps = 1e-6f, kLog2e = 1.44269504088896f;
__device__ __forceinline__ float phi(float u) {
  const float e = __builtin_amdgcn_fmed3f(__builtin_amdgcn_exp2f(__builtin_fmaf(u, kLog2e, -kEps * kLog2e)), 0.f, 1.f);
  return fmax1(u, kEps) + e;
}
__device__ __forceinline__ u32x2 phi4(f32x4 v) { return (u32x2){pk2(phi(v[0]), phi(v[1])), pk2(phi(v[2]), phi(v[3]))}; }
__device__ __forceinline__ float sigmoidf(float z) {
  return __builtin_amdgcn_rcpf(1.f + __builtin_amdgcn_exp2f(-1.44269504088896f * z));
}
__device__ __forceinline__ u64 next_tag(u64 e) {
  const u64 s = ((u64)(unsigned)__builtin_amdgcn_readfirstlane((unsigned)(e >> 32)) << 32) |
                (unsigned)__builtin_amdgcn_readfirstlane((unsigned)e);
  return s * 6364136223846793005ull + 1442695040888963407ull;
}
__device__ __forceinline__ u64 uniform64(u64 e) {
  return ((u64)(unsigned)__builtin_amdgcn_readfirstlane((unsigned)(e >> 32)) << 32) |
         (unsigned)__builtin_amdgcn_readfirstlane((unsigned)e);
}
__device__ __forceinline__ void poll_flags(const gu64* p, u64 tag) {
#pragma nounroll
  for (unsigned spins = 0; spins < SPIN_CAP; ++spins) {
    u64 v = __hip_atomic_load(p, RLX_AGENT);
    if (__all(v == tag)) break;
    __builtin_amdgcn_s_sleep(2);
  }
  asm volatile("" ::: "memory");
}
__device__ __forceinline__ void publish(u32x4 v, __amdgpu_buffer_rsrc_t r, unsigned voff) {
#pragma unroll
  for (int i = 0; i < 4; ++i) v[i] = v[i] < SENT - 1u ? v[i] : SENT - 1u;
  __builtin_amdgcn_raw_buffer_store_b128(v, r, voff, 0, 16);
}
__device__ __forceinline__ unsigned lane_id() {
  unsigned l = __builtin_amdgcn_mbcnt_hi(~0u, __builtin_amdgcn_mbcnt_lo(~0u, 0u));
  asm volatile("" : "+v"(l));
  return l;
}
__device__ __forceinline__ int wrap7(int c) { return c >= 7 ? c - 7 : c; }

__global__ __launch_bounds__(512, 2) void gated_mix_kernel(const char* __restrict__ x, const char* __restrict__ wqkvA,
                                                           const char* __restrict__ woA, const char* __restrict__ wqkvB,
                                                           const char* __restrict__ woB,
                                                           const unsigned short* __restrict__ gate_w, char* __restrict__ out,
                                                           char* ws) {
  __shared__ __attribute__((aligned(1024))) char lds[LDS_B];
  const unsigned L0 = (unsigned)(uintptr_t)(LAS char*)lds;
  const int bid = blockIdx.x;
  const int xcd = bid & 7, idx = bid >> 3;
  const int xj = xcd >> 1, ci = idx >> 3;
  const int br = ci >> 1;
  const int hp = 2 * (br ? (xj ^ 1) : xj) + (ci & 1);
  const int rt = (xcd & 1) * 8 + (idx & 7);
  const int rot = wrap7(wrap7((idx & 7) + 2 * ci + (xj & 1)));
  const int cg = idx & 7, rg = 4 * xcd + ci;

  const int tid = threadIdx.x;
  const int lane = tid & 63;
  const int w = __builtin_amdgcn_readfirstlane(tid >> 6) & 7;
  const int wl = w & 3;
  const int li = lane & 15, g = lane >> 4, q = (lane >> 2) & 3, p = lane & 3;
  const bool g3 = g == 3;
  const f32x4 z4 = {0.f, 0.f, 0.f, 0.f};
  STAMP(0); STAMPC(18);
  unsigned vx0 = (lane >> 3) * XROW + (((lane & 7) ^ (lane >> 4)) << 4);
  unsigned vx1 = vx0 ^ 64u;
  unsigned vw0, vw1;
  {
    const unsigned s0 = wl * 64 + lane, s1 = s0 + 256;
    const unsigned p0 = (s0 * 9363u) >> 16, p1 = (s1 * 9363u) >> 16;
    const unsigned k0 = (p0 & 56) | ((p0 >> 1) & 3) | ((p0 & 1) << 2);
    const unsigned k1 = (p1 & 56) | ((p1 >> 1) & 3) | ((p1 & 1) << 2);
    vw0 = k0 * WROW + hp * 112 + (s0 - p0 * 7) * 16;
    vw1 = k1 * WROW + hp * 112 + (s1 - p1 * 7) * 16;
  }
  asm volatile("" : "+v"(vx0), "+v"(vx1), "+v"(vw0), "+v"(vw1));
  asm volatile("" ::"s"(x), "s"(wqkvA), "s"(woA), "s"(wqkvB), "s"(woB), "s"(gate_w), "s"(out), "s"(ws));
  const __amdgpu_buffer_rsrc_t wsr = __builtin_amdgcn_make_buffer_rsrc(ws, 0, WS_END, 0x00020000);
  u64 tagO;
  auto wo_run = [&](int j0, int j1) {
    const unsigned vwo = (lane >> 3) * XROW + cg * 128 + (((lane & 7) ^ (((lane >> 4) & 3) << 1)) << 4);
    const u32x4 sa = make_srd(woA, kD * XROW), sb = make_srd(woB, kD * XROW);
    unsigned so = (wl + 7 * cg + 4 * (j0 % 14)) * (8 * XROW);
    so = so >= kD * XROW ? so - kD * XROW : so;
#pragma unroll
    for (int j = j0; j < j1; ++j) {
      dma16(j < 14 ? sa : sb, vwo, so, L0 + (wl + 4 * j) * 1024);
      so += 32 * XROW;
      so = so >= kD * XROW ? so - kD * XROW : so;
    }
  };
  const u32x4 srdX = make_srd(x, kS * XROW);
  const u32x4 srdW = make_srd(br ? wqkvB : wqkvA, kD * WROW);
  const bool l3 = wl == 3;
  const int xr = rt * 128 + wl * 32 - (br ? 0 : 16);
  const unsigned sx01 = (xr < 0 ? 0 : xr) * XROW, sx23 = (xr + 16) * XROW;
  const bool l3a = l3 && !br;
  const u32x4 srd2 = l3a ? srdX : srdW;
  const unsigned v2 = l3 ? vw0 : vw1;
  const unsigned v2a = l3a ? vx0 : v2, v2b = l3a ? vx1 : v2;
  const unsigned st2 = l3a ? 128u : 64u * WROW, s2a = l3a ? (rt * 128 + 112) * XROW : 0u;
  const unsigned s2d = l3a ? 8u * XROW : (unsigned)XROW, s2e = l3a ? 8u * XROW : 2u * XROW;
  const unsigned sub2 = (l3 ? wl : wl + 4) * 1024;
  auto chunk = [&](unsigned sl, unsigned lq, unsigned lk, int cc) {
    const unsigned sw = cc * (64 * WROW), sx = cc * 128, s2 = cc * st2 + s2a;
#pragma unroll
    for (int j = 0; j < 4; ++j)
      dma16(srdX, (j & 1) ? vx1 : vx0, (j < 2 ? sx01 : sx23) + (j & 1) * 8 * XROW + sx, sl + (wl * 4 + j) * 1024);
    dma16(srdW, vw0, sw + XROW, lk + wl * 1024);
    dma16(srdW, vw0, sw + 2 * XROW, lk + WM_B + wl * 1024);
    dma16(srd2, v2b, s2 + s2d, l3a ? sl + 17408 : lk + sub2);
    dma16(srd2, v2b, s2 + s2e, l3a ? sl + 17408 : lk + WM_B + sub2);
    if (!br) {
      dma16(srdW, vw0, sw, lq + wl * 1024);
      dma16(srd2, v2a, s2, l3a ? sl + 16384 : lq + sub2);
    }
  };
  unsigned gv = 0, pt = 0;
  u64 epoch = 0;
  {
    int wx = w;
    asm volatile("" : "+s"(wx));
    if (wx < 4) {
      gv = ((const unsigned*)gate_w)[tid < 224 ? tid : 0];
      epoch = __hip_atomic_load((gu64*)(ws + WS_E), RLX_AGENT);
      const char* pb_ = wx == 0 ? ws : (wx == 1 ? out + rg * 64 * XROW : (wx == 2 ? woA : woB));
      pt = __hip_atomic_load((GAS unsigned*)(pb_ + (lane & 7) * (wx == 0 ? PT_WS : (wx == 1 ? 7168 : 50176))), RLX_AGENT);
    } else {
#pragma nounroll
      for (int c = 0; c < 2; ++c) {
        const unsigned sl = L0 + (br ? (c ? b_slot(1) : b_slot(0)) : (c ? a_slot(1) : a_slot(0)));
        chunk(sl, sl + A_W, sl + (br ? B_W : A_W + WM_B), wrap7(rot + c));
      }
    }
  }

    const int rh = w & 1, hh = w >> 1;
    const unsigned obs = WS_O + ((rt * 8 + rh * 4) * 32 + hp * 2 + hh) * 1024;
    const unsigned kvs = WS_KVP + (hp * 4 + w) * 16 * 1024;
    auto setup = [&](int goff) {
      if (tid < 60) *(LAS unsigned*)(uintptr_t)(L0 + SYNC + tid * 4) = 0u;
      const unsigned lo = (gv & 0xffffu) * 0x10001u, hi = (gv >> 16) * 0x10001u;
      const int pos = tid - rot * 32;
      if (tid < 224) lds_st128(L0 + goff + (pos < 0 ? pos + 224 : pos) * 16, (u32x4){lo, lo, hi, hi});
    };
    u64 clr = 0;
    auto clrp = [&]() { return (const gu64*)(ws + WS_CLR) + hp * 16 + (lane_id() & 15); };
    auto hook = [&](int c, bool before, bool isb) {
      if (c == 0 && !before && isb)
        __builtin_amdgcn_raw_buffer_store_b128((u32x4){SENT, SENT, SENT, SENT}, wsr, lane_id() * 16 + kvs + rt * 1024, 0, 16);
      if (c == 3 && before) {
        asm volatile("" : "+v"(epoch) : "v"(pt));
        tagO = next_tag(epoch);
        *(LAS u64*)(uintptr_t)(L0 + SYNC + SY_TAG) = tagO;
        VM_WAIT(0);
      }
      if (c == 3 && !before && isb)
        __builtin_amdgcn_raw_buffer_store_b64((u32x2){(unsigned)tagO, (unsigned)(tagO >> 32)}, wsr,
                                              (lane_id() | w) == 0 ? WS_CLR + (hp * 16 + rt) * 8 : 0x7ffffff0u, 0, 16);
      if (c == 6 && !before && isb) clr = __hip_atomic_load(clrp(), RLX_AGENT);
    };
    const unsigned xsw = li * 128 + ((g ^ (li >> 1)) << 4);
    const unsigned wln = (8 * g + 2 * q) * 112 + 8 * p + 56 * hh;

  if (__builtin_expect(w >= 4, 1)) {
    if (!br) {
      int cc = wrap7(rot + 1);
      unsigned sl = L0 + a_slot(2);
#pragma nounroll
      for (int c = 2; c < 7; ++c) {
        vm_bar<10>();
        cc = wrap7(cc + 1);
        chunk(sl, sl + A_W, sl + A_W + WM_B, cc);
        sl = sl == L0 + a_slot(0) ? L0 + a_slot(1) : sl + A_SLOT;
      }
      vm_bar<10>();
      if (cg >= 7) { vm_bar<0>(); STAMP_FLUSH(); return; }
      wo_run(0, 10);
      vm_bar<10>();
      wo_run(10, 28);
      VM_WAIT(14);
      sig(L0 + SYNC + SY_WOA + wl * 4);
      VM_WAIT(0);
      asm volatile("" ::"v"(gv), "v"(epoch), "v"(pt));
      sig(L0 + SYNC + SY_WOB + wl * 4);
    } else {
      auto instr = [&](int n) {
        if (n < 56) {
          const int c = n >> 3, j = n & 7, cc = wrap7(c + rot);
          const unsigned sl = L0 + b_slot(c), sw = cc * (64 * WROW), sx = cc * 128;
          if (j < 4) dma16(srdX, (j & 1) ? vx1 : vx0, sx01 + j * 8 * XROW + sx, sl + (wl * 4 + j) * 1024);
          else dma16(srdW, j < 6 ? vw0 : v2, sw + ((j & 1) + 1) * XROW, sl + B_W + (j & 1) * WM_B + (j < 6 ? wl * 1024 : sub2));
        } else {
          const int c = (n - 56) >> 1;
          dma16(srdW, (n & 1) ? v2 : vw0, wrap7(c + rot) * (64 * WROW), L0 + B_WQ + c * WM_B + ((n & 1) ? sub2 : wl * 1024));
        }
      };
      STAMP(1);
      bf16x8 xq[14][2];
      const unsigned xl = L0 + (wl * 32 + li) * 128 + ((g ^ (li >> 1)) << 4);
#pragma unroll
      for (int c = 0; c < 7; ++c) {
        vm_bar<8>();
#pragma unroll
        for (int ks = 0; ks < 2; ++ks) {
          const unsigned xa = (xl ^ (ks * 64)) + b_slot(c);
          xq[c * 2 + ks][0] = as_frag(lds_ld128(xa));
          xq[c * 2 + ks][1] = as_frag(lds_ld128(xa + 2048));
        }
#pragma unroll
        for (int n = 8 * c + 16; n < (c < 6 ? 8 * c + 24 : 70); ++n) instr(n);
      }
      STAMP(4);
      VM_WAIT(0);
      asm volatile("" ::"v"(gv), "v"(epoch), "v"(pt));
      sig(L0 + SYNC + SY_WQ + wl * 4);
      wait4(L0 + SYNC + SY_WQ);
      wait4(L0 + SYNC + SY_EX);
      STAMP(3);
      f32x4 aq[2][2][2];
#pragma unroll
      for (int i = 0; i < 8; ++i) aq[i >> 2][(i >> 1) & 1][i & 1] = (f32x4){kEps, kEps, kEps, kEps};
      const unsigned wb = L0 + B_WQ + (8 * g + 2 * q) * 112 + 8 * p;
      const unsigned gl = L0 + B_G + (8 * g + q) * 8;
      const bool p3 = p == 3;
      const unsigned d1 = p3 ? 32u : 112u;
      const unsigned qa0 = opaque(p3 ? gl : wb + 32), qstep = opaque(p3 ? 256u : 3584u);
      bf16x8 fq[3][4];
      auto rdq = [&](int s) {
        const unsigned wk = wb + s * 3584;
        const unsigned a = qa0 + s * qstep;
        fq[s % 3][0] = lds_tr2(wk, wk + 112);
        fq[s % 3][1] = lds_tr2(a, a + d1);
        fq[s % 3][2] = lds_tr2(wk + 56, wk + 56 + 112);
        fq[s % 3][3] = lds_tr2(wk + 88, wk + 88 + 112);
      };
      rdq(0);
      rdq(1);
#pragma unroll
      for (int s = 0; s < 14; ++s) {
        SB();
        if (s + 2 < 14) rdq(s + 2);
#pragma unroll
        for (int i = 0; i < 4; ++i) {
          aq[0][i >> 1][i & 1] = mfma(fq[s % 3][i], xq[s][0], aq[0][i >> 1][i & 1]);
          aq[1][i >> 1][i & 1] = mfma(fq[s % 3][i], xq[s][1], aq[1][i >> 1][i & 1]);
        }
        interleave<8, 8>();
      }
      sig(L0 + SYNC + SY_QD + wl * 4);
      STAMP(5);
      u32x4 pqf[2][2];
      float omg[2];
#pragma unroll
      for (int t = 0; t < 2; ++t) {
        omg[t] = 1.f - sigmoidf(row3_bcast(aq[t][0][1][0]));
#pragma unroll
        for (int h = 0; h < 2; ++h) {
          const u32x2 a = phi4(aq[t][h][0]), b = phi4(aq[t][h][1]);
          pqf[t][h] = (u32x4){a[0], a[1], g3 ? 0u : b[0], g3 ? 0u : b[1]};
        }
      }
      tagO = uniform64(*(LAS u64*)(uintptr_t)(L0 + SYNC + SY_TAG));
      __builtin_amdgcn_s_setprio(2);
      STAMP(6);
      wait4(L0 + SYNC + SY_KV);
      STAMP(8);
      const unsigned ob = WS_O + ((rt * 8 + wl * 2) * 32 + 16 + hp * 2) * 1024 + lane * 16;
#pragma unroll
      for (int h = 0; h < 2; ++h) {
        const unsigned kb = L0 + B_KVL + h * 2048;
        const bf16x8 kv0 = as_frag(lds_ld128(kb + lane * 16)), kv1 = as_frag(lds_ld128(kb + 1024 + lane * 16));
        const bf16x8 zf = as_frag(lds_ld128(kb + 1024 + (g * 16 + 12) * 16));
#pragma unroll
        for (int t = 0; t < 2; ++t) {
          const f32x4 n0 = mfma(kv0, as_frag(pqf[t][h]), z4);
          const f32x4 n1 = mfma(kv1, as_frag(pqf[t][h]), z4);
          const f32x4 dn = mfma(zf, as_frag(pqf[t][h]), z4);
          const float s = omg[t] * __builtin_amdgcn_rcpf(dn[0] + 1e-6f);
          const u32x2 a = pk4(n0 * s), b = pk4(n1 * s);
          __builtin_amdgcn_raw_buffer_store_b128((u32x4){a[0], a[1], g3 ? 0u : b[0], g3 ? 0u : b[1]}, wsr, ob + (t * 32 + h) * 1024, 0, 16);
        }
      }
      STAMP(9);
      VM_WAIT(0);
      if (lane == 0) __hip_atomic_store((gu64*)(ws + WS_FB) + ((rt * 4 + wl) * 8 + hp) * FS, tagO, RLX_AGENT);
      STAMP(10); STAMPC(19);
      if (cg >= 7) { STAMP_FLUSH(); return; }
    }
  } else if (__builtin_expect(br, 1)) {
      __builtin_amdgcn_s_setprio(2);
      setup(B_G);
      f32x4 ak[4][2], av[4][2];
      {
        const float one = li == 12 ? 1.f : 0.f, i0 = rh ? one : 0.f, i1 = rh ? 0.f : one;
#pragma unroll
        for (int t = 0; t < 4; ++t) {
          ak[t][0] = ak[t][1] = (f32x4){kEps, kEps, kEps, kEps};
          av[t][0] = (f32x4){i0, i0, i0, i0};
          av[t][1] = (f32x4){i1, i1, i1, i1};
        }
      }
      bf16x8 xf[2][4], fw[2][4];
      unsigned xb, wb;
      const bool z0 = p == 3 && rh, z1 = p == 3 && !rh;
      const unsigned zaddr = L0 + SYNC + SY_ZERO;
      auto rd = [&](int ks, int part) {
        const unsigned xa = xb ^ (ks * 64), wk = wb + ks * 3584;
        if (part & 1) {
          const unsigned a = wk + WM_B + (((part >> 1) ^ rh) << 5);
          const bool z = (part >> 1) ? z1 : z0;
          fw[ks][part] = lds_tr2(z ? zaddr : a, z ? zaddr : a + 112);
        } else {
          fw[ks][part] = lds_tr2(wk + (part >> 1) * 32, wk + (part >> 1) * 32 + 112);
        }
        if (part < 2) {
          xf[ks][part * 2] = as_frag(lds_ld128(xa + part * 4096));
          xf[ks][part * 2 + 1] = as_frag(lds_ld128(xa + part * 4096 + 2048));
        }
      };
      auto mm = [&](int ks, int part) {
#pragma unroll
        for (int t = 0; t < 4; ++t) {
          if (part & 1) av[t][part >> 1] = mfma(xf[ks][t], fw[ks][part], av[t][part >> 1]);
          else ak[t][part >> 1] = mfma(xf[ks][t], fw[ks][part], ak[t][part >> 1]);
        }
      };
#pragma unroll
      for (int c = 0; c < 7; ++c) {
        xb = opaque(L0 + b_slot(c) + rh * 8192 + xsw);
        wb = opaque(L0 + b_slot(c) + B_W + wln);
        hook(c, true, true);
        LDS_BAR();
        hook(c, false, true);
        if (c == 0) { STAMP(2); STAMPC(16); }
        if (c == 3) STAMP(3);
        if (c == 6) { STAMP(4); STAMPC(17); }
        SB();
#pragma unroll
        for (int ks = 0; ks < 2; ++ks) {
#pragma unroll
          for (int part = 0; part < 4; ++part) rd(ks, part);
          if (ks == 1 || c > 0) {
#pragma unroll
            for (int part = 0; part < 4; ++part) mm(ks ^ 1, part);
            interleave<16, 12>();
          }
          SB();
        }
      }
      sig(L0 + SYNC + SY_D6 + w * 4);
      mm(1, 0); mm(1, 2);
      SB();
      mm(1, 3); mm(1, 1);
      STAMP(5);
      f32x4 kv[2][2];
      {
        u32x2 pk[4][2], vf[4][2];
#pragma unroll
        for (int t = 0; t < 4; ++t) {
          pk[t][0] = phi4(ak[t][0]);
          pk[t][1] = phi4(ak[t][1]);
          vf[t][0] = pk4(av[t][0]);
          vf[t][1] = pk4(av[t][1]);
        }
#pragma unroll
        for (int e = 1; e >= 0; --e)
#pragma unroll
          for (int d = 0; d < 2; ++d) {
            kv[e][d] = mfma(frag2(pk[0][d], pk[1][d]), frag2(vf[0][e], vf[1][e]), z4);
            kv[e][d] = mfma(frag2(pk[2][d], pk[3][d]), frag2(vf[2][e], vf[3][e]), kv[e][d]);
          }
      }
      lds_st128(L0 + B_EX + w * 2048 + lane * 16, __builtin_bit_cast(u32x4, kv[1][0]));
      lds_st128(L0 + B_EX + w * 2048 + 1024 + lane * 16, __builtin_bit_cast(u32x4, kv[1][1]));
      sig(L0 + SYNC + SY_EX + w * 4);
      wait1(L0 + SYNC + SY_EX + (w ^ 1) * 4);
      const f32x4 s0 = kv[0][0] + __builtin_bit_cast(f32x4, lds_ld128(L0 + B_EX + (w ^ 1) * 2048 + lane * 16));
      const f32x4 s1 = kv[0][1] + __builtin_bit_cast(f32x4, lds_ld128(L0 + B_EX + (w ^ 1) * 2048 + 1024 + lane * 16));
      {
        const u32x2 a = pk4(s0), b = pk4(s1);
        publish((u32x4){a[0], a[1], b[0], b[1]}, wsr, lane * 16 + kvs + rt * 1024);
      }
      STAMP(6);
      if (!__all(clr == tagO)) poll_flags(clrp(), tagO);
      wo_run(0, QWO);
      STAMP(7);
      f32x4 sum0, sum1;
      {
        const unsigned e0 = q == g ? ((p & 1) ? 0x3f800000u : 0x3f80u) : 0u;
        const unsigned ea = p < 2 ? e0 : 0u, eb = p < 2 ? 0u : e0;
        const bf16x8 sel0 = as_frag((u32x4){ea, eb, 0u, 0u}), sel1 = as_frag((u32x4){0u, 0u, ea, eb});
        unsigned vo = kvs + lane * 16;
#pragma nounroll
        for (unsigned spins = 0; spins < SPIN_CAP; ++spins) {
          asm volatile("" : "+v"(vo)::"memory");
          u32x4 pr[16];
#pragma unroll
          for (int r = 0; r < 16; ++r) pr[r] = __builtin_amdgcn_raw_buffer_load_b128(wsr, vo, ((r + 2 * rt) & 15) * 1024, 16);
          SB();
          sum0 = sum1 = z4;
#pragma unroll
          for (int r = 0; r < 16; ++r) {
            sum0 = mfma(sel0, as_frag(pr[r]), sum0);
            sum1 = mfma(sel1, as_frag(pr[r]), sum1);
          }
          if (!__any(sum0[0] != sum0[0])) break;
          unsigned m = 0;
#pragma unroll
          for (int r = 0; r < 16; ++r) m = max(max(max(max(m, pr[r][0]), pr[r][1]), pr[r][2]), pr[r][3]);
          if (!__any(m == SENT)) break;
        }
      }
      STAMP(8);
      lds_st128(L0 + B_KVL + w * 1024 + lane * 16, __builtin_bit_cast(u32x4, frag2(pk4(sum0), pk4(sum1))));
      sig(L0 + SYNC + SY_KV + w * 4);
      STAMP(10); STAMPC(19);
      __builtin_amdgcn_s_setprio(0);
      if (cg >= 7) { STAMP_FLUSH(); return; }
      sig(L0 + SYNC + SY_WOA + w * 4);
      wait4(L0 + SYNC + SY_D6);
      wait4(L0 + SYNC + SY_QD);
      wo_run(QWO, 28);
  } else {
      asm volatile("; role A");
      setup(A_G);
      f32x4 aq[4][2], ak[5][2], av[5][2];
#pragma unroll
      for (int t = 0; t < 5; ++t)
#pragma unroll
        for (int f = 0; f < 2; ++f) { ak[t][f] = av[t][f] = z4; if (t < 4) aq[t][f] = z4; }
      const unsigned gl = L0 + A_G + (8 * g + q) * 8;
      const bool p3 = p == 3;
      const unsigned d1 = p3 ? 32u : 112u;
      bf16x8 xf[2][5], fq[2][2], fk[2][2], fv[2][2];
      unsigned xb, wb;
      auto rd = [&](int c, int ks, int part) {
        const unsigned xa = xb ^ (ks * 64), wk = wb + ks * 3584;
        if (part == 0) {
          fk[ks][0] = lds_tr2(wk + WM_B, wk + WM_B + 112);
          xf[ks][0] = as_frag(lds_ld128(xa));
          xf[ks][1] = as_frag(lds_ld128(xa + 2048));
        } else if (part == 1) {
          fv[ks][0] = lds_tr2(wk + 2 * WM_B, wk + 2 * WM_B + 112);
          xf[ks][2] = as_frag(lds_ld128(xa + 4096));
        } else if (part == 2) {
          fq[ks][0] = lds_tr2(wk, wk + 112);
          xf[ks][3] = as_frag(lds_ld128(xa + 6144));
        } else if (part == 3) {
          fk[ks][1] = lds_tr2(wk + WM_B + 32, wk + WM_B + 32 + 112);
          xf[ks][4] = as_frag(lds_ld128(xa + 8192));
        } else if (part == 4) {
          fv[ks][1] = lds_tr2(wk + 2 * WM_B + 32, wk + 2 * WM_B + 32 + 112);
        } else {
          const unsigned a = p3 ? gl + c * 512 + ks * 256 : wk + 32;
          fq[ks][1] = lds_tr2(a, a + d1);
        }
      };
      auto mm = [&](int ks, int part) {
        const int f = part / 3, m = part % 3;
#pragma unroll
        for (int t = 0; t < 5; ++t) {
          if (m == 0) ak[t][f] = mfma(fk[ks][f], xf[ks][t], ak[t][f]);
          if (m == 1) av[t][f] = mfma(xf[ks][t], fv[ks][f], av[t][f]);
          if (m == 2 && t > 0) aq[t - 1][f] = mfma(fq[ks][f], xf[ks][t], aq[t - 1][f]);
        }
      };
#pragma unroll
      for (int c = 0; c < 7; ++c) {
        xb = opaque(L0 + a_slot(c) + rh * 8192 + xsw);
        wb = opaque(L0 + a_slot(c) + A_W + wln);
        hook(c, true, false);
        LDS_BAR();
        hook(c, false, false);
        if (c == 0) { STAMP(2); STAMPC(16); }
        if (c == 3) STAMP(3);
        if (c == 6) { STAMP(4); STAMPC(17); }
        SB();
#pragma unroll
        for (int ks = 0; ks < 2; ++ks) {
#pragma unroll
          for (int part = 0; part < 6; ++part) rd(c, ks, part);
          if (ks == 1 || c > 0) {
#pragma unroll
            for (int part = 0; part < 6; ++part) mm(ks ^ 1, part);
            interleave<28, 17>();
          }
          SB();
        }
      }
      sig(L0 + SYNC + SY_D6 + w * 4);
      mm(1, 0); mm(1, 3); mm(1, 2); mm(1, 5);
      SB();
      mm(1, 1); mm(1, 4);
      STAMP(5);
      const bool odd = li & 1;
      const int kk0 = 4 * g + (li & 1);
      const bool own0 = kk0 <= li, own1 = kk0 + 2 <= li;
      const float sc = 0.18898223650461362f * kLog2e;
      const float ninf = (rt == 0 && rh == 0) ? -__builtin_inff() : 0.f;
      const unsigned l16 = lane_id() * 16;
      bf16x8 kf[5];
#pragma unroll
      for (int t = 0; t < 5; ++t) kf[t] = frag2(pk4(ak[t][0]), pk4(ak[t][1]));
      f32x4 sp[4], so[4];
#pragma unroll
      for (int t = 0; t < 4; ++t) {
        const u32x2 b = pk4(aq[t][1]);
        const bf16x8 qf = frag2(pk4(aq[t][0]), (u32x2){g3 ? 0u : b[0], g3 ? 0u : b[1]});
        sp[t] = mfma(kf[t], qf, t == 0 ? (f32x4){ninf, ninf, ninf, ninf} : z4);
        so[t] = mfma(kf[t + 1], qf, z4);
      }
      float ug;
      {
        float z0 = aq[0][1][0], z1 = aq[1][1][0], z2 = aq[2][1][0], z3 = aq[3][1][0];
        swap32(z0, z1);
        swap32(z2, z3);
        swap16(z1, z3);
        ug = 1.f + __builtin_amdgcn_exp2f(-kLog2e * z3);
      }
      u32x2 vf[5][2];
#pragma unroll
      for (int t = 0; t < 5; ++t) {
        vf[t][0] = pk4(av[t][0]);
        vf[t][1] = pk4(av[t][1]);
      }
      float l0[4], l1[4], bc[4];
#pragma unroll
      for (int t = 0; t < 4; ++t) {
        float a0 = so[t][0], a2 = so[t][2], b0 = sp[t][0], b2 = sp[t][2];
        const float c0 = own0 ? a0 : b0, c1 = own0 ? so[t][1] : sp[t][1], c2 = own1 ? a2 : b2, c3 = own1 ? so[t][3] : sp[t][3];
        l0[t] = odd ? c1 : c0;
        l1[t] = odd ? c3 : c2;
      }
      rows_bcast4(rows_reduce4<true>(fmax1(l0[0], l1[0]), fmax1(l0[1], l1[1]), fmax1(l0[2], l1[2]), fmax1(l0[3], l1[3])) * -sc, bc);
#pragma unroll
      for (int t = 0; t < 4; ++t) {
        l0[t] = __builtin_amdgcn_exp2f(__builtin_fmaf(l0[t], sc, bc[t]));
        l1[t] = __builtin_amdgcn_exp2f(__builtin_fmaf(l1[t], sc, bc[t]));
      }
      rows_bcast4(__builtin_amdgcn_rcpf(rows_reduce4<false>(l0[0] + l1[0], l0[1] + l1[1], l0[2] + l1[2], l0[3] + l1[3]) * ug), bc);
      const unsigned sh = odd ? 16u : 0u;
      const unsigned mk0 = own0 ? 0u : 0xffffu, mk1 = own1 ? 0u : 0xffffu;
#pragma unroll
      for (int t = 0; t < 4; ++t) {
        const unsigned pe = pk2(l0[t] * bc[t], l1[t] * bc[t]), ph = pe >> 16;
        const bf16x8 pf = as_frag((u32x4){(pe & mk0) << sh, (ph & mk1) << sh, (pe & (mk0 ^ 0xffffu)) << sh, (ph & (mk1 ^ 0xffffu)) << sh});
        const f32x4 r0 = mfma(frag2(vf[t][0], vf[t + 1][0]), pf, z4);
        const f32x4 r1 = mfma(frag2(vf[t][1], vf[t + 1][1]), pf, z4);
        const u32x2 c0 = pk4(r0), c1 = pk4(r1);
        __builtin_amdgcn_raw_buffer_store_b128((u32x4){c0[0], c0[1], g3 ? 0u : c1[0], g3 ? 0u : c1[1]}, wsr, l16 + obs + t * 32768, 0, 16);
      }
      STAMP(9);
      VM_WAIT(0);
      if (l16 == 0) __hip_atomic_store((gu64*)(ws + WS_FA) + ((rt * 2 + rh) * 16 + hp * 2 + hh) * FS, tagO, RLX_AGENT);
      STAMP(10); STAMPC(19);
      if (cg >= 7) {
        if (bid == 56 && w == 0) {
#pragma nounroll
          for (int k = 0; k < 4; ++k) {
            const int i = (k & 1) * 64 + lane;
            poll_flags(k < 2 ? (gu64*)(ws + WS_CLR) + i : (gu64*)(ws + WS_FA) + ((i >> 3) * 32 + (i & 7) * 2) * FS, tagO);
          }
          if (lane == 0) __hip_atomic_store((gu64*)(ws + WS_E), tagO, RLX_AGENT);
        }
        STAMP_FLUSH();
        return;
      }
  }

  const int pb = w >> 2;
  wait4(L0 + SYNC + (pb ? SY_WOB : SY_WOA));
  const unsigned sw = (2 * g + (q >> 1)) & 3;
  const unsigned rb = L0 + (pb * 448 + 4 * g + q) * 128 + 8 * p;
  unsigned wa[4], wa2[4];
#pragma unroll
  for (int v = 0; v < 4; ++v) {
    wa[v] = opaque(rb + ((v ^ sw) << 5));
    wa2[v] = opaque(wa[v] + (g3 ? 0u : 2048u));
  }
  auto wfrag = [&](int h, int ct) {
    const int v = ct ^ (2 * (h & 1));
    return lds_tr2(wa[v] + h * 3584, wa2[v] + h * 3584);
  };
  constexpr int NPRE = 8;
  bf16x8 wf[16][4];
#pragma unroll
  for (int h = 0; h < NPRE; ++h)
#pragma unroll
    for (int ct = 0; ct < 4; ++ct) wf[h][ct] = wfrag(h, ct);
  STAMP(11);
  const unsigned ub = WS_O + ((rg * 4 + wl) * 32 + pb * 16) * 1024;
  tagO = uniform64(*(LAS u64*)(uintptr_t)(L0 + SYNC + SY_TAG));
  {
    const gu64* fp = pb ? (gu64*)(ws + WS_FB) + ((rg * 2 + (wl >> 1)) * 8 + (li & 7)) * FS : (gu64*)(ws + WS_FA) + (rg * 16 + li) * FS;
    const u64 f0 = __hip_atomic_load(fp, RLX_AGENT);
    if (br && !pb) { VM_WAIT(0); sig(L0 + SYNC + SY_WOB + wl * 4); }
    if (!__all(f0 == tagO)) poll_flags(fp, tagO);
  }
  STAMP(12);
  u32x4 of[16];
  {
    unsigned vo = ub + lane * 16;
    asm volatile("" : "+v"(vo)::"memory");
#pragma unroll
    for (int h = 0; h < 16; ++h) of[h] = __builtin_amdgcn_raw_buffer_load_b128(wsr, vo, ((h + 2 * cg) & 15) * 1024, 16);
    SB();
  }
  f32x4 acc[4] = {z4, z4, z4, z4};
#pragma unroll
  for (int h = 0; h < 16; ++h) {
#pragma unroll
    for (int ct = 0; ct < 4; ++ct) acc[ct] = mfma(wf[h][ct], as_frag(of[h]), acc[ct]);
    SB();
    if (h + NPRE < 16) {
#pragma unroll
      for (int ct = 0; ct < 4; ++ct) wf[h + NPRE][ct] = wfrag(h + NPRE, ct);
    }
    SB();
  }
  STAMP(13);
  const unsigned xa = L0 + P2X + wl * 4096 + lane * 16;
  if (!pb) {
    wait4(L0 + SYNC + SY_D6);
#pragma unroll
    for (int ct = 0; ct < 4; ++ct) lds_st128(xa + ct * 1024, __builtin_bit_cast(u32x4, acc[ct]));
    sig(L0 + SYNC + SY_P2 + wl * 4);
  } else {
    f32x4 pa[4];
    for (;;) {
      const unsigned f = *(volatile LAS unsigned*)(uintptr_t)(L0 + SYNC + SY_P2 + wl * 4);
#pragma unroll
      for (int ct = 0; ct < 4; ++ct) pa[ct] = *(volatile LAS f32x4*)(uintptr_t)(xa + ct * 1024);
      if (__builtin_amdgcn_readfirstlane(f)) break;
      __builtin_amdgcn_s_sleep(1);
    }
    char* orow = out + (rg * 64 + wl * 16 + li) * XROW + cg * 128 + (g & 1) * 32 + (g >> 1) * 16;
#pragma unroll
    for (int k = 0; k < 2; ++k) {
      const u32x2 a = pk4(acc[2 * k] + pa[2 * k]), b = pk4(acc[2 * k + 1] + pa[2 * k + 1]);
      const auto r0 = __builtin_amdgcn_permlane16_swap(a[0], b[0], false, false);
      const auto r1 = __builtin_amdgcn_permlane16_swap(a[1], b[1], false, false);
      *(u32x4*)(orow + k * 64) = (u32x4){r0[0], r1[0], r0[1], r1[1]};
    }
  }
  STAMP(14);
  STAMP(15);
  STAMP_FLUSH();
}

}

extern "C" void kernel_launch(void* const* d_in, const int* in_sizes, int n_in, void* d_out, int out_size, void* d_ws,
                              size_t ws_size, hipStream_t stream) {
  (void)in_sizes; (void)n_in; (void)out_size; (void)ws_size;
  gated_mix_kernel<<<256, 512, 0, stream>>>((const char*)d_in[0], (const char*)d_in[1], (const char*)d_in[2],
                                            (const char*)d_in[4], (const char*)d_in[5],
                                            (const unsigned short*)d_in[6], (char*)d_out, (char*)d_ws);
}
```

```cpp
#define DIAG_A 0
#define DIAG_B 4
#define DIAG_ROLE 2
#include <hip/hip_runtime.h>
#include <stdint.h>

typedef __bf16 bf16x2 __attribute__((ext_vector_type(2)));
typedef __bf16 bf16x4 __attribute__((ext_vector_type(4)));
typedef __bf16 bf16x8 __attribute__((ext_vector_type(8)));
typedef float f32x2 __attribute__((ext_vector_type(2)));
typedef float f32x4 __attribute__((ext_vector_type(4)));
typedef unsigned u32x2 __attribute__((ext_vector_type(2)));
typedef unsigned u32x4 __attribute__((ext_vector_type(4)));
typedef unsigned long long u64;
#define LAS __attribute__((address_space(3)))
#define GAS __attribute__((address_space(1)))
typedef GAS u64 gu64;
#define RLX_AGENT __ATOMIC_RELAXED, __HIP_MEMORY_SCOPE_AGENT

namespace {

constexpr int kS = 2048, kD = 448;
constexpr int XROW = kD * 2;
constexpr int WROW = 3 * kD * 2;
constexpr int WM_B = 64 * 112;

constexpr int A_FRONT = 34816, A_SLOT = 144 * 128 + 3 * WM_B, A_W = 144 * 128;
constexpr int A_G = A_FRONT + 3 * A_SLOT;
constexpr int B_SLOT = 128 * 128 + 2 * WM_B, B_W = 128 * 128;
constexpr int B_WQ = 97536, B_G = B_WQ + 7 * WM_B, B_EX = B_G + 3584, B_KVL = B_EX + 8192;
constexpr int P2X = 114688;
constexpr int SYNC = 163584;
constexpr int LDS_B = 163840;
static_assert(A_G + 3584 <= SYNC && B_KVL + 4096 <= SYNC && A_FRONT + 2 * A_SLOT == 114688, "lds map");
__host__ __device__ constexpr int a_slot(int c) { return A_FRONT + ((c + 2) % 3) * A_SLOT; }
__host__ __device__ constexpr int b_slot(int c) { return ((c + 2) % 3) * B_SLOT; }
enum { SY_WOA = 0, SY_WOB = 16, SY_D6 = 32, SY_D6Q = 48, SY_WQ = 64, SY_QD = 80, SY_EX = 96, SY_KV = 112, SY_P2 = 128, SY_ZERO = 192, SY_TAG = 240 };

constexpr int WS_E = 0;
constexpr int WS_CLR = 4096;
constexpr int FS = 16;
constexpr int WS_FA = 8192;
constexpr int WS_FB = WS_FA + 16 * 2 * 16 * 8 * FS;
constexpr int WS_KVP = WS_FB + 16 * 4 * 8 * 8 * FS;
constexpr int WS_O = WS_KVP + 8 * 4 * 16 * 1024;
constexpr int WS_END = WS_O + 16 * 8 * 32 * 1024;

constexpr int PT_WS = (WS_END / 8) & ~127;
constexpr unsigned SPIN_CAP = 1u << 14;
constexpr unsigned SENT = 0xffffffffu;
#define QWO 14

#define STAMP(i) do { if (i == DIAG_A) dta = (unsigned)__builtin_amdgcn_s_memrealtime(); if (i == DIAG_B) dtb = (unsigned)__builtin_amdgcn_s_memrealtime(); } while (0)
#define STAMPC(i) do {} while (0)
#define STAMP_FLUSH() do { if (br * 2 + (w >> 2) == DIAG_ROLE) { unsigned d = (dtb && dta) ? (dtb - dta) * 8u : 0u; d = d > 100000u ? 100000u : d; const unsigned t0 = dt0; while ((unsigned)__builtin_amdgcn_s_memrealtime() - t0 < 4000u + d) __builtin_amdgcn_s_sleep(2); } } while (0)

__device__ __forceinline__ unsigned pk2(float a, float b) {
  f32x2 v = {a, b};
  bf16x2 r = __builtin_convertvector(v, bf16x2);
  return __builtin_bit_cast(unsigned, r);
}
__device__ __forceinline__ u32x2 pk4(f32x4 v) { return (u32x2){pk2(v[0], v[1]), pk2(v[2], v[3])}; }
__device__ __forceinline__ bf16x8 as_frag(u32x4 v) { return __builtin_bit_cast(bf16x8, v); }
__device__ __forceinline__ bf16x8 frag2(u32x2 a, u32x2 b) { return as_frag((u32x4){a[0], a[1], b[0], b[1]}); }
__device__ __forceinline__ f32x4 mfma(bf16x8 a, bf16x8 b, f32x4 c) {
  return __builtin_amdgcn_mfma_f32_16x16x32_bf16(a, b, c, 0, 0, 0);
}
__device__ __forceinline__ bf16x8 lds_tr2(unsigned a0, unsigned a1) {
  bf16x4 lo = __builtin_amdgcn_ds_read_tr16_b64_v4bf16((LAS bf16x4*)(uintptr_t)a0);
  bf16x4 hi = __builtin_amdgcn_ds_read_tr16_b64_v4bf16((LAS bf16x4*)(uintptr_t)a1);
  return __builtin_shufflevector(lo, hi, 0, 1, 2, 3, 4, 5, 6, 7);
}
__device__ __forceinline__ u32x4 lds_ld128(unsigned a) { return *(LAS u32x4*)(uintptr_t)a; }
__device__ __forceinline__ void lds_st128(unsigned a, u32x4 v) { *(LAS u32x4*)(uintptr_t)a = v; }
__device__ __forceinline__ void lds_st64(unsigned a, u32x2 v) { *(LAS u32x2*)(uintptr_t)a = v; }
__device__ __forceinline__ unsigned opaque(unsigned v) { asm volatile("" : "+v"(v)); return v; }

__device__ __forceinline__ void sig(unsigned a) {
  asm volatile("" ::: "memory");
  *(volatile LAS unsigned*)(uintptr_t)a = 1u;
  asm volatile("" ::: "memory");
  __builtin_amdgcn_sched_barrier(0);
}
__device__ __forceinline__ void wait1(unsigned a) {
  while (__builtin_amdgcn_readfirstlane(*(volatile LAS unsigned*)(uintptr_t)a) == 0u) __builtin_amdgcn_s_sleep(1);
  asm volatile("" ::: "memory");
}
__device__ __forceinline__ void wait4(unsigned a) {
  for (;;) {
    const u32x4 v = *(volatile LAS u32x4*)(uintptr_t)a;
    if (__builtin_amdgcn_readfirstlane(v[0] & v[1] & v[2] & v[3])) break;
    __builtin_amdgcn_s_sleep(1);
  }
  asm volatile("" ::: "memory");
}

__device__ __forceinline__ u32x4 make_srd(const void* p, unsigned bytes) {
  uint64_t a = (uint64_t)p;
  u32x4 r;
  r[0] = __builtin_amdgcn_readfirstlane((unsigned)a);
  r[1] = __builtin_amdgcn_readfirstlane((unsigned)(a >> 32) & 0xffffu);
  r[2] = bytes;
  r[3] = 0x00020000u;
  return r;
}
__device__ __forceinline__ void dma16(u32x4 srd, unsigned voff, unsigned soff, unsigned ldsaddr) {
  asm volatile(
      "s_mov_b32 m0, %3\n\t"
      "s_nop 3\n\t"
      "buffer_load_dwordx4 %0, %1, %2 offen lds"
      :
      : "v"(voff), "s"(srd), "s"(soff), "s"(ldsaddr));
}
#define VM_BAR(N) asm volatile("s_waitcnt vmcnt(" #N ") lgkmcnt(0)\n\ts_barrier" ::: "memory")
#define LDS_BAR() asm volatile("s_waitcnt lgkmcnt(0)\n\ts_barrier" ::: "memory")
#define VM_WAIT(N) asm volatile("s_waitcnt vmcnt(" #N ")" ::: "memory")
template <int N> __device__ __forceinline__ void vm_bar() { asm volatile("s_waitcnt vmcnt(%0) lgkmcnt(0)\n\ts_barrier" ::"n"(N) : "memory"); }
#define SB() __builtin_amdgcn_sched_barrier(0)

__device__ __forceinline__ float fmax1(float a, float b) { return __builtin_amdgcn_fmed3f(a, b, 3.0e38f); }
__device__ __forceinline__ void swap16(float& a, float& b) {
  auto r = __builtin_amdgcn_permlane16_swap(__float_as_uint(a), __float_as_uint(b), false, false);
  const unsigned r0 = r[0], r1 = r[1];
  a = __uint_as_float(r0);
  b = __uint_as_float(r1);
}
__device__ __forceinline__ void swap32(float& a, float& b) {
  auto r = __builtin_amdgcn_permlane32_swap(__float_as_uint(a), __float_as_uint(b), false, false);
  const unsigned r0 = r[0], r1 = r[1];
  a = __uint_as_float(r0);
  b = __uint_as_float(r1);
}
template <bool MAX>
__device__ __forceinline__ float rows_reduce4(float v0, float v1, float v2, float v3) {
  swap32(v0, v1);
  swap32(v2, v3);
  float a = MAX ? fmax1(v0, v1) : v0 + v1, b = MAX ? fmax1(v2, v3) : v2 + v3;
  swap16(a, b);
  return MAX ? fmax1(a, b) : a + b;
}
__device__ __forceinline__ void rows_bcast4(float x, float (&o)[4]) {
  float y = x;
  swap16(x, y);
  o[0] = x; o[1] = x; o[2] = y; o[3] = y;
  swap32(o[0], o[1]);
  swap32(o[2], o[3]);
}
__device__ __forceinline__ float row3_bcast(float v) {
  float a = v;
  swap32(a, v);
  a = v;
  swap16(a, v);
  return v;
}
template <int NM, int ND>
__device__ __forceinline__ void interleave() {
#pragma unroll
  for (int i = 0; i < NM; ++i) {
    __builtin_amdgcn_sched_group_barrier(0x8, 1, 0);
    if (i < ND) {
      __builtin_amdgcn_sched_group_barrier(0x2, 1, 0);
      __builtin_amdgcn_sched_group_barrier(0x100, 1, 0);
    }
  }
}
constexpr float kEps = 1e-6f, kLog2e = 1.44269504088896f;
__device__ __forceinline__ float phi(float u) {
  const float e = __builtin_amdgcn_fmed3f(__builtin_amdgcn_exp2f(__builtin_fmaf(u, kLog2e, -kEps * kLog2e)), 0.f, 1.f);
  return fmax1(u, kEps) + e;
}
__device__ __forceinline__ u32x2 phi4(f32x4 v) { return (u32x2){pk2(phi(v[0]), phi(v[1])), pk2(phi(v[2]), phi(v[3]))}; }
__device__ __forceinline__ float sigmoidf(float z) {
  return __builtin_amdgcn_rcpf(1.f + __builtin_amdgcn_exp2f(-1.44269504088896f * z));
}
__device__ __forceinline__ u64 next_tag(u64 e) {
  const u64 s = ((u64)(unsigned)__builtin_amdgcn_readfirstlane((unsigned)(e >> 32)) << 32) |
                (unsigned)__builtin_amdgcn_readfirstlane((unsigned)e);
  return s * 6364136223846793005ull + 1442695040888963407ull;
}
__device__ __forceinline__ u64 uniform64(u64 e) {
  return ((u64)(unsigned)__builtin_amdgcn_readfirstlane((unsigned)(e >> 32)) << 32) |
         (unsigned)__builtin_amdgcn_readfirstlane((unsigned)e);
}
__device__ __forceinline__ void poll_flags(const gu64* p, u64 tag) {
#pragma nounroll
  for (unsigned spins = 0; spins < SPIN_CAP; ++spins) {
    u64 v = __hip_atomic_load(p, RLX_AGENT);
    if (__all(v == tag)) break;
    __builtin_amdgcn_s_sleep(2);
  }
  asm volatile("" ::: "memory");
}
__device__ __forceinline__ void publish(u32x4 v, __amdgpu_buffer_rsrc_t r, unsigned voff) {
#pragma unroll
  for (int i = 0; i < 4; ++i) v[i] = v[i] < SENT - 1u ? v[i] : SENT - 1u;
  __builtin_amdgcn_raw_buffer_store_b128(v, r, voff, 0, 16);
}
__device__ __forceinline__ unsigned lane_id() {
  unsigned l = __builtin_amdgcn_mbcnt_hi(~0u, __builtin_amdgcn_mbcnt_lo(~0u, 0u));
  asm volatile("" : "+v"(l));
  return l;
}
__device__ __forceinline__ int wrap7(int c) { return c >= 7 ? c - 7 : c; }

__global__ __launch_bounds__(512, 2) void gated_mix_kernel(const char* __restrict__ x, const char* __restrict__ wqkvA,
                                                           const char* __restrict__ woA, const char* __restrict__ wqkvB,
                                                           const char* __restrict__ woB,
                                                           const unsigned short* __restrict__ gate_w, char* __restrict__ out,
                                                           char* ws) {
  __shared__ __attribute__((aligned(1024))) char lds[LDS_B];
  const unsigned L0 = (unsigned)(uintptr_t)(LAS char*)lds;
  const int bid = blockIdx.x;
  const int xcd = bid & 7, idx = bid >> 3;
  const int xj = xcd >> 1, ci = idx >> 3;
  const int br = ci >> 1;
  const int hp = 2 * (br ? (xj ^ 1) : xj) + (ci & 1);
  const int rt = (xcd & 1) * 8 + (idx & 7);
  const int rot = wrap7(wrap7((idx & 7) + 2 * ci + (xj & 1)));
  const int cg = idx & 7, rg = 4 * xcd + ci;

  const int tid = threadIdx.x;
  const int lane = tid & 63;
  const int w = __builtin_amdgcn_readfirstlane(tid >> 6) & 7;
  const int wl = w & 3;
  const int li = lane & 15, g = lane >> 4, q = (lane >> 2) & 3, p = lane & 3;
  const bool g3 = g == 3;
  const f32x4 z4 = {0.f, 0.f, 0.f, 0.f};
  unsigned dta = 0, dtb = 0;
  const unsigned dt0 = (unsigned)__builtin_amdgcn_s_memrealtime();
  STAMP(0); STAMPC(18);
  unsigned vx0 = (lane >> 3) * XROW + (((lane & 7) ^ (lane >> 4)) << 4);
  unsigned vx1 = vx0 ^ 64u;
  unsigned vw0, vw1;
  {
    const unsigned s0 = wl * 64 + lane, s1 = s0 + 256;
    const unsigned p0 = (s0 * 9363u) >> 16, p1 = (s1 * 9363u) >> 16;
    const unsigned k0 = (p0 & 56) | ((p0 >> 1) & 3) | ((p0 & 1) << 2);
    const unsigned k1 = (p1 & 56) | ((p1 >> 1) & 3) | ((p1 & 1) << 2);
    vw0 = k0 * WROW + hp * 112 + (s0 - p0 * 7) * 16;
    vw1 = k1 * WROW + hp * 112 + (s1 - p1 * 7) * 16;
  }
  asm volatile("" : "+v"(vx0), "+v"(vx1), "+v"(vw0), "+v"(vw1));
  asm volatile("" ::"s"(x), "s"(wqkvA), "s"(woA), "s"(wqkvB), "s"(woB), "s"(gate_w), "s"(out), "s"(ws));
  const __amdgpu_buffer_rsrc_t wsr = __builtin_amdgcn_make_buffer_rsrc(ws, 0, WS_END, 0x00020000);
  u64 tagO;
  auto wo_run = [&](int j0, int j1) {
    const unsigned vwo = (lane >> 3) * XROW + cg * 128 + (((lane & 7) ^ (((lane >> 4) & 3) << 1)) << 4);
    const u32x4 sa = make_srd(woA, kD * XROW), sb = make_srd(woB, kD * XROW);
    unsigned so = (wl + 7 * cg + 4 * (j0 % 14)) * (8 * XROW);
    so = so >= kD * XROW ? so - kD * XROW : so;
#pragma unroll
    for (int j = j0; j < j1; ++j) {
      dma16(j < 14 ? sa : sb, vwo, so, L0 + (wl + 4 * j) * 1024);
      so += 32 * XROW;
      so = so >= kD * XROW ? so - kD * XROW : so;
    }
  };
  const u32x4 srdX = make_srd(x, kS * XROW);
  const u32x4 srdW = make_srd(br ? wqkvB : wqkvA, kD * WROW);
  const bool l3 = wl == 3;
  const int xr = rt * 128 + wl * 32 - (br ? 0 : 16);
  const unsigned sx01 = (xr < 0 ? 0 : xr) * XROW, sx23 = (xr + 16) * XROW;
  const bool l3a = l3 && !br;
  const u32x4 srd2 = l3a ? srdX : srdW;
  const unsigned v2 = l3 ? vw0 : vw1;
  const unsigned v2a = l3a ? vx0 : v2, v2b = l3a ? vx1 : v2;
  const unsigned st2 = l3a ? 128u : 64u * WROW, s2a = l3a ? (rt * 128 + 112) * XROW : 0u;
  const unsigned s2d = l3a ? 8u * XROW : (unsigned)XROW, s2e = l3a ? 8u * XROW : 2u * XROW;
  const unsigned sub2 = (l3 ? wl : wl + 4) * 1024;
  auto chunk = [&](unsigned sl, unsigned lq, unsigned lk, int cc) {
    const unsigned sw = cc * (64 * WROW), sx = cc * 128, s2 = cc * st2 + s2a;
#pragma unroll
    for (int j = 0; j < 4; ++j)
      dma16(srdX, (j & 1) ? vx1 : vx0, (j < 2 ? sx01 : sx23) + (j & 1) * 8 * XROW + sx, sl + (wl * 4 + j) * 1024);
    dma16(srdW, vw0, sw + XROW, lk + wl * 1024);
    dma16(srdW, vw0, sw + 2 * XROW, lk + WM_B + wl * 1024);
    dma16(srd2, v2b, s2 + s2d, l3a ? sl + 17408 : lk + sub2);
    dma16(srd2, v2b, s2 + s2e, l3a ? sl + 17408 : lk + WM_B + sub2);
    if (!br) {
      dma16(srdW, vw0, sw, lq + wl * 1024);
      dma16(srd2, v2a, s2, l3a ? sl + 16384 : lq + sub2);
    }
  };
  unsigned gv = 0, pt = 0;
  u64 epoch = 0;
  {
    int wx = w;
    asm volatile("" : "+s"(wx));
    if (wx < 4) {
      gv = ((const unsigned*)gate_w)[tid < 224 ? tid : 0];
      epoch = __hip_atomic_load((gu64*)(ws + WS_E), RLX_AGENT);
      const char* pb_ = wx == 0 ? ws : (wx == 1 ? out + rg * 64 * XROW : (wx == 2 ? woA : woB));
      pt = __hip_atomic_load((GAS unsigned*)(pb_ + (lane & 7) * (wx == 0 ? PT_WS : (wx == 1 ? 7168 : 50176))), RLX_AGENT);
    } else {
#pragma nounroll
      for (int c = 0; c < 2; ++c) {
        const unsigned sl = L0 + (br ? (c ? b_slot(1) : b_slot(0)) : (c ? a_slot(1) : a_slot(0)));
        chunk(sl, sl + A_W, sl + (br ? B_W : A_W + WM_B), wrap7(rot + c));
      }
    }
  }

    const int rh = w & 1, hh = w >> 1;
    const unsigned obs = WS_O + ((rt * 8 + rh * 4) * 32 + hp * 2 + hh) * 1024;
    const unsigned kvs = WS_KVP + (hp * 4 + w) * 16 * 1024;
    auto setup = [&](int goff) {
      if (tid < 60) *(LAS unsigned*)(uintptr_t)(L0 + SYNC + tid * 4) = 0u;
      const unsigned lo = (gv & 0xffffu) * 0x10001u, hi = (gv >> 16) * 0x10001u;
      const int pos = tid - rot * 32;
      if (tid < 224) lds_st128(L0 + goff + (pos < 0 ? pos + 224 : pos) * 16, (u32x4){lo, lo, hi, hi});
    };
    u64 clr = 0;
    auto clrp = [&]() { return (const gu64*)(ws + WS_CLR) + hp * 16 + (lane_id() & 15); };
    auto hook = [&](int c, bool before, bool isb) {
      if (c == 0 && !before && isb)
        __builtin_amdgcn_raw_buffer_store_b128((u32x4){SENT, SENT, SENT, SENT}, wsr, lane_id() * 16 + kvs + rt * 1024, 0, 16);
      if (c == 3 && before) {
        asm volatile("" : "+v"(epoch) : "v"(pt));
        tagO = next_tag(epoch);
        *(LAS u64*)(uintptr_t)(L0 + SYNC + SY_TAG) = tagO;
        VM_WAIT(0);
      }
      if (c == 3 && !before && isb)
        __builtin_amdgcn_raw_buffer_store_b64((u32x2){(unsigned)tagO, (unsigned)(tagO >> 32)}, wsr,
                                              (lane_id() | w) == 0 ? WS_CLR + (hp * 16 + rt) * 8 : 0x7ffffff0u, 0, 16);
      if (c == 6 && !before && isb) clr = __hip_atomic_load(clrp(), RLX_AGENT);
    };
    const unsigned xsw = li * 128 + ((g ^ (li >> 1)) << 4);
    const unsigned wln = (8 * g + 2 * q) * 112 + 8 * p + 56 * hh;

  if (__builtin_expect(w >= 4, 1)) {
    if (!br) {
      int cc = wrap7(rot + 1);
      unsigned sl = L0 + a_slot(2);
#pragma nounroll
      for (int c = 2; c < 7; ++c) {
        vm_bar<10>();
        cc = wrap7(cc + 1);
        chunk(sl, sl + A_W, sl + A_W + WM_B, cc);
        sl = sl == L0 + a_slot(0) ? L0 + a_slot(1) : sl + A_SLOT;
      }
      vm_bar<10>();
      if (cg >= 7) { vm_bar<0>(); STAMP_FLUSH(); return; }
      wo_run(0, 10);
      vm_bar<10>();
      wo_run(10, 28);
      VM_WAIT(14);
      sig(L0 + SYNC + SY_WOA + wl * 4);
      VM_WAIT(0);
      sig(L0 + SYNC + SY_WOB + wl * 4);
    } else {
      auto instr = [&](int n) {
        if (n < 56) {
          const int c = n >> 3, j = n & 7, cc = wrap7(c + rot);
          const unsigned sl = L0 + b_slot(c), sw = cc * (64 * WROW), sx = cc * 128;
          if (j < 4) dma16(srdX, (j & 1) ? vx1 : vx0, sx01 + j * 8 * XROW + sx, sl + (wl * 4 + j) * 1024);
          else dma16(srdW, j < 6 ? vw0 : v2, sw + ((j & 1) + 1) * XROW, sl + B_W + (j & 1) * WM_B + (j < 6 ? wl * 1024 : sub2));
        } else {
          const int c = (n - 56) >> 1;
          dma16(srdW, (n & 1) ? v2 : vw0, wrap7(c + rot) * (64 * WROW), L0 + B_WQ + c * WM_B + ((n & 1) ? sub2 : wl * 1024));
        }
      };
      STAMP(1);
      bf16x8 xq[14][2];
      const unsigned xl = L0 + (wl * 32 + li) * 128 + ((g ^ (li >> 1)) << 4);
#pragma unroll
      for (int c = 0; c < 7; ++c) {
        vm_bar<8>();
#pragma unroll
        for (int ks = 0; ks < 2; ++ks) {
          const unsigned xa = (xl ^ (ks * 64)) + b_slot(c);
          xq[c * 2 + ks][0] = as_frag(lds_ld128(xa));
          xq[c * 2 + ks][1] = as_frag(lds_ld128(xa + 2048));
        }
#pragma unroll
        for (int n = 8 * c + 16; n < (c < 6 ? 8 * c + 24 : 70); ++n) instr(n);
      }
      STAMP(4);
      VM_WAIT(0);
      sig(L0 + SYNC + SY_WQ + wl * 4);
      wait4(L0 + SYNC + SY_WQ);
      wait4(L0 + SYNC + SY_EX);
      STAMP(3);
      f32x4 aq[2][2][2];
#pragma unroll
      for (int i = 0; i < 8; ++i) aq[i >> 2][(i >> 1) & 1][i & 1] = (f32x4){kEps, kEps, kEps, kEps};
      const unsigned wb = L0 + B_WQ + (8 * g + 2 * q) * 112 + 8 * p;
      const unsigned gl = L0 + B_G + (8 * g + q) * 8;
      const bool p3 = p == 3;
      const unsigned d1 = p3 ? 32u : 112u;
      const unsigned qa0 = opaque(p3 ? gl : wb + 32), qstep = opaque(p3 ? 256u : 3584u);
      bf16x8 fq[3][4];
      auto rdq = [&](int s) {
        const unsigned wk = wb + s * 3584;
        const unsigned a = qa0 + s * qstep;
        fq[s % 3][0] = lds_tr2(wk, wk + 112);
        fq[s % 3][1] = lds_tr2(a, a + d1);
        fq[s % 3][2] = lds_tr2(wk + 56, wk + 56 + 112);
        fq[s % 3][3] = lds_tr2(wk + 88, wk + 88 + 112);
      };
      rdq(0);
      rdq(1);
#pragma unroll
      for (int s = 0; s < 14; ++s) {
        SB();
        if (s + 2 < 14) rdq(s + 2);
#pragma unroll
        for (int i = 0; i < 4; ++i) {
          aq[0][i >> 1][i & 1] = mfma(fq[s % 3][i], xq[s][0], aq[0][i >> 1][i & 1]);
          aq[1][i >> 1][i & 1] = mfma(fq[s % 3][i], xq[s][1], aq[1][i >> 1][i & 1]);
        }
        interleave<8, 8>();
      }
      sig(L0 + SYNC + SY_QD + wl * 4);
      STAMP(5);
      u32x4 pqf[2][2];
      float omg[2];
#pragma unroll
      for (int t = 0; t < 2; ++t) {
        omg[t] = 1.f - sigmoidf(row3_bcast(aq[t][0][1][0]));
#pragma unroll
        for (int h = 0; h < 2; ++h) {
          const u32x2 a = phi4(aq[t][h][0]), b = phi4(aq[t][h][1]);
          pqf[t][h] = (u32x4){a[0], a[1], g3 ? 0u : b[0], g3 ? 0u : b[1]};
        }
      }
      tagO = uniform64(*(LAS u64*)(uintptr_t)(L0 + SYNC + SY_TAG));
      __builtin_amdgcn_s_setprio(2);
      STAMP(6);
      wait4(L0 + SYNC + SY_KV);
      STAMP(8);
      const unsigned ob = WS_O + ((rt * 8 + wl * 2) * 32 + 16 + hp * 2) * 1024 + lane * 16;
#pragma unroll
      for (int h = 0; h < 2; ++h) {
        const unsigned kb = L0 + B_KVL + h * 2048;
        const bf16x8 kv0 = as_frag(lds_ld128(kb + lane * 16)), kv1 = as_frag(lds_ld128(kb + 1024 + lane * 16));
        const bf16x8 zf = as_frag(lds_ld128(kb + 1024 + (g * 16 + 12) * 16));
#pragma unroll
        for (int t = 0; t < 2; ++t) {
          const f32x4 n0 = mfma(kv0, as_frag(pqf[t][h]), z4);
          const f32x4 n1 = mfma(kv1, as_frag(pqf[t][h]), z4);
          const f32x4 dn = mfma(zf, as_frag(pqf[t][h]), z4);
          const float s = omg[t] * __builtin_amdgcn_rcpf(dn[0] + 1e-6f);
          const u32x2 a = pk4(n0 * s), b = pk4(n1 * s);
          __builtin_amdgcn_raw_buffer_store_b128((u32x4){a[0], a[1], g3 ? 0u : b[0], g3 ? 0u : b[1]}, wsr, ob + (t * 32 + h) * 1024, 0, 16);
        }
      }
      STAMP(9);
      VM_WAIT(0);
      if (lane == 0) __hip_atomic_store((gu64*)(ws + WS_FB) + ((rt * 4 + wl) * 8 + hp) * FS, tagO, RLX_AGENT);
      STAMP(10); STAMPC(19);
      if (cg >= 7) { STAMP_FLUSH(); return; }
    }
  } else if (__builtin_expect(br, 1)) {
      __builtin_amdgcn_s_setprio(2);
      setup(B_G);
      f32x4 ak[4][2], av[4][2];
      {
        const float one = li == 12 ? 1.f : 0.f, i0 = rh ? one : 0.f, i1 = rh ? 0.f : one;
#pragma unroll
        for (int t = 0; t < 4; ++t) {
          ak[t][0] = ak[t][1] = (f32x4){kEps, kEps, kEps, kEps};
          av[t][0] = (f32x4){i0, i0, i0, i0};
          av[t][1] = (f32x4){i1, i1, i1, i1};
        }
      }
      bf16x8 xf[2][4], fw[2][4];
      unsigned xb, wb;
      const bool z0 = p == 3 && rh, z1 = p == 3 && !rh;
      const unsigned zaddr = L0 + SYNC + SY_ZERO;
      auto rd = [&](int ks, int part) {
        const unsigned xa = xb ^ (ks * 64), wk = wb + ks * 3584;
        if (part & 1) {
          const unsigned a = wk + WM_B + (((part >> 1) ^ rh) << 5);
          const bool z = (part >> 1) ? z1 : z0;
          fw[ks][part] = lds_tr2(z ? zaddr : a, z ? zaddr : a + 112);
        } else {
          fw[ks][part] = lds_tr2(wk + (part >> 1) * 32, wk + (part >> 1) * 32 + 112);
        }
        if (part < 2) {
          xf[ks][part * 2] = as_frag(lds_ld128(xa + part * 4096));
          xf[ks][part * 2 + 1] = as_frag(lds_ld128(xa + part * 4096 + 2048));
        }
      };
      auto mm = [&](int ks, int part) {
#pragma unroll
        for (int t = 0; t < 4; ++t) {
          if (part & 1) av[t][part >> 1] = mfma(xf[ks][t], fw[ks][part], av[t][part >> 1]);
          else ak[t][part >> 1] = mfma(xf[ks][t], fw[ks][part], ak[t][part >> 1]);
        }
      };
#pragma unroll
      for (int c = 0; c < 7; ++c) {
        xb = opaque(L0 + b_slot(c) + rh * 8192 + xsw);
        wb = opaque(L0 + b_slot(c) + B_W + wln);
        hook(c, true, true);
        LDS_BAR();
        hook(c, false, true);
        if (c == 0) { STAMP(2); STAMPC(16); }
        if (c == 3) STAMP(3);
        if (c == 6) { STAMP(4); STAMPC(17); }
        SB();
#pragma unroll
        for (int ks = 0; ks < 2; ++ks) {
#pragma unroll
          for (int part = 0; part < 4; ++part) rd(ks, part);
          if (ks == 1 || c > 0) {
#pragma unroll
            for (int part = 0; part < 4; ++part) mm(ks ^ 1, part);
            interleave<16, 12>();
          }
          SB();
        }
      }
      sig(L0 + SYNC + SY_D6 + w * 4);
      mm(1, 0); mm(1, 2);
      SB();
      mm(1, 3); mm(1, 1);
      STAMP(5);
      f32x4 kv[2][2];
      {
        u32x2 pk[4][2], vf[4][2];
#pragma unroll
        for (int t = 0; t < 4; ++t) {
          pk[t][0] = phi4(ak[t][0]);
          pk[t][1] = phi4(ak[t][1]);
          vf[t][0] = pk4(av[t][0]);
          vf[t][1] = pk4(av[t][1]);
        }
#pragma unroll
        for (int e = 1; e >= 0; --e)
#pragma unroll
          for (int d = 0; d < 2; ++d) {
            kv[e][d] = mfma(frag2(pk[0][d], pk[1][d]), frag2(vf[0][e], vf[1][e]), z4);
            kv[e][d] = mfma(frag2(pk[2][d], pk[3][d]), frag2(vf[2][e], vf[3][e]), kv[e][d]);
          }
      }
      lds_st128(L0 + B_EX + w * 2048 + lane * 16, __builtin_bit_cast(u32x4, kv[1][0]));
      lds_st128(L0 + B_EX + w * 2048 + 1024 + lane * 16, __builtin_bit_cast(u32x4, kv[1][1]));
      sig(L0 + SYNC + SY_EX + w * 4);
      wait1(L0 + SYNC + SY_EX + (w ^ 1) * 4);
      const f32x4 s0 = kv[0][0] + __builtin_bit_cast(f32x4, lds_ld128(L0 + B_EX + (w ^ 1) * 2048 + lane * 16));
      const f32x4 s1 = kv[0][1] + __builtin_bit_cast(f32x4, lds_ld128(L0 + B_EX + (w ^ 1) * 2048 + 1024 + lane * 16));
      {
        const u32x2 a = pk4(s0), b = pk4(s1);
        publish((u32x4){a[0], a[1], b[0], b[1]}, wsr, lane * 16 + kvs + rt * 1024);
      }
      STAMP(6);
      if (!__all(clr == tagO)) poll_flags(clrp(), tagO);
      wo_run(0, QWO);
      STAMP(7);
      f32x4 sum0, sum1;
      {
        const unsigned e0 = q == g ? ((p & 1) ? 0x3f800000u : 0x3f80u) : 0u;
        const unsigned ea = p < 2 ? e0 : 0u, eb = p < 2 ? 0u : e0;
        const bf16x8 sel0 = as_frag((u32x4){ea, eb, 0u, 0u}), sel1 = as_frag((u32x4){0u, 0u, ea, eb});
        unsigned vo = kvs + lane * 16;
#pragma nounroll
        for (unsigned spins = 0; spins < SPIN_CAP; ++spins) {
          asm volatile("" : "+v"(vo)::"memory");
          u32x4 pr[16];
#pragma unroll
          for (int r = 0; r < 16; ++r) pr[r] = __builtin_amdgcn_raw_buffer_load_b128(wsr, vo, ((r + 2 * rt) & 15) * 1024, 16);
          SB();
          sum0 = sum1 = z4;
#pragma unroll
          for (int r = 0; r < 16; ++r) {
            sum0 = mfma(sel0, as_frag(pr[r]), sum0);
            sum1 = mfma(sel1, as_frag(pr[r]), sum1);
          }
          if (!__any(sum0[0] != sum0[0])) break;
          unsigned m = 0;
#pragma unroll
          for (int r = 0; r < 16; ++r) m = max(max(max(max(m, pr[r][0]), pr[r][1]), pr[r][2]), pr[r][3]);
          if (!__any(m == SENT)) break;
        }
      }
      STAMP(8);
      lds_st128(L0 + B_KVL + w * 1024 + lane * 16, __builtin_bit_cast(u32x4, frag2(pk4(sum0), pk4(sum1))));
      sig(L0 + SYNC + SY_KV + w * 4);
      STAMP(10); STAMPC(19);
      __builtin_amdgcn_s_setprio(0);
      if (cg >= 7) { STAMP_FLUSH(); return; }
      sig(L0 + SYNC + SY_WOA + w * 4);
      wait4(L0 + SYNC + SY_D6);
      wait4(L0 + SYNC + SY_QD);
      wo_run(QWO, 28);
  } else {
      asm volatile("; role A");
      setup(A_G);
      f32x4 aq[4][2], ak[5][2], av[5][2];
#pragma unroll
      for (int t = 0; t < 5; ++t)
#pragma unroll
        for (int f = 0; f < 2; ++f) { ak[t][f] = av[t][f] = z4; if (t < 4) aq[t][f] = z4; }
      const unsigned gl = L0 + A_G + (8 * g + q) * 8;
      const bool p3 = p == 3;
      const unsigned d1 = p3 ? 32u : 112u;
      bf16x8 xf[2][5], fq[2][2], fk[2][2], fv[2][2];
      unsigned xb, wb;
      auto rd = [&](int c, int ks, int part) {
        const unsigned xa = xb ^ (ks * 64), wk = wb + ks * 3584;
        if (part == 0) {
          fk[ks][0] = lds_tr2(wk + WM_B, wk + WM_B + 112);
          xf[ks][0] = as_frag(lds_ld128(xa));
          xf[ks][1] = as_frag(lds_ld128(xa + 2048));
        } else if (part == 1) {
          fv[ks][0] = lds_tr2(wk + 2 * WM_B, wk + 2 * WM_B + 112);
          xf[ks][2] = as_frag(lds_ld128(xa + 4096));
        } else if (part == 2) {
          fq[ks][0] = lds_tr2(wk, wk + 112);
          xf[ks][3] = as_frag(lds_ld128(xa + 6144));
        } else if (part == 3) {
          fk[ks][1] = lds_tr2(wk + WM_B + 32, wk + WM_B + 32 + 112);
          xf[ks][4] = as_frag(lds_ld128(xa + 8192));
        } else if (part == 4) {
          fv[ks][1] = lds_tr2(wk + 2 * WM_B + 32, wk + 2 * WM_B + 32 + 112);
        } else {
          const unsigned a = p3 ? gl + c * 512 + ks * 256 : wk + 32;
          fq[ks][1] = lds_tr2(a, a + d1);
        }
      };
      auto mm = [&](int ks, int part) {
        const int f = part / 3, m = part % 3;
#pragma unroll
        for (int t = 0; t < 5; ++t) {
          if (m == 0) ak[t][f] = mfma(fk[ks][f], xf[ks][t], ak[t][f]);
          if (m == 1) av[t][f] = mfma(xf[ks][t], fv[ks][f], av[t][f]);
          if (m == 2 && t > 0) aq[t - 1][f] = mfma(fq[ks][f], xf[ks][t], aq[t - 1][f]);
        }
      };
#pragma unroll
      for (int c = 0; c < 7; ++c) {
        xb = opaque(L0 + a_slot(c) + rh * 8192 + xsw);
        wb = opaque(L0 + a_slot(c) + A_W + wln);
        hook(c, true, false);
        LDS_BAR();
        hook(c, false, false);
        if (c == 0) { STAMP(2); STAMPC(16); }
        if (c == 3) STAMP(3);
        if (c == 6) { STAMP(4); STAMPC(17); }
        SB();
#pragma unroll
        for (int ks = 0; ks < 2; ++ks) {
#pragma unroll
          for (int part = 0; part < 6; ++part) rd(c, ks, part);
          if (ks == 1 || c > 0) {
#pragma unroll
            for (int part = 0; part < 6; ++part) mm(ks ^ 1, part);
            interleave<28, 17>();
          }
          SB();
        }
      }
      sig(L0 + SYNC + SY_D6 + w * 4);
      mm(1, 0); mm(1, 3); mm(1, 2); mm(1, 5);
      SB();
      mm(1, 1); mm(1, 4);
      STAMP(5);
      const bool odd = li & 1;
      const int kk0 = 4 * g + (li & 1);
      const bool own0 = kk0 <= li, own1 = kk0 + 2 <= li;
      const float sc = 0.18898223650461362f * kLog2e;
      const float ninf = (rt == 0 && rh == 0) ? -__builtin_inff() : 0.f;
      const unsigned l16 = lane_id() * 16;
      bf16x8 kf[5];
#pragma unroll
      for (int t = 0; t < 5; ++t) kf[t] = frag2(pk4(ak[t][0]), pk4(ak[t][1]));
      f32x4 sp[4], so[4];
#pragma unroll
      for (int t = 0; t < 4; ++t) {
        const u32x2 b = pk4(aq[t][1]);
        const bf16x8 qf = frag2(pk4(aq[t][0]), (u32x2){g3 ? 0u : b[0], g3 ? 0u : b[1]});
        sp[t] = mfma(kf[t], qf, t == 0 ? (f32x4){ninf, ninf, ninf, ninf} : z4);
        so[t] = mfma(kf[t + 1], qf, z4);
      }
      float ug;
      {
        float z0 = aq[0][1][0], z1 = aq[1][1][0], z2 = aq[2][1][0], z3 = aq[3][1][0];
        swap32(z0, z1);
        swap32(z2, z3);
        swap16(z1, z3);
        ug = 1.f + __builtin_amdgcn_exp2f(-kLog2e * z3);
      }
      u32x2 vf[5][2];
#pragma unroll
      for (int t = 0; t < 5; ++t) {
        vf[t][0] = pk4(av[t][0]);
        vf[t][1] = pk4(av[t][1]);
      }
      float l0[4], l1[4], bc[4];
#pragma unroll
      for (int t = 0; t < 4; ++t) {
        float a0 = so[t][0], a2 = so[t][2], b0 = sp[t][0], b2 = sp[t][2];
        const float c0 = own0 ? a0 : b0, c1 = own0 ? so[t][1] : sp[t][1], c2 = own1 ? a2 : b2, c3 = own1 ? so[t][3] : sp[t][3];
        l0[t] = odd ? c1 : c0;
        l1[t] = odd ? c3 : c2;
      }
      rows_bcast4(rows_reduce4<true>(fmax1(l0[0], l1[0]), fmax1(l0[1], l1[1]), fmax1(l0[2], l1[2]), fmax1(l0[3], l1[3])) * -sc, bc);
#pragma unroll
      for (int t = 0; t < 4; ++t) {
        l0[t] = __builtin_amdgcn_exp2f(__builtin_fmaf(l0[t], sc, bc[t]));
        l1[t] = __builtin_amdgcn_exp2f(__builtin_fmaf(l1[t], sc, bc[t]));
      }
      rows_bcast4(__builtin_amdgcn_rcpf(rows_reduce4<false>(l0[0] + l1[0], l0[1] + l1[1], l0[2] + l1[2], l0[3] + l1[3]) * ug), bc);
      const unsigned sh = odd ? 16u : 0u;
      const unsigned mk0 = own0 ? 0u : 0xffffu, mk1 = own1 ? 0u : 0xffffu;
#pragma unroll
      for (int t = 0; t < 4; ++t) {
        const unsigned pe = pk2(l0[t] * bc[t], l1[t] * bc[t]), ph = pe >> 16;
        const bf16x8 pf = as_frag((u32x4){(pe & mk0) << sh, (ph & mk1) << sh, (pe & (mk0 ^ 0xffffu)) << sh, (ph & (mk1 ^ 0xffffu)) << sh});
        const f32x4 r0 = mfma(frag2(vf[t][0], vf[t + 1][0]), pf, z4);
        const f32x4 r1 = mfma(frag2(vf[t][1], vf[t + 1][1]), pf, z4);
        const u32x2 c0 = pk4(r0), c1 = pk4(r1);
        __builtin_amdgcn_raw_buffer_store_b128((u32x4){c0[0], c0[1], g3 ? 0u : c1[0], g3 ? 0u : c1[1]}, wsr, l16 + obs + t * 32768, 0, 16);
      }
      STAMP(9);
      VM_WAIT(0);
      if (l16 == 0) __hip_atomic_store((gu64*)(ws + WS_FA) + ((rt * 2 + rh) * 16 + hp * 2 + hh) * FS, tagO, RLX_AGENT);
      STAMP(10); STAMPC(19);
      if (cg >= 7) {
        if (bid == 56 && w == 0) {
#pragma nounroll
          for (int k = 0; k < 4; ++k) {
            const int i = (k & 1) * 64 + lane;
            poll_flags(k < 2 ? (gu64*)(ws + WS_CLR) + i : (gu64*)(ws + WS_FA) + ((i >> 3) * 32 + (i & 7) * 2) * FS, tagO);
          }
          if (lane == 0) __hip_atomic_store((gu64*)(ws + WS_E), tagO, RLX_AGENT);
        }
        STAMP_FLUSH();
        return;
      }
  }

  const int pb = w >> 2;
  const gu64* fp = pb ? (gu64*)(ws + WS_FB) + ((rg * 2 + (wl >> 1)) * 8 + (li & 7)) * FS : (gu64*)(ws + WS_FA) + (rg * 16 + li) * FS;
  const u64 f0 = __hip_atomic_load(fp, RLX_AGENT);
  wait4(L0 + SYNC + (pb ? SY_WOB : SY_WOA));
  const unsigned sw = (2 * g + (q >> 1)) & 3;
  const unsigned rb = L0 + (pb * 448 + 4 * g + q) * 128 + 8 * p;
  unsigned wa[4], wa2[4];
#pragma unroll
  for (int v = 0; v < 4; ++v) {
    wa[v] = opaque(rb + ((v ^ sw) << 5));
    wa2[v] = opaque(wa[v] + (g3 ? 0u : 2048u));
  }
  auto wfrag = [&](int h, int ct) {
    const int v = ct ^ (2 * (h & 1));
    return lds_tr2(wa[v] + h * 3584, wa2[v] + h * 3584);
  };
  constexpr int NPRE = 8;
  bf16x8 wf[16][4];
#pragma unroll
  for (int h = 0; h < NPRE; ++h)
#pragma unroll
    for (int ct = 0; ct < 4; ++ct) wf[h][ct] = wfrag(h, ct);
  STAMP(11);
  const unsigned ub = WS_O + ((rg * 4 + wl) * 32 + pb * 16) * 1024;
  tagO = uniform64(*(LAS u64*)(uintptr_t)(L0 + SYNC + SY_TAG));
  if (br && !pb) { VM_WAIT(0); sig(L0 + SYNC + SY_WOB + wl * 4); }
  if (!__all(f0 == tagO)) poll_flags(fp, tagO);
  asm volatile("" ::"v"(gv), "v"(epoch), "v"(pt));
  STAMP(12);
  u32x4 of[16];
  {
    unsigned vo = ub + lane * 16;
    asm volatile("" : "+v"(vo)::"memory");
#pragma unroll
    for (int h = 0; h < 16; ++h) of[h] = __builtin_amdgcn_raw_buffer_load_b128(wsr, vo, ((h + 2 * cg) & 15) * 1024, 16);
    SB();
  }
  f32x4 acc[4] = {z4, z4, z4, z4};
#pragma unroll
  for (int h = 0; h < 16; ++h) {
#pragma unroll
    for (int ct = 0; ct < 4; ++ct) acc[ct] = mfma(wf[h][ct], as_frag(of[h]), acc[ct]);
    SB();
    if (h + NPRE < 16) {
#pragma unroll
      for (int ct = 0; ct < 4; ++ct) wf[h + NPRE][ct] = wfrag(h + NPRE, ct);
    }
    SB();
  }
  STAMP(13);
  const unsigned xa = L0 + P2X + wl * 4096 + lane * 16;
  if (!pb) {
    wait4(L0 + SYNC + SY_D6);
#pragma unroll
    for (int ct = 0; ct < 4; ++ct) lds_st128(xa + ct * 1024, __builtin_bit_cast(u32x4, acc[ct]));
    sig(L0 + SYNC + SY_P2 + wl * 4);
  } else {
    f32x4 pa[4];
    for (;;) {
      const unsigned f = *(volatile LAS unsigned*)(uintptr_t)(L0 + SYNC + SY_P2 + wl * 4);
#pragma unroll
      for (int ct = 0; ct < 4; ++ct) pa[ct] = *(volatile LAS f32x4*)(uintptr_t)(xa + ct * 1024);
      if (__builtin_amdgcn_readfirstlane(f)) break;
      __builtin_amdgcn_s_sleep(1);
    }
    char* orow = out + (rg * 64 + wl * 16 + li) * XROW + cg * 128 + (g & 1) * 32 + (g >> 1) * 16;
#pragma unroll
    for (int k = 0; k < 2; ++k) {
      const u32x2 a = pk4(acc[2 * k] + pa[2 * k]), b = pk4(acc[2 * k + 1] + pa[2 * k + 1]);
      const auto r0 = __builtin_amdgcn_permlane16_swap(a[0], b[0], false, false);
      const auto r1 = __builtin_amdgcn_permlane16_swap(a[1], b[1], false, false);
      *(u32x4*)(orow + k * 64) = (u32x4){r0[0], r1[0], r0[1], r1[1]};
    }
  }
  STAMP(14);
  STAMP(15);
  STAMP_FLUSH();
}

}

extern "C" void kernel_launch(void* const* d_in, const int* in_sizes, int n_in, void* d_out, int out_size, void* d_ws,
                              size_t ws_size, hipStream_t stream) {
  (void)in_sizes; (void)n_in; (void)out_size; (void)ws_size;
  gated_mix_kernel<<<256, 512, 0, stream>>>((const char*)d_in[0], (const char*)d_in[1], (const char*)d_in[2],
                                            (const char*)d_in[4], (const char*)d_in[5],
                                            (const unsigned short*)d_in[6], (char*)d_out, (char*)d_ws);
}
```

```cpp
#include <hip/hip_runtime.h>
#include <stdint.h>

typedef __bf16 bf16x2 __attribute__((ext_vector_type(2)));
typedef __bf16 bf16x4 __attribute__((ext_vector_type(4)));
typedef __bf16 bf16x8 __attribute__((ext_vector_type(8)));
typedef float f32x2 __attribute__((ext_vector_type(2)));
typedef float f32x4 __attribute__((ext_vector_type(4)));
typedef unsigned u32x2 __attribute__((ext_vector_type(2)));
typedef unsigned u32x4 __attribute__((ext_vector_type(4)));
typedef unsigned long long u64;
#define LAS __attribute__((address_space(3)))
#define GAS __attribute__((address_space(1)))
typedef GAS u64 gu64;
#define RLX_AGENT __ATOMIC_RELAXED, __HIP_MEMORY_SCOPE_AGENT

namespace {

constexpr int kS = 2048, kD = 448;
constexpr int XROW = kD * 2;
constexpr int WROW = 3 * kD * 2;
constexpr int WM_B = 64 * 112;

constexpr int A_FRONT = 34816, A_SLOT = 144 * 128 + 3 * WM_B, A_W = 144 * 128;
constexpr int A_G = A_FRONT + 3 * A_SLOT;
constexpr int B_SLOT = 128 * 128 + 2 * WM_B, B_W = 128 * 128;
constexpr int B_WQ = 97536, B_G = B_WQ + 7 * WM_B, B_EX = B_G + 3584, B_KVL = B_EX + 8192;
constexpr int P2X = 114688;
constexpr int SYNC = 163584;
constexpr int LDS_B = 163840;
static_assert(A_G + 3584 <= SYNC && B_KVL + 4096 <= SYNC && A_FRONT + 2 * A_SLOT == 114688, "lds map");
__host__ __device__ constexpr int a_slot(int c) { return A_FRONT + ((c + 2) % 3) * A_SLOT; }
__host__ __device__ constexpr int b_slot(int c) { return ((c + 2) % 3) * B_SLOT; }
enum { SY_WOA = 0, SY_WOB = 16, SY_D6 = 32, SY_ST = 48, SY_WQ = 64, SY_QD = 80, SY_EX = 96, SY_KV = 112, SY_P2 = 128, SY_ZERO = 192, SY_TAG = 240 };

constexpr int WS_E = 0;
constexpr int WS_CLR = 4096;
constexpr int FS = 16;
constexpr int WS_FA = 8192;
constexpr int WS_FB = WS_FA + 16 * 2 * 16 * 8 * FS;
constexpr int WS_KVP = WS_FB + 16 * 4 * 8 * 8 * FS;
constexpr int WS_O = WS_KVP + 8 * 4 * 16 * 1024;
constexpr int WS_END = WS_O + 16 * 8 * 32 * 1024;

constexpr int PT_WS = (WS_END / 8) & ~127;
constexpr unsigned SPIN_CAP = 1u << 14;
constexpr unsigned SENT = 0xffffffffu;
#define NQE 0
static_assert(NQE <= 3, "");
#define QWO 14

#define STAMP(i) do {} while (0)
#define STAMPC(i) do {} while (0)
#define STAMP_FLUSH() do {} while (0)

__device__ __forceinline__ unsigned pk2(float a, float b) {
  f32x2 v = {a, b};
  bf16x2 r = __builtin_convertvector(v, bf16x2);
  return __builtin_bit_cast(unsigned, r);
}
__device__ __forceinline__ u32x2 pk4(f32x4 v) { return (u32x2){pk2(v[0], v[1]), pk2(v[2], v[3])}; }
__device__ __forceinline__ bf16x8 as_frag(u32x4 v) { return __builtin_bit_cast(bf16x8, v); }
__device__ __forceinline__ bf16x8 frag2(u32x2 a, u32x2 b) { return as_frag((u32x4){a[0], a[1], b[0], b[1]}); }
__device__ __forceinline__ f32x4 mfma(bf16x8 a, bf16x8 b, f32x4 c) {
  return __builtin_amdgcn_mfma_f32_16x16x32_bf16(a, b, c, 0, 0, 0);
}
__device__ __forceinline__ bf16x8 lds_tr2(unsigned a0, unsigned a1) {
  bf16x4 lo = __builtin_amdgcn_ds_read_tr16_b64_v4bf16((LAS bf16x4*)(uintptr_t)a0);
  bf16x4 hi = __builtin_amdgcn_ds_read_tr16_b64_v4bf16((LAS bf16x4*)(uintptr_t)a1);
  return __builtin_shufflevector(lo, hi, 0, 1, 2, 3, 4, 5, 6, 7);
}
__device__ __forceinline__ u32x4 lds_ld128(unsigned a) { return *(LAS u32x4*)(uintptr_t)a; }
__device__ __forceinline__ void lds_st128(unsigned a, u32x4 v) { *(LAS u32x4*)(uintptr_t)a = v; }
__device__ __forceinline__ void lds_st64(unsigned a, u32x2 v) { *(LAS u32x2*)(uintptr_t)a = v; }
__device__ __forceinline__ unsigned opaque(unsigned v) { asm volatile("" : "+v"(v)); return v; }

__device__ __forceinline__ void sig(unsigned a) {
  asm volatile("" ::: "memory");
  *(volatile LAS unsigned*)(uintptr_t)a = 1u;
  asm volatile("" ::: "memory");
  __builtin_amdgcn_sched_barrier(0);
}
__device__ __forceinline__ void wait1(unsigned a) {
  while (__builtin_amdgcn_readfirstlane(*(volatile LAS unsigned*)(uintptr_t)a) == 0u) __builtin_amdgcn_s_sleep(1);
  asm volatile("" ::: "memory");
}
__device__ __forceinline__ void wait4(unsigned a) {
  for (;;) {
    const u32x4 v = *(volatile LAS u32x4*)(uintptr_t)a;
    if (__builtin_amdgcn_readfirstlane(v[0] & v[1] & v[2] & v[3])) break;
    __builtin_amdgcn_s_sleep(1);
  }
  asm volatile("" ::: "memory");
}

__device__ __forceinline__ void wait4_hot(unsigned a) {
  for (;;) {
    const u32x4 v = *(volatile LAS u32x4*)(uintptr_t)a;
    if (__builtin_amdgcn_readfirstlane(v[0] & v[1] & v[2] & v[3])) break;
  }
  asm volatile("" ::: "memory");
}

__device__ __forceinline__ u32x4 make_srd(const void* p, unsigned bytes) {
  uint64_t a = (uint64_t)p;
  u32x4 r;
  r[0] = __builtin_amdgcn_readfirstlane((unsigned)a);
  r[1] = __builtin_amdgcn_readfirstlane((unsigned)(a >> 32) & 0xffffu);
  r[2] = bytes;
  r[3] = 0x00020000u;
  return r;
}
__device__ __forceinline__ void dma16(u32x4 srd, unsigned voff, unsigned soff, unsigned ldsaddr) {
  asm volatile(
      "s_mov_b32 m0, %3\n\t"
      "s_nop 3\n\t"
      "buffer_load_dwordx4 %0, %1, %2 offen lds"
      :
      : "v"(voff), "s"(srd), "s"(soff), "s"(ldsaddr));
}
#define VM_BAR(N) asm volatile("s_waitcnt vmcnt(" #N ") lgkmcnt(0)\n\ts_barrier" ::: "memory")
#define LDS_BAR() asm volatile("s_waitcnt lgkmcnt(0)\n\ts_barrier" ::: "memory")
#define VM_WAIT(N) asm volatile("s_waitcnt vmcnt(" #N ")" ::: "memory")
template <int N> __device__ __forceinline__ void vm_bar() { asm volatile("s_waitcnt vmcnt(%0) lgkmcnt(0)\n\ts_barrier" ::"n"(N) : "memory"); }
#define SB() __builtin_amdgcn_sched_barrier(0)

__device__ __forceinline__ float fmax1(float a, float b) { return __builtin_amdgcn_fmed3f(a, b, 3.0e38f); }
__device__ __forceinline__ void swap16(float& a, float& b) {
  auto r = __builtin_amdgcn_permlane16_swap(__float_as_uint(a), __float_as_uint(b), false, false);
  const unsigned r0 = r[0], r1 = r[1];
  a = __uint_as_float(r0);
  b = __uint_as_float(r1);
}
__device__ __forceinline__ void swap32(float& a, float& b) {
  auto r = __builtin_amdgcn_permlane32_swap(__float_as_uint(a), __float_as_uint(b), false, false);
  const unsigned r0 = r[0], r1 = r[1];
  a = __uint_as_float(r0);
  b = __uint_as_float(r1);
}
template <bool MAX>
__device__ __forceinline__ float rows_reduce4(float v0, float v1, float v2, float v3) {
  swap32(v0, v1);
  swap32(v2, v3);
  float a = MAX ? fmax1(v0, v1) : v0 + v1, b = MAX ? fmax1(v2, v3) : v2 + v3;
  swap16(a, b);
  return MAX ? fmax1(a, b) : a + b;
}
__device__ __forceinline__ void rows_bcast4(float x, float (&o)[4]) {
  float y = x;
  swap16(x, y);
  o[0] = x; o[1] = x; o[2] = y; o[3] = y;
  swap32(o[0], o[1]);
  swap32(o[2], o[3]);
}
__device__ __forceinline__ float row3_bcast(float v) {
  float a = v;
  swap32(a, v);
  a = v;
  swap16(a, v);
  return v;
}
template <int NM, int ND>
__device__ __forceinline__ void interleave() {
#pragma unroll
  for (int i = 0; i < NM; ++i) {
    __builtin_amdgcn_sched_group_barrier(0x8, 1, 0);
    if (i < ND) {
      __builtin_amdgcn_sched_group_barrier(0x2, 1, 0);
      __builtin_amdgcn_sched_group_barrier(0x100, 1, 0);
    }
  }
}
constexpr float kEps = 1e-6f, kLog2e = 1.44269504088896f;
__device__ __forceinline__ float phi(float u) {
  const float e = __builtin_amdgcn_fmed3f(__builtin_amdgcn_exp2f(__builtin_fmaf(u, kLog2e, -kEps * kLog2e)), 0.f, 1.f);
  return fmax1(u, kEps) + e;
}
__device__ __forceinline__ u32x2 phi4(f32x4 v) { return (u32x2){pk2(phi(v[0]), phi(v[1])), pk2(phi(v[2]), phi(v[3]))}; }
__device__ __forceinline__ float sigmoidf(float z) {
  return __builtin_amdgcn_rcpf(1.f + __builtin_amdgcn_exp2f(-1.44269504088896f * z));
}
__device__ __forceinline__ u64 next_tag(u64 e) {
  const u64 s = ((u64)(unsigned)__builtin_amdgcn_readfirstlane((unsigned)(e >> 32)) << 32) |
                (unsigned)__builtin_amdgcn_readfirstlane((unsigned)e);
  return s * 6364136223846793005ull + 1442695040888963407ull;
}
__device__ __forceinline__ u64 uniform64(u64 e) {
  return ((u64)(unsigned)__builtin_amdgcn_readfirstlane((unsigned)(e >> 32)) << 32) |
         (unsigned)__builtin_amdgcn_readfirstlane((unsigned)e);
}
__device__ __forceinline__ void poll_flags(const gu64* p, u64 tag) {
#pragma nounroll
  for (unsigned spins = 0; spins < SPIN_CAP; ++spins) {
    u64 v = __hip_atomic_load(p, RLX_AGENT);
    if (__all(v == tag)) break;
    __builtin_amdgcn_s_sleep(2);
  }
  asm volatile("" ::: "memory");
}
__device__ __forceinline__ void publish(u32x4 v, __amdgpu_buffer_rsrc_t r, unsigned voff) {
#pragma unroll
  for (int i = 0; i < 4; ++i) v[i] = v[i] < SENT - 1u ? v[i] : SENT - 1u;
  __builtin_amdgcn_raw_buffer_store_b128(v, r, voff, 0, 16);
}
__device__ __forceinline__ unsigned lane_id() {
  unsigned l = __builtin_amdgcn_mbcnt_hi(~0u, __builtin_amdgcn_mbcnt_lo(~0u, 0u));
  asm volatile("" : "+v"(l));
  return l;
}
__device__ __forceinline__ int wrap7(int c) { return c >= 7 ? c - 7 : c; }

__global__ __launch_bounds__(512, 2) void gated_mix_kernel(const char* __restrict__ x, const char* __restrict__ wqkvA,
                                                           const char* __restrict__ woA, const char* __restrict__ wqkvB,
                                                           const char* __restrict__ woB,
                                                           const unsigned short* __restrict__ gate_w, char* __restrict__ out,
                                                           char* ws) {
  __shared__ __attribute__((aligned(1024))) char lds[LDS_B];
  const unsigned L0 = (unsigned)(uintptr_t)(LAS char*)lds;
  const int bid = blockIdx.x;
  const int xcd = bid & 7, idx = bid >> 3;
  const int xj = xcd >> 1, ci = idx >> 3;
  const int br = ci >> 1;
  const int hp = 2 * (br ? (xj ^ 1) : xj) + (ci & 1);
  const int rt = (xcd & 1) * 8 + (idx & 7);
  const int rot = wrap7(wrap7((idx & 7) + 2 * ci + (xj & 1)));
  const int cg = idx & 7, rg = 4 * xcd + ci;

  const int tid = threadIdx.x;
  const int lane = tid & 63;
  const int w = __builtin_amdgcn_readfirstlane(tid >> 6) & 7;
  const int wl = w & 3;
  const int li = lane & 15, g = lane >> 4, q = (lane >> 2) & 3, p = lane & 3;
  const bool g3 = g == 3;
  const f32x4 z4 = {0.f, 0.f, 0.f, 0.f};
  STAMP(0); STAMPC(18);
  unsigned vx0 = (lane >> 3) * XROW + (((lane & 7) ^ (lane >> 4)) << 4);
  unsigned vx1 = vx0 ^ 64u;
  unsigned vw0, vw1;
  {
    const unsigned s0 = wl * 64 + lane, s1 = s0 + 256;
    const unsigned p0 = (s0 * 9363u) >> 16, p1 = (s1 * 9363u) >> 16;
    const unsigned k0 = (p0 & 56) | ((p0 >> 1) & 3) | ((p0 & 1) << 2);
    const unsigned k1 = (p1 & 56) | ((p1 >> 1) & 3) | ((p1 & 1) << 2);
    vw0 = k0 * WROW + hp * 112 + (s0 - p0 * 7) * 16;
    vw1 = k1 * WROW + hp * 112 + (s1 - p1 * 7) * 16;
  }
  asm volatile("" : "+v"(vx0), "+v"(vx1), "+v"(vw0), "+v"(vw1));
  asm volatile("" ::"s"(x), "s"(wqkvA), "s"(woA), "s"(wqkvB), "s"(woB), "s"(gate_w), "s"(out), "s"(ws));
  const __amdgpu_buffer_rsrc_t wsr = __builtin_amdgcn_make_buffer_rsrc(ws, 0, WS_END, 0x00020000);
  u64 tagO;
  auto wo_run = [&](int j0, int j1) {
    const unsigned vwo = (lane >> 3) * XROW + cg * 128 + (((lane & 7) ^ (((lane >> 4) & 3) << 1)) << 4);
    const u32x4 sa = make_srd(woA, kD * XROW), sb = make_srd(woB, kD * XROW);
    unsigned so = (wl + 7 * cg + 4 * (j0 % 14)) * (8 * XROW);
    so = so >= kD * XROW ? so - kD * XROW : so;
#pragma unroll
    for (int j = j0; j < j1; ++j) {
      dma16(j < 14 ? sa : sb, vwo, so, L0 + (wl + 4 * j) * 1024);
      so += 32 * XROW;
      so = so >= kD * XROW ? so - kD * XROW : so;
    }
  };
  const u32x4 srdX = make_srd(x, kS * XROW);
  const u32x4 srdW = make_srd(br ? wqkvB : wqkvA, kD * WROW);
  const bool l3 = wl == 3;
  const int xr = rt * 128 + wl * 32 - (br ? 0 : 16);
  const unsigned sx01 = (xr < 0 ? 0 : xr) * XROW, sx23 = (xr + 16) * XROW;
  const bool l3a = l3 && !br;
  const u32x4 srd2 = l3a ? srdX : srdW;
  const unsigned v2 = l3 ? vw0 : vw1;
  const unsigned v2a = l3a ? vx0 : v2, v2b = l3a ? vx1 : v2;
  const unsigned st2 = l3a ? 128u : 64u * WROW, s2a = l3a ? (rt * 128 + 112) * XROW : 0u;
  const unsigned s2d = l3a ? 8u * XROW : (unsigned)XROW, s2e = l3a ? 8u * XROW : 2u * XROW;
  const unsigned sub2 = (l3 ? wl : wl + 4) * 1024;
  auto chunk = [&](unsigned sl, unsigned lq, unsigned lk, int cc) {
    const unsigned sw = cc * (64 * WROW), sx = cc * 128, s2 = cc * st2 + s2a;
#pragma unroll
    for (int j = 0; j < 4; ++j)
      dma16(srdX, (j & 1) ? vx1 : vx0, (j < 2 ? sx01 : sx23) + (j & 1) * 8 * XROW + sx, sl + (wl * 4 + j) * 1024);
    dma16(srdW, vw0, sw + XROW, lk + wl * 1024);
    dma16(srdW, vw0, sw + 2 * XROW, lk + WM_B + wl * 1024);
    dma16(srd2, v2b, s2 + s2d, l3a ? sl + 17408 : lk + sub2);
    dma16(srd2, v2b, s2 + s2e, l3a ? sl + 17408 : lk + WM_B + sub2);
    if (!br) {
      dma16(srdW, vw0, sw, lq + wl * 1024);
      dma16(srd2, v2a, s2, l3a ? sl + 16384 : lq + sub2);
    }
  };
  unsigned gv = 0, pt = 0;
  u64 epoch = 0;
  {
    int wx = w;
    asm volatile("" : "+s"(wx));
    if (wx < 4) {
      gv = ((const unsigned*)gate_w)[tid < 224 ? tid : 0];
      epoch = __hip_atomic_load((gu64*)(ws + WS_E), RLX_AGENT);
      const char* pb_ = wx == 0 ? ws : (wx == 1 ? out + rg * 64 * XROW : (wx == 2 ? woA : woB));
      pt = __hip_atomic_load((GAS unsigned*)(pb_ + (lane & 7) * (wx == 0 ? PT_WS : (wx == 1 ? 7168 : 50176))), RLX_AGENT);
    } else {
#pragma nounroll
      for (int c = 0; c < 2; ++c) {
        const unsigned sl = L0 + (br ? (c ? b_slot(1) : b_slot(0)) : (c ? a_slot(1) : a_slot(0)));
        chunk(sl, sl + A_W, sl + (br ? B_W : A_W + WM_B), wrap7(rot + c));
      }
    }
  }

    const int rh = w & 1, hh = w >> 1;
    const unsigned obs = WS_O + ((rt * 8 + rh * 4) * 32 + hp * 2 + hh) * 1024;
    const unsigned kvs = WS_KVP + (hp * 4 + w) * 16 * 1024;
    auto setup = [&](int goff) {
      if (tid < 60) *(LAS unsigned*)(uintptr_t)(L0 + SYNC + tid * 4) = 0u;
      const unsigned lo = (gv & 0xffffu) * 0x10001u, hi = (gv >> 16) * 0x10001u;
      const int pos = tid - rot * 32;
      if (tid < 224) lds_st128(L0 + goff + (pos < 0 ? pos + 224 : pos) * 16, (u32x4){lo, lo, hi, hi});
    };
    u64 clr = 0;
    auto clrp = [&]() { return (const gu64*)(ws + WS_CLR) + hp * 16 + (lane_id() & 15); };
    auto hook = [&](int c, bool before, bool isb) {
      if (c == 0 && !before && isb)
        __builtin_amdgcn_raw_buffer_store_b128((u32x4){SENT, SENT, SENT, SENT}, wsr, lane_id() * 16 + kvs + rt * 1024, 0, 16);
      if (c == 3 && before) {
        asm volatile("" : "+v"(epoch) : "v"(pt));
        tagO = next_tag(epoch);
        *(LAS u64*)(uintptr_t)(L0 + SYNC + SY_TAG) = tagO;
        VM_WAIT(0);
      }
      if (c == 3 && !before && isb)
        __builtin_amdgcn_raw_buffer_store_b64((u32x2){(unsigned)tagO, (unsigned)(tagO >> 32)}, wsr,
                                              (lane_id() | w) == 0 ? WS_CLR + (hp * 16 + rt) * 8 : 0x7ffffff0u, 0, 16);
      if (c == 6 && !before && isb) clr = __hip_atomic_load(clrp(), RLX_AGENT);
      if (c == 6 && !before) {
        const unsigned l = lane_id(), u = (l >> 3) & 3;
        const unsigned o = isb ? WS_O + ((rt * 8 + w * 2 + (u >> 1)) * 32 + 16 + hp * 2 + (u & 1)) * 1024 : obs + u * 32768;
        pt = __builtin_amdgcn_raw_buffer_load_b32(wsr, o + (l & 7) * 128, 0, 16);
      }
    };
    const unsigned xsw = li * 128 + ((g ^ (li >> 1)) << 4);
    const unsigned wln = (8 * g + 2 * q) * 112 + 8 * p + 56 * hh;

  if (__builtin_expect(w >= 4, 1)) {
    if (!br) {
      int cc = wrap7(rot + 1);
      unsigned sl = L0 + a_slot(2);
#pragma nounroll
      for (int c = 2; c < 7; ++c) {
        vm_bar<10>();
        cc = wrap7(cc + 1);
        chunk(sl, sl + A_W, sl + A_W + WM_B, cc);
        sl = sl == L0 + a_slot(0) ? L0 + a_slot(1) : sl + A_SLOT;
      }
      vm_bar<10>();
      if (cg >= 7) { vm_bar<0>(); STAMP_FLUSH(); return; }
      wo_run(0, 10);
      vm_bar<10>();
      wo_run(10, 28);
      VM_WAIT(14);
      sig(L0 + SYNC + SY_WOA + wl * 4);
      VM_WAIT(0);
      sig(L0 + SYNC + SY_WOB + wl * 4);
    } else {
      auto instr = [&](int n) {
        if (n < 56) {
          const int c = n >> 3, j = n & 7, cc = wrap7(c + rot);
          const unsigned sl = L0 + b_slot(c), sw = cc * (64 * WROW), sx = cc * 128;
          if (j < 4) dma16(srdX, (j & 1) ? vx1 : vx0, sx01 + j * 8 * XROW + sx, sl + (wl * 4 + j) * 1024);
          else dma16(srdW, j < 6 ? vw0 : v2, sw + ((j & 1) + 1) * XROW, sl + B_W + (j & 1) * WM_B + (j < 6 ? wl * 1024 : sub2));
        } else {
          const int c = (n - 56) >> 1;
          dma16(srdW, (n & 1) ? v2 : vw0, wrap7(c + rot) * (64 * WROW), L0 + B_WQ + c * WM_B + ((n & 1) ? sub2 : wl * 1024));
        }
      };
      STAMP(1);
      bf16x8 xq[14][2];
      const unsigned xl = L0 + (wl * 32 + li) * 128 + ((g ^ (li >> 1)) << 4);
      f32x4 aq[2][2][2];
#pragma unroll
      for (int i = 0; i < 8; ++i) aq[i >> 2][(i >> 1) & 1][i & 1] = (f32x4){kEps, kEps, kEps, kEps};
      const unsigned wb = L0 + B_WQ + (8 * g + 2 * q) * 112 + 8 * p;
      const unsigned gl = L0 + B_G + (8 * g + q) * 8;
      const bool p3 = p == 3;
      const unsigned d1 = p3 ? 32u : 112u;
      const unsigned qa0 = opaque(p3 ? gl : wb + 32), qstep = opaque(p3 ? 256u : 3584u);
      bf16x8 fq[3][4];
      auto rdq = [&](int s) {
        const unsigned wk = wb + s * 3584;
        const unsigned a = qa0 + s * qstep;
        fq[s % 3][0] = lds_tr2(wk, wk + 112);
        fq[s % 3][1] = lds_tr2(a, a + d1);
        fq[s % 3][2] = lds_tr2(wk + 56, wk + 56 + 112);
        fq[s % 3][3] = lds_tr2(wk + 88, wk + 88 + 112);
      };
      auto mmq = [&](int s) {
#pragma unroll
        for (int i = 0; i < 4; ++i) {
          aq[0][i >> 1][i & 1] = mfma(fq[s % 3][i], xq[s][0], aq[0][i >> 1][i & 1]);
          aq[1][i >> 1][i & 1] = mfma(fq[s % 3][i], xq[s][1], aq[1][i >> 1][i & 1]);
        }
      };
#pragma unroll
      for (int c = 0; c < 7; ++c) {
        if (c + 1 >= 2 && c + 1 < 2 + NQE) vm_bar<10>(); else vm_bar<8>();
#pragma unroll
        for (int ks = 0; ks < 2; ++ks) {
          const unsigned xa = (xl ^ (ks * 64)) + b_slot(c);
          xq[c * 2 + ks][0] = as_frag(lds_ld128(xa));
          xq[c * 2 + ks][1] = as_frag(lds_ld128(xa + 2048));
        }
        const bool qe = c >= 2 && c < 2 + NQE;
        if (qe) { rdq(2 * c - 4); rdq(2 * c - 3); }
        if (c < 5) {
#pragma unroll
          for (int n = 8 * c + 16; n < 8 * c + 24; ++n) instr(n);
          if (c < NQE) { instr(56 + 2 * c); instr(57 + 2 * c); }
        } else {
#pragma unroll
          for (int n = 56 + 2 * NQE + (c - 5) * 8; n < (c == 5 ? 64 + 2 * NQE : 70); ++n) instr(n);
        }
        if (qe) { mmq(2 * c - 4); mmq(2 * c - 3); }
        SB();
      }
      STAMP(4);
      VM_WAIT(0);
      sig(L0 + SYNC + SY_WQ + wl * 4);
      wait4(L0 + SYNC + SY_WQ);
      wait4(L0 + SYNC + SY_EX);
      STAMP(3);
      rdq(2 * NQE);
      rdq(2 * NQE + 1);
#pragma unroll
      for (int s = 2 * NQE; s < 14; ++s) {
        SB();
        if (s + 2 < 14) rdq(s + 2);
        mmq(s);
        interleave<8, 8>();
      }
      sig(L0 + SYNC + SY_QD + wl * 4);
      STAMP(5);
      u32x4 pqf[2][2];
      float omg[2];
#pragma unroll
      for (int t = 0; t < 2; ++t) {
        omg[t] = 1.f - sigmoidf(row3_bcast(aq[t][0][1][0]));
#pragma unroll
        for (int h = 0; h < 2; ++h) {
          const u32x2 a = phi4(aq[t][h][0]), b = phi4(aq[t][h][1]);
          pqf[t][h] = (u32x4){a[0], a[1], g3 ? 0u : b[0], g3 ? 0u : b[1]};
        }
      }
      tagO = uniform64(*(LAS u64*)(uintptr_t)(L0 + SYNC + SY_TAG));
      unsigned ob = WS_O + ((rt * 8 + wl * 2) * 32 + 16 + hp * 2) * 1024 + lane * 16;
      unsigned kb = L0 + B_KVL + lane * 16, zb = L0 + B_KVL + 1024 + (g * 16 + 12) * 16;
      asm volatile("" : "+v"(pqf[0][0]), "+v"(pqf[0][1]), "+v"(pqf[1][0]), "+v"(pqf[1][1]), "+v"(omg[0]), "+v"(omg[1]), "+v"(ob),
                   "+v"(kb), "+v"(zb), "+s"(tagO));
      __builtin_amdgcn_s_setprio(2);
      STAMP(6);
      wait4_hot(L0 + SYNC + SY_KV);
      STAMP(8);
      {
        bf16x8 kv0[2], kv1[2], zf[2];
#pragma unroll
        for (int h = 0; h < 2; ++h) zf[h] = as_frag(lds_ld128(zb + h * 2048));
#pragma unroll
        for (int h = 0; h < 2; ++h) {
          kv0[h] = as_frag(lds_ld128(kb + h * 2048));
          kv1[h] = as_frag(lds_ld128(kb + h * 2048 + 1024));
        }
        SB();
        f32x4 dn[4], n0[4], n1[4];
#pragma unroll
        for (int i = 0; i < 4; ++i) dn[i] = mfma(zf[i >> 1], as_frag(pqf[i & 1][i >> 1]), (f32x4){kEps, kEps, kEps, kEps});
#pragma unroll
        for (int i = 0; i < 4; ++i) {
          n0[i] = mfma(kv0[i >> 1], as_frag(pqf[i & 1][i >> 1]), z4);
          n1[i] = mfma(kv1[i >> 1], as_frag(pqf[i & 1][i >> 1]), z4);
        }
        SB();
#pragma unroll
        for (int i = 0; i < 4; ++i) {
          const float s = omg[i & 1] * __builtin_amdgcn_rcpf(dn[i][0]);
          const u32x2 a = pk4(n0[i] * s), b = pk4(n1[i] * s);
          __builtin_amdgcn_raw_buffer_store_b128((u32x4){a[0], a[1], g3 ? 0u : b[0], b[1]}, wsr, ob + ((i & 1) * 32 + (i >> 1)) * 1024, 0, 16);
        }
      }
      STAMP(9);
      VM_WAIT(0);
      if (lane == 0) __hip_atomic_store((gu64*)(ws + WS_FB) + ((rt * 4 + wl) * 8 + hp) * FS, tagO, RLX_AGENT);
      STAMP(10); STAMPC(19);
      if (cg >= 7) { STAMP_FLUSH(); return; }
    }
  } else if (__builtin_expect(br, 1)) {
      __builtin_amdgcn_s_setprio(2);
      setup(B_G);
      f32x4 ak[4][2], av[4][2];
      {
        const float one = li == 12 ? 1.f : 0.f, i0 = rh ? one : 0.f, i1 = rh ? 0.f : one;
#pragma unroll
        for (int t = 0; t < 4; ++t) {
          ak[t][0] = ak[t][1] = (f32x4){kEps, kEps, kEps, kEps};
          av[t][0] = (f32x4){i0, i0, i0, i0};
          av[t][1] = (f32x4){i1, i1, i1, i1};
        }
      }
      bf16x8 xf[2][4], fw[2][4];
      unsigned xb, wb;
      const bool z0 = p == 3 && rh, z1 = p == 3 && !rh;
      const unsigned zaddr = L0 + SYNC + SY_ZERO;
      auto rd = [&](int ks, int part) {
        const unsigned xa = xb ^ (ks * 64), wk = wb + ks * 3584;
        if (part & 1) {
          const unsigned a = wk + WM_B + (((part >> 1) ^ rh) << 5);
          const bool z = (part >> 1) ? z1 : z0;
          fw[ks][part] = lds_tr2(z ? zaddr : a, z ? zaddr : a + 112);
        } else {
          fw[ks][part] = lds_tr2(wk + (part >> 1) * 32, wk + (part >> 1) * 32 + 112);
        }
        if (part < 2) {
          xf[ks][part * 2] = as_frag(lds_ld128(xa + part * 4096));
          xf[ks][part * 2 + 1] = as_frag(lds_ld128(xa + part * 4096 + 2048));
        }
      };
      auto mm = [&](int ks, int part) {
#pragma unroll
        for (int t = 0; t < 4; ++t) {
          if (part & 1) av[t][part >> 1] = mfma(xf[ks][t], fw[ks][part], av[t][part >> 1]);
          else ak[t][part >> 1] = mfma(xf[ks][t], fw[ks][part], ak[t][part >> 1]);
        }
      };
#pragma unroll
      for (int c = 0; c < 7; ++c) {
        xb = opaque(L0 + b_slot(c) + rh * 8192 + xsw);
        wb = opaque(L0 + b_slot(c) + B_W + wln);
        hook(c, true, true);
        LDS_BAR();
        hook(c, false, true);
        if (c == 0) { STAMP(2); STAMPC(16); }
        if (c == 3) STAMP(3);
        if (c == 6) { STAMP(4); STAMPC(17); }
        SB();
#pragma unroll
        for (int ks = 0; ks < 2; ++ks) {
#pragma unroll
          for (int part = 0; part < 4; ++part) rd(ks, part);
          if (ks == 1 || c > 0) {
#pragma unroll
            for (int part = 0; part < 4; ++part) mm(ks ^ 1, part);
            interleave<16, 12>();
          }
          SB();
        }
      }
      sig(L0 + SYNC + SY_D6 + w * 4);
      mm(1, 0); mm(1, 2);
      SB();
      mm(1, 3); mm(1, 1);
      STAMP(5);
      f32x4 kv[2][2];
      {
        u32x2 pk[4][2], vf[4][2];
#pragma unroll
        for (int t = 0; t < 4; ++t) {
          pk[t][0] = phi4(ak[t][0]);
          pk[t][1] = phi4(ak[t][1]);
          vf[t][0] = pk4(av[t][0]);
          vf[t][1] = pk4(av[t][1]);
        }
#pragma unroll
        for (int e = 1; e >= 0; --e)
#pragma unroll
          for (int d = 0; d < 2; ++d) {
            kv[e][d] = mfma(frag2(pk[0][d], pk[1][d]), frag2(vf[0][e], vf[1][e]), z4);
            kv[e][d] = mfma(frag2(pk[2][d], pk[3][d]), frag2(vf[2][e], vf[3][e]), kv[e][d]);
          }
      }
      lds_st128(L0 + B_EX + w * 2048 + lane * 16, __builtin_bit_cast(u32x4, kv[1][0]));
      lds_st128(L0 + B_EX + w * 2048 + 1024 + lane * 16, __builtin_bit_cast(u32x4, kv[1][1]));
      sig(L0 + SYNC + SY_EX + w * 4);
      wait1(L0 + SYNC + SY_EX + (w ^ 1) * 4);
      const f32x4 s0 = kv[0][0] + __builtin_bit_cast(f32x4, lds_ld128(L0 + B_EX + (w ^ 1) * 2048 + lane * 16));
      const f32x4 s1 = kv[0][1] + __builtin_bit_cast(f32x4, lds_ld128(L0 + B_EX + (w ^ 1) * 2048 + 1024 + lane * 16));
      {
        const u32x2 a = pk4(s0), b = pk4(s1);
        publish((u32x4){a[0], a[1], b[0], b[1]}, wsr, lane * 16 + kvs + rt * 1024);
      }
      STAMP(6);
      if (!__all(clr == tagO)) poll_flags(clrp(), tagO);
      wo_run(0, QWO);
      STAMP(7);
      f32x4 sum0, sum1;
      {
        const unsigned e0 = q == g ? ((p & 1) ? 0x3f800000u : 0x3f80u) : 0u;
        const unsigned ea = p < 2 ? e0 : 0u, eb = p < 2 ? 0u : e0;
        const bf16x8 sel0 = as_frag((u32x4){ea, eb, 0u, 0u}), sel1 = as_frag((u32x4){0u, 0u, ea, eb});
        unsigned vo = kvs + lane * 16;
#pragma nounroll
        for (unsigned spins = 0; spins < SPIN_CAP; ++spins) {
          asm volatile("" : "+v"(vo)::"memory");
          u32x4 pr[16];
#pragma unroll
          for (int r = 0; r < 16; ++r) pr[r] = __builtin_amdgcn_raw_buffer_load_b128(wsr, vo, ((r + 2 * rt) & 15) * 1024, 16);
          SB();
          sum0 = sum1 = z4;
#pragma unroll
          for (int r = 0; r < 16; ++r) {
            sum0 = mfma(sel0, as_frag(pr[r]), sum0);
            sum1 = mfma(sel1, as_frag(pr[r]), sum1);
          }
          if (!__any(sum0[0] != sum0[0])) break;
          unsigned m = 0;
#pragma unroll
          for (int r = 0; r < 16; ++r) m = max(max(max(max(m, pr[r][0]), pr[r][1]), pr[r][2]), pr[r][3]);
          if (!__any(m == SENT)) break;
        }
      }
      STAMP(8);
      lds_st128(L0 + B_KVL + w * 1024 + lane * 16, __builtin_bit_cast(u32x4, frag2(pk4(sum0), pk4(sum1))));
      sig(L0 + SYNC + SY_KV + w * 4);
      STAMP(10); STAMPC(19);
      __builtin_amdgcn_s_setprio(0);
      if (cg >= 7) { STAMP_FLUSH(); return; }
      sig(L0 + SYNC + SY_WOA + w * 4);
      wait4(L0 + SYNC + SY_D6);
      wait4(L0 + SYNC + SY_QD);
      wo_run(QWO, 28);
  } else {
      asm volatile("; role A");
      setup(A_G);
      f32x4 aq[4][2], ak[5][2], av[5][2];
#pragma unroll
      for (int t = 0; t < 5; ++t)
#pragma unroll
        for (int f = 0; f < 2; ++f) { ak[t][f] = av[t][f] = z4; if (t < 4) aq[t][f] = z4; }
      const unsigned gl = L0 + A_G + (8 * g + q) * 8;
      const bool p3 = p == 3;
      const unsigned d1 = p3 ? 32u : 112u;
      bf16x8 xf[2][5], fq[2][2], fk[2][2], fv[2][2];
      unsigned xb, wb;
      auto rd = [&](int c, int ks, int part) {
        const unsigned xa = xb ^ (ks * 64), wk = wb + ks * 3584;
        if (part == 0) {
          fk[ks][0] = lds_tr2(wk + WM_B, wk + WM_B + 112);
          xf[ks][0] = as_frag(lds_ld128(xa));
          xf[ks][1] = as_frag(lds_ld128(xa + 2048));
        } else if (part == 1) {
          fv[ks][0] = lds_tr2(wk + 2 * WM_B, wk + 2 * WM_B + 112);
          xf[ks][2] = as_frag(lds_ld128(xa + 4096));
        } else if (part == 2) {
          fq[ks][0] = lds_tr2(wk, wk + 112);
          xf[ks][3] = as_frag(lds_ld128(xa + 6144));
        } else if (part == 3) {
          fk[ks][1] = lds_tr2(wk + WM_B + 32, wk + WM_B + 32 + 112);
          xf[ks][4] = as_frag(lds_ld128(xa + 8192));
        } else if (part == 4) {
          fv[ks][1] = lds_tr2(wk + 2 * WM_B + 32, wk + 2 * WM_B + 32 + 112);
        } else {
          const unsigned a = p3 ? gl + c * 512 + ks * 256 : wk + 32;
          fq[ks][1] = lds_tr2(a, a + d1);
        }
      };
      auto mm = [&](int ks, int part) {
        const int f = part / 3, m = part % 3;
#pragma unroll
        for (int t = 0; t < 5; ++t) {
          if (m == 0) ak[t][f] = mfma(fk[ks][f], xf[ks][t], ak[t][f]);
          if (m == 1) av[t][f] = mfma(xf[ks][t], fv[ks][f], av[t][f]);
          if (m == 2 && t > 0) aq[t - 1][f] = mfma(fq[ks][f], xf[ks][t], aq[t - 1][f]);
        }
      };
#pragma unroll
      for (int c = 0; c < 7; ++c) {
        xb = opaque(L0 + a_slot(c) + rh * 8192 + xsw);
        wb = opaque(L0 + a_slot(c) + A_W + wln);
        hook(c, true, false);
        LDS_BAR();
        hook(c, false, false);
        if (c == 0) { STAMP(2); STAMPC(16); }
        if (c == 3) STAMP(3);
        if (c == 6) { STAMP(4); STAMPC(17); }
        SB();
#pragma unroll
        for (int ks = 0; ks < 2; ++ks) {
#pragma unroll
          for (int part = 0; part < 6; ++part) rd(c, ks, part);
          if (ks == 1 || c > 0) {
#pragma unroll
            for (int part = 0; part < 6; ++part) mm(ks ^ 1, part);
            interleave<28, 17>();
          }
          SB();
        }
      }
      sig(L0 + SYNC + SY_D6 + w * 4);
      mm(1, 0); mm(1, 3); mm(1, 2); mm(1, 5);
      SB();
      mm(1, 1); mm(1, 4);
      STAMP(5);
      const bool odd = li & 1;
      const int kk0 = 4 * g + (li & 1);
      const bool own0 = kk0 <= li, own1 = kk0 + 2 <= li;
      const float sc = 0.18898223650461362f * kLog2e;
      const float ninf = (rt == 0 && rh == 0) ? -__builtin_inff() : 0.f;
      const unsigned l16 = lane_id() * 16;
      bf16x8 kf[5];
#pragma unroll
      for (int t = 0; t < 5; ++t) kf[t] = frag2(pk4(ak[t][0]), pk4(ak[t][1]));
      f32x4 sp[4], so[4];
#pragma unroll
      for (int t = 0; t < 4; ++t) {
        const u32x2 b = pk4(aq[t][1]);
        const bf16x8 qf = frag2(pk4(aq[t][0]), (u32x2){g3 ? 0u : b[0], g3 ? 0u : b[1]});
        sp[t] = mfma(kf[t], qf, t == 0 ? (f32x4){ninf, ninf, ninf, ninf} : z4);
        so[t] = mfma(kf[t + 1], qf, z4);
      }
      float ug;
      {
        float z0 = aq[0][1][0], z1 = aq[1][1][0], z2 = aq[2][1][0], z3 = aq[3][1][0];
        swap32(z0, z1);
        swap32(z2, z3);
        swap16(z1, z3);
        ug = 1.f + __builtin_amdgcn_exp2f(-kLog2e * z3);
      }
      u32x2 vf[5][2];
#pragma unroll
      for (int t = 0; t < 5; ++t) {
        vf[t][0] = pk4(av[t][0]);
        vf[t][1] = pk4(av[t][1]);
      }
      float l0[4], l1[4], bc[4];
#pragma unroll
      for (int t = 0; t < 4; ++t) {
        float a0 = so[t][0], a2 = so[t][2], b0 = sp[t][0], b2 = sp[t][2];
        const float c0 = own0 ? a0 : b0, c1 = own0 ? so[t][1] : sp[t][1], c2 = own1 ? a2 : b2, c3 = own1 ? so[t][3] : sp[t][3];
        l0[t] = odd ? c1 : c0;
        l1[t] = odd ? c3 : c2;
      }
      rows_bcast4(rows_reduce4<true>(fmax1(l0[0], l1[0]), fmax1(l0[1], l1[1]), fmax1(l0[2], l1[2]), fmax1(l0[3], l1[3])) * -sc, bc);
#pragma unroll
      for (int t = 0; t < 4; ++t) {
        l0[t] = __builtin_amdgcn_exp2f(__builtin_fmaf(l0[t], sc, bc[t]));
        l1[t] = __builtin_amdgcn_exp2f(__builtin_fmaf(l1[t], sc, bc[t]));
      }
      rows_bcast4(__builtin_amdgcn_rcpf(rows_reduce4<false>(l0[0] + l1[0], l0[1] + l1[1], l0[2] + l1[2], l0[3] + l1[3]) * ug), bc);
      const unsigned sh = odd ? 16u : 0u;
      const unsigned mk0 = own0 ? 0u : 0xffffu, mk1 = own1 ? 0u : 0xffffu;
#pragma unroll
      for (int t = 0; t < 4; ++t) {
        const unsigned pe = pk2(l0[t] * bc[t], l1[t] * bc[t]), ph = pe >> 16;
        const bf16x8 pf = as_frag((u32x4){(pe & mk0) << sh, (ph & mk1) << sh, (pe & (mk0 ^ 0xffffu)) << sh, (ph & (mk1 ^ 0xffffu)) << sh});
        const f32x4 r0 = mfma(frag2(vf[t][0], vf[t + 1][0]), pf, z4);
        const f32x4 r1 = mfma(frag2(vf[t][1], vf[t + 1][1]), pf, z4);
        const u32x2 c0 = pk4(r0), c1 = pk4(r1);
        __builtin_amdgcn_raw_buffer_store_b128((u32x4){c0[0], c0[1], g3 ? 0u : c1[0], g3 ? 0u : c1[1]}, wsr, l16 + obs + t * 32768, 0, 16);
      }
      STAMP(9);
      VM_WAIT(0);
      if (l16 == 0) __hip_atomic_store((gu64*)(ws + WS_FA) + ((rt * 2 + rh) * 16 + hp * 2 + hh) * FS, tagO, RLX_AGENT);
      STAMP(10); STAMPC(19);
      if (cg >= 7) {
        if (bid == 56 && w == 0) {
#pragma nounroll
          for (int k = 0; k < 4; ++k) {
            const int i = (k & 1) * 64 + lane;
            poll_flags(k < 2 ? (gu64*)(ws + WS_CLR) + i : (gu64*)(ws + WS_FA) + ((i >> 3) * 32 + (i & 7) * 2) * FS, tagO);
          }
          if (lane == 0) __hip_atomic_store((gu64*)(ws + WS_E), tagO, RLX_AGENT);
        }
        STAMP_FLUSH();
        return;
      }
  }

  const int pb = w >> 2;
  const gu64* fp = pb ? (gu64*)(ws + WS_FB) + ((rg * 2 + (wl >> 1)) * 8 + (li & 7)) * FS : (gu64*)(ws + WS_FA) + (rg * 16 + li) * FS;
  const u64 f0 = __hip_atomic_load(fp, RLX_AGENT);
  wait4(L0 + SYNC + (pb ? SY_WOB : SY_WOA));
  const unsigned sw = (2 * g + (q >> 1)) & 3;
  const unsigned rb = L0 + (pb * 448 + 4 * g + q) * 128 + 8 * p;
  unsigned wa[4], wa2[4];
#pragma unroll
  for (int v = 0; v < 4; ++v) {
    wa[v] = opaque(rb + ((v ^ sw) << 5));
    wa2[v] = opaque(wa[v] + (g3 ? 0u : 2048u));
  }
  auto wfrag = [&](int h, int ct) {
    const int v = ct ^ (2 * (h & 1));
    return lds_tr2(wa[v] + h * 3584, wa2[v] + h * 3584);
  };
  constexpr int NPRE = 8;
  bf16x8 wf[16][4];
#pragma unroll
  for (int h = 0; h < NPRE; ++h)
#pragma unroll
    for (int ct = 0; ct < 4; ++ct) wf[h][ct] = wfrag(h, ct);
  STAMP(11);
  const unsigned ub = WS_O + ((rg * 4 + wl) * 32 + pb * 16) * 1024;
  tagO = uniform64(*(LAS u64*)(uintptr_t)(L0 + SYNC + SY_TAG));
  if (br && !pb) { VM_WAIT(0); sig(L0 + SYNC + SY_WOB + wl * 4); }
  if (!__all(f0 == tagO)) poll_flags(fp, tagO);
  asm volatile("" ::"v"(gv), "v"(epoch), "v"(pt));
  STAMP(12);
  u32x4 of[16];
  {
    unsigned vo = ub + lane * 16;
    asm volatile("" : "+v"(vo)::"memory");
#pragma unroll
    for (int h = 0; h < 16; ++h) of[h] = __builtin_amdgcn_raw_buffer_load_b128(wsr, vo, ((h + 2 * cg) & 15) * 1024, 16);
    SB();
  }
  f32x4 acc[4] = {z4, z4, z4, z4};
#pragma unroll
  for (int h = 0; h < 16; ++h) {
#pragma unroll
    for (int ct = 0; ct < 4; ++ct) acc[ct] = mfma(wf[h][ct], as_frag(of[h]), acc[ct]);
    SB();
    if (h + NPRE < 16) {
#pragma unroll
      for (int ct = 0; ct < 4; ++ct) wf[h + NPRE][ct] = wfrag(h + NPRE, ct);
    }
    SB();
  }
  STAMP(13);
  const unsigned xa = L0 + P2X + wl * 4096 + lane * 16;
  if (!pb) {
    wait4(L0 + SYNC + SY_D6);
#pragma unroll
    for (int ct = 0; ct < 4; ++ct) lds_st128(xa + ct * 1024, __builtin_bit_cast(u32x4, acc[ct]));
    sig(L0 + SYNC + SY_P2 + wl * 4);
  } else {
    f32x4 pa[4];
    for (;;) {
      const unsigned f = *(volatile LAS unsigned*)(uintptr_t)(L0 + SYNC + SY_P2 + wl * 4);
#pragma unroll
      for (int ct = 0; ct < 4; ++ct) pa[ct] = *(volatile LAS f32x4*)(uintptr_t)(xa + ct * 1024);
      if (__builtin_amdgcn_readfirstlane(f)) break;
      __builtin_amdgcn_s_sleep(1);
    }
    char* orow = out + (rg * 64 + wl * 16 + li) * XROW + cg * 128 + (g & 1) * 32 + (g >> 1) * 16;
#pragma unroll
    for (int k = 0; k < 2; ++k) {
      const u32x2 a = pk4(acc[2 * k] + pa[2 * k]), b = pk4(acc[2 * k + 1] + pa[2 * k + 1]);
      const auto r0 = __builtin_amdgcn_permlane16_swap(a[0], b[0], false, false);
      const auto r1 = __builtin_amdgcn_permlane16_swap(a[1], b[1], false, false);
      *(u32x4*)(orow + k * 64) = (u32x4){r0[0], r1[0], r0[1], r1[1]};
    }
  }
  STAMP(14);
  STAMP(15);
  STAMP_FLUSH();
}

}

extern "C" void kernel_launch(void* const* d_in, const int* in_sizes, int n_in, void* d_out, int out_size, void* d_ws,
                              size_t ws_size, hipStream_t stream) {
  (void)in_sizes; (void)n_in; (void)out_size; (void)ws_size;
  gated_mix_kernel<<<256, 512, 0, stream>>>((const char*)d_in[0], (const char*)d_in[1], (const char*)d_in[2],
                                            (const char*)d_in[4], (const char*)d_in[5],
                                            (const unsigned short*)d_in[6], (char*)d_out, (char*)d_ws);
}
```

```cpp
#include <hip/hip_runtime.h>
#include <stdint.h>

typedef __bf16 bf16x2 __attribute__((ext_vector_type(2)));
typedef __bf16 bf16x4 __attribute__((ext_vector_type(4)));
typedef __bf16 bf16x8 __attribute__((ext_vector_type(8)));
typedef float f32x2 __attribute__((ext_vector_type(2)));
typedef float f32x4 __attribute__((ext_vector_type(4)));
typedef unsigned u32x2 __attribute__((ext_vector_type(2)));
typedef unsigned u32x4 __attribute__((ext_vector_type(4)));
typedef unsigned long long u64;
#define LAS __attribute__((address_space(3)))
#define GAS __attribute__((address_space(1)))
typedef GAS u64 gu64;
#define RLX_AGENT __ATOMIC_RELAXED, __HIP_MEMORY_SCOPE_AGENT

namespace {

constexpr int kS = 2048, kD = 448;
constexpr int XROW = kD * 2;
constexpr int WROW = 3 * kD * 2;
constexpr int WM_B = 64 * 112;

constexpr int A_FRONT = 34816, A_SLOT = 144 * 128 + 3 * WM_B, A_W = 144 * 128;
constexpr int A_G = A_FRONT + 3 * A_SLOT;
constexpr int B_SLOT = 128 * 128 + 2 * WM_B, B_W = 128 * 128;
constexpr int B_WQ = 97536, B_G = B_WQ + 7 * WM_B, B_EX = B_G + 3584, B_KVL = B_EX + 8192;
constexpr int P2X = 114688;
constexpr int SYNC = 163584;
constexpr int LDS_B = 163840;
static_assert(A_G + 3584 <= SYNC && B_KVL + 4096 <= SYNC && A_FRONT + 2 * A_SLOT == 114688, "lds map");
__host__ __device__ constexpr int a_slot(int c) { return A_FRONT + ((c + 2) % 3) * A_SLOT; }
__host__ __device__ constexpr int b_slot(int c) { return ((c + 2) % 3) * B_SLOT; }
enum { SY_WOA = 0, SY_WOB = 16, SY_D6 = 32, SY_D6Q = 48, SY_WQ = 64, SY_QD = 80, SY_EX = 96, SY_KV = 112, SY_P2 = 128, SY_ZERO = 192, SY_TAG = 240 };

constexpr int WS_E = 0;
constexpr int WS_CLR = 4096;
constexpr int FS = 16;
constexpr int WS_FA = 8192;
constexpr int WS_FB = WS_FA + 16 * 2 * 16 * 8 * FS;
constexpr int WS_KVP = WS_FB + 16 * 4 * 8 * 8 * FS;
constexpr int WS_O = WS_KVP + 8 * 4 * 16 * 1024;
constexpr int WS_END = WS_O + 16 * 8 * 32 * 1024;

constexpr int PT_WS = (WS_END / 8) & ~127;
constexpr unsigned SPIN_CAP = 1u << 14;
constexpr unsigned SENT = 0xffffffffu;
#define QWO 14

#define STAMP(i) do {} while (0)
#define STAMPC(i) do {} while (0)
#define STAMP_FLUSH() do {} while (0)

__device__ __forceinline__ unsigned pk2(float a, float b) {
  f32x2 v = {a, b};
  bf16x2 r = __builtin_convertvector(v, bf16x2);
  return __builtin_bit_cast(unsigned, r);
}
__device__ __forceinline__ u32x2 pk4(f32x4 v) { return (u32x2){pk2(v[0], v[1]), pk2(v[2], v[3])}; }
__device__ __forceinline__ bf16x8 as_frag(u32x4 v) { return __builtin_bit_cast(bf16x8, v); }
__device__ __forceinline__ bf16x8 frag2(u32x2 a, u32x2 b) { return as_frag((u32x4){a[0], a[1], b[0], b[1]}); }
__device__ __forceinline__ f32x4 mfma(bf16x8 a, bf16x8 b, f32x4 c) {
  return __builtin_amdgcn_mfma_f32_16x16x32_bf16(a, b, c, 0, 0, 0);
}
__device__ __forceinline__ bf16x8 lds_tr2(unsigned a0, unsigned a1) {
  bf16x4 lo = __builtin_amdgcn_ds_read_tr16_b64_v4bf16((LAS bf16x4*)(uintptr_t)a0);
  bf16x4 hi = __builtin_amdgcn_ds_read_tr16_b64_v4bf16((LAS bf16x4*)(uintptr_t)a1);
  return __builtin_shufflevector(lo, hi, 0, 1, 2, 3, 4, 5, 6, 7);
}
__device__ __forceinline__ u32x4 lds_ld128(unsigned a) { return *(LAS u32x4*)(uintptr_t)a; }
__device__ __forceinline__ void lds_st128(unsigned a, u32x4 v) { *(LAS u32x4*)(uintptr_t)a = v; }
__device__ __forceinline__ void lds_st64(unsigned a, u32x2 v) { *(LAS u32x2*)(uintptr_t)a = v; }
__device__ __forceinline__ unsigned opaque(unsigned v) { asm volatile("" : "+v"(v)); return v; }

__device__ __forceinline__ void sig(unsigned a) {
  asm volatile("" ::: "memory");
  *(volatile LAS unsigned*)(uintptr_t)a = 1u;
  asm volatile("" ::: "memory");
  __builtin_amdgcn_sched_barrier(0);
}
__device__ __forceinline__ void wait1(unsigned a) {
  while (__builtin_amdgcn_readfirstlane(*(volatile LAS unsigned*)(uintptr_t)a) == 0u) __builtin_amdgcn_s_sleep(1);
  asm volatile("" ::: "memory");
}
__device__ __forceinline__ void wait4(unsigned a) {
  for (;;) {
    const u32x4 v = *(volatile LAS u32x4*)(uintptr_t)a;
    if (__builtin_amdgcn_readfirstlane(v[0] & v[1] & v[2] & v[3])) break;
    __builtin_amdgcn_s_sleep(1);
  }
  asm volatile("" ::: "memory");
}

__device__ __forceinline__ u32x4 make_srd(const void* p, unsigned bytes) {
  uint64_t a = (uint64_t)p;
  u32x4 r;
  r[0] = __builtin_amdgcn_readfirstlane((unsigned)a);
  r[1] = __builtin_amdgcn_readfirstlane((unsigned)(a >> 32) & 0xffffu);
  r[2] = bytes;
  r[3] = 0x00020000u;
  return r;
}
__device__ __forceinline__ void dma16(u32x4 srd, unsigned voff, unsigned soff, unsigned ldsaddr) {
  asm volatile(
      "s_mov_b32 m0, %3\n\t"
      "s_nop 3\n\t"
      "buffer_load_dwordx4 %0, %1, %2 offen lds"
      :
      : "v"(voff), "s"(srd), "s"(soff), "s"(ldsaddr));
}
#define VM_BAR(N) asm volatile("s_waitcnt vmcnt(" #N ") lgkmcnt(0)\n\ts_barrier" ::: "memory")
#define LDS_BAR() asm volatile("s_waitcnt lgkmcnt(0)\n\ts_barrier" ::: "memory")
#define VM_WAIT(N) asm volatile("s_waitcnt vmcnt(" #N ")" ::: "memory")
template <int N> __device__ __forceinline__ void vm_bar() { asm volatile("s_waitcnt vmcnt(%0) lgkmcnt(0)\n\ts_barrier" ::"n"(N) : "memory"); }
#define SB() __builtin_amdgcn_sched_barrier(0)

__device__ __forceinline__ float fmax1(float a, float b) { return __builtin_amdgcn_fmed3f(a, b, 3.0e38f); }
__device__ __forceinline__ void swap16(float& a, float& b) {
  auto r = __builtin_amdgcn_permlane16_swap(__float_as_uint(a), __float_as_uint(b), false, false);
  const unsigned r0 = r[0], r1 = r[1];
  a = __uint_as_float(r0);
  b = __uint_as_float(r1);
}
__device__ __forceinline__ void swap32(float& a, float& b) {
  auto r = __builtin_amdgcn_permlane32_swap(__float_as_uint(a), __float_as_uint(b), false, false);
  const unsigned r0 = r[0], r1 = r[1];
  a = __uint_as_float(r0);
  b = __uint_as_float(r1);
}
template <bool MAX>
__device__ __forceinline__ float rows_reduce4(float v0, float v1, float v2, float v3) {
  swap32(v0, v1);
  swap32(v2, v3);
  float a = MAX ? fmax1(v0, v1) : v0 + v1, b = MAX ? fmax1(v2, v3) : v2 + v3;
  swap16(a, b);
  return MAX ? fmax1(a, b) : a + b;
}
__device__ __forceinline__ void rows_bcast4(float x, float (&o)[4]) {
  float y = x;
  swap16(x, y);
  o[0] = x; o[1] = x; o[2] = y; o[3] = y;
  swap32(o[0], o[1]);
  swap32(o[2], o[3]);
}
__device__ __forceinline__ float row3_bcast(float v) {
  float a = v;
  swap32(a, v);
  a = v;
  swap16(a, v);
  return v;
}
template <int NM, int ND>
__device__ __forceinline__ void interleave() {
#pragma unroll
  for (int i = 0; i < NM; ++i) {
    __builtin_amdgcn_sched_group_barrier(0x8, 1, 0);
    if (i < ND) {
      __builtin_amdgcn_sched_group_barrier(0x2, 1, 0);
      __builtin_amdgcn_sched_group_barrier(0x100, 1, 0);
    }
  }
}
constexpr float kEps = 1e-6f, kLog2e = 1.44269504088896f;
__device__ __forceinline__ float phi(float u) {
  const float e = __builtin_amdgcn_fmed3f(__builtin_amdgcn_exp2f(__builtin_fmaf(u, kLog2e, -kEps * kLog2e)), 0.f, 1.f);
  return fmax1(u, kEps) + e;
}
__device__ __forceinline__ u32x2 phi4(f32x4 v) { return (u32x2){pk2(phi(v[0]), phi(v[1])), pk2(phi(v[2]), phi(v[3]))}; }
__device__ __forceinline__ float sigmoidf(float z) {
  return __builtin_amdgcn_rcpf(1.f + __builtin_amdgcn_exp2f(-1.44269504088896f * z));
}
__device__ __forceinline__ u64 next_tag(u64 e) {
  const u64 s = ((u64)(unsigned)__builtin_amdgcn_readfirstlane((unsigned)(e >> 32)) << 32) |
                (unsigned)__builtin_amdgcn_readfirstlane((unsigned)e);
  return s * 6364136223846793005ull + 1442695040888963407ull;
}
__device__ __forceinline__ u64 uniform64(u64 e) {
  return ((u64)(unsigned)__builtin_amdgcn_readfirstlane((unsigned)(e >> 32)) << 32) |
         (unsigned)__builtin_amdgcn_readfirstlane((unsigned)e);
}
__device__ __forceinline__ void poll_flags(const gu64* p, u64 tag) {
#pragma nounroll
  for (unsigned spins = 0; spins < SPIN_CAP; ++spins) {
    u64 v = __hip_atomic_load(p, RLX_AGENT);
    if (__all(v == tag)) break;
    __builtin_amdgcn_s_sleep(2);
  }
  asm volatile("" ::: "memory");
}
__device__ __forceinline__ void publish(u32x4 v, __amdgpu_buffer_rsrc_t r, unsigned voff) {
#pragma unroll
  for (int i = 0; i < 4; ++i) v[i] = v[i] < SENT - 1u ? v[i] : SENT - 1u;
  __builtin_amdgcn_raw_buffer_store_b128(v, r, voff, 0, 16);
}
__device__ __forceinline__ unsigned lane_id() {
  unsigned l = __builtin_amdgcn_mbcnt_hi(~0u, __builtin_amdgcn_mbcnt_lo(~0u, 0u));
  asm volatile("" : "+v"(l));
  return l;
}
__device__ __forceinline__ int wrap7(int c) { return c >= 7 ? c - 7 : c; }

__global__ __launch_bounds__(512, 2) void gated_mix_kernel(const char* __restrict__ x, const char* __restrict__ wqkvA,
                                                           const char* __restrict__ woA, const char* __restrict__ wqkvB,
                                                           const char* __restrict__ woB,
                                                           const unsigned short* __restrict__ gate_w, char* __restrict__ out,
                                                           char* ws) {
  __shared__ __attribute__((aligned(1024))) char lds[LDS_B];
  const unsigned L0 = (unsigned)(uintptr_t)(LAS char*)lds;
  const int bid = blockIdx.x;
  const int xcd = bid & 7, idx = bid >> 3;
  const int xj = xcd >> 1, ci = idx >> 3;
  const int br = ci >> 1;
  const int hp = 2 * (br ? (xj ^ 1) : xj) + (ci & 1);
  const int rt = (xcd & 1) * 8 + (idx & 7);
  const int rot = wrap7(wrap7((idx & 7) + 2 * ci + (xj & 1)));
  const int cg = idx & 7, rg = 4 * xcd + ci;

  const int tid = threadIdx.x;
  const int lane = tid & 63;
  const int w = __builtin_amdgcn_readfirstlane(tid >> 6) & 7;
  const int wl = w & 3;
  const int li = lane & 15, g = lane >> 4, q = (lane >> 2) & 3, p = lane & 3;
  const bool g3 = g == 3;
  const f32x4 z4 = {0.f, 0.f, 0.f, 0.f};
  STAMP(0); STAMPC(18);
  unsigned vx0 = (lane >> 3) * XROW + (((lane & 7) ^ (lane >> 4)) << 4);
  unsigned vx1 = vx0 ^ 64u;
  unsigned vw0, vw1;
  {
    const unsigned s0 = wl * 64 + lane, s1 = s0 + 256;
    const unsigned p0 = (s0 * 9363u) >> 16, p1 = (s1 * 9363u) >> 16;
    const unsigned k0 = (p0 & 56) | ((p0 >> 1) & 3) | ((p0 & 1) << 2);
    const unsigned k1 = (p1 & 56) | ((p1 >> 1) & 3) | ((p1 & 1) << 2);
    vw0 = k0 * WROW + hp * 112 + (s0 - p0 * 7) * 16;
    vw1 = k1 * WROW + hp * 112 + (s1 - p1 * 7) * 16;
  }
  asm volatile("" : "+v"(vx0), "+v"(vx1), "+v"(vw0), "+v"(vw1));
  asm volatile("" ::"s"(x), "s"(wqkvA), "s"(woA), "s"(wqkvB), "s"(woB), "s"(gate_w), "s"(out), "s"(ws));
  const __amdgpu_buffer_rsrc_t wsr = __builtin_amdgcn_make_buffer_rsrc(ws, 0, WS_END, 0x00020000);
  u64 tagO;
  auto wo_run = [&](int j0, int j1) {
    const unsigned vwo = (lane >> 3) * XROW + cg * 128 + (((lane & 7) ^ (((lane >> 4) & 3) << 1)) << 4);
    const u32x4 sa = make_srd(woA, kD * XROW), sb = make_srd(woB, kD * XROW);
    unsigned so = (wl + 7 * cg + 4 * (j0 % 14)) * (8 * XROW);
    so = so >= kD * XROW ? so - kD * XROW : so;
#pragma unroll
    for (int j = j0; j < j1; ++j) {
      dma16(j < 14 ? sa : sb, vwo, so, L0 + (wl + 4 * j) * 1024);
      so += 32 * XROW;
      so = so >= kD * XROW ? so - kD * XROW : so;
    }
  };
  const u32x4 srdX = make_srd(x, kS * XROW);
  const u32x4 srdW = make_srd(br ? wqkvB : wqkvA, kD * WROW);
  const bool l3 = wl == 3;
  const int xr = rt * 128 + wl * 32 - (br ? 0 : 16);
  const unsigned sx01 = (xr < 0 ? 0 : xr) * XROW, sx23 = (xr + 16) * XROW;
  const bool l3a = l3 && !br;
  const u32x4 srd2 = l3a ? srdX : srdW;
  const unsigned v2 = l3 ? vw0 : vw1;
  const unsigned v2a = l3a ? vx0 : v2, v2b = l3a ? vx1 : v2;
  const unsigned st2 = l3a ? 128u : 64u * WROW, s2a = l3a ? (rt * 128 + 112) * XROW : 0u;
  const unsigned s2d = l3a ? 8u * XROW : (unsigned)XROW, s2e = l3a ? 8u * XROW : 2u * XROW;
  const unsigned sub2 = (l3 ? wl : wl + 4) * 1024;
  auto chunk = [&](unsigned sl, unsigned lq, unsigned lk, int cc) {
    const unsigned sw = cc * (64 * WROW), sx = cc * 128, s2 = cc * st2 + s2a;
#pragma unroll
    for (int j = 0; j < 4; ++j)
      dma16(srdX, (j & 1) ? vx1 : vx0, (j < 2 ? sx01 : sx23) + (j & 1) * 8 * XROW + sx, sl + (wl * 4 + j) * 1024);
    dma16(srdW, vw0, sw + XROW, lk + wl * 1024);
    dma16(srdW, vw0, sw + 2 * XROW, lk + WM_B + wl * 1024);
    dma16(srd2, v2b, s2 + s2d, l3a ? sl + 17408 : lk + sub2);
    dma16(srd2, v2b, s2 + s2e, l3a ? sl + 17408 : lk + WM_B + sub2);
    if (!br) {
      dma16(srdW, vw0, sw, lq + wl * 1024);
      dma16(srd2, v2a, s2, l3a ? sl + 16384 : lq + sub2);
    }
  };
  unsigned gv = 0, pt = 0;
  u64 epoch = 0;
  {
    int wx = w;
    asm volatile("" : "+s"(wx));
    if (wx < 4) {
      gv = ((const unsigned*)gate_w)[tid < 224 ? tid : 0];
      epoch = __hip_atomic_load((gu64*)(ws + WS_E), RLX_AGENT);
      const char* pb_ = wx == 0 ? ws : (wx == 1 ? out + rg * 64 * XROW : (wx == 2 ? woA : woB));
      pt = __hip_atomic_load((GAS unsigned*)(pb_ + (lane & 7) * (wx == 0 ? PT_WS : (wx == 1 ? 7168 : 50176))), RLX_AGENT);
    } else {
#pragma nounroll
      for (int c = 0; c < 2; ++c) {
        const unsigned sl = L0 + (br ? (c ? b_slot(1) : b_slot(0)) : (c ? a_slot(1) : a_slot(0)));
        chunk(sl, sl + A_W, sl + (br ? B_W : A_W + WM_B), wrap7(rot + c));
      }
    }
  }

    const int rh = w & 1, hh = w >> 1;
    const unsigned obs = WS_O + ((rt * 8 + rh * 4) * 32 + hp * 2 + hh) * 1024;
    const unsigned kvs = WS_KVP + (hp * 4 + w) * 16 * 1024;
    auto setup = [&](int goff) {
      if (tid < 60) *(LAS unsigned*)(uintptr_t)(L0 + SYNC + tid * 4) = 0u;
      const unsigned lo = (gv & 0xffffu) * 0x10001u, hi = (gv >> 16) * 0x10001u;
      const int pos = tid - rot * 32;
      if (tid < 224) lds_st128(L0 + goff + (pos < 0 ? pos + 224 : pos) * 16, (u32x4){lo, lo, hi, hi});
    };
    u64 clr = 0;
    unsigned pw = 0;
    auto clrp = [&]() { return (const gu64*)(ws + WS_CLR) + hp * 16 + (lane_id() & 15); };
    auto hook = [&](int c, bool before, bool isb) {
      if (c == 0 && !before && isb)
        __builtin_amdgcn_raw_buffer_store_b128((u32x4){SENT, SENT, SENT, SENT}, wsr, lane_id() * 16 + kvs + rt * 1024, 0, 16);
      if (c == 3 && before) {
        asm volatile("" : "+v"(epoch) : "v"(pt));
        tagO = next_tag(epoch);
        *(LAS u64*)(uintptr_t)(L0 + SYNC + SY_TAG) = tagO;
        VM_WAIT(0);
      }
      if (c == 3 && !before && isb)
        __builtin_amdgcn_raw_buffer_store_b64((u32x2){(unsigned)tagO, (unsigned)(tagO >> 32)}, wsr,
                                              (lane_id() | w) == 0 ? WS_CLR + (hp * 16 + rt) * 8 : 0x7ffffff0u, 0, 16);
      if (c == 6 && !before && isb) clr = __hip_atomic_load(clrp(), RLX_AGENT);
      if (c == 6 && !before) {
        const unsigned l = lane_id(), u = (l >> 3) & 3;
        const unsigned o = isb ? WS_O + ((rt * 8 + w * 2 + (u >> 1)) * 32 + 16 + hp * 2 + (u & 1)) * 1024 : obs + u * 32768;
        pw = __builtin_amdgcn_raw_buffer_load_b32(wsr, o + (l & 7) * 128, 0, 16);
      }
    };
    const unsigned xsw = li * 128 + ((g ^ (li >> 1)) << 4);
    const unsigned wln = (8 * g + 2 * q) * 112 + 8 * p + 56 * hh;

  if (__builtin_expect(w >= 4, 1)) {
    if (!br) {
      int cc = wrap7(rot + 1);
      unsigned sl = L0 + a_slot(2);
#pragma nounroll
      for (int c = 2; c < 7; ++c) {
        vm_bar<10>();
        cc = wrap7(cc + 1);
        chunk(sl, sl + A_W, sl + A_W + WM_B, cc);
        sl = sl == L0 + a_slot(0) ? L0 + a_slot(1) : sl + A_SLOT;
      }
      vm_bar<10>();
      if (cg >= 7) { vm_bar<0>(); STAMP_FLUSH(); return; }
      wo_run(0, 10);
      vm_bar<10>();
      wo_run(10, 28);
      VM_WAIT(14);
      sig(L0 + SYNC + SY_WOA + wl * 4);
      VM_WAIT(0);
      asm volatile("" ::"v"(gv), "v"(epoch), "v"(pt));
      sig(L0 + SYNC + SY_WOB + wl * 4);
    } else {
      auto instr = [&](int n) {
        if (n < 56) {
          const int c = n >> 3, j = n & 7, cc = wrap7(c + rot);
          const unsigned sl = L0 + b_slot(c), sw = cc * (64 * WROW), sx = cc * 128;
          if (j < 4) dma16(srdX, (j & 1) ? vx1 : vx0, sx01 + j * 8 * XROW + sx, sl + (wl * 4 + j) * 1024);
          else dma16(srdW, j < 6 ? vw0 : v2, sw + ((j & 1) + 1) * XROW, sl + B_W + (j & 1) * WM_B + (j < 6 ? wl * 1024 : sub2));
        } else {
          const int c = (n - 56) >> 1;
          dma16(srdW, (n & 1) ? v2 : vw0, wrap7(c + rot) * (64 * WROW), L0 + B_WQ + c * WM_B + ((n & 1) ? sub2 : wl * 1024));
        }
      };
      STAMP(1);
      bf16x8 xq[14][2];
      const unsigned xl = L0 + (wl * 32 + li) * 128 + ((g ^ (li >> 1)) << 4);
#pragma unroll
      for (int c = 0; c < 7; ++c) {
        vm_bar<8>();
#pragma unroll
        for (int ks = 0; ks < 2; ++ks) {
          const unsigned xa = (xl ^ (ks * 64)) + b_slot(c);
          xq[c * 2 + ks][0] = as_frag(lds_ld128(xa));
          xq[c * 2 + ks][1] = as_frag(lds_ld128(xa + 2048));
        }
#pragma unroll
        for (int n = 8 * c + 16; n < (c < 6 ? 8 * c + 24 : 70); ++n) instr(n);
      }
      STAMP(4);
      VM_WAIT(0);
      asm volatile("" ::"v"(gv), "v"(epoch), "v"(pt));
      sig(L0 + SYNC + SY_WQ + wl * 4);
      wait4(L0 + SYNC + SY_WQ);
      wait4(L0 + SYNC + SY_EX);
      STAMP(3);
      f32x4 aq[2][2][2];
#pragma unroll
      for (int i = 0; i < 8; ++i) aq[i >> 2][(i >> 1) & 1][i & 1] = (f32x4){kEps, kEps, kEps, kEps};
      const unsigned wb = L0 + B_WQ + (8 * g + 2 * q) * 112 + 8 * p;
      const unsigned gl = L0 + B_G + (8 * g + q) * 8;
      const bool p3 = p == 3;
      const unsigned d1 = p3 ? 32u : 112u;
      const unsigned qa0 = opaque(p3 ? gl : wb + 32), qstep = opaque(p3 ? 256u : 3584u);
      bf16x8 fq[3][4];
      auto rdq = [&](int s) {
        const unsigned wk = wb + s * 3584;
        const unsigned a = qa0 + s * qstep;
        fq[s % 3][0] = lds_tr2(wk, wk + 112);
        fq[s % 3][1] = lds_tr2(a, a + d1);
        fq[s % 3][2] = lds_tr2(wk + 56, wk + 56 + 112);
        fq[s % 3][3] = lds_tr2(wk + 88, wk + 88 + 112);
      };
      rdq(0);
      rdq(1);
#pragma unroll
      for (int s = 0; s < 14; ++s) {
        SB();
        if (s + 2 < 14) rdq(s + 2);
#pragma unroll
        for (int i = 0; i < 4; ++i) {
          aq[0][i >> 1][i & 1] = mfma(fq[s % 3][i], xq[s][0], aq[0][i >> 1][i & 1]);
          aq[1][i >> 1][i & 1] = mfma(fq[s % 3][i], xq[s][1], aq[1][i >> 1][i & 1]);
        }
        interleave<8, 8>();
      }
      sig(L0 + SYNC + SY_QD + wl * 4);
      STAMP(5);
      u32x4 pqf[2][2];
      float omg[2];
#pragma unroll
      for (int t = 0; t < 2; ++t) {
        omg[t] = 1.f - sigmoidf(row3_bcast(aq[t][0][1][0]));
#pragma unroll
        for (int h = 0; h < 2; ++h) {
          const u32x2 a = phi4(aq[t][h][0]), b = phi4(aq[t][h][1]);
          pqf[t][h] = (u32x4){a[0], a[1], g3 ? 0u : b[0], g3 ? 0u : b[1]};
        }
      }
      tagO = uniform64(*(LAS u64*)(uintptr_t)(L0 + SYNC + SY_TAG));
      unsigned ob = WS_O + ((rt * 8 + wl * 2) * 32 + 16 + hp * 2) * 1024 + lane * 16;
      unsigned kb = L0 + B_KVL + lane * 16, zb = L0 + B_KVL + 1024 + (g * 16 + 12) * 16;
      asm volatile("" : "+v"(pqf[0][0]), "+v"(pqf[0][1]), "+v"(pqf[1][0]), "+v"(pqf[1][1]), "+v"(omg[0]), "+v"(omg[1]), "+v"(ob),
                   "+v"(kb), "+v"(zb), "+s"(tagO));
      __builtin_amdgcn_s_setprio(2);
      STAMP(6);
      wait4(L0 + SYNC + SY_KV);
      STAMP(8);
      {
        bf16x8 kv0[2], kv1[2], zf[2];
#pragma unroll
        for (int h = 0; h < 2; ++h) zf[h] = as_frag(lds_ld128(zb + h * 2048));
#pragma unroll
        for (int h = 0; h < 2; ++h) {
          kv0[h] = as_frag(lds_ld128(kb + h * 2048));
          kv1[h] = as_frag(lds_ld128(kb + h * 2048 + 1024));
        }
        SB();
        f32x4 dn[4], n0[4], n1[4];
#pragma unroll
        for (int i = 0; i < 4; ++i) dn[i] = mfma(zf[i >> 1], as_frag(pqf[i & 1][i >> 1]), (f32x4){kEps, kEps, kEps, kEps});
#pragma unroll
        for (int i = 0; i < 4; ++i) {
          n0[i] = mfma(kv0[i >> 1], as_frag(pqf[i & 1][i >> 1]), z4);
          n1[i] = mfma(kv1[i >> 1], as_frag(pqf[i & 1][i >> 1]), z4);
        }
        SB();
#pragma unroll
        for (int i = 0; i < 4; ++i) {
          const float s = omg[i & 1] * __builtin_amdgcn_rcpf(dn[i][0]);
          const u32x2 a = pk4(n0[i] * s), b = pk4(n1[i] * s);
          __builtin_amdgcn_raw_buffer_store_b128((u32x4){a[0], a[1], g3 ? 0u : b[0], b[1]}, wsr, ob + ((i & 1) * 32 + (i >> 1)) * 1024, 0, 16);
        }
      }
      STAMP(9);
      VM_WAIT(0);
      if (lane == 0) __hip_atomic_store((gu64*)(ws + WS_FB) + ((rt * 4 + wl) * 8 + hp) * FS, tagO, RLX_AGENT);
      STAMP(10); STAMPC(19);
      if (cg >= 7) { STAMP_FLUSH(); return; }
    }
  } else if (__builtin_expect(br, 1)) {
      __builtin_amdgcn_s_setprio(2);
      setup(B_G);
      f32x4 ak[4][2], av[4][2];
      {
        const float one = li == 12 ? 1.f : 0.f, i0 = rh ? one : 0.f, i1 = rh ? 0.f : one;
#pragma unroll
        for (int t = 0; t < 4; ++t) {
          ak[t][0] = ak[t][1] = (f32x4){kEps, kEps, kEps, kEps};
          av[t][0] = (f32x4){i0, i0, i0, i0};
          av[t][1] = (f32x4){i1, i1, i1, i1};
        }
      }
      bf16x8 xf[2][4], fw[2][4];
      unsigned xb, wb;
      const bool z0 = p == 3 && rh, z1 = p == 3 && !rh;
      const unsigned zaddr = L0 + SYNC + SY_ZERO;
      auto rd = [&](int ks, int part) {
        const unsigned xa = xb ^ (ks * 64), wk = wb + ks * 3584;
        if (part & 1) {
          const unsigned a = wk + WM_B + (((part >> 1) ^ rh) << 5);
          const bool z = (part >> 1) ? z1 : z0;
          fw[ks][part] = lds_tr2(z ? zaddr : a, z ? zaddr : a + 112);
        } else {
          fw[ks][part] = lds_tr2(wk + (part >> 1) * 32, wk + (part >> 1) * 32 + 112);
        }
        if (part < 2) {
          xf[ks][part * 2] = as_frag(lds_ld128(xa + part * 4096));
          xf[ks][part * 2 + 1] = as_frag(lds_ld128(xa + part * 4096 + 2048));
        }
      };
      auto mm = [&](int ks, int part) {
#pragma unroll
        for (int t = 0; t < 4; ++t) {
          if (part & 1) av[t][part >> 1] = mfma(xf[ks][t], fw[ks][part], av[t][part >> 1]);
          else ak[t][part >> 1] = mfma(xf[ks][t], fw[ks][part], ak[t][part >> 1]);
        }
      };
#pragma unroll
      for (int c = 0; c < 7; ++c) {
        xb = opaque(L0 + b_slot(c) + rh * 8192 + xsw);
        wb = opaque(L0 + b_slot(c) + B_W + wln);
        hook(c, true, true);
        LDS_BAR();
        hook(c, false, true);
        if (c == 0) { STAMP(2); STAMPC(16); }
        if (c == 3) STAMP(3);
        if (c == 6) { STAMP(4); STAMPC(17); }
        SB();
#pragma unroll
        for (int ks = 0; ks < 2; ++ks) {
#pragma unroll
          for (int part = 0; part < 4; ++part) rd(ks, part);
          if (ks == 1 || c > 0) {
#pragma unroll
            for (int part = 0; part < 4; ++part) mm(ks ^ 1, part);
            interleave<16, 12>();
          }
          SB();
        }
      }
      sig(L0 + SYNC + SY_D6 + w * 4);
      mm(1, 0); mm(1, 2);
      SB();
      mm(1, 3); mm(1, 1);
      STAMP(5);
      f32x4 kv[2][2];
      {
        u32x2 pk[4][2], vf[4][2];
#pragma unroll
        for (int t = 0; t < 4; ++t) {
          pk[t][0] = phi4(ak[t][0]);
          pk[t][1] = phi4(ak[t][1]);
          vf[t][0] = pk4(av[t][0]);
          vf[t][1] = pk4(av[t][1]);
        }
#pragma unroll
        for (int e = 1; e >= 0; --e)
#pragma unroll
          for (int d = 0; d < 2; ++d) {
            kv[e][d] = mfma(frag2(pk[0][d], pk[1][d]), frag2(vf[0][e], vf[1][e]), z4);
            kv[e][d] = mfma(frag2(pk[2][d], pk[3][d]), frag2(vf[2][e], vf[3][e]), kv[e][d]);
          }
      }
      lds_st128(L0 + B_EX + w * 2048 + lane * 16, __builtin_bit_cast(u32x4, kv[1][0]));
      lds_st128(L0 + B_EX + w * 2048 + 1024 + lane * 16, __builtin_bit_cast(u32x4, kv[1][1]));
      sig(L0 + SYNC + SY_EX + w * 4);
      wait1(L0 + SYNC + SY_EX + (w ^ 1) * 4);
      const f32x4 s0 = kv[0][0] + __builtin_bit_cast(f32x4, lds_ld128(L0 + B_EX + (w ^ 1) * 2048 + lane * 16));
      const f32x4 s1 = kv[0][1] + __builtin_bit_cast(f32x4, lds_ld128(L0 + B_EX + (w ^ 1) * 2048 + 1024 + lane * 16));
      {
        const u32x2 a = pk4(s0), b = pk4(s1);
        publish((u32x4){a[0], a[1], b[0], b[1]}, wsr, lane * 16 + kvs + rt * 1024);
      }
      STAMP(6);
      if (!__all(clr == tagO)) poll_flags(clrp(), tagO);
      wo_run(0, QWO);
      STAMP(7);
      f32x4 sum0, sum1;
      {
        const unsigned e0 = q == g ? ((p & 1) ? 0x3f800000u : 0x3f80u) : 0u;
        const unsigned ea = p < 2 ? e0 : 0u, eb = p < 2 ? 0u : e0;
        const bf16x8 sel0 = as_frag((u32x4){ea, eb, 0u, 0u}), sel1 = as_frag((u32x4){0u, 0u, ea, eb});
        unsigned vo = kvs + lane * 16;
#pragma nounroll
        for (unsigned spins = 0; spins < SPIN_CAP; ++spins) {
          asm volatile("" : "+v"(vo)::"memory");
          u32x4 pr[16];
#pragma unroll
          for (int r = 0; r < 16; ++r) pr[r] = __builtin_amdgcn_raw_buffer_load_b128(wsr, vo, ((r + 2 * rt) & 15) * 1024, 16);
          SB();
          sum0 = sum1 = z4;
#pragma unroll
          for (int r = 0; r < 16; ++r) {
            sum0 = mfma(sel0, as_frag(pr[r]), sum0);
            sum1 = mfma(sel1, as_frag(pr[r]), sum1);
          }
          if (!__any(sum0[0] != sum0[0])) break;
          unsigned m = 0;
#pragma unroll
          for (int r = 0; r < 16; ++r) m = max(max(max(max(m, pr[r][0]), pr[r][1]), pr[r][2]), pr[r][3]);
          if (!__any(m == SENT)) break;
        }
      }
      asm volatile("" ::"v"(pw));
      STAMP(8);
      lds_st128(L0 + B_KVL + w * 1024 + lane * 16, __builtin_bit_cast(u32x4, frag2(pk4(sum0), pk4(sum1))));
      sig(L0 + SYNC + SY_KV + w * 4);
      STAMP(10); STAMPC(19);
      __builtin_amdgcn_s_setprio(0);
      if (cg >= 7) { STAMP_FLUSH(); return; }
      sig(L0 + SYNC + SY_WOA + w * 4);
      wait4(L0 + SYNC + SY_D6);
      wait4(L0 + SYNC + SY_QD);
      wo_run(QWO, 28);
  } else {
      asm volatile("; role A");
      setup(A_G);
      f32x4 aq[4][2], ak[5][2], av[5][2];
#pragma unroll
      for (int t = 0; t < 5; ++t)
#pragma unroll
        for (int f = 0; f < 2; ++f) { ak[t][f] = av[t][f] = z4; if (t < 4) aq[t][f] = z4; }
      const unsigned gl = L0 + A_G + (8 * g + q) * 8;
      const bool p3 = p == 3;
      const unsigned d1 = p3 ? 32u : 112u;
      bf16x8 xf[2][5], fq[2][2], fk[2][2], fv[2][2];
      unsigned xb, wb;
      auto rd = [&](int c, int ks, int part) {
        const unsigned xa = xb ^ (ks * 64), wk = wb + ks * 3584;
        if (part == 0) {
          fk[ks][0] = lds_tr2(wk + WM_B, wk + WM_B + 112);
          xf[ks][0] = as_frag(lds_ld128(xa));
          xf[ks][1] = as_frag(lds_ld128(xa + 2048));
        } else if (part == 1) {
          fv[ks][0] = lds_tr2(wk + 2 * WM_B, wk + 2 * WM_B + 112);
          xf[ks][2] = as_frag(lds_ld128(xa + 4096));
        } else if (part == 2) {
          fq[ks][0] = lds_tr2(wk, wk + 112);
          xf[ks][3] = as_frag(lds_ld128(xa + 6144));
        } else if (part == 3) {
          fk[ks][1] = lds_tr2(wk + WM_B + 32, wk + WM_B + 32 + 112);
          xf[ks][4] = as_frag(lds_ld128(xa + 8192));
        } else if (part == 4) {
          fv[ks][1] = lds_tr2(wk + 2 * WM_B + 32, wk + 2 * WM_B + 32 + 112);
        } else {
          const unsigned a = p3 ? gl + c * 512 + ks * 256 : wk + 32;
          fq[ks][1] = lds_tr2(a, a + d1);
        }
      };
      auto mm = [&](int ks, int part) {
        const int f = part / 3, m = part % 3;
#pragma unroll
        for (int t = 0; t < 5; ++t) {
          if (m == 0) ak[t][f] = mfma(fk[ks][f], xf[ks][t], ak[t][f]);
          if (m == 1) av[t][f] = mfma(xf[ks][t], fv[ks][f], av[t][f]);
          if (m == 2 && t > 0) aq[t - 1][f] = mfma(fq[ks][f], xf[ks][t], aq[t - 1][f]);
        }
      };
#pragma unroll
      for (int c = 0; c < 7; ++c) {
        xb = opaque(L0 + a_slot(c) + rh * 8192 + xsw);
        wb = opaque(L0 + a_slot(c) + A_W + wln);
        hook(c, true, false);
        LDS_BAR();
        hook(c, false, false);
        if (c == 0) { STAMP(2); STAMPC(16); }
        if (c == 3) STAMP(3);
        if (c == 6) { STAMP(4); STAMPC(17); }
        SB();
#pragma unroll
        for (int ks = 0; ks < 2; ++ks) {
#pragma unroll
          for (int part = 0; part < 6; ++part) rd(c, ks, part);
          if (ks == 1 || c > 0) {
#pragma unroll
            for (int part = 0; part < 6; ++part) mm(ks ^ 1, part);
            interleave<28, 17>();
          }
          SB();
        }
      }
      sig(L0 + SYNC + SY_D6 + w * 4);
      mm(1, 0); mm(1, 3); mm(1, 2); mm(1, 5);
      SB();
      mm(1, 1); mm(1, 4);
      STAMP(5);
      const bool odd = li & 1;
      const int kk0 = 4 * g + (li & 1);
      const bool own0 = kk0 <= li, own1 = kk0 + 2 <= li;
      const float sc = 0.18898223650461362f * kLog2e;
      const float ninf = (rt == 0 && rh == 0) ? -__builtin_inff() : 0.f;
      const unsigned l16 = lane_id() * 16;
      bf16x8 kf[5];
#pragma unroll
      for (int t = 0; t < 5; ++t) kf[t] = frag2(pk4(ak[t][0]), pk4(ak[t][1]));
      f32x4 sp[4], so[4];
#pragma unroll
      for (int t = 0; t < 4; ++t) {
        const u32x2 b = pk4(aq[t][1]);
        const bf16x8 qf = frag2(pk4(aq[t][0]), (u32x2){g3 ? 0u : b[0], g3 ? 0u : b[1]});
        sp[t] = mfma(kf[t], qf, t == 0 ? (f32x4){ninf, ninf, ninf, ninf} : z4);
        so[t] = mfma(kf[t + 1], qf, z4);
      }
      float ug;
      {
        float z0 = aq[0][1][0], z1 = aq[1][1][0], z2 = aq[2][1][0], z3 = aq[3][1][0];
        swap32(z0, z1);
        swap32(z2, z3);
        swap16(z1, z3);
        ug = 1.f + __builtin_amdgcn_exp2f(-kLog2e * z3);
      }
      u32x2 vf[5][2];
#pragma unroll
      for (int t = 0; t < 5; ++t) {
        vf[t][0] = pk4(av[t][0]);
        vf[t][1] = pk4(av[t][1]);
      }
      float l0[4], l1[4], bc[4];
#pragma unroll
      for (int t = 0; t < 4; ++t) {
        float a0 = so[t][0], a2 = so[t][2], b0 = sp[t][0], b2 = sp[t][2];
        const float c0 = own0 ? a0 : b0, c1 = own0 ? so[t][1] : sp[t][1], c2 = own1 ? a2 : b2, c3 = own1 ? so[t][3] : sp[t][3];
        l0[t] = odd ? c1 : c0;
        l1[t] = odd ? c3 : c2;
      }
      rows_bcast4(rows_reduce4<true>(fmax1(l0[0], l1[0]), fmax1(l0[1], l1[1]), fmax1(l0[2], l1[2]), fmax1(l0[3], l1[3])) * -sc, bc);
#pragma unroll
      for (int t = 0; t < 4; ++t) {
        l0[t] = __builtin_amdgcn_exp2f(__builtin_fmaf(l0[t], sc, bc[t]));
        l1[t] = __builtin_amdgcn_exp2f(__builtin_fmaf(l1[t], sc, bc[t]));
      }
      rows_bcast4(__builtin_amdgcn_rcpf(rows_reduce4<false>(l0[0] + l1[0], l0[1] + l1[1], l0[2] + l1[2], l0[3] + l1[3]) * ug), bc);
      const unsigned sh = odd ? 16u : 0u;
      const unsigned mk0 = own0 ? 0u : 0xffffu, mk1 = own1 ? 0u : 0xffffu;
#pragma unroll
      for (int t = 0; t < 4; ++t) {
        const unsigned pe = pk2(l0[t] * bc[t], l1[t] * bc[t]), ph = pe >> 16;
        const bf16x8 pf = as_frag((u32x4){(pe & mk0) << sh, (ph & mk1) << sh, (pe & (mk0 ^ 0xffffu)) << sh, (ph & (mk1 ^ 0xffffu)) << sh});
        const f32x4 r0 = mfma(frag2(vf[t][0], vf[t + 1][0]), pf, z4);
        const f32x4 r1 = mfma(frag2(vf[t][1], vf[t + 1][1]), pf, z4);
        const u32x2 c0 = pk4(r0), c1 = pk4(r1);
        __builtin_amdgcn_raw_buffer_store_b128((u32x4){c0[0], c0[1], g3 ? 0u : c1[0], g3 ? 0u : c1[1]}, wsr, l16 + obs + t * 32768, 0, 16);
      }
      STAMP(9);
      VM_WAIT(0);
      asm volatile("" ::"v"(pw));
      if (l16 == 0) __hip_atomic_store((gu64*)(ws + WS_FA) + ((rt * 2 + rh) * 16 + hp * 2 + hh) * FS, tagO, RLX_AGENT);
      STAMP(10); STAMPC(19);
      if (cg >= 7) {
        if (bid == 56 && w == 0) {
#pragma nounroll
          for (int k = 0; k < 4; ++k) {
            const int i = (k & 1) * 64 + lane;
            poll_flags(k < 2 ? (gu64*)(ws + WS_CLR) + i : (gu64*)(ws + WS_FA) + ((i >> 3) * 32 + (i & 7) * 2) * FS, tagO);
          }
          if (lane == 0) __hip_atomic_store((gu64*)(ws + WS_E), tagO, RLX_AGENT);
        }
        STAMP_FLUSH();
        return;
      }
  }

  const int pb = w >> 2;
  wait4(L0 + SYNC + (pb ? SY_WOB : SY_WOA));
  const unsigned sw = (2 * g + (q >> 1)) & 3;
  const unsigned rb = L0 + (pb * 448 + 4 * g + q) * 128 + 8 * p;
  unsigned wa[4], wa2[4];
#pragma unroll
  for (int v = 0; v < 4; ++v) {
    wa[v] = opaque(rb + ((v ^ sw) << 5));
    wa2[v] = opaque(wa[v] + (g3 ? 0u : 2048u));
  }
  auto wfrag = [&](int h, int ct) {
    const int v = ct ^ (2 * (h & 1));
    return lds_tr2(wa[v] + h * 3584, wa2[v] + h * 3584);
  };
  constexpr int NPRE = 8;
  bf16x8 wf[16][4];
#pragma unroll
  for (int h = 0; h < NPRE; ++h)
#pragma unroll
    for (int ct = 0; ct < 4; ++ct) wf[h][ct] = wfrag(h, ct);
  STAMP(11);
  const unsigned ub = WS_O + ((rg * 4 + wl) * 32 + pb * 16) * 1024;
  tagO = uniform64(*(LAS u64*)(uintptr_t)(L0 + SYNC + SY_TAG));
  {
    const gu64* fp = pb ? (gu64*)(ws + WS_FB) + ((rg * 2 + (wl >> 1)) * 8 + (li & 7)) * FS : (gu64*)(ws + WS_FA) + (rg * 16 + li) * FS;
    const u64 f0 = __hip_atomic_load(fp, RLX_AGENT);
    if (br && !pb) { VM_WAIT(0); sig(L0 + SYNC + SY_WOB + wl * 4); }
    if (!__all(f0 == tagO)) poll_flags(fp, tagO);
  }
  STAMP(12);
  u32x4 of[16];
  {
    unsigned vo = ub + lane * 16;
    asm volatile("" : "+v"(vo)::"memory");
#pragma unroll
    for (int h = 0; h < 16; ++h) of[h] = __builtin_amdgcn_raw_buffer_load_b128(wsr, vo, ((h + 2 * cg) & 15) * 1024, 16);
    SB();
  }
  f32x4 acc[4] = {z4, z4, z4, z4};
#pragma unroll
  for (int h = 0; h < 16; ++h) {
#pragma unroll
    for (int ct = 0; ct < 4; ++ct) acc[ct] = mfma(wf[h][ct], as_frag(of[h]), acc[ct]);
    SB();
    if (h + NPRE < 16) {
#pragma unroll
      for (int ct = 0; ct < 4; ++ct) wf[h + NPRE][ct] = wfrag(h + NPRE, ct);
    }
    SB();
  }
  STAMP(13);
  const unsigned xa = L0 + P2X + wl * 4096 + lane * 16;
  if (!pb) {
    wait4(L0 + SYNC + SY_D6);
#pragma unroll
    for (int ct = 0; ct < 4; ++ct) lds_st128(xa + ct * 1024, __builtin_bit_cast(u32x4, acc[ct]));
    sig(L0 + SYNC + SY_P2 + wl * 4);
  } else {
    f32x4 pa[4];
    for (;;) {
      const unsigned f = *(volatile LAS unsigned*)(uintptr_t)(L0 + SYNC + SY_P2 + wl * 4);
#pragma unroll
      for (int ct = 0; ct < 4; ++ct) pa[ct] = *(volatile LAS f32x4*)(uintptr_t)(xa + ct * 1024);
      if (__builtin_amdgcn_readfirstlane(f)) break;
      __builtin_amdgcn_s_sleep(1);
    }
    char* orow = out + (rg * 64 + wl * 16 + li) * XROW + cg * 128 + (g & 1) * 32 + (g >> 1) * 16;
#pragma unroll
    for (int k = 0; k < 2; ++k) {
      const u32x2 a = pk4(acc[2 * k] + pa[2 * k]), b = pk4(acc[2 * k + 1] + pa[2 * k + 1]);
      const auto r0 = __builtin_amdgcn_permlane16_swap(a[0], b[0], false, false);
      const auto r1 = __builtin_amdgcn_permlane16_swap(a[1], b[1], false, false);
      *(u32x4*)(orow + k * 64) = (u32x4){r0[0], r1[0], r0[1], r1[1]};
    }
  }
  STAMP(14);
  STAMP(15);
  STAMP_FLUSH();
}

}

extern "C" void kernel_launch(void* const* d_in, const int* in_sizes, int n_in, void* d_out, int out_size, void* d_ws,
                              size_t ws_size, hipStream_t stream) {
  (void)in_sizes; (void)n_in; (void)out_size; (void)ws_size;
  gated_mix_kernel<<<256, 512, 0, stream>>>((const char*)d_in[0], (const char*)d_in[1], (const char*)d_in[2],
                                            (const char*)d_in[4], (const char*)d_in[5],
                                            (const unsigned short*)d_in[6], (char*)d_out, (char*)d_ws);
}
```

```cpp
#define DIAG_A 0
#define DIAG_B 4
#define DIAG_ROLE 0
#include <hip/hip_runtime.h>
#include <stdint.h>

typedef __bf16 bf16x2 __attribute__((ext_vector_type(2)));
typedef __bf16 bf16x4 __attribute__((ext_vector_type(4)));
typedef __bf16 bf16x8 __attribute__((ext_vector_type(8)));
typedef float f32x2 __attribute__((ext_vector_type(2)));
typedef float f32x4 __attribute__((ext_vector_type(4)));
typedef unsigned u32x2 __attribute__((ext_vector_type(2)));
typedef unsigned u32x4 __attribute__((ext_vector_type(4)));
typedef unsigned long long u64;
#define LAS __attribute__((address_space(3)))
#define GAS __attribute__((address_space(1)))
typedef GAS u64 gu64;
#define RLX_AGENT __ATOMIC_RELAXED, __HIP_MEMORY_SCOPE_AGENT

namespace {

constexpr int kS = 2048, kD = 448;
constexpr int XROW = kD * 2;
constexpr int WROW = 3 * kD * 2;
constexpr int WM_B = 64 * 112;

constexpr int A_FRONT = 34816, A_SLOT = 144 * 128 + 3 * WM_B, A_W = 144 * 128;
constexpr int A_G = A_FRONT + 3 * A_SLOT;
constexpr int B_SLOT = 128 * 128 + 2 * WM_B, B_W = 128 * 128;
constexpr int B_WQ = 97536, B_G = B_WQ + 7 * WM_B, B_EX = B_G + 3584, B_KVL = B_EX + 8192;
constexpr int P2X = 114688;
constexpr int SYNC = 163584;
constexpr int LDS_B = 163840;
static_assert(A_G + 3584 <= SYNC && B_KVL + 4096 <= SYNC && A_FRONT + 2 * A_SLOT == 114688, "lds map");
__host__ __device__ constexpr int a_slot(int c) { return A_FRONT + ((c + 2) % 3) * A_SLOT; }
__host__ __device__ constexpr int b_slot(int c) { return ((c + 2) % 3) * B_SLOT; }
enum { SY_WOA = 0, SY_WOB = 16, SY_D6 = 32, SY_D6Q = 48, SY_WQ = 64, SY_QD = 80, SY_EX = 96, SY_KV = 112, SY_P2 = 128, SY_ZERO = 192, SY_TAG = 240 };

constexpr int WS_E = 0;
constexpr int WS_CLR = 4096;
constexpr int FS = 16;
constexpr int WS_FA = 8192;
constexpr int WS_FB = WS_FA + 16 * 2 * 16 * 8 * FS;
constexpr int WS_KVP = WS_FB + 16 * 4 * 8 * 8 * FS;
constexpr int WS_O = WS_KVP + 8 * 4 * 16 * 1024;
constexpr int WS_END = WS_O + 16 * 8 * 32 * 1024;

constexpr int PT_WS = (WS_END / 8) & ~127;
constexpr unsigned SPIN_CAP = 1u << 14;
constexpr unsigned SENT = 0xffffffffu;
#define QWO 14

#define STAMP(i) do { if (i == DIAG_A) dta = (unsigned)__builtin_amdgcn_s_memrealtime(); if (i == DIAG_B) dtb = (unsigned)__builtin_amdgcn_s_memrealtime(); } while (0)
#define STAMPC(i) do {} while (0)
#define STAMP_FLUSH() do { if (br * 2 + (w >> 2) == DIAG_ROLE) { unsigned d = (dtb && dta) ? (dtb - dta) * 8u : 0u; d = d > 100000u ? 100000u : d; const unsigned t0 = dt0; while ((unsigned)__builtin_amdgcn_s_memrealtime() - t0 < 4000u + d) __builtin_amdgcn_s_sleep(2); } } while (0)

__device__ __forceinline__ unsigned pk2(float a, float b) {
  f32x2 v = {a, b};
  bf16x2 r = __builtin_convertvector(v, bf16x2);
  return __builtin_bit_cast(unsigned, r);
}
__device__ __forceinline__ u32x2 pk4(f32x4 v) { return (u32x2){pk2(v[0], v[1]), pk2(v[2], v[3])}; }
__device__ __forceinline__ bf16x8 as_frag(u32x4 v) { return __builtin_bit_cast(bf16x8, v); }
__device__ __forceinline__ bf16x8 frag2(u32x2 a, u32x2 b) { return as_frag((u32x4){a[0], a[1], b[0], b[1]}); }
__device__ __forceinline__ f32x4 mfma(bf16x8 a, bf16x8 b, f32x4 c) {
  return __builtin_amdgcn_mfma_f32_16x16x32_bf16(a, b, c, 0, 0, 0);
}
__device__ __forceinline__ bf16x8 lds_tr2(unsigned a0, unsigned a1) {
  bf16x4 lo = __builtin_amdgcn_ds_read_tr16_b64_v4bf16((LAS bf16x4*)(uintptr_t)a0);
  bf16x4 hi = __builtin_amdgcn_ds_read_tr16_b64_v4bf16((LAS bf16x4*)(uintptr_t)a1);
  return __builtin_shufflevector(lo, hi, 0, 1, 2, 3, 4, 5, 6, 7);
}
__device__ __forceinline__ u32x4 lds_ld128(unsigned a) { return *(LAS u32x4*)(uintptr_t)a; }
__device__ __forceinline__ void lds_st128(unsigned a, u32x4 v) { *(LAS u32x4*)(uintptr_t)a = v; }
__device__ __forceinline__ void lds_st64(unsigned a, u32x2 v) { *(LAS u32x2*)(uintptr_t)a = v; }
__device__ __forceinline__ unsigned opaque(unsigned v) { asm volatile("" : "+v"(v)); return v; }

__device__ __forceinline__ void sig(unsigned a) {
  asm volatile("" ::: "memory");
  *(volatile LAS unsigned*)(uintptr_t)a = 1u;
  asm volatile("" ::: "memory");
  __builtin_amdgcn_sched_barrier(0);
}
__device__ __forceinline__ void wait1(unsigned a) {
  while (__builtin_amdgcn_readfirstlane(*(volatile LAS unsigned*)(uintptr_t)a) == 0u) __builtin_amdgcn_s_sleep(1);
  asm volatile("" ::: "memory");
}
__device__ __forceinline__ void wait4(unsigned a) {
  for (;;) {
    const u32x4 v = *(volatile LAS u32x4*)(uintptr_t)a;
    if (__builtin_amdgcn_readfirstlane(v[0] & v[1] & v[2] & v[3])) break;
    __builtin_amdgcn_s_sleep(1);
  }
  asm volatile("" ::: "memory");
}

__device__ __forceinline__ u32x4 make_srd(const void* p, unsigned bytes) {
  uint64_t a = (uint64_t)p;
  u32x4 r;
  r[0] = __builtin_amdgcn_readfirstlane((unsigned)a);
  r[1] = __builtin_amdgcn_readfirstlane((unsigned)(a >> 32) & 0xffffu);
  r[2] = bytes;
  r[3] = 0x00020000u;
  return r;
}
__device__ __forceinline__ void dma16(u32x4 srd, unsigned voff, unsigned soff, unsigned ldsaddr) {
  asm volatile(
      "s_mov_b32 m0, %3\n\t"
      "s_nop 3\n\t"
      "buffer_load_dwordx4 %0, %1, %2 offen lds"
      :
      : "v"(voff), "s"(srd), "s"(soff), "s"(ldsaddr));
}
#define VM_BAR(N) asm volatile("s_waitcnt vmcnt(" #N ") lgkmcnt(0)\n\ts_barrier" ::: "memory")
#define LDS_BAR() asm volatile("s_waitcnt lgkmcnt(0)\n\ts_barrier" ::: "memory")
#define VM_WAIT(N) asm volatile("s_waitcnt vmcnt(" #N ")" ::: "memory")
template <int N> __device__ __forceinline__ void vm_bar() { asm volatile("s_waitcnt vmcnt(%0) lgkmcnt(0)\n\ts_barrier" ::"n"(N) : "memory"); }
#define SB() __builtin_amdgcn_sched_barrier(0)

__device__ __forceinline__ float fmax1(float a, float b) { return __builtin_amdgcn_fmed3f(a, b, 3.0e38f); }
__device__ __forceinline__ void swap16(float& a, float& b) {
  auto r = __builtin_amdgcn_permlane16_swap(__float_as_uint(a), __float_as_uint(b), false, false);
  const unsigned r0 = r[0], r1 = r[1];
  a = __uint_as_float(r0);
  b = __uint_as_float(r1);
}
__device__ __forceinline__ void swap32(float& a, float& b) {
  auto r = __builtin_amdgcn_permlane32_swap(__float_as_uint(a), __float_as_uint(b), false, false);
  const unsigned r0 = r[0], r1 = r[1];
  a = __uint_as_float(r0);
  b = __uint_as_float(r1);
}
template <bool MAX>
__device__ __forceinline__ float rows_reduce4(float v0, float v1, float v2, float v3) {
  swap32(v0, v1);
  swap32(v2, v3);
  float a = MAX ? fmax1(v0, v1) : v0 + v1, b = MAX ? fmax1(v2, v3) : v2 + v3;
  swap16(a, b);
  return MAX ? fmax1(a, b) : a + b;
}
__device__ __forceinline__ void rows_bcast4(float x, float (&o)[4]) {
  float y = x;
  swap16(x, y);
  o[0] = x; o[1] = x; o[2] = y; o[3] = y;
  swap32(o[0], o[1]);
  swap32(o[2], o[3]);
}
__device__ __forceinline__ float row3_bcast(float v) {
  float a = v;
  swap32(a, v);
  a = v;
  swap16(a, v);
  return v;
}
template <int NM, int ND>
__device__ __forceinline__ void interleave() {
#pragma unroll
  for (int i = 0; i < NM; ++i) {
    __builtin_amdgcn_sched_group_barrier(0x8, 1, 0);
    if (i < ND) {
      __builtin_amdgcn_sched_group_barrier(0x2, 1, 0);
      __builtin_amdgcn_sched_group_barrier(0x100, 1, 0);
    }
  }
}
constexpr float kEps = 1e-6f, kLog2e = 1.44269504088896f;
__device__ __forceinline__ float phi(float u) {
  const float e = __builtin_amdgcn_fmed3f(__builtin_amdgcn_exp2f(__builtin_fmaf(u, kLog2e, -kEps * kLog2e)), 0.f, 1.f);
  return fmax1(u, kEps) + e;
}
__device__ __forceinline__ u32x2 phi4(f32x4 v) { return (u32x2){pk2(phi(v[0]), phi(v[1])), pk2(phi(v[2]), phi(v[3]))}; }
__device__ __forceinline__ float sigmoidf(float z) {
  return __builtin_amdgcn_rcpf(1.f + __builtin_amdgcn_exp2f(-1.44269504088896f * z));
}
__device__ __forceinline__ u64 next_tag(u64 e) {
  const u64 s = ((u64)(unsigned)__builtin_amdgcn_readfirstlane((unsigned)(e >> 32)) << 32) |
                (unsigned)__builtin_amdgcn_readfirstlane((unsigned)e);
  return s * 6364136223846793005ull + 1442695040888963407ull;
}
__device__ __forceinline__ u64 uniform64(u64 e) {
  return ((u64)(unsigned)__builtin_amdgcn_readfirstlane((unsigned)(e >> 32)) << 32) |
         (unsigned)__builtin_amdgcn_readfirstlane((unsigned)e);
}
__device__ __forceinline__ void poll_flags(const gu64* p, u64 tag) {
#pragma nounroll
  for (unsigned spins = 0; spins < SPIN_CAP; ++spins) {
    u64 v = __hip_atomic_load(p, RLX_AGENT);
    if (__all(v == tag)) break;
    __builtin_amdgcn_s_sleep(2);
  }
  asm volatile("" ::: "memory");
}
__device__ __forceinline__ void publish(u32x4 v, __amdgpu_buffer_rsrc_t r, unsigned voff) {
#pragma unroll
  for (int i = 0; i < 4; ++i) v[i] = v[i] < SENT - 1u ? v[i] : SENT - 1u;
  __builtin_amdgcn_raw_buffer_store_b128(v, r, voff, 0, 16);
}
__device__ __forceinline__ unsigned lane_id() {
  unsigned l = __builtin_amdgcn_mbcnt_hi(~0u, __builtin_amdgcn_mbcnt_lo(~0u, 0u));
  asm volatile("" : "+v"(l));
  return l;
}
__device__ __forceinline__ int wrap7(int c) { return c >= 7 ? c - 7 : c; }

__global__ __launch_bounds__(512, 2) void gated_mix_kernel(const char* __restrict__ x, const char* __restrict__ wqkvA,
                                                           const char* __restrict__ woA, const char* __restrict__ wqkvB,
                                                           const char* __restrict__ woB,
                                                           const unsigned short* __restrict__ gate_w, char* __restrict__ out,
                                                           char* ws) {
  __shared__ __attribute__((aligned(1024))) char lds[LDS_B];
  const unsigned L0 = (unsigned)(uintptr_t)(LAS char*)lds;
  const int bid = blockIdx.x;
  const int xcd = bid & 7, idx = bid >> 3;
  const int xj = xcd >> 1, ci = idx >> 3;
  const int br = ci >> 1;
  const int hp = 2 * (br ? (xj ^ 1) : xj) + (ci & 1);
  const int rt = (xcd & 1) * 8 + (idx & 7);
  const int rot = wrap7(wrap7((idx & 7) + 2 * ci + (xj & 1)));
  const int cg = idx & 7, rg = 4 * xcd + ci;

  const int tid = threadIdx.x;
  const int lane = tid & 63;
  const int w = __builtin_amdgcn_readfirstlane(tid >> 6) & 7;
  const int wl = w & 3;
  const int li = lane & 15, g = lane >> 4, q = (lane >> 2) & 3, p = lane & 3;
  const bool g3 = g == 3;
  const f32x4 z4 = {0.f, 0.f, 0.f, 0.f};
  unsigned dta = 0, dtb = 0;
  const unsigned dt0 = (unsigned)__builtin_amdgcn_s_memrealtime();
  STAMP(0); STAMPC(18);
  unsigned vx0 = (lane >> 3) * XROW + (((lane & 7) ^ (lane >> 4)) << 4);
  unsigned vx1 = vx0 ^ 64u;
  unsigned vw0, vw1;
  {
    const unsigned s0 = wl * 64 + lane, s1 = s0 + 256;
    const unsigned p0 = (s0 * 9363u) >> 16, p1 = (s1 * 9363u) >> 16;
    const unsigned k0 = (p0 & 56) | ((p0 >> 1) & 3) | ((p0 & 1) << 2);
    const unsigned k1 = (p1 & 56) | ((p1 >> 1) & 3) | ((p1 & 1) << 2);
    vw0 = k0 * WROW + hp * 112 + (s0 - p0 * 7) * 16;
    vw1 = k1 * WROW + hp * 112 + (s1 - p1 * 7) * 16;
  }
  asm volatile("" : "+v"(vx0), "+v"(vx1), "+v"(vw0), "+v"(vw1));
  asm volatile("" ::"s"(x), "s"(wqkvA), "s"(woA), "s"(wqkvB), "s"(woB), "s"(gate_w), "s"(out), "s"(ws));
  const __amdgpu_buffer_rsrc_t wsr = __builtin_amdgcn_make_buffer_rsrc(ws, 0, WS_END, 0x00020000);
  u64 tagO;
  auto wo_run = [&](int j0, int j1) {
    const unsigned vwo = (lane >> 3) * XROW + cg * 128 + (((lane & 7) ^ (((lane >> 4) & 3) << 1)) << 4);
    const u32x4 sa = make_srd(woA, kD * XROW), sb = make_srd(woB, kD * XROW);
    unsigned so = (wl + 7 * cg + 4 * (j0 % 14)) * (8 * XROW);
    so = so >= kD * XROW ? so - kD * XROW : so;
#pragma unroll
    for (int j = j0; j < j1; ++j) {
      dma16(j < 14 ? sa : sb, vwo, so, L0 + (wl + 4 * j) * 1024);
      so += 32 * XROW;
      so = so >= kD * XROW ? so - kD * XROW : so;
    }
  };
  const u32x4 srdX = make_srd(x, kS * XROW);
  const u32x4 srdW = make_srd(br ? wqkvB : wqkvA, kD * WROW);
  const bool l3 = wl == 3;
  const int xr = rt * 128 + wl * 32 - (br ? 0 : 16);
  const unsigned sx01 = (xr < 0 ? 0 : xr) * XROW, sx23 = (xr + 16) * XROW;
  const bool l3a = l3 && !br;
  const u32x4 srd2 = l3a ? srdX : srdW;
  const unsigned v2 = l3 ? vw0 : vw1;
  const unsigned v2a = l3a ? vx0 : v2, v2b = l3a ? vx1 : v2;
  const unsigned st2 = l3a ? 128u : 64u * WROW, s2a = l3a ? (rt * 128 + 112) * XROW : 0u;
  const unsigned s2d = l3a ? 8u * XROW : (unsigned)XROW, s2e = l3a ? 8u * XROW : 2u * XROW;
  const unsigned sub2 = (l3 ? wl : wl + 4) * 1024;
  auto chunk = [&](unsigned sl, unsigned lq, unsigned lk, int cc) {
    const unsigned sw = cc * (64 * WROW), sx = cc * 128, s2 = cc * st2 + s2a;
#pragma unroll
    for (int j = 0; j < 4; ++j)
      dma16(srdX, (j & 1) ? vx1 : vx0, (j < 2 ? sx01 : sx23) + (j & 1) * 8 * XROW + sx, sl + (wl * 4 + j) * 1024);
    dma16(srdW, vw0, sw + XROW, lk + wl * 1024);
    dma16(srdW, vw0, sw + 2 * XROW, lk + WM_B + wl * 1024);
    dma16(srd2, v2b, s2 + s2d, l3a ? sl + 17408 : lk + sub2);
    dma16(srd2, v2b, s2 + s2e, l3a ? sl + 17408 : lk + WM_B + sub2);
    if (!br) {
      dma16(srdW, vw0, sw, lq + wl * 1024);
      dma16(srd2, v2a, s2, l3a ? sl + 16384 : lq + sub2);
    }
  };
  unsigned gv = 0, pt = 0;
  u64 epoch = 0;
  {
    int wx = w;
    asm volatile("" : "+s"(wx));
    if (wx < 4) {
      gv = ((const unsigned*)gate_w)[tid < 224 ? tid : 0];
      epoch = __hip_atomic_load((gu64*)(ws + WS_E), RLX_AGENT);
      const char* pb_ = wx == 0 ? ws : (wx == 1 ? out + rg * 64 * XROW : (wx == 2 ? woA : woB));
      pt = __hip_atomic_load((GAS unsigned*)(pb_ + (lane & 7) * (wx == 0 ? PT_WS : (wx == 1 ? 7168 : 50176))), RLX_AGENT);
    } else {
#pragma nounroll
      for (int c = 0; c < 2; ++c) {
        const unsigned sl = L0 + (br ? (c ? b_slot(1) : b_slot(0)) : (c ? a_slot(1) : a_slot(0)));
        chunk(sl, sl + A_W, sl + (br ? B_W : A_W + WM_B), wrap7(rot + c));
      }
    }
  }

    const int rh = w & 1, hh = w >> 1;
    const unsigned obs = WS_O + ((rt * 8 + rh * 4) * 32 + hp * 2 + hh) * 1024;
    const unsigned kvs = WS_KVP + (hp * 4 + w) * 16 * 1024;
    auto setup = [&](int goff) {
      if (tid < 60) *(LAS unsigned*)(uintptr_t)(L0 + SYNC + tid * 4) = 0u;
      const unsigned lo = (gv & 0xffffu) * 0x10001u, hi = (gv >> 16) * 0x10001u;
      const int pos = tid - rot * 32;
      if (tid < 224) lds_st128(L0 + goff + (pos < 0 ? pos + 224 : pos) * 16, (u32x4){lo, lo, hi, hi});
    };
    u64 clr = 0;
    unsigned pw = 0;
    auto clrp = [&]() { return (const gu64*)(ws + WS_CLR) + hp * 16 + (lane_id() & 15); };
    auto hook = [&](int c, bool before, bool isb) {
      if (c == 0 && !before && isb)
        __builtin_amdgcn_raw_buffer_store_b128((u32x4){SENT, SENT, SENT, SENT}, wsr, lane_id() * 16 + kvs + rt * 1024, 0, 16);
      if (c == 3 && before) {
        asm volatile("" : "+v"(epoch) : "v"(pt));
        tagO = next_tag(epoch);
        *(LAS u64*)(uintptr_t)(L0 + SYNC + SY_TAG) = tagO;
        VM_WAIT(0);
      }
      if (c == 3 && !before && isb)
        __builtin_amdgcn_raw_buffer_store_b64((u32x2){(unsigned)tagO, (unsigned)(tagO >> 32)}, wsr,
                                              (lane_id() | w) == 0 ? WS_CLR + (hp * 16 + rt) * 8 : 0x7ffffff0u, 0, 16);
      if (c == 6 && !before && isb) clr = __hip_atomic_load(clrp(), RLX_AGENT);
      if (c == 6 && !before) {
        const unsigned l = lane_id(), u = (l >> 3) & 3;
        const unsigned o = isb ? WS_O + ((rt * 8 + w * 2 + (u >> 1)) * 32 + 16 + hp * 2 + (u & 1)) * 1024 : obs + u * 32768;
        pw = __builtin_amdgcn_raw_buffer_load_b32(wsr, o + (l & 7) * 128, 0, 16);
      }
    };
    const unsigned xsw = li * 128 + ((g ^ (li >> 1)) << 4);
    const unsigned wln = (8 * g + 2 * q) * 112 + 8 * p + 56 * hh;

  if (__builtin_expect(w >= 4, 1)) {
    if (!br) {
      int cc = wrap7(rot + 1);
      unsigned sl = L0 + a_slot(2);
#pragma nounroll
      for (int c = 2; c < 7; ++c) {
        vm_bar<10>();
        cc = wrap7(cc + 1);
        chunk(sl, sl + A_W, sl + A_W + WM_B, cc);
        sl = sl == L0 + a_slot(0) ? L0 + a_slot(1) : sl + A_SLOT;
      }
      vm_bar<10>();
      if (cg >= 7) { vm_bar<0>(); STAMP_FLUSH(); return; }
      wo_run(0, 4);
      vm_bar<4>();
      wo_run(4, 28);
      VM_WAIT(14);
      sig(L0 + SYNC + SY_WOA + wl * 4);
      VM_WAIT(0);
      asm volatile("" ::"v"(gv), "v"(epoch), "v"(pt));
      sig(L0 + SYNC + SY_WOB + wl * 4);
    } else {
      auto instr = [&](int n) {
        if (n < 56) {
          const int c = n >> 3, j = n & 7, cc = wrap7(c + rot);
          const unsigned sl = L0 + b_slot(c), sw = cc * (64 * WROW), sx = cc * 128;
          if (j < 4) dma16(srdX, (j & 1) ? vx1 : vx0, sx01 + j * 8 * XROW + sx, sl + (wl * 4 + j) * 1024);
          else dma16(srdW, j < 6 ? vw0 : v2, sw + ((j & 1) + 1) * XROW, sl + B_W + (j & 1) * WM_B + (j < 6 ? wl * 1024 : sub2));
        } else {
          const int c = (n - 56) >> 1;
          dma16(srdW, (n & 1) ? v2 : vw0, wrap7(c + rot) * (64 * WROW), L0 + B_WQ + c * WM_B + ((n & 1) ? sub2 : wl * 1024));
        }
      };
      STAMP(1);
      bf16x8 xq[14][2];
      const unsigned xl = L0 + (wl * 32 + li) * 128 + ((g ^ (li >> 1)) << 4);
#pragma unroll
      for (int c = 0; c < 7; ++c) {
        if (c < 6) vm_bar<8>(); else vm_bar<4>();
#pragma unroll
        for (int ks = 0; ks < 2; ++ks) {
          const unsigned xa = (xl ^ (ks * 64)) + b_slot(c);
          xq[c * 2 + ks][0] = as_frag(lds_ld128(xa));
          xq[c * 2 + ks][1] = as_frag(lds_ld128(xa + 2048));
        }
#pragma unroll
        for (int n = (c < 6 ? 8 * c + 16 : 56 + 4); n < (c < 5 ? 8 * c + 24 : (c == 5 ? 56 + 4 : 70)); ++n) instr(n);
      }
      STAMP(4);
      VM_WAIT(0);
      asm volatile("" ::"v"(gv), "v"(epoch), "v"(pt));
      sig(L0 + SYNC + SY_WQ + wl * 4);
      wait4(L0 + SYNC + SY_WQ);
      wait4(L0 + SYNC + SY_EX);
      STAMP(3);
      f32x4 aq[2][2][2];
#pragma unroll
      for (int i = 0; i < 8; ++i) aq[i >> 2][(i >> 1) & 1][i & 1] = (f32x4){kEps, kEps, kEps, kEps};
      const unsigned wb = L0 + B_WQ + (8 * g + 2 * q) * 112 + 8 * p;
      const unsigned gl = L0 + B_G + (8 * g + q) * 8;
      const bool p3 = p == 3;
      const unsigned d1 = p3 ? 32u : 112u;
      const unsigned qa0 = opaque(p3 ? gl : wb + 32), qstep = opaque(p3 ? 256u : 3584u);
      bf16x8 fq[3][4];
      auto rdq = [&](int s) {
        const unsigned wk = wb + s * 3584;
        const unsigned a = qa0 + s * qstep;
        fq[s % 3][0] = lds_tr2(wk, wk + 112);
        fq[s % 3][1] = lds_tr2(a, a + d1);
        fq[s % 3][2] = lds_tr2(wk + 56, wk + 56 + 112);
        fq[s % 3][3] = lds_tr2(wk + 88, wk + 88 + 112);
      };
      rdq(0);
      rdq(1);
#pragma unroll
      for (int s = 0; s < 14; ++s) {
        SB();
        if (s + 2 < 14) rdq(s + 2);
#pragma unroll
        for (int i = 0; i < 4; ++i) {
          aq[0][i >> 1][i & 1] = mfma(fq[s % 3][i], xq[s][0], aq[0][i >> 1][i & 1]);
          aq[1][i >> 1][i & 1] = mfma(fq[s % 3][i], xq[s][1], aq[1][i >> 1][i & 1]);
        }
        interleave<8, 8>();
      }
      sig(L0 + SYNC + SY_QD + wl * 4);
      STAMP(5);
      u32x4 pqf[2][2];
      float omg[2];
#pragma unroll
      for (int t = 0; t < 2; ++t) {
        omg[t] = 1.f - sigmoidf(row3_bcast(aq[t][0][1][0]));
#pragma unroll
        for (int h = 0; h < 2; ++h) {
          const u32x2 a = phi4(aq[t][h][0]), b = phi4(aq[t][h][1]);
          pqf[t][h] = (u32x4){a[0], a[1], g3 ? 0u : b[0], g3 ? 0u : b[1]};
        }
      }
      tagO = uniform64(*(LAS u64*)(uintptr_t)(L0 + SYNC + SY_TAG));
      unsigned ob = WS_O + ((rt * 8 + wl * 2) * 32 + 16 + hp * 2) * 1024 + lane * 16;
      unsigned kb = L0 + B_KVL + lane * 16, zb = L0 + B_KVL + 1024 + (g * 16 + 12) * 16;
      asm volatile("" : "+v"(pqf[0][0]), "+v"(pqf[0][1]), "+v"(pqf[1][0]), "+v"(pqf[1][1]), "+v"(omg[0]), "+v"(omg[1]), "+v"(ob),
                   "+v"(kb), "+v"(zb), "+s"(tagO));
      __builtin_amdgcn_s_setprio(2);
      STAMP(6);
      wait4(L0 + SYNC + SY_KV);
      STAMP(8);
      {
        bf16x8 kv0[2], kv1[2], zf[2];
#pragma unroll
        for (int h = 0; h < 2; ++h) zf[h] = as_frag(lds_ld128(zb + h * 2048));
#pragma unroll
        for (int h = 0; h < 2; ++h) {
          kv0[h] = as_frag(lds_ld128(kb + h * 2048));
          kv1[h] = as_frag(lds_ld128(kb + h * 2048 + 1024));
        }
        SB();
        f32x4 dn[4], n0[4], n1[4];
#pragma unroll
        for (int i = 0; i < 4; ++i) dn[i] = mfma(zf[i >> 1], as_frag(pqf[i & 1][i >> 1]), (f32x4){kEps, kEps, kEps, kEps});
#pragma unroll
        for (int i = 0; i < 4; ++i) {
          n0[i] = mfma(kv0[i >> 1], as_frag(pqf[i & 1][i >> 1]), z4);
          n1[i] = mfma(kv1[i >> 1], as_frag(pqf[i & 1][i >> 1]), z4);
        }
        SB();
#pragma unroll
        for (int i = 0; i < 4; ++i) {
          const float s = omg[i & 1] * __builtin_amdgcn_rcpf(dn[i][0]);
          const u32x2 a = pk4(n0[i] * s), b = pk4(n1[i] * s);
          __builtin_amdgcn_raw_buffer_store_b128((u32x4){a[0], a[1], g3 ? 0u : b[0], b[1]}, wsr, ob + ((i & 1) * 32 + (i >> 1)) * 1024, 0, 16);
        }
      }
      STAMP(9);
      VM_WAIT(0);
      if (lane == 0) __hip_atomic_store((gu64*)(ws + WS_FB) + ((rt * 4 + wl) * 8 + hp) * FS, tagO, RLX_AGENT);
      STAMP(10); STAMPC(19);
      if (cg >= 7) { STAMP_FLUSH(); return; }
    }
  } else if (__builtin_expect(br, 1)) {
      __builtin_amdgcn_s_setprio(2);
      setup(B_G);
      f32x4 ak[4][2], av[4][2];
      {
        const float one = li == 12 ? 1.f : 0.f, i0 = rh ? one : 0.f, i1 = rh ? 0.f : one;
#pragma unroll
        for (int t = 0; t < 4; ++t) {
          ak[t][0] = ak[t][1] = (f32x4){kEps, kEps, kEps, kEps};
          av[t][0] = (f32x4){i0, i0, i0, i0};
          av[t][1] = (f32x4){i1, i1, i1, i1};
        }
      }
      bf16x8 xf[2][4], fw[2][4];
      unsigned xb, wb;
      const bool z0 = p == 3 && rh, z1 = p == 3 && !rh;
      const unsigned zaddr = L0 + SYNC + SY_ZERO;
      auto rd = [&](int ks, int part) {
        const unsigned xa = xb ^ (ks * 64), wk = wb + ks * 3584;
        if (part & 1) {
          const unsigned a = wk + WM_B + (((part >> 1) ^ rh) << 5);
          const bool z = (part >> 1) ? z1 : z0;
          fw[ks][part] = lds_tr2(z ? zaddr : a, z ? zaddr : a + 112);
        } else {
          fw[ks][part] = lds_tr2(wk + (part >> 1) * 32, wk + (part >> 1) * 32 + 112);
        }
        if (part < 2) {
          xf[ks][part * 2] = as_frag(lds_ld128(xa + part * 4096));
          xf[ks][part * 2 + 1] = as_frag(lds_ld128(xa + part * 4096 + 2048));
        }
      };
      auto mm = [&](int ks, int part) {
#pragma unroll
        for (int t = 0; t < 4; ++t) {
          if (part & 1) av[t][part >> 1] = mfma(xf[ks][t], fw[ks][part], av[t][part >> 1]);
          else ak[t][part >> 1] = mfma(xf[ks][t], fw[ks][part], ak[t][part >> 1]);
        }
      };
#pragma unroll
      for (int c = 0; c < 7; ++c) {
        xb = opaque(L0 + b_slot(c) + rh * 8192 + xsw);
        wb = opaque(L0 + b_slot(c) + B_W + wln);
        hook(c, true, true);
        LDS_BAR();
        hook(c, false, true);
        if (c == 0) { STAMP(2); STAMPC(16); }
        if (c == 3) STAMP(3);
        if (c == 6) { STAMP(4); STAMPC(17); }
        SB();
#pragma unroll
        for (int ks = 0; ks < 2; ++ks) {
#pragma unroll
          for (int part = 0; part < 4; ++part) rd(ks, part);
          if (ks == 1 || c > 0) {
#pragma unroll
            for (int part = 0; part < 4; ++part) mm(ks ^ 1, part);
            interleave<16, 12>();
          }
          SB();
        }
      }
      sig(L0 + SYNC + SY_D6 + w * 4);
      mm(1, 0); mm(1, 2);
      SB();
      mm(1, 3); mm(1, 1);
      STAMP(5);
      f32x4 kv[2][2];
      {
        u32x2 pk[4][2], vf[4][2];
#pragma unroll
        for (int t = 0; t < 4; ++t) {
          pk[t][0] = phi4(ak[t][0]);
          pk[t][1] = phi4(ak[t][1]);
          vf[t][0] = pk4(av[t][0]);
          vf[t][1] = pk4(av[t][1]);
        }
#pragma unroll
        for (int e = 1; e >= 0; --e)
#pragma unroll
          for (int d = 0; d < 2; ++d) {
            kv[e][d] = mfma(frag2(pk[0][d], pk[1][d]), frag2(vf[0][e], vf[1][e]), z4);
            kv[e][d] = mfma(frag2(pk[2][d], pk[3][d]), frag2(vf[2][e], vf[3][e]), kv[e][d]);
          }
      }
      lds_st128(L0 + B_EX + w * 2048 + lane * 16, __builtin_bit_cast(u32x4, kv[1][0]));
      lds_st128(L0 + B_EX + w * 2048 + 1024 + lane * 16, __builtin_bit_cast(u32x4, kv[1][1]));
      sig(L0 + SYNC + SY_EX + w * 4);
      wait1(L0 + SYNC + SY_EX + (w ^ 1) * 4);
      const f32x4 s0 = kv[0][0] + __builtin_bit_cast(f32x4, lds_ld128(L0 + B_EX + (w ^ 1) * 2048 + lane * 16));
      const f32x4 s1 = kv[0][1] + __builtin_bit_cast(f32x4, lds_ld128(L0 + B_EX + (w ^ 1) * 2048 + 1024 + lane * 16));
      {
        const u32x2 a = pk4(s0), b = pk4(s1);
        publish((u32x4){a[0], a[1], b[0], b[1]}, wsr, lane * 16 + kvs + rt * 1024);
      }
      STAMP(6);
      if (!__all(clr == tagO)) poll_flags(clrp(), tagO);
      wo_run(0, QWO);
      STAMP(7);
      f32x4 sum0, sum1;
      {
        const unsigned e0 = q == g ? ((p & 1) ? 0x3f800000u : 0x3f80u) : 0u;
        const unsigned ea = p < 2 ? e0 : 0u, eb = p < 2 ? 0u : e0;
        const bf16x8 sel0 = as_frag((u32x4){ea, eb, 0u, 0u}), sel1 = as_frag((u32x4){0u, 0u, ea, eb});
        unsigned vo = kvs + lane * 16;
#pragma nounroll
        for (unsigned spins = 0; spins < SPIN_CAP; ++spins) {
          asm volatile("" : "+v"(vo)::"memory");
          u32x4 pr[16];
#pragma unroll
          for (int r = 0; r < 16; ++r) pr[r] = __builtin_amdgcn_raw_buffer_load_b128(wsr, vo, ((r + 2 * rt) & 15) * 1024, 16);
          SB();
          sum0 = sum1 = z4;
#pragma unroll
          for (int r = 0; r < 16; ++r) {
            sum0 = mfma(sel0, as_frag(pr[r]), sum0);
            sum1 = mfma(sel1, as_frag(pr[r]), sum1);
          }
          if (!__any(sum0[0] != sum0[0])) break;
          unsigned m = 0;
#pragma unroll
          for (int r = 0; r < 16; ++r) m = max(max(max(max(m, pr[r][0]), pr[r][1]), pr[r][2]), pr[r][3]);
          if (!__any(m == SENT)) break;
        }
      }
      asm volatile("" ::"v"(pw));
      STAMP(8);
      lds_st128(L0 + B_KVL + w * 1024 + lane * 16, __builtin_bit_cast(u32x4, frag2(pk4(sum0), pk4(sum1))));
      sig(L0 + SYNC + SY_KV + w * 4);
      STAMP(10); STAMPC(19);
      __builtin_amdgcn_s_setprio(0);
      if (cg >= 7) { STAMP_FLUSH(); return; }
      sig(L0 + SYNC + SY_WOA + w * 4);
      wait4(L0 + SYNC + SY_D6);
      wait4(L0 + SYNC + SY_QD);
      wo_run(QWO, 28);
  } else {
      asm volatile("; role A");
      setup(A_G);
      f32x4 aq[4][2], ak[5][2], av[5][2];
#pragma unroll
      for (int t = 0; t < 5; ++t)
#pragma unroll
        for (int f = 0; f < 2; ++f) { ak[t][f] = av[t][f] = z4; if (t < 4) aq[t][f] = z4; }
      const unsigned gl = L0 + A_G + (8 * g + q) * 8;
      const bool p3 = p == 3;
      const unsigned d1 = p3 ? 32u : 112u;
      bf16x8 xf[2][5], fq[2][2], fk[2][2], fv[2][2];
      unsigned xb, wb;
      auto rd = [&](int c, int ks, int part) {
        const unsigned xa = xb ^ (ks * 64), wk = wb + ks * 3584;
        if (part == 0) {
          fk[ks][0] = lds_tr2(wk + WM_B, wk + WM_B + 112);
          xf[ks][0] = as_frag(lds_ld128(xa));
          xf[ks][1] = as_frag(lds_ld128(xa + 2048));
        } else if (part == 1) {
          fv[ks][0] = lds_tr2(wk + 2 * WM_B, wk + 2 * WM_B + 112);
          xf[ks][2] = as_frag(lds_ld128(xa + 4096));
        } else if (part == 2) {
          fq[ks][0] = lds_tr2(wk, wk + 112);
          xf[ks][3] = as_frag(lds_ld128(xa + 6144));
        } else if (part == 3) {
          fk[ks][1] = lds_tr2(wk + WM_B + 32, wk + WM_B + 32 + 112);
          xf[ks][4] = as_frag(lds_ld128(xa + 8192));
        } else if (part == 4) {
          fv[ks][1] = lds_tr2(wk + 2 * WM_B + 32, wk + 2 * WM_B + 32 + 112);
        } else {
          const unsigned a = p3 ? gl + c * 512 + ks * 256 : wk + 32;
          fq[ks][1] = lds_tr2(a, a + d1);
        }
      };
      auto mm = [&](int ks, int part) {
        const int f = part / 3, m = part % 3;
#pragma unroll
        for (int t = 0; t < 5; ++t) {
          if (m == 0) ak[t][f] = mfma(fk[ks][f], xf[ks][t], ak[t][f]);
          if (m == 1) av[t][f] = mfma(xf[ks][t], fv[ks][f], av[t][f]);
          if (m == 2 && t > 0) aq[t - 1][f] = mfma(fq[ks][f], xf[ks][t], aq[t - 1][f]);
        }
      };
#pragma unroll
      for (int c = 0; c < 7; ++c) {
        xb = opaque(L0 + a_slot(c) + rh * 8192 + xsw);
        wb = opaque(L0 + a_slot(c) + A_W + wln);
        hook(c, true, false);
        LDS_BAR();
        hook(c, false, false);
        if (c == 0) { STAMP(2); STAMPC(16); }
        if (c == 3) STAMP(3);
        if (c == 6) { STAMP(4); STAMPC(17); }
        SB();
#pragma unroll
        for (int ks = 0; ks < 2; ++ks) {
#pragma unroll
          for (int part = 0; part < 6; ++part) rd(c, ks, part);
          if (ks == 1 || c > 0) {
#pragma unroll
            for (int part = 0; part < 6; ++part) mm(ks ^ 1, part);
            interleave<28, 17>();
          }
          SB();
        }
      }
      sig(L0 + SYNC + SY_D6 + w * 4);
      mm(1, 0); mm(1, 3); mm(1, 2); mm(1, 5);
      SB();
      mm(1, 1); mm(1, 4);
      STAMP(5);
      const bool odd = li & 1;
      const int kk0 = 4 * g + (li & 1);
      const bool own0 = kk0 <= li, own1 = kk0 + 2 <= li;
      const float sc = 0.18898223650461362f * kLog2e;
      const float ninf = (rt == 0 && rh == 0) ? -__builtin_inff() : 0.f;
      const unsigned l16 = lane_id() * 16;
      bf16x8 kf[5];
#pragma unroll
      for (int t = 0; t < 5; ++t) kf[t] = frag2(pk4(ak[t][0]), pk4(ak[t][1]));
      f32x4 sp[4], so[4];
#pragma unroll
      for (int t = 0; t < 4; ++t) {
        const u32x2 b = pk4(aq[t][1]);
        const bf16x8 qf = frag2(pk4(aq[t][0]), (u32x2){g3 ? 0u : b[0], g3 ? 0u : b[1]});
        sp[t] = mfma(kf[t], qf, t == 0 ? (f32x4){ninf, ninf, ninf, ninf} : z4);
        so[t] = mfma(kf[t + 1], qf, z4);
      }
      float ug;
      {
        float z0 = aq[0][1][0], z1 = aq[1][1][0], z2 = aq[2][1][0], z3 = aq[3][1][0];
        swap32(z0, z1);
        swap32(z2, z3);
        swap16(z1, z3);
        ug = 1.f + __builtin_amdgcn_exp2f(-kLog2e * z3);
      }
      u32x2 vf[5][2];
#pragma unroll
      for (int t = 0; t < 5; ++t) {
        vf[t][0] = pk4(av[t][0]);
        vf[t][1] = pk4(av[t][1]);
      }
      float l0[4], l1[4], bc[4];
#pragma unroll
      for (int t = 0; t < 4; ++t) {
        float a0 = so[t][0], a2 = so[t][2], b0 = sp[t][0], b2 = sp[t][2];
        const float c0 = own0 ? a0 : b0, c1 = own0 ? so[t][1] : sp[t][1], c2 = own1 ? a2 : b2, c3 = own1 ? so[t][3] : sp[t][3];
        l0[t] = odd ? c1 : c0;
        l1[t] = odd ? c3 : c2;
      }
      rows_bcast4(rows_reduce4<true>(fmax1(l0[0], l1[0]), fmax1(l0[1], l1[1]), fmax1(l0[2], l1[2]), fmax1(l0[3], l1[3])) * -sc, bc);
#pragma unroll
      for (int t = 0; t < 4; ++t) {
        l0[t] = __builtin_amdgcn_exp2f(__builtin_fmaf(l0[t], sc, bc[t]));
        l1[t] = __builtin_amdgcn_exp2f(__builtin_fmaf(l1[t], sc, bc[t]));
      }
      rows_bcast4(__builtin_amdgcn_rcpf(rows_reduce4<false>(l0[0] + l1[0], l0[1] + l1[1], l0[2] + l1[2], l0[3] + l1[3]) * ug), bc);
      const unsigned sh = odd ? 16u : 0u;
      const unsigned mk0 = own0 ? 0u : 0xffffu, mk1 = own1 ? 0u : 0xffffu;
#pragma unroll
      for (int t = 0; t < 4; ++t) {
        const unsigned pe = pk2(l0[t] * bc[t], l1[t] * bc[t]), ph = pe >> 16;
        const bf16x8 pf = as_frag((u32x4){(pe & mk0) << sh, (ph & mk1) << sh, (pe & (mk0 ^ 0xffffu)) << sh, (ph & (mk1 ^ 0xffffu)) << sh});
        const f32x4 r0 = mfma(frag2(vf[t][0], vf[t + 1][0]), pf, z4);
        const f32x4 r1 = mfma(frag2(vf[t][1], vf[t + 1][1]), pf, z4);
        const u32x2 c0 = pk4(r0), c1 = pk4(r1);
        __builtin_amdgcn_raw_buffer_store_b128((u32x4){c0[0], c0[1], g3 ? 0u : c1[0], g3 ? 0u : c1[1]}, wsr, l16 + obs + t * 32768, 0, 16);
      }
      STAMP(9);
      VM_WAIT(0);
      asm volatile("" ::"v"(pw));
      if (l16 == 0) __hip_atomic_store((gu64*)(ws + WS_FA) + ((rt * 2 + rh) * 16 + hp * 2 + hh) * FS, tagO, RLX_AGENT);
      STAMP(10); STAMPC(19);
      if (cg >= 7) {
        if (bid == 56 && w == 0) {
#pragma nounroll
          for (int k = 0; k < 4; ++k) {
            const int i = (k & 1) * 64 + lane;
            poll_flags(k < 2 ? (gu64*)(ws + WS_CLR) + i : (gu64*)(ws + WS_FA) + ((i >> 3) * 32 + (i & 7) * 2) * FS, tagO);
          }
          if (lane == 0) __hip_atomic_store((gu64*)(ws + WS_E), tagO, RLX_AGENT);
        }
        STAMP_FLUSH();
        return;
      }
  }

  const int pb = w >> 2;
  wait4(L0 + SYNC + (pb ? SY_WOB : SY_WOA));
  const unsigned sw = (2 * g + (q >> 1)) & 3;
  const unsigned rb = L0 + (pb * 448 + 4 * g + q) * 128 + 8 * p;
  unsigned wa[4], wa2[4];
#pragma unroll
  for (int v = 0; v < 4; ++v) {
    wa[v] = opaque(rb + ((v ^ sw) << 5));
    wa2[v] = opaque(wa[v] + (g3 ? 0u : 2048u));
  }
  auto wfrag = [&](int h, int ct) {
    const int v = ct ^ (2 * (h & 1));
    return lds_tr2(wa[v] + h * 3584, wa2[v] + h * 3584);
  };
  constexpr int NPRE = 8;
  bf16x8 wf[16][4];
#pragma unroll
  for (int h = 0; h < NPRE; ++h)
#pragma unroll
    for (int ct = 0; ct < 4; ++ct) wf[h][ct] = wfrag(h, ct);
  STAMP(11);
  const unsigned ub = WS_O + ((rg * 4 + wl) * 32 + pb * 16) * 1024;
  tagO = uniform64(*(LAS u64*)(uintptr_t)(L0 + SYNC + SY_TAG));
  {
    const gu64* fp = pb ? (gu64*)(ws + WS_FB) + ((rg * 2 + (wl >> 1)) * 8 + (li & 7)) * FS : (gu64*)(ws + WS_FA) + (rg * 16 + li) * FS;
    const u64 f0 = __hip_atomic_load(fp, RLX_AGENT);
    if (br && !pb) { VM_WAIT(0); sig(L0 + SYNC + SY_WOB + wl * 4); }
    if (!__all(f0 == tagO)) poll_flags(fp, tagO);
  }
  STAMP(12);
  u32x4 of[16];
  {
    unsigned vo = ub + lane * 16;
    asm volatile("" : "+v"(vo)::"memory");
#pragma unroll
    for (int h = 0; h < 16; ++h) of[h] = __builtin_amdgcn_raw_buffer_load_b128(wsr, vo, ((h + 2 * cg) & 15) * 1024, 16);
    SB();
  }
  f32x4 acc[4] = {z4, z4, z4, z4};
#pragma unroll
  for (int h = 0; h < 16; ++h) {
#pragma unroll
    for (int ct = 0; ct < 4; ++ct) acc[ct] = mfma(wf[h][ct], as_frag(of[h]), acc[ct]);
    SB();
    if (h + NPRE < 16) {
#pragma unroll
      for (int ct = 0; ct < 4; ++ct) wf[h + NPRE][ct] = wfrag(h + NPRE, ct);
    }
    SB();
  }
  STAMP(13);
  const unsigned xa = L0 + P2X + wl * 4096 + lane * 16;
  if (!pb) {
    wait4(L0 + SYNC + SY_D6);
#pragma unroll
    for (int ct = 0; ct < 4; ++ct) lds_st128(xa + ct * 1024, __builtin_bit_cast(u32x4, acc[ct]));
    sig(L0 + SYNC + SY_P2 + wl * 4);
  } else {
    f32x4 pa[4];
    for (;;) {
      const unsigned f = *(volatile LAS unsigned*)(uintptr_t)(L0 + SYNC + SY_P2 + wl * 4);
#pragma unroll
      for (int ct = 0; ct < 4; ++ct) pa[ct] = *(volatile LAS f32x4*)(uintptr_t)(xa + ct * 1024);
      if (__builtin_amdgcn_readfirstlane(f)) break;
      __builtin_amdgcn_s_sleep(1);
    }
    char* orow = out + (rg * 64 + wl * 16 + li) * XROW + cg * 128 + (g & 1) * 32 + (g >> 1) * 16;
#pragma unroll
    for (int k = 0; k < 2; ++k) {
      const u32x2 a = pk4(acc[2 * k] + pa[2 * k]), b = pk4(acc[2 * k + 1] + pa[2 * k + 1]);
      const auto r0 = __builtin_amdgcn_permlane16_swap(a[0], b[0], false, false);
      const auto r1 = __builtin_amdgcn_permlane16_swap(a[1], b[1], false, false);
      *(u32x4*)(orow + k * 64) = (u32x4){r0[0], r1[0], r0[1], r1[1]};
    }
  }
  STAMP(14);
  STAMP(15);
  STAMP_FLUSH();
}

}

extern "C" void kernel_launch(void* const* d_in, const int* in_sizes, int n_in, void* d_out, int out_size, void* d_ws,
                              size_t ws_size, hipStream_t stream) {
  (void)in_sizes; (void)n_in; (void)out_size; (void)ws_size;
  gated_mix_kernel<<<256, 512, 0, stream>>>((const char*)d_in[0], (const char*)d_in[1], (const char*)d_in[2],
                                            (const char*)d_in[4], (const char*)d_in[5],
                                            (const unsigned short*)d_in[6], (char*)d_out, (char*)d_ws);
}
```

```cpp
#include <hip/hip_runtime.h>
#include <stdint.h>

typedef __bf16 bf16x2 __attribute__((ext_vector_type(2)));
typedef __bf16 bf16x4 __attribute__((ext_vector_type(4)));
typedef __bf16 bf16x8 __attribute__((ext_vector_type(8)));
typedef float f32x2 __attribute__((ext_vector_type(2)));
typedef float f32x4 __attribute__((ext_vector_type(4)));
typedef unsigned u32x2 __attribute__((ext_vector_type(2)));
typedef unsigned u32x4 __attribute__((ext_vector_type(4)));
typedef unsigned long long u64;
#define INL __attribute__((always_inline))
#define LAS __attribute__((address_space(3)))
#define GAS __attribute__((address_space(1)))
typedef GAS u64 gu64;
#define RLX_AGENT __ATOMIC_RELAXED, __HIP_MEMORY_SCOPE_AGENT

namespace {

constexpr int kS = 2048, kD = 448;
constexpr int XROW = kD * 2;
constexpr int WROW = 3 * kD * 2;
constexpr int WM_B = 64 * 112;

constexpr int A_FRONT = 34816, A_SLOT = 144 * 128 + 3 * WM_B, A_W = 144 * 128;
constexpr int A_G = A_FRONT + 3 * A_SLOT;
constexpr int B_SLOT = 128 * 128 + 2 * WM_B, B_W = 128 * 128;
constexpr int B_WQ = 97536, B_G = B_WQ + 7 * WM_B, B_EX = B_G + 3584, B_KVL = B_EX + 8192;
constexpr int P2X = 114688;
constexpr int SYNC = 163584;
constexpr int LDS_B = 163840;
static_assert(A_G + 3584 <= SYNC && B_KVL + 4096 <= SYNC && A_FRONT + 2 * A_SLOT == 114688, "lds map");
__host__ __device__ constexpr int a_slot(int c) { return A_FRONT + ((c + 2) % 3) * A_SLOT; }
__host__ __device__ constexpr int b_slot(int c) { return ((c + 2) % 3) * B_SLOT; }
enum { SY_WOA = 0, SY_WOB = 16, SY_D6 = 32, SY_D6Q = 48, SY_WQ = 64, SY_QD = 80, SY_EX = 96, SY_KV = 112, SY_P2 = 128, SY_ZERO = 192, SY_TAG = 240 };

constexpr int WS_E = 0;
constexpr int WS_CLR = 4096;
constexpr int FS = 16;
constexpr int WS_FA = 8192;
constexpr int WS_FB = WS_FA + 16 * 2 * 16 * 8 * FS;
constexpr int WS_KVP = WS_FB + 16 * 4 * 8 * 8 * FS;
constexpr int WS_O = WS_KVP + 8 * 4 * 16 * 1024;
constexpr int WS_END = WS_O + 16 * 8 * 32 * 1024;

constexpr int PT_WS = (WS_END / 8) & ~127;
constexpr unsigned SPIN_CAP = 1u << 14;
constexpr unsigned SENT = 0xffffffffu;
#define QWO 14

#define STAMP(i) do {} while (0)
#define STAMPC(i) do {} while (0)
#define STAMP_FLUSH() do {} while (0)

__device__ __forceinline__ unsigned pk2(float a, float b) {
  f32x2 v = {a, b};
  bf16x2 r = __builtin_convertvector(v, bf16x2);
  return __builtin_bit_cast(unsigned, r);
}
__device__ __forceinline__ u32x2 pk4(f32x4 v) { return (u32x2){pk2(v[0], v[1]), pk2(v[2], v[3])}; }
__device__ __forceinline__ bf16x8 as_frag(u32x4 v) { return __builtin_bit_cast(bf16x8, v); }
__device__ __forceinline__ bf16x8 frag2(u32x2 a, u32x2 b) { return as_frag((u32x4){a[0], a[1], b[0], b[1]}); }
__device__ __forceinline__ f32x4 mfma(bf16x8 a, bf16x8 b, f32x4 c) {
  return __builtin_amdgcn_mfma_f32_16x16x32_bf16(a, b, c, 0, 0, 0);
}
__device__ __forceinline__ bf16x8 lds_tr2(unsigned a0, unsigned a1) {
  bf16x4 lo = __builtin_amdgcn_ds_read_tr16_b64_v4bf16((LAS bf16x4*)(uintptr_t)a0);
  bf16x4 hi = __builtin_amdgcn_ds_read_tr16_b64_v4bf16((LAS bf16x4*)(uintptr_t)a1);
  return __builtin_shufflevector(lo, hi, 0, 1, 2, 3, 4, 5, 6, 7);
}
__device__ __forceinline__ u32x4 lds_ld128(unsigned a) { return *(LAS u32x4*)(uintptr_t)a; }
__device__ __forceinline__ void lds_st128(unsigned a, u32x4 v) { *(LAS u32x4*)(uintptr_t)a = v; }
__device__ __forceinline__ void lds_st64(unsigned a, u32x2 v) { *(LAS u32x2*)(uintptr_t)a = v; }
__device__ __forceinline__ unsigned opaque(unsigned v) { asm volatile("" : "+v"(v)); return v; }

__device__ __forceinline__ void sig(unsigned a) {
  asm volatile("" ::: "memory");
  *(volatile LAS unsigned*)(uintptr_t)a = 1u;
  asm volatile("" ::: "memory");
  __builtin_amdgcn_sched_barrier(0);
}
__device__ __forceinline__ void wait1(unsigned a) {
  while (__builtin_amdgcn_readfirstlane(*(volatile LAS unsigned*)(uintptr_t)a) == 0u) __builtin_amdgcn_s_sleep(1);
  asm volatile("" ::: "memory");
}
__device__ __forceinline__ void wait4(unsigned a) {
  for (;;) {
    const u32x4 v = *(volatile LAS u32x4*)(uintptr_t)a;
    if (__builtin_amdgcn_readfirstlane(v[0] & v[1] & v[2] & v[3])) break;
    __builtin_amdgcn_s_sleep(1);
  }
  asm volatile("" ::: "memory");
}

__device__ __forceinline__ u32x4 make_srd(const void* p, unsigned bytes) {
  uint64_t a = (uint64_t)p;
  u32x4 r;
  r[0] = __builtin_amdgcn_readfirstlane((unsigned)a);
  r[1] = __builtin_amdgcn_readfirstlane((unsigned)(a >> 32) & 0xffffu);
  r[2] = bytes;
  r[3] = 0x00020000u;
  return r;
}
__device__ __forceinline__ void dma16(u32x4 srd, unsigned voff, unsigned soff, unsigned ldsaddr) {
  asm volatile(
      "s_mov_b32 m0, %3\n\t"
      "s_nop 3\n\t"
      "buffer_load_dwordx4 %0, %1, %2 offen lds"
      :
      : "v"(voff), "s"(srd), "s"(soff), "s"(ldsaddr));
}
#define VM_BAR(N) asm volatile("s_waitcnt vmcnt(" #N ") lgkmcnt(0)\n\ts_barrier" ::: "memory")
#define LDS_BAR() asm volatile("s_waitcnt lgkmcnt(0)\n\ts_barrier" ::: "memory")
#define VM_WAIT(N) asm volatile("s_waitcnt vmcnt(" #N ")" ::: "memory")
template <int N> __device__ __forceinline__ void vm_bar() { asm volatile("s_waitcnt vmcnt(%0) lgkmcnt(0)\n\ts_barrier" ::"n"(N) : "memory"); }
#define SB() __builtin_amdgcn_sched_barrier(0)

__device__ __forceinline__ float fmax1(float a, float b) { return __builtin_amdgcn_fmed3f(a, b, 3.0e38f); }
__device__ __forceinline__ void swap16(float& a, float& b) {
  auto r = __builtin_amdgcn_permlane16_swap(__float_as_uint(a), __float_as_uint(b), false, false);
  const unsigned r0 = r[0], r1 = r[1];
  a = __uint_as_float(r0);
  b = __uint_as_float(r1);
}
__device__ __forceinline__ void swap32(float& a, float& b) {
  auto r = __builtin_amdgcn_permlane32_swap(__float_as_uint(a), __float_as_uint(b), false, false);
  const unsigned r0 = r[0], r1 = r[1];
  a = __uint_as_float(r0);
  b = __uint_as_float(r1);
}
template <bool MAX>
__device__ __forceinline__ float rows_reduce4(float v0, float v1, float v2, float v3) {
  swap32(v0, v1);
  swap32(v2, v3);
  float a = MAX ? fmax1(v0, v1) : v0 + v1, b = MAX ? fmax1(v2, v3) : v2 + v3;
  swap16(a, b);
  return MAX ? fmax1(a, b) : a + b;
}
__device__ __forceinline__ void rows_bcast4(float x, float (&o)[4]) {
  float y = x;
  swap16(x, y);
  o[0] = x; o[1] = x; o[2] = y; o[3] = y;
  swap32(o[0], o[1]);
  swap32(o[2], o[3]);
}
__device__ __forceinline__ float row3_bcast(float v) {
  float a = v;
  swap32(a, v);
  a = v;
  swap16(a, v);
  return v;
}
template <int NM, int ND>
__device__ __forceinline__ void interleave() {
#pragma unroll
  for (int i = 0; i < NM; ++i) {
    __builtin_amdgcn_sched_group_barrier(0x8, 1, 0);
    if (i < ND) {
      __builtin_amdgcn_sched_group_barrier(0x2, 1, 0);
      __builtin_amdgcn_sched_group_barrier(0x100, 1, 0);
    }
  }
}
constexpr float kEps = 1e-6f, kLog2e = 1.44269504088896f;
__device__ __forceinline__ float phi(float u) {
  const float e = __builtin_amdgcn_fmed3f(__builtin_amdgcn_exp2f(__builtin_fmaf(u, kLog2e, -kEps * kLog2e)), 0.f, 1.f);
  return fmax1(u, kEps) + e;
}
__device__ __forceinline__ u32x2 phi4(f32x4 v) { return (u32x2){pk2(phi(v[0]), phi(v[1])), pk2(phi(v[2]), phi(v[3]))}; }
__device__ __forceinline__ float sigmoidf(float z) {
  return __builtin_amdgcn_rcpf(1.f + __builtin_amdgcn_exp2f(-1.44269504088896f * z));
}
__device__ __forceinline__ u64 next_tag(u64 e) {
  const u64 s = ((u64)(unsigned)__builtin_amdgcn_readfirstlane((unsigned)(e >> 32)) << 32) |
                (unsigned)__builtin_amdgcn_readfirstlane((unsigned)e);
  return s * 6364136223846793005ull + 1442695040888963407ull;
}
__device__ __forceinline__ u64 uniform64(u64 e) {
  return ((u64)(unsigned)__builtin_amdgcn_readfirstlane((unsigned)(e >> 32)) << 32) |
         (unsigned)__builtin_amdgcn_readfirstlane((unsigned)e);
}
__device__ __forceinline__ void poll_flags(const gu64* p, u64 tag) {
#pragma nounroll
  for (unsigned spins = 0; spins < SPIN_CAP; ++spins) {
    u64 v = __hip_atomic_load(p, RLX_AGENT);
    if (__all(v == tag)) break;
    __builtin_amdgcn_s_sleep(2);
  }
  asm volatile("" ::: "memory");
}
__device__ __forceinline__ void publish(u32x4 v, __amdgpu_buffer_rsrc_t r, unsigned voff) {
#pragma unroll
  for (int i = 0; i < 4; ++i) v[i] = v[i] < SENT - 1u ? v[i] : SENT - 1u;
  __builtin_amdgcn_raw_buffer_store_b128(v, r, voff, 0, 16);
}
__device__ __forceinline__ unsigned lane_id() {
  unsigned l = __builtin_amdgcn_mbcnt_hi(~0u, __builtin_amdgcn_mbcnt_lo(~0u, 0u));
  asm volatile("" : "+v"(l));
  return l;
}
__device__ __forceinline__ int wrap7(int c) { return c >= 7 ? c - 7 : c; }

__global__ __launch_bounds__(512, 2) void gated_mix_kernel(const char* __restrict__ x, const char* __restrict__ wqkvA,
                                                           const char* __restrict__ woA, const char* __restrict__ wqkvB,
                                                           const char* __restrict__ woB,
                                                           const unsigned short* __restrict__ gate_w, char* __restrict__ out,
                                                           char* ws) {
  __shared__ __attribute__((aligned(1024))) char lds[LDS_B];
  const unsigned L0 = (unsigned)(uintptr_t)(LAS char*)lds;
  const int bid = blockIdx.x;
  const int xcd = bid & 7, idx = bid >> 3;
  const int xj = xcd >> 1, ci = idx >> 3;
  const int br = ci >> 1;
  const int hp = 2 * (br ? (xj ^ 1) : xj) + (ci & 1);
  const int rt = (xcd & 1) * 8 + (idx & 7);
  const int rot = wrap7(wrap7((idx & 7) + 2 * ci + (xj & 1)));
  const int cg = idx & 7, rg = 4 * xcd + ci;

  const int tid = threadIdx.x;
  const int lane = tid & 63;
  const int w = __builtin_amdgcn_readfirstlane(tid >> 6) & 7;
  const int wl = w & 3;
  const int li = lane & 15, g = lane >> 4, q = (lane >> 2) & 3, p = lane & 3;
  const bool g3 = g == 3;
  const f32x4 z4 = {0.f, 0.f, 0.f, 0.f};
  STAMP(0); STAMPC(18);
  unsigned vx0 = (lane >> 3) * XROW + (((lane & 7) ^ (lane >> 4)) << 4);
  unsigned vx1 = vx0 ^ 64u;
  unsigned vw0, vw1;
  {
    const unsigned s0 = wl * 64 + lane, s1 = s0 + 256;
    const unsigned p0 = (s0 * 9363u) >> 16, p1 = (s1 * 9363u) >> 16;
    const unsigned k0 = (p0 & 56) | ((p0 >> 1) & 3) | ((p0 & 1) << 2);
    const unsigned k1 = (p1 & 56) | ((p1 >> 1) & 3) | ((p1 & 1) << 2);
    vw0 = k0 * WROW + hp * 112 + (s0 - p0 * 7) * 16;
    vw1 = k1 * WROW + hp * 112 + (s1 - p1 * 7) * 16;
  }
  asm volatile("" : "+v"(vx0), "+v"(vx1), "+v"(vw0), "+v"(vw1));
  asm volatile("" ::"s"(x), "s"(wqkvA), "s"(woA), "s"(wqkvB), "s"(woB), "s"(gate_w), "s"(out), "s"(ws));
  const __amdgpu_buffer_rsrc_t wsr = __builtin_amdgcn_make_buffer_rsrc(ws, 0, WS_END, 0x00020000);
  u64 tagO = 0;
  auto wo_run = [&](int j0, int j1) INL {
    const unsigned vwo = (lane >> 3) * XROW + cg * 128 + (((lane & 7) ^ (((lane >> 4) & 3) << 1)) << 4);
    const u32x4 sa = make_srd(woA, kD * XROW), sb = make_srd(woB, kD * XROW);
    unsigned so = (wl + 7 * cg + 4 * (j0 % 14)) * (8 * XROW);
    so = so >= kD * XROW ? so - kD * XROW : so;
#pragma unroll
    for (int j = j0; j < j1; ++j) {
      dma16(j < 14 ? sa : sb, vwo, so, L0 + (wl + 4 * j) * 1024);
      so += 32 * XROW;
      so = so >= kD * XROW ? so - kD * XROW : so;
    }
  };
  const u32x4 srdX = make_srd(x, kS * XROW);
  const u32x4 srdW = make_srd(br ? wqkvB : wqkvA, kD * WROW);
  const bool l3 = wl == 3;
  const int xr = rt * 128 + wl * 32 - (br ? 0 : 16);
  const unsigned sx01 = (xr < 0 ? 0 : xr) * XROW, sx23 = (xr + 16) * XROW;
  const bool l3a = l3 && !br;
  const u32x4 srd2 = l3a ? srdX : srdW;
  const unsigned v2 = l3 ? vw0 : vw1;
  const unsigned v2a = l3a ? vx0 : v2, v2b = l3a ? vx1 : v2;
  const unsigned st2 = l3a ? 128u : 64u * WROW, s2a = l3a ? (rt * 128 + 112) * XROW : 0u;
  const unsigned s2d = l3a ? 8u * XROW : (unsigned)XROW, s2e = l3a ? 8u * XROW : 2u * XROW;
  const unsigned sub2 = (l3 ? wl : wl + 4) * 1024;
  auto chunk = [&](unsigned sl, unsigned lq, unsigned lk, int cc) INL {
    const unsigned sw = cc * (64 * WROW), sx = cc * 128, s2 = cc * st2 + s2a;
#pragma unroll
    for (int j = 0; j < 4; ++j)
      dma16(srdX, (j & 1) ? vx1 : vx0, (j < 2 ? sx01 : sx23) + (j & 1) * 8 * XROW + sx, sl + (wl * 4 + j) * 1024);
    dma16(srdW, vw0, sw + XROW, lk + wl * 1024);
    dma16(srdW, vw0, sw + 2 * XROW, lk + WM_B + wl * 1024);
    dma16(srd2, v2b, s2 + s2d, l3a ? sl + 17408 : lk + sub2);
    dma16(srd2, v2b, s2 + s2e, l3a ? sl + 17408 : lk + WM_B + sub2);
    if (!br) {
      dma16(srdW, vw0, sw, lq + wl * 1024);
      dma16(srd2, v2a, s2, l3a ? sl + 16384 : lq + sub2);
    }
  };
  unsigned gv = 0, pt = 0;
  u64 epoch = 0;
  {
    int wx = w;
    asm volatile("" : "+s"(wx));
    if (wx < 4) {
      gv = ((const unsigned*)gate_w)[tid < 224 ? tid : 0];
      epoch = __hip_atomic_load((gu64*)(ws + WS_E), RLX_AGENT);
      const char* pb_ = wx == 0 ? ws : (wx == 1 ? out + rg * 64 * XROW : (wx == 2 ? woA : woB));
      pt = __hip_atomic_load((GAS unsigned*)(pb_ + (lane & 7) * (wx == 0 ? PT_WS : (wx == 1 ? 7168 : 50176))), RLX_AGENT);
    } else {
#pragma nounroll
      for (int c = 0; c < 2; ++c) {
        const unsigned sl = L0 + (br ? (c ? b_slot(1) : b_slot(0)) : (c ? a_slot(1) : a_slot(0)));
        chunk(sl, sl + A_W, sl + (br ? B_W : A_W + WM_B), wrap7(rot + c));
      }
    }
  }

    const int rh = w & 1, hh = w >> 1;
    const unsigned obs = WS_O + ((rt * 8 + rh * 4) * 32 + hp * 2 + hh) * 1024;
    const unsigned kvs = WS_KVP + (hp * 4 + w) * 16 * 1024;
    auto setup = [&](int goff) INL {
      if (tid < 60) *(LAS unsigned*)(uintptr_t)(L0 + SYNC + tid * 4) = 0u;
      const unsigned lo = (gv & 0xffffu) * 0x10001u, hi = (gv >> 16) * 0x10001u;
      const int pos = tid - rot * 32;
      if (tid < 224) lds_st128(L0 + goff + (pos < 0 ? pos + 224 : pos) * 16, (u32x4){lo, lo, hi, hi});
    };
    u64 clr = 0;
    unsigned pw = 0;
    auto clrp = [&]() INL { return (const gu64*)(ws + WS_CLR) + hp * 16 + (lane_id() & 15); };
    auto hook = [&](int c, bool before, bool isb) INL {
      if (c == 0 && !before && isb)
        __builtin_amdgcn_raw_buffer_store_b128((u32x4){SENT, SENT, SENT, SENT}, wsr, lane_id() * 16 + kvs + rt * 1024, 0, 16);
      if (c == 3 && before) {
        asm volatile("" : "+v"(epoch) : "v"(pt));
        tagO = next_tag(epoch);
        *(LAS u64*)(uintptr_t)(L0 + SYNC + SY_TAG) = tagO;
        VM_WAIT(0);
      }
      if (c == 3 && !before && isb)
        __builtin_amdgcn_raw_buffer_store_b64((u32x2){(unsigned)tagO, (unsigned)(tagO >> 32)}, wsr,
                                              (lane_id() | w) == 0 ? WS_CLR + (hp * 16 + rt) * 8 : 0x7ffffff0u, 0, 16);
      if (c == 6 && !before && isb) clr = __hip_atomic_load(clrp(), RLX_AGENT);
      if (c == 6 && !before) {
        const unsigned l = lane_id(), u = (l >> 3) & 3;
        const unsigned o = isb ? WS_O + ((rt * 8 + w * 2 + (u >> 1)) * 32 + 16 + hp * 2 + (u & 1)) * 1024 : obs + u * 32768;
        pw = __builtin_amdgcn_raw_buffer_load_b32(wsr, o + (l & 7) * 128, 0, 16);
      }
    };
    const unsigned xsw = li * 128 + ((g ^ (li >> 1)) << 4);
    const unsigned wln = (8 * g + 2 * q) * 112 + 8 * p + 56 * hh;

  if (__builtin_expect(w >= 4, 1)) {
    if (!br) {
      int cc = wrap7(rot + 1);
      unsigned sl = L0 + a_slot(2);
#pragma nounroll
      for (int c = 2; c < 7; ++c) {
        vm_bar<10>();
        cc = wrap7(cc + 1);
        chunk(sl, sl + A_W, sl + A_W + WM_B, cc);
        sl = sl == L0 + a_slot(0) ? L0 + a_slot(1) : sl + A_SLOT;
      }
      vm_bar<10>();
      if (cg >= 7) { vm_bar<0>(); STAMP_FLUSH(); return; }
      wo_run(0, 4);
      vm_bar<4>();
      wo_run(4, 28);
      VM_WAIT(14);
      sig(L0 + SYNC + SY_WOA + wl * 4);
      VM_WAIT(0);
      asm volatile("" ::"v"(gv), "v"(epoch), "v"(pt));
      sig(L0 + SYNC + SY_WOB + wl * 4);
    } else {
      auto instr = [&](int n) INL {
        if (n < 56) {
          const int c = n >> 3, j = n & 7, cc = wrap7(c + rot);
          const unsigned sl = L0 + b_slot(c), sw = cc * (64 * WROW), sx = cc * 128;
          if (j < 4) dma16(srdX, (j & 1) ? vx1 : vx0, sx01 + j * 8 * XROW + sx, sl + (wl * 4 + j) * 1024);
          else dma16(srdW, j < 6 ? vw0 : v2, sw + ((j & 1) + 1) * XROW, sl + B_W + (j & 1) * WM_B + (j < 6 ? wl * 1024 : sub2));
        } else {
          const int c = (n - 56) >> 1;
          dma16(srdW, (n & 1) ? v2 : vw0, wrap7(c + rot) * (64 * WROW), L0 + B_WQ + c * WM_B + ((n & 1) ? sub2 : wl * 1024));
        }
      };
      STAMP(1);
      bf16x8 xq[14][2];
      const unsigned xl = L0 + (wl * 32 + li) * 128 + ((g ^ (li >> 1)) << 4);
#pragma unroll
      for (int c = 0; c < 7; ++c) {
        if (c < 6) vm_bar<8>(); else vm_bar<4>();
#pragma unroll
        for (int ks = 0; ks < 2; ++ks) {
          const unsigned xa = (xl ^ (ks * 64)) + b_slot(c);
          xq[c * 2 + ks][0] = as_frag(lds_ld128(xa));
          xq[c * 2 + ks][1] = as_frag(lds_ld128(xa + 2048));
        }
#pragma unroll
        for (int n = (c < 6 ? 8 * c + 16 : 56 + 4); n < (c < 5 ? 8 * c + 24 : (c == 5 ? 56 + 4 : 70)); ++n) instr(n);
      }
      STAMP(4);
      VM_WAIT(0);
      asm volatile("" ::"v"(gv), "v"(epoch), "v"(pt));
      sig(L0 + SYNC + SY_WQ + wl * 4);
      wait4(L0 + SYNC + SY_WQ);
      wait4(L0 + SYNC + SY_EX);
      STAMP(3);
      f32x4 aq[2][2][2];
#pragma unroll
      for (int i = 0; i < 8; ++i) aq[i >> 2][(i >> 1) & 1][i & 1] = (f32x4){kEps, kEps, kEps, kEps};
      const unsigned wb = L0 + B_WQ + (8 * g + 2 * q) * 112 + 8 * p;
      const unsigned gl = L0 + B_G + (8 * g + q) * 8;
      const bool p3 = p == 3;
      const unsigned d1 = p3 ? 32u : 112u;
      const unsigned qa0 = opaque(p3 ? gl : wb + 32), qstep = opaque(p3 ? 256u : 3584u);
      bf16x8 fq[3][4];
      auto rdq = [&](int s) INL {
        const unsigned wk = wb + s * 3584;
        const unsigned a = qa0 + s * qstep;
        fq[s % 3][0] = lds_tr2(wk, wk + 112);
        fq[s % 3][1] = lds_tr2(a, a + d1);
        fq[s % 3][2] = lds_tr2(wk + 56, wk + 56 + 112);
        fq[s % 3][3] = lds_tr2(wk + 88, wk + 88 + 112);
      };
      rdq(0);
      rdq(1);
#pragma unroll
      for (int s = 0; s < 14; ++s) {
        SB();
        if (s + 2 < 14) rdq(s + 2);
#pragma unroll
        for (int i = 0; i < 4; ++i) {
          aq[0][i >> 1][i & 1] = mfma(fq[s % 3][i], xq[s][0], aq[0][i >> 1][i & 1]);
          aq[1][i >> 1][i & 1] = mfma(fq[s % 3][i], xq[s][1], aq[1][i >> 1][i & 1]);
        }
        interleave<8, 8>();
      }
      sig(L0 + SYNC + SY_QD + wl * 4);
      STAMP(5);
      u32x4 pqf[2][2];
      float omg[2];
#pragma unroll
      for (int t = 0; t < 2; ++t) {
        omg[t] = 1.f - sigmoidf(row3_bcast(aq[t][0][1][0]));
#pragma unroll
        for (int h = 0; h < 2; ++h) {
          const u32x2 a = phi4(aq[t][h][0]), b = phi4(aq[t][h][1]);
          pqf[t][h] = (u32x4){a[0], a[1], g3 ? 0u : b[0], g3 ? 0u : b[1]};
        }
      }
      tagO = uniform64(*(LAS u64*)(uintptr_t)(L0 + SYNC + SY_TAG));
      unsigned ob = WS_O + ((rt * 8 + wl * 2) * 32 + 16 + hp * 2) * 1024 + lane * 16;
      unsigned kb = L0 + B_KVL + lane * 16, zb = L0 + B_KVL + 1024 + (g * 16 + 12) * 16;
      asm volatile("" : "+v"(pqf[0][0]), "+v"(pqf[0][1]), "+v"(pqf[1][0]), "+v"(pqf[1][1]), "+v"(omg[0]), "+v"(omg[1]), "+v"(ob),
                   "+v"(kb), "+v"(zb), "+s"(tagO));
      __builtin_amdgcn_s_setprio(2);
      STAMP(6);
      wait4(L0 + SYNC + SY_KV);
      STAMP(8);
      {
        bf16x8 kv0[2], kv1[2], zf[2];
#pragma unroll
        for (int h = 0; h < 2; ++h) zf[h] = as_frag(lds_ld128(zb + h * 2048));
#pragma unroll
        for (int h = 0; h < 2; ++h) {
          kv0[h] = as_frag(lds_ld128(kb + h * 2048));
          kv1[h] = as_frag(lds_ld128(kb + h * 2048 + 1024));
        }
        SB();
        f32x4 dn[4], n0[4], n1[4];
#pragma unroll
        for (int i = 0; i < 4; ++i) dn[i] = mfma(zf[i >> 1], as_frag(pqf[i & 1][i >> 1]), (f32x4){kEps, kEps, kEps, kEps});
#pragma unroll
        for (int i = 0; i < 4; ++i) {
          n0[i] = mfma(kv0[i >> 1], as_frag(pqf[i & 1][i >> 1]), z4);
          n1[i] = mfma(kv1[i >> 1], as_frag(pqf[i & 1][i >> 1]), z4);
        }
        SB();
#pragma unroll
        for (int i = 0; i < 4; ++i) {
          const float s = omg[i & 1] * __builtin_amdgcn_rcpf(dn[i][0]);
          const u32x2 a = pk4(n0[i] * s), b = pk4(n1[i] * s);
          __builtin_amdgcn_raw_buffer_store_b128((u32x4){a[0], a[1], g3 ? 0u : b[0], b[1]}, wsr, ob + ((i & 1) * 32 + (i >> 1)) * 1024, 0, 16);
        }
      }
      STAMP(9);
      VM_WAIT(0);
      if (lane == 0) __hip_atomic_store((gu64*)(ws + WS_FB) + ((rt * 4 + wl) * 8 + hp) * FS, tagO, RLX_AGENT);
      STAMP(10); STAMPC(19);
      if (cg >= 7) { STAMP_FLUSH(); return; }
    }
  } else if (__builtin_expect(br, 1)) {
      __builtin_amdgcn_s_setprio(2);
      setup(B_G);
      f32x4 ak[4][2], av[4][2];
      {
        const float one = li == 12 ? 1.f : 0.f, i0 = rh ? one : 0.f, i1 = rh ? 0.f : one;
#pragma unroll
        for (int t = 0; t < 4; ++t) {
          ak[t][0] = ak[t][1] = (f32x4){kEps, kEps, kEps, kEps};
          av[t][0] = (f32x4){i0, i0, i0, i0};
          av[t][1] = (f32x4){i1, i1, i1, i1};
        }
      }
      bf16x8 xf[2][4], fw[2][4];
      unsigned xb, wb;
      const bool z0 = p == 3 && rh, z1 = p == 3 && !rh;
      const unsigned zaddr = L0 + SYNC + SY_ZERO;
      auto rd = [&](int ks, int part) INL {
        const unsigned xa = xb ^ (ks * 64), wk = wb + ks * 3584;
        if (part & 1) {
          const unsigned a = wk + WM_B + (((part >> 1) ^ rh) << 5);
          const bool z = (part >> 1) ? z1 : z0;
          fw[ks][part] = lds_tr2(z ? zaddr : a, z ? zaddr : a + 112);
        } else {
          fw[ks][part] = lds_tr2(wk + (part >> 1) * 32, wk + (part >> 1) * 32 + 112);
        }
        if (part < 2) {
          xf[ks][part * 2] = as_frag(lds_ld128(xa + part * 4096));
          xf[ks][part * 2 + 1] = as_frag(lds_ld128(xa + part * 4096 + 2048));
        }
      };
      auto mm = [&](int ks, int part) INL {
#pragma unroll
        for (int t = 0; t < 4; ++t) {
          if (part & 1) av[t][part >> 1] = mfma(xf[ks][t], fw[ks][part], av[t][part >> 1]);
          else ak[t][part >> 1] = mfma(xf[ks][t], fw[ks][part], ak[t][part >> 1]);
        }
      };
      auto half = [&](int ks, bool compute) INL {
#pragma unroll
        for (int part = 0; part < 4; ++part) rd(ks, part);
        if (compute) {
#pragma unroll
          for (int part = 0; part < 4; ++part) mm(ks ^ 1, part);
          interleave<16, 12>();
        }
        SB();
      };
      unsigned sl = b_slot(0);
      xb = L0 + sl + rh * 8192 + xsw;
      wb = L0 + sl + B_W + wln;
      LDS_BAR();
      hook(0, false, true);
      STAMP(2); STAMPC(16);
      half(0, false);
      half(1, true);
#pragma nounroll
      for (int c = 1; c < 7; ++c) {
        sl = sl == b_slot(0) ? b_slot(1) : sl + B_SLOT;
        xb = L0 + sl + rh * 8192 + xsw;
        wb = L0 + sl + B_W + wln;
        if (c == 3) hook(3, true, true);
        LDS_BAR();
        if (c == 3) hook(3, false, true);
        if (c == 6) { hook(6, false, true); STAMP(4); STAMPC(17); }
        SB();
        half(0, true);
        half(1, true);
      }
      sig(L0 + SYNC + SY_D6 + w * 4);
      mm(1, 0); mm(1, 2);
      SB();
      mm(1, 3); mm(1, 1);
      STAMP(5);
      f32x4 kv[2][2];
      {
        u32x2 pk[4][2], vf[4][2];
#pragma unroll
        for (int t = 0; t < 4; ++t) {
          pk[t][0] = phi4(ak[t][0]);
          pk[t][1] = phi4(ak[t][1]);
          vf[t][0] = pk4(av[t][0]);
          vf[t][1] = pk4(av[t][1]);
        }
#pragma unroll
        for (int e = 1; e >= 0; --e)
#pragma unroll
          for (int d = 0; d < 2; ++d) {
            kv[e][d] = mfma(frag2(pk[0][d], pk[1][d]), frag2(vf[0][e], vf[1][e]), z4);
            kv[e][d] = mfma(frag2(pk[2][d], pk[3][d]), frag2(vf[2][e], vf[3][e]), kv[e][d]);
          }
      }
      lds_st128(L0 + B_EX + w * 2048 + lane * 16, __builtin_bit_cast(u32x4, kv[1][0]));
      lds_st128(L0 + B_EX + w * 2048 + 1024 + lane * 16, __builtin_bit_cast(u32x4, kv[1][1]));
      sig(L0 + SYNC + SY_EX + w * 4);
      wait1(L0 + SYNC + SY_EX + (w ^ 1) * 4);
      const f32x4 s0 = kv[0][0] + __builtin_bit_cast(f32x4, lds_ld128(L0 + B_EX + (w ^ 1) * 2048 + lane * 16));
      const f32x4 s1 = kv[0][1] + __builtin_bit_cast(f32x4, lds_ld128(L0 + B_EX + (w ^ 1) * 2048 + 1024 + lane * 16));
      {
        const u32x2 a = pk4(s0), b = pk4(s1);
        publish((u32x4){a[0], a[1], b[0], b[1]}, wsr, lane * 16 + kvs + rt * 1024);
      }
      STAMP(6);
      if (!__all(clr == tagO)) poll_flags(clrp(), tagO);
      wo_run(0, QWO);
      STAMP(7);
      f32x4 sum0, sum1;
      {
        const unsigned e0 = q == g ? ((p & 1) ? 0x3f800000u : 0x3f80u) : 0u;
        const unsigned ea = p < 2 ? e0 : 0u, eb = p < 2 ? 0u : e0;
        const bf16x8 sel0 = as_frag((u32x4){ea, eb, 0u, 0u}), sel1 = as_frag((u32x4){0u, 0u, ea, eb});
        unsigned vo = kvs + lane * 16;
#pragma nounroll
        for (unsigned spins = 0; spins < SPIN_CAP; ++spins) {
          asm volatile("" : "+v"(vo)::"memory");
          u32x4 pr[16];
#pragma unroll
          for (int r = 0; r < 16; ++r) pr[r] = __builtin_amdgcn_raw_buffer_load_b128(wsr, vo, ((r + 2 * rt) & 15) * 1024, 16);
          SB();
          sum0 = sum1 = z4;
#pragma unroll
          for (int r = 0; r < 16; ++r) {
            sum0 = mfma(sel0, as_frag(pr[r]), sum0);
            sum1 = mfma(sel1, as_frag(pr[r]), sum1);
          }
          if (!__any(sum0[0] != sum0[0])) break;
          unsigned m = 0;
#pragma unroll
          for (int r = 0; r < 16; ++r) m = max(max(max(max(m, pr[r][0]), pr[r][1]), pr[r][2]), pr[r][3]);
          if (!__any(m == SENT)) break;
        }
      }
      asm volatile("" ::"v"(pw));
      STAMP(8);
      lds_st128(L0 + B_KVL + w * 1024 + lane * 16, __builtin_bit_cast(u32x4, frag2(pk4(sum0), pk4(sum1))));
      sig(L0 + SYNC + SY_KV + w * 4);
      STAMP(10); STAMPC(19);
      __builtin_amdgcn_s_setprio(0);
      if (cg >= 7) { STAMP_FLUSH(); return; }
      sig(L0 + SYNC + SY_WOA + w * 4);
      wait4(L0 + SYNC + SY_D6);
      wait4(L0 + SYNC + SY_QD);
      wo_run(QWO, 28);
  } else {
      asm volatile("; role A");
      setup(A_G);
      f32x4 aq[4][2], ak[5][2], av[5][2];
#pragma unroll
      for (int t = 0; t < 5; ++t)
#pragma unroll
        for (int f = 0; f < 2; ++f) { ak[t][f] = av[t][f] = z4; if (t < 4) aq[t][f] = z4; }
      const unsigned gl = L0 + A_G + (8 * g + q) * 8;
      const bool p3 = p == 3;
      const unsigned d1 = p3 ? 32u : 112u;
      bf16x8 xf[2][5], fq[2][2], fk[2][2], fv[2][2];
      unsigned xb, wb, gq;
      auto rd = [&](int ks, int part) INL {
        const unsigned xa = xb ^ (ks * 64), wk = wb + ks * 3584;
        if (part == 0) {
          fk[ks][0] = lds_tr2(wk + WM_B, wk + WM_B + 112);
          xf[ks][0] = as_frag(lds_ld128(xa));
          xf[ks][1] = as_frag(lds_ld128(xa + 2048));
        } else if (part == 1) {
          fv[ks][0] = lds_tr2(wk + 2 * WM_B, wk + 2 * WM_B + 112);
          xf[ks][2] = as_frag(lds_ld128(xa + 4096));
        } else if (part == 2) {
          fq[ks][0] = lds_tr2(wk, wk + 112);
          xf[ks][3] = as_frag(lds_ld128(xa + 6144));
        } else if (part == 3) {
          fk[ks][1] = lds_tr2(wk + WM_B + 32, wk + WM_B + 32 + 112);
          xf[ks][4] = as_frag(lds_ld128(xa + 8192));
        } else if (part == 4) {
          fv[ks][1] = lds_tr2(wk + 2 * WM_B + 32, wk + 2 * WM_B + 32 + 112);
        } else {
          const unsigned a = p3 ? gq + ks * 256 : wk + 32;
          fq[ks][1] = lds_tr2(a, a + d1);
        }
      };
      auto mm = [&](int ks, int part) INL {
        const int f = part / 3, m = part % 3;
#pragma unroll
        for (int t = 0; t < 5; ++t) {
          if (m == 0) ak[t][f] = mfma(fk[ks][f], xf[ks][t], ak[t][f]);
          if (m == 1) av[t][f] = mfma(xf[ks][t], fv[ks][f], av[t][f]);
          if (m == 2 && t > 0) aq[t - 1][f] = mfma(fq[ks][f], xf[ks][t], aq[t - 1][f]);
        }
      };
      auto half = [&](int ks, bool compute) INL {
#pragma unroll
        for (int part = 0; part < 6; ++part) rd(ks, part);
        if (compute) {
#pragma unroll
          for (int part = 0; part < 6; ++part) mm(ks ^ 1, part);
          interleave<28, 17>();
        }
        SB();
      };
      unsigned sl = a_slot(0);
      xb = L0 + sl + rh * 8192 + xsw;
      wb = L0 + sl + A_W + wln;
      gq = gl;
      LDS_BAR();
      STAMP(2); STAMPC(16);
      half(0, false);
      half(1, true);
#pragma nounroll
      for (int c = 1; c < 7; ++c) {
        sl = sl == a_slot(0) ? a_slot(1) : sl + A_SLOT;
        xb = L0 + sl + rh * 8192 + xsw;
        wb = L0 + sl + A_W + wln;
        gq += 512;
        if (c == 3) hook(3, true, false);
        LDS_BAR();
        if (c == 6) { hook(6, false, false); STAMP(4); STAMPC(17); }
        SB();
        half(0, true);
        half(1, true);
      }
      sig(L0 + SYNC + SY_D6 + w * 4);
      mm(1, 0); mm(1, 3); mm(1, 2); mm(1, 5);
      SB();
      mm(1, 1); mm(1, 4);
      STAMP(5);
      const bool odd = li & 1;
      const int kk0 = 4 * g + (li & 1);
      const bool own0 = kk0 <= li, own1 = kk0 + 2 <= li;
      const float sc = 0.18898223650461362f * kLog2e;
      const float ninf = (rt == 0 && rh == 0) ? -__builtin_inff() : 0.f;
      const unsigned l16 = lane_id() * 16;
      bf16x8 kf[5];
#pragma unroll
      for (int t = 0; t < 5; ++t) kf[t] = frag2(pk4(ak[t][0]), pk4(ak[t][1]));
      f32x4 sp[4], so[4];
#pragma unroll
      for (int t = 0; t < 4; ++t) {
        const u32x2 b = pk4(aq[t][1]);
        const bf16x8 qf = frag2(pk4(aq[t][0]), (u32x2){g3 ? 0u : b[0], g3 ? 0u : b[1]});
        sp[t] = mfma(kf[t], qf, t == 0 ? (f32x4){ninf, ninf, ninf, ninf} : z4);
        so[t] = mfma(kf[t + 1], qf, z4);
      }
      float ug;
      {
        float z0 = aq[0][1][0], z1 = aq[1][1][0], z2 = aq[2][1][0], z3 = aq[3][1][0];
        swap32(z0, z1);
        swap32(z2, z3);
        swap16(z1, z3);
        ug = 1.f + __builtin_amdgcn_exp2f(-kLog2e * z3);
      }
      u32x2 vf[5][2];
#pragma unroll
      for (int t = 0; t < 5; ++t) {
        vf[t][0] = pk4(av[t][0]);
        vf[t][1] = pk4(av[t][1]);
      }
      float l0[4], l1[4], bc[4];
#pragma unroll
      for (int t = 0; t < 4; ++t) {
        float a0 = so[t][0], a2 = so[t][2], b0 = sp[t][0], b2 = sp[t][2];
        const float c0 = own0 ? a0 : b0, c1 = own0 ? so[t][1] : sp[t][1], c2 = own1 ? a2 : b2, c3 = own1 ? so[t][3] : sp[t][3];
        l0[t] = odd ? c1 : c0;
        l1[t] = odd ? c3 : c2;
      }
      rows_bcast4(rows_reduce4<true>(fmax1(l0[0], l1[0]), fmax1(l0[1], l1[1]), fmax1(l0[2], l1[2]), fmax1(l0[3], l1[3])) * -sc, bc);
#pragma unroll
      for (int t = 0; t < 4; ++t) {
        l0[t] = __builtin_amdgcn_exp2f(__builtin_fmaf(l0[t], sc, bc[t]));
        l1[t] = __builtin_amdgcn_exp2f(__builtin_fmaf(l1[t], sc, bc[t]));
      }
      rows_bcast4(__builtin_amdgcn_rcpf(rows_reduce4<false>(l0[0] + l1[0], l0[1] + l1[1], l0[2] + l1[2], l0[3] + l1[3]) * ug), bc);
      const unsigned sh = odd ? 16u : 0u;
      const unsigned mk0 = own0 ? 0u : 0xffffu, mk1 = own1 ? 0u : 0xffffu;
#pragma unroll
      for (int t = 0; t < 4; ++t) {
        const unsigned pe = pk2(l0[t] * bc[t], l1[t] * bc[t]), ph = pe >> 16;
        const bf16x8 pf = as_frag((u32x4){(pe & mk0) << sh, (ph & mk1) << sh, (pe & (mk0 ^ 0xffffu)) << sh, (ph & (mk1 ^ 0xffffu)) << sh});
        const f32x4 r0 = mfma(frag2(vf[t][0], vf[t + 1][0]), pf, z4);
        const f32x4 r1 = mfma(frag2(vf[t][1], vf[t + 1][1]), pf, z4);
        const u32x2 c0 = pk4(r0), c1 = pk4(r1);
        __builtin_amdgcn_raw_buffer_store_b128((u32x4){c0[0], c0[1], g3 ? 0u : c1[0], g3 ? 0u : c1[1]}, wsr, l16 + obs + t * 32768, 0, 16);
      }
      STAMP(9);
      VM_WAIT(0);
      asm volatile("" ::"v"(pw));
      if (l16 == 0) __hip_atomic_store((gu64*)(ws + WS_FA) + ((rt * 2 + rh) * 16 + hp * 2 + hh) * FS, tagO, RLX_AGENT);
      STAMP(10); STAMPC(19);
      if (cg >= 7) {
        if (bid == 56 && w == 0) {
#pragma nounroll
          for (int k = 0; k < 4; ++k) {
            const int i = (k & 1) * 64 + lane;
            poll_flags(k < 2 ? (gu64*)(ws + WS_CLR) + i : (gu64*)(ws + WS_FA) + ((i >> 3) * 32 + (i & 7) * 2) * FS, tagO);
          }
          if (lane == 0) __hip_atomic_store((gu64*)(ws + WS_E), tagO, RLX_AGENT);
        }
        STAMP_FLUSH();
        return;
      }
  }

  const int pb = w >> 2;
  wait4(L0 + SYNC + (pb ? SY_WOB : SY_WOA));
  const unsigned sw = (2 * g + (q >> 1)) & 3;
  const unsigned rb = L0 + (pb * 448 + 4 * g + q) * 128 + 8 * p;
  unsigned wa[4], wa2[4];
#pragma unroll
  for (int v = 0; v < 4; ++v) {
    wa[v] = opaque(rb + ((v ^ sw) << 5));
    wa2[v] = opaque(wa[v] + (g3 ? 0u : 2048u));
  }
  auto wfrag = [&](int h, int ct) INL {
    const int v = ct ^ (2 * (h & 1));
    return lds_tr2(wa[v] + h * 3584, wa2[v] + h * 3584);
  };
  constexpr int NPRE = 8;
  bf16x8 wf[16][4];
#pragma unroll
  for (int h = 0; h < NPRE; ++h)
#pragma unroll
    for (int ct = 0; ct < 4; ++ct) wf[h][ct] = wfrag(h, ct);
  STAMP(11);
  const unsigned ub = WS_O + ((rg * 4 + wl) * 32 + pb * 16) * 1024;
  tagO = uniform64(*(LAS u64*)(uintptr_t)(L0 + SYNC + SY_TAG));
  {
    const gu64* fp = pb ? (gu64*)(ws + WS_FB) + ((rg * 2 + (wl >> 1)) * 8 + (li & 7)) * FS : (gu64*)(ws + WS_FA) + (rg * 16 + li) * FS;
    const u64 f0 = __hip_atomic_load(fp, RLX_AGENT);
    if (br && !pb) { VM_WAIT(0); sig(L0 + SYNC + SY_WOB + wl * 4); }
    if (!__all(f0 == tagO)) poll_flags(fp, tagO);
  }
  STAMP(12);
  u32x4 of[16];
  {
    unsigned vo = ub + lane * 16;
    asm volatile("" : "+v"(vo)::"memory");
#pragma unroll
    for (int h = 0; h < 16; ++h) of[h] = __builtin_amdgcn_raw_buffer_load_b128(wsr, vo, ((h + 2 * cg) & 15) * 1024, 16);
    SB();
  }
  f32x4 acc[4] = {z4, z4, z4, z4};
#pragma unroll
  for (int h = 0; h < 16; ++h) {
#pragma unroll
    for (int ct = 0; ct < 4; ++ct) acc[ct] = mfma(wf[h][ct], as_frag(of[h]), acc[ct]);
    SB();
    if (h + NPRE < 16) {
#pragma unroll
      for (int ct = 0; ct < 4; ++ct) wf[h + NPRE][ct] = wfrag(h + NPRE, ct);
    }
    SB();
  }
  STAMP(13);
  const unsigned xa = L0 + P2X + wl * 4096 + lane * 16;
  if (!pb) {
    wait4(L0 + SYNC + SY_D6);
#pragma unroll
    for (int ct = 0; ct < 4; ++ct) lds_st128(xa + ct * 1024, __builtin_bit_cast(u32x4, acc[ct]));
    sig(L0 + SYNC + SY_P2 + wl * 4);
  } else {
    f32x4 pa[4];
    for (;;) {
      const unsigned f = *(volatile LAS unsigned*)(uintptr_t)(L0 + SYNC + SY_P2 + wl * 4);
#pragma unroll
      for (int ct = 0; ct < 4; ++ct) pa[ct] = *(volatile LAS f32x4*)(uintptr_t)(xa + ct * 1024);
      if (__builtin_amdgcn_readfirstlane(f)) break;
      __builtin_amdgcn_s_sleep(1);
    }
    char* orow = out + (rg * 64 + wl * 16 + li) * XROW + cg * 128 + (g & 1) * 32 + (g >> 1) * 16;
#pragma unroll
    for (int k = 0; k < 2; ++k) {
      const u32x2 a = pk4(acc[2 * k] + pa[2 * k]), b = pk4(acc[2 * k + 1] + pa[2 * k + 1]);
      const auto r0 = __builtin_amdgcn_permlane16_swap(a[0], b[0], false, false);
      const auto r1 = __builtin_amdgcn_permlane16_swap(a[1], b[1], false, false);
      *(u32x4*)(orow + k * 64) = (u32x4){r0[0], r1[0], r0[1], r1[1]};
    }
  }
  STAMP(14);
  STAMP(15);
  STAMP_FLUSH();
}

}

extern "C" void kernel_launch(void* const* d_in, const int* in_sizes, int n_in, void* d_out, int out_size, void* d_ws,
                              size_t ws_size, hipStream_t stream) {
  (void)in_sizes; (void)n_in; (void)out_size; (void)ws_size;
  gated_mix_kernel<<<256, 512, 0, stream>>>((const char*)d_in[0], (const char*)d_in[1], (const char*)d_in[2],
                                            (const char*)d_in[4], (const char*)d_in[5],
                                            (const unsigned short*)d_in[6], (char*)d_out, (char*)d_ws);
}
```

```cpp
#include <hip/hip_runtime.h>
#include <stdint.h>

typedef __bf16 bf16x2 __attribute__((ext_vector_type(2)));
typedef __bf16 bf16x4 __attribute__((ext_vector_type(4)));
typedef __bf16 bf16x8 __attribute__((ext_vector_type(8)));
typedef float f32x2 __attribute__((ext_vector_type(2)));
typedef float f32x4 __attribute__((ext_vector_type(4)));
typedef unsigned u32x2 __attribute__((ext_vector_type(2)));
typedef unsigned u32x4 __attribute__((ext_vector_type(4)));
typedef unsigned long long u64;
#define LAS __attribute__((address_space(3)))
#define GAS __attribute__((address_space(1)))
typedef GAS u64 gu64;
#define RLX_AGENT __ATOMIC_RELAXED, __HIP_MEMORY_SCOPE_AGENT

namespace {

constexpr int kS = 2048, kD = 448;
constexpr int XROW = kD * 2;
constexpr int WROW = 3 * kD * 2;
constexpr int WM_B = 64 * 112;

constexpr int A_FRONT = 34816, A_SLOT = 144 * 128 + 3 * WM_B, A_W = 144 * 128;
constexpr int A_G = A_FRONT + 3 * A_SLOT;
constexpr int B_SLOT = 128 * 128 + 2 * WM_B, B_W = 128 * 128;
constexpr int B_WQ = 97536, B_G = B_WQ + 7 * WM_B, B_EX = B_G + 3584, B_KVL = B_EX + 8192;
constexpr int P2X = 114688;
constexpr int SYNC = 163584;
constexpr int LDS_B = 163840;
static_assert(A_G + 3584 <= SYNC && B_KVL + 4096 <= SYNC && A_FRONT + 2 * A_SLOT == 114688, "lds map");
__host__ __device__ constexpr int a_slot(int c) { return A_FRONT + ((c + 2) % 3) * A_SLOT; }
__host__ __device__ constexpr int b_slot(int c) { return ((c + 2) % 3) * B_SLOT; }
enum { SY_WOA = 0, SY_WOB = 16, SY_D6 = 32, SY_D6Q = 48, SY_WQ = 64, SY_QD = 80, SY_EX = 96, SY_KV = 112, SY_P2 = 128, SY_ZERO = 192, SY_TAG = 240 };

constexpr int WS_E = 0;
constexpr int WS_CLR = 4096;
constexpr int FS = 16;
constexpr int WS_FA = 8192;
constexpr int WS_FB = WS_FA + 16 * 2 * 16 * 8 * FS;
constexpr int WS_KVP = WS_FB + 16 * 4 * 8 * 8 * FS;
constexpr int WS_O = WS_KVP + 8 * 4 * 16 * 1024;
constexpr int WS_END = WS_O + 16 * 8 * 32 * 1024;

constexpr int PT_WS = (WS_END / 8) & ~127;
constexpr unsigned SPIN_CAP = 1u << 14;
constexpr unsigned SENT = 0xffffffffu;
#define QWO 14

#define STAMP(i) do {} while (0)
#define STAMPC(i) do {} while (0)
#define STAMP_FLUSH() do {} while (0)

__device__ __forceinline__ unsigned pk2(float a, float b) {
  f32x2 v = {a, b};
  bf16x2 r = __builtin_convertvector(v, bf16x2);
  return __builtin_bit_cast(unsigned, r);
}
__device__ __forceinline__ u32x2 pk4(f32x4 v) { return (u32x2){pk2(v[0], v[1]), pk2(v[2], v[3])}; }
__device__ __forceinline__ bf16x8 as_frag(u32x4 v) { return __builtin_bit_cast(bf16x8, v); }
__device__ __forceinline__ bf16x8 frag2(u32x2 a, u32x2 b) { return as_frag((u32x4){a[0], a[1], b[0], b[1]}); }
__device__ __forceinline__ f32x4 mfma(bf16x8 a, bf16x8 b, f32x4 c) {
  return __builtin_amdgcn_mfma_f32_16x16x32_bf16(a, b, c, 0, 0, 0);
}
__device__ __forceinline__ bf16x8 lds_tr2(unsigned a0, unsigned a1) {
  bf16x4 lo = __builtin_amdgcn_ds_read_tr16_b64_v4bf16((LAS bf16x4*)(uintptr_t)a0);
  bf16x4 hi = __builtin_amdgcn_ds_read_tr16_b64_v4bf16((LAS bf16x4*)(uintptr_t)a1);
  return __builtin_shufflevector(lo, hi, 0, 1, 2, 3, 4, 5, 6, 7);
}
__device__ __forceinline__ u32x4 lds_ld128(unsigned a) { return *(LAS u32x4*)(uintptr_t)a; }
__device__ __forceinline__ void lds_st128(unsigned a, u32x4 v) { *(LAS u32x4*)(uintptr_t)a = v; }
__device__ __forceinline__ void lds_st64(unsigned a, u32x2 v) { *(LAS u32x2*)(uintptr_t)a = v; }
__device__ __forceinline__ unsigned opaque(unsigned v) { asm volatile("" : "+v"(v)); return v; }

__device__ __forceinline__ void sig(unsigned a) {
  asm volatile("" ::: "memory");
  *(volatile LAS unsigned*)(uintptr_t)a = 1u;
  asm volatile("" ::: "memory");
  __builtin_amdgcn_sched_barrier(0);
}
__device__ __forceinline__ void wait1(unsigned a) {
  while (__builtin_amdgcn_readfirstlane(*(volatile LAS unsigned*)(uintptr_t)a) == 0u) __builtin_amdgcn_s_sleep(1);
  asm volatile("" ::: "memory");
}
__device__ __forceinline__ void wait4(unsigned a) {
  for (;;) {
    const u32x4 v = *(volatile LAS u32x4*)(uintptr_t)a;
    if (__builtin_amdgcn_readfirstlane(v[0] & v[1] & v[2] & v[3])) break;
    __builtin_amdgcn_s_sleep(1);
  }
  asm volatile("" ::: "memory");
}

__device__ __forceinline__ u32x4 make_srd(const void* p, unsigned bytes) {
  uint64_t a = (uint64_t)p;
  u32x4 r;
  r[0] = __builtin_amdgcn_readfirstlane((unsigned)a);
  r[1] = __builtin_amdgcn_readfirstlane((unsigned)(a >> 32) & 0xffffu);
  r[2] = bytes;
  r[3] = 0x00020000u;
  return r;
}
__device__ __forceinline__ void dma16(u32x4 srd, unsigned voff, unsigned soff, unsigned ldsaddr) {
  asm volatile(
      "s_mov_b32 m0, %3\n\t"
      "s_nop 3\n\t"
      "buffer_load_dwordx4 %0, %1, %2 offen lds"
      :
      : "v"(voff), "s"(srd), "s"(soff), "s"(ldsaddr));
}
#define VM_BAR(N) asm volatile("s_waitcnt vmcnt(" #N ") lgkmcnt(0)\n\ts_barrier" ::: "memory")
#define LDS_BAR() asm volatile("s_waitcnt lgkmcnt(0)\n\ts_barrier" ::: "memory")
#define VM_WAIT(N) asm volatile("s_waitcnt vmcnt(" #N ")" ::: "memory")
template <int N> __device__ __forceinline__ void vm_bar() { asm volatile("s_waitcnt vmcnt(%0) lgkmcnt(0)\n\ts_barrier" ::"n"(N) : "memory"); }
#define SB() __builtin_amdgcn_sched_barrier(0)

__device__ __forceinline__ float fmax1(float a, float b) { return __builtin_amdgcn_fmed3f(a, b, 3.0e38f); }
__device__ __forceinline__ void swap16(float& a, float& b) {
  auto r = __builtin_amdgcn_permlane16_swap(__float_as_uint(a), __float_as_uint(b), false, false);
  const unsigned r0 = r[0], r1 = r[1];
  a = __uint_as_float(r0);
  b = __uint_as_float(r1);
}
__device__ __forceinline__ void swap32(float& a, float& b) {
  auto r = __builtin_amdgcn_permlane32_swap(__float_as_uint(a), __float_as_uint(b), false, false);
  const unsigned r0 = r[0], r1 = r[1];
  a = __uint_as_float(r0);
  b = __uint_as_float(r1);
}
template <bool MAX>
__device__ __forceinline__ float rows_reduce4(float v0, float v1, float v2, float v3) {
  swap32(v0, v1);
  swap32(v2, v3);
  float a = MAX ? fmax1(v0, v1) : v0 + v1, b = MAX ? fmax1(v2, v3) : v2 + v3;
  swap16(a, b);
  return MAX ? fmax1(a, b) : a + b;
}
__device__ __forceinline__ void rows_bcast4(float x, float (&o)[4]) {
  float y = x;
  swap16(x, y);
  o[0] = x; o[1] = x; o[2] = y; o[3] = y;
  swap32(o[0], o[1]);
  swap32(o[2], o[3]);
}
__device__ __forceinline__ float row3_bcast(float v) {
  float a = v;
  swap32(a, v);
  a = v;
  swap16(a, v);
  return v;
}
template <int NM, int ND>
__device__ __forceinline__ void interleave() {
#pragma unroll
  for (int i = 0; i < NM; ++i) {
    __builtin_amdgcn_sched_group_barrier(0x8, 1, 0);
    if (i < ND) {
      __builtin_amdgcn_sched_group_barrier(0x2, 1, 0);
      __builtin_amdgcn_sched_group_barrier(0x100, 1, 0);
    }
  }
}
constexpr float kEps = 1e-6f, kLog2e = 1.44269504088896f;
__device__ __forceinline__ float phi(float u) {
  const float e = __builtin_amdgcn_fmed3f(__builtin_amdgcn_exp2f(__builtin_fmaf(u, kLog2e, -kEps * kLog2e)), 0.f, 1.f);
  return fmax1(u, kEps) + e;
}
__device__ __forceinline__ u32x2 phi4(f32x4 v) { return (u32x2){pk2(phi(v[0]), phi(v[1])), pk2(phi(v[2]), phi(v[3]))}; }
__device__ __forceinline__ float sigmoidf(float z) {
  return __builtin_amdgcn_rcpf(1.f + __builtin_amdgcn_exp2f(-1.44269504088896f * z));
}
__device__ __forceinline__ u64 next_tag(u64 e) {
  const u64 s = ((u64)(unsigned)__builtin_amdgcn_readfirstlane((unsigned)(e >> 32)) << 32) |
                (unsigned)__builtin_amdgcn_readfirstlane((unsigned)e);
  return s * 6364136223846793005ull + 1442695040888963407ull;
}
__device__ __forceinline__ u64 uniform64(u64 e) {
  return ((u64)(unsigned)__builtin_amdgcn_readfirstlane((unsigned)(e >> 32)) << 32) |
         (unsigned)__builtin_amdgcn_readfirstlane((unsigned)e);
}
__device__ __forceinline__ void poll_flags(const gu64* p, u64 tag) {
#pragma nounroll
  for (unsigned spins = 0; spins < SPIN_CAP; ++spins) {
    u64 v = __hip_atomic_load(p, RLX_AGENT);
    if (__all(v == tag)) break;
    __builtin_amdgcn_s_sleep(2);
  }
  asm volatile("" ::: "memory");
}
__device__ __forceinline__ void publish(u32x4 v, __amdgpu_buffer_rsrc_t r, unsigned voff) {
#pragma unroll
  for (int i = 0; i < 4; ++i) v[i] = v[i] < SENT - 1u ? v[i] : SENT - 1u;
  __builtin_amdgcn_raw_buffer_store_b128(v, r, voff, 0, 16);
}
__device__ __forceinline__ unsigned lane_id() {
  unsigned l = __builtin_amdgcn_mbcnt_hi(~0u, __builtin_amdgcn_mbcnt_lo(~0u, 0u));
  asm volatile("" : "+v"(l));
  return l;
}
__device__ __forceinline__ int wrap7(int c) { return c >= 7 ? c - 7 : c; }

__global__ __launch_bounds__(512, 2) void gated_mix_kernel(const char* __restrict__ x, const char* __restrict__ wqkvA,
                                                           const char* __restrict__ woA, const char* __restrict__ wqkvB,
                                                           const char* __restrict__ woB,
                                                           const unsigned short* __restrict__ gate_w, char* __restrict__ out,
                                                           char* ws) {
  __shared__ __attribute__((aligned(1024))) char lds[LDS_B];
  const unsigned L0 = (unsigned)(uintptr_t)(LAS char*)lds;
  const int bid = blockIdx.x;
  const int xcd = bid & 7, idx = bid >> 3;
  const int xj = xcd >> 1, ci = idx >> 3;
  const int br = ci >> 1;
  const int hp = 2 * (br ? (xj ^ 1) : xj) + (ci & 1);
  const int rt = (xcd & 1) * 8 + (idx & 7);
  const int rot = wrap7(wrap7((idx & 7) + 2 * ci + (xj & 1)));
  const int cg = idx & 7, rg = 4 * xcd + ci;

  const int tid = threadIdx.x;
  const int lane = tid & 63;
  const int w = __builtin_amdgcn_readfirstlane(tid >> 6) & 7;
  const int wl = w & 3;
  const int li = lane & 15, g = lane >> 4, q = (lane >> 2) & 3, p = lane & 3;
  const bool g3 = g == 3;
  const f32x4 z4 = {0.f, 0.f, 0.f, 0.f};
  STAMP(0); STAMPC(18);
  unsigned vx0 = (lane >> 3) * XROW + (((lane & 7) ^ (lane >> 4)) << 4);
  unsigned vx1 = vx0 ^ 64u;
  unsigned vw0, vw1;
  {
    const unsigned s0 = wl * 64 + lane, s1 = s0 + 256;
    const unsigned p0 = (s0 * 9363u) >> 16, p1 = (s1 * 9363u) >> 16;
    const unsigned k0 = (p0 & 56) | ((p0 >> 1) & 3) | ((p0 & 1) << 2);
    const unsigned k1 = (p1 & 56) | ((p1 >> 1) & 3) | ((p1 & 1) << 2);
    vw0 = k0 * WROW + hp * 112 + (s0 - p0 * 7) * 16;
    vw1 = k1 * WROW + hp * 112 + (s1 - p1 * 7) * 16;
  }
  asm volatile("" : "+v"(vx0), "+v"(vx1), "+v"(vw0), "+v"(vw1));
  asm volatile("" ::"s"(x), "s"(wqkvA), "s"(woA), "s"(wqkvB), "s"(woB), "s"(gate_w), "s"(out), "s"(ws));
  const __amdgpu_buffer_rsrc_t wsr = __builtin_amdgcn_make_buffer_rsrc(ws, 0, WS_END, 0x00020000);
  u64 tagO;
  auto wo_run = [&](int j0, int j1) {
    const unsigned vwo = (lane >> 3) * XROW + cg * 128 + (((lane & 7) ^ (((lane >> 4) & 3) << 1)) << 4);
    const u32x4 sa = make_srd(woA, kD * XROW), sb = make_srd(woB, kD * XROW);
    unsigned so = (wl + 7 * cg + 4 * (j0 % 14)) * (8 * XROW);
    so = so >= kD * XROW ? so - kD * XROW : so;
#pragma unroll
    for (int j = j0; j < j1; ++j) {
      dma16(j < 14 ? sa : sb, vwo, so, L0 + (wl + 4 * j) * 1024);
      so += 32 * XROW;
      so = so >= kD * XROW ? so - kD * XROW : so;
    }
  };
  const u32x4 srdX = make_srd(x, kS * XROW);
  const u32x4 srdW = make_srd(br ? wqkvB : wqkvA, kD * WROW);
  const bool l3 = wl == 3;
  const int xr = rt * 128 + wl * 32 - (br ? 0 : 16);
  const unsigned sx01 = (xr < 0 ? 0 : xr) * XROW, sx23 = (xr + 16) * XROW;
  const bool l3a = l3 && !br;
  const u32x4 srd2 = l3a ? srdX : srdW;
  const unsigned v2 = l3 ? vw0 : vw1;
  const unsigned v2a = l3a ? vx0 : v2, v2b = l3a ? vx1 : v2;
  const unsigned st2 = l3a ? 128u : 64u * WROW, s2a = l3a ? (rt * 128 + 112) * XROW : 0u;
  const unsigned s2d = l3a ? 8u * XROW : (unsigned)XROW, s2e = l3a ? 8u * XROW : 2u * XROW;
  const unsigned sub2 = (l3 ? wl : wl + 4) * 1024;
  auto chunk = [&](unsigned sl, unsigned lq, unsigned lk, int cc) {
    const unsigned sw = cc * (64 * WROW), sx = cc * 128, s2 = cc * st2 + s2a;
#pragma unroll
    for (int j = 0; j < 4; ++j)
      dma16(srdX, (j & 1) ? vx1 : vx0, (j < 2 ? sx01 : sx23) + (j & 1) * 8 * XROW + sx, sl + (wl * 4 + j) * 1024);
    dma16(srdW, vw0, sw + XROW, lk + wl * 1024);
    dma16(srdW, vw0, sw + 2 * XROW, lk + WM_B + wl * 1024);
    dma16(srd2, v2b, s2 + s2d, l3a ? sl + 17408 : lk + sub2);
    dma16(srd2, v2b, s2 + s2e, l3a ? sl + 17408 : lk + WM_B + sub2);
    if (!br) {
      dma16(srdW, vw0, sw, lq + wl * 1024);
      dma16(srd2, v2a, s2, l3a ? sl + 16384 : lq + sub2);
    }
  };
  unsigned gv = 0, pt = 0;
  u64 epoch = 0;
  {
    int wx = w;
    asm volatile("" : "+s"(wx));
    if (wx < 4) {
      gv = ((const unsigned*)gate_w)[tid < 224 ? tid : 0];
      epoch = __hip_atomic_load((gu64*)(ws + WS_E), RLX_AGENT);
      const char* pb_ = wx == 0 ? ws : (wx == 1 ? out + rg * 64 * XROW : (wx == 2 ? woA : woB));
      pt = __hip_atomic_load((GAS unsigned*)(pb_ + (lane & 7) * (wx == 0 ? PT_WS : (wx == 1 ? 7168 : 50176))), RLX_AGENT);
    } else {
#pragma nounroll
      for (int c = 0; c < 2; ++c) {
        const unsigned sl = L0 + (br ? (c ? b_slot(1) : b_slot(0)) : (c ? a_slot(1) : a_slot(0)));
        chunk(sl, sl + A_W, sl + (br ? B_W : A_W + WM_B), wrap7(rot + c));
      }
    }
  }

    const int rh = w & 1, hh = w >> 1;
    const unsigned obs = WS_O + ((rt * 8 + rh * 4) * 32 + hp * 2 + hh) * 1024;
    const unsigned kvs = WS_KVP + (hp * 4 + w) * 16 * 1024;
    auto setup = [&](int goff) {
      if (tid < 60) *(LAS unsigned*)(uintptr_t)(L0 + SYNC + tid * 4) = 0u;
      const unsigned lo = (gv & 0xffffu) * 0x10001u, hi = (gv >> 16) * 0x10001u;
      const int pos = tid - rot * 32;
      if (tid < 224) lds_st128(L0 + goff + (pos < 0 ? pos + 224 : pos) * 16, (u32x4){lo, lo, hi, hi});
    };
    u64 clr = 0;
    unsigned pw = 0;
    auto clrp = [&]() { return (const gu64*)(ws + WS_CLR) + hp * 16 + (lane_id() & 15); };
    auto hook = [&](int c, bool before, bool isb) {
      if (c == 0 && !before && isb)
        __builtin_amdgcn_raw_buffer_store_b128((u32x4){SENT, SENT, SENT, SENT}, wsr, lane_id() * 16 + kvs + rt * 1024, 0, 16);
      if (c == 3 && before) {
        asm volatile("" : "+v"(epoch) : "v"(pt));
        tagO = next_tag(epoch);
        *(LAS u64*)(uintptr_t)(L0 + SYNC + SY_TAG) = tagO;
        VM_WAIT(0);
      }
      if (c == 3 && !before && isb)
        __builtin_amdgcn_raw_buffer_store_b64((u32x2){(unsigned)tagO, (unsigned)(tagO >> 32)}, wsr,
                                              (lane_id() | w) == 0 ? WS_CLR + (hp * 16 + rt) * 8 : 0x7ffffff0u, 0, 16);
      if (c == 6 && !before && isb) clr = __hip_atomic_load(clrp(), RLX_AGENT);
      if (c == 6 && !before) {
        const unsigned l = lane_id(), u = (l >> 3) & 3;
        const unsigned o = isb ? WS_O + ((rt * 8 + w * 2 + (u >> 1)) * 32 + 16 + hp * 2 + (u & 1)) * 1024 : obs + u * 32768;
        pw = __builtin_amdgcn_raw_buffer_load_b32(wsr, o + (l & 7) * 128, 0, 16);
      }
    };
    const unsigned xsw = li * 128 + ((g ^ (li >> 1)) << 4);
    const unsigned wln = (8 * g + 2 * q) * 112 + 8 * p + 56 * hh;

  if (__builtin_expect(w >= 4, 1)) {
    if (!br) {
      int cc = wrap7(rot + 1);
      unsigned sl = L0 + a_slot(2);
#pragma nounroll
      for (int c = 2; c < 7; ++c) {
        vm_bar<10>();
        cc = wrap7(cc + 1);
        chunk(sl, sl + A_W, sl + A_W + WM_B, cc);
        sl = sl == L0 + a_slot(0) ? L0 + a_slot(1) : sl + A_SLOT;
      }
      vm_bar<10>();
      if (cg >= 7) { vm_bar<0>(); STAMP_FLUSH(); return; }
      wo_run(0, 4);
      vm_bar<4>();
      wo_run(4, 28);
      VM_WAIT(14);
      sig(L0 + SYNC + SY_WOA + wl * 4);
      VM_WAIT(0);
      asm volatile("" ::"v"(gv), "v"(epoch), "v"(pt));
      sig(L0 + SYNC + SY_WOB + wl * 4);
    } else {
      auto instr = [&](int n) {
        if (n < 56) {
          const int c = n >> 3, j = n & 7, cc = wrap7(c + rot);
          const unsigned sl = L0 + b_slot(c), sw = cc * (64 * WROW), sx = cc * 128;
          if (j < 4) dma16(srdX, (j & 1) ? vx1 : vx0, sx01 + j * 8 * XROW + sx, sl + (wl * 4 + j) * 1024);
          else dma16(srdW, j < 6 ? vw0 : v2, sw + ((j & 1) + 1) * XROW, sl + B_W + (j & 1) * WM_B + (j < 6 ? wl * 1024 : sub2));
        } else {
          const int c = (n - 56) >> 1;
          dma16(srdW, (n & 1) ? v2 : vw0, wrap7(c + rot) * (64 * WROW), L0 + B_WQ + c * WM_B + ((n & 1) ? sub2 : wl * 1024));
        }
      };
      STAMP(1);
      bf16x8 xq[14][2];
      const unsigned xl = L0 + (wl * 32 + li) * 128 + ((g ^ (li >> 1)) << 4);
#pragma unroll
      for (int c = 0; c < 7; ++c) {
        if (c < 6) vm_bar<8>(); else vm_bar<4>();
#pragma unroll
        for (int ks = 0; ks < 2; ++ks) {
          const unsigned xa = (xl ^ (ks * 64)) + b_slot(c);
          xq[c * 2 + ks][0] = as_frag(lds_ld128(xa));
          xq[c * 2 + ks][1] = as_frag(lds_ld128(xa + 2048));
        }
#pragma unroll
        for (int n = (c < 6 ? 8 * c + 16 : 56 + 4); n < (c < 5 ? 8 * c + 24 : (c == 5 ? 56 + 4 : 70)); ++n) instr(n);
      }
      STAMP(4);
      VM_WAIT(0);
      asm volatile("" ::"v"(gv), "v"(epoch), "v"(pt));
      sig(L0 + SYNC + SY_WQ + wl * 4);
      wait4(L0 + SYNC + SY_WQ);
      wait4(L0 + SYNC + SY_EX);
      STAMP(3);
      f32x4 aq[2][2][2];
#pragma unroll
      for (int i = 0; i < 8; ++i) aq[i >> 2][(i >> 1) & 1][i & 1] = (f32x4){kEps, kEps, kEps, kEps};
      const unsigned wb = L0 + B_WQ + (8 * g + 2 * q) * 112 + 8 * p;
      const unsigned gl = L0 + B_G + (8 * g + q) * 8;
      const bool p3 = p == 3;
      const unsigned d1 = p3 ? 32u : 112u;
      const unsigned qa0 = opaque(p3 ? gl : wb + 32), qstep = opaque(p3 ? 256u : 3584u);
      bf16x8 fq[3][4];
      auto rdq = [&](int s) {
        const unsigned wk = wb + s * 3584;
        const unsigned a = qa0 + s * qstep;
        fq[s % 3][0] = lds_tr2(wk, wk + 112);
        fq[s % 3][1] = lds_tr2(a, a + d1);
        fq[s % 3][2] = lds_tr2(wk + 56, wk + 56 + 112);
        fq[s % 3][3] = lds_tr2(wk + 88, wk + 88 + 112);
      };
      rdq(0);
      rdq(1);
#pragma unroll
      for (int s = 0; s < 14; ++s) {
        SB();
        if (s + 2 < 14) rdq(s + 2);
#pragma unroll
        for (int i = 0; i < 4; ++i) {
          aq[0][i >> 1][i & 1] = mfma(fq[s % 3][i], xq[s][0], aq[0][i >> 1][i & 1]);
          aq[1][i >> 1][i & 1] = mfma(fq[s % 3][i], xq[s][1], aq[1][i >> 1][i & 1]);
        }
        interleave<8, 8>();
      }
      sig(L0 + SYNC + SY_QD + wl * 4);
      STAMP(5);
      u32x4 pqf[2][2];
      float omg[2];
#pragma unroll
      for (int t = 0; t < 2; ++t) {
        omg[t] = 1.f - sigmoidf(row3_bcast(aq[t][0][1][0]));
#pragma unroll
        for (int h = 0; h < 2; ++h) {
          const u32x2 a = phi4(aq[t][h][0]), b = phi4(aq[t][h][1]);
          pqf[t][h] = (u32x4){a[0], a[1], g3 ? 0u : b[0], g3 ? 0u : b[1]};
        }
      }
      tagO = uniform64(*(LAS u64*)(uintptr_t)(L0 + SYNC + SY_TAG));
      unsigned ob = WS_O + ((rt * 8 + wl * 2) * 32 + 16 + hp * 2) * 1024 + lane * 16;
      unsigned kb = L0 + B_KVL + lane * 16, zb = L0 + B_KVL + 1024 + (g * 16 + 12) * 16;
      asm volatile("" : "+v"(pqf[0][0]), "+v"(pqf[0][1]), "+v"(pqf[1][0]), "+v"(pqf[1][1]), "+v"(omg[0]), "+v"(omg[1]), "+v"(ob),
                   "+v"(kb), "+v"(zb), "+s"(tagO));
      __builtin_amdgcn_s_setprio(2);
      STAMP(6);
      wait4(L0 + SYNC + SY_KV);
      STAMP(8);
      {
        bf16x8 kv0[2], kv1[2], zf[2];
#pragma unroll
        for (int h = 0; h < 2; ++h) zf[h] = as_frag(lds_ld128(zb + h * 2048));
#pragma unroll
        for (int h = 0; h < 2; ++h) {
          kv0[h] = as_frag(lds_ld128(kb + h * 2048));
          kv1[h] = as_frag(lds_ld128(kb + h * 2048 + 1024));
        }
        SB();
        f32x4 dn[4], n0[4], n1[4];
#pragma unroll
        for (int i = 0; i < 4; ++i) dn[i] = mfma(zf[i >> 1], as_frag(pqf[i & 1][i >> 1]), (f32x4){kEps, kEps, kEps, kEps});
#pragma unroll
        for (int i = 0; i < 4; ++i) {
          n0[i] = mfma(kv0[i >> 1], as_frag(pqf[i & 1][i >> 1]), z4);
          n1[i] = mfma(kv1[i >> 1], as_frag(pqf[i & 1][i >> 1]), z4);
        }
        SB();
#pragma unroll
        for (int i = 0; i < 4; ++i) {
          const float s = omg[i & 1] * __builtin_amdgcn_rcpf(dn[i][0]);
          const u32x2 a = pk4(n0[i] * s), b = pk4(n1[i] * s);
          __builtin_amdgcn_raw_buffer_store_b128((u32x4){a[0], a[1], g3 ? 0u : b[0], b[1]}, wsr, ob + ((i & 1) * 32 + (i >> 1)) * 1024, 0, 16);
        }
      }
      STAMP(9);
      VM_WAIT(0);
      if (lane == 0) __hip_atomic_store((gu64*)(ws + WS_FB) + ((rt * 4 + wl) * 8 + hp) * FS, tagO, RLX_AGENT);
      STAMP(10); STAMPC(19);
      if (cg >= 7) { STAMP_FLUSH(); return; }
    }
  } else if (__builtin_expect(br, 1)) {
      __builtin_amdgcn_s_setprio(2);
      setup(B_G);
      f32x4 ak[4][2], av[4][2];
      {
        const float one = li == 12 ? 1.f : 0.f, i0 = rh ? one : 0.f, i1 = rh ? 0.f : one;
#pragma unroll
        for (int t = 0; t < 4; ++t) {
          ak[t][0] = ak[t][1] = (f32x4){kEps, kEps, kEps, kEps};
          av[t][0] = (f32x4){i0, i0, i0, i0};
          av[t][1] = (f32x4){i1, i1, i1, i1};
        }
      }
      bf16x8 xf[2][4], fw[2][4];
      unsigned xb, wb;
      const bool z0 = p == 3 && rh, z1 = p == 3 && !rh;
      const unsigned zaddr = L0 + SYNC + SY_ZERO;
      auto rd = [&](int ks, int part) {
        const unsigned xa = xb ^ (ks * 64), wk = wb + ks * 3584;
        if (part & 1) {
          const unsigned a = wk + WM_B + (((part >> 1) ^ rh) << 5);
          const bool z = (part >> 1) ? z1 : z0;
          fw[ks][part] = lds_tr2(z ? zaddr : a, z ? zaddr : a + 112);
        } else {
          fw[ks][part] = lds_tr2(wk + (part >> 1) * 32, wk + (part >> 1) * 32 + 112);
        }
        if (part < 2) {
          xf[ks][part * 2] = as_frag(lds_ld128(xa + part * 4096));
          xf[ks][part * 2 + 1] = as_frag(lds_ld128(xa + part * 4096 + 2048));
        }
      };
      auto mm = [&](int ks, int part) {
#pragma unroll
        for (int t = 0; t < 4; ++t) {
          if (part & 1) av[t][part >> 1] = mfma(xf[ks][t], fw[ks][part], av[t][part >> 1]);
          else ak[t][part >> 1] = mfma(xf[ks][t], fw[ks][part], ak[t][part >> 1]);
        }
      };
#pragma unroll
      for (int c = 0; c < 7; ++c) {
        xb = opaque(L0 + b_slot(c) + rh * 8192 + xsw);
        wb = opaque(L0 + b_slot(c) + B_W + wln);
        hook(c, true, true);
        LDS_BAR();
        hook(c, false, true);
        if (c == 0) { STAMP(2); STAMPC(16); }
        if (c == 3) STAMP(3);
        if (c == 6) { STAMP(4); STAMPC(17); }
        SB();
#pragma unroll
        for (int ks = 0; ks < 2; ++ks) {
#pragma unroll
          for (int part = 0; part < 4; ++part) rd(ks, part);
          if (ks == 1 || c > 0) {
#pragma unroll
            for (int part = 0; part < 4; ++part) mm(ks ^ 1, part);
            interleave<16, 12>();
          }
          SB();
        }
      }
      sig(L0 + SYNC + SY_D6 + w * 4);
      mm(1, 0); mm(1, 2);
      SB();
      mm(1, 3); mm(1, 1);
      STAMP(5);
      f32x4 kv[2][2];
      {
        u32x2 pk[4][2], vf[4][2];
#pragma unroll
        for (int t = 0; t < 4; ++t) {
          pk[t][0] = phi4(ak[t][0]);
          pk[t][1] = phi4(ak[t][1]);
          vf[t][0] = pk4(av[t][0]);
          vf[t][1] = pk4(av[t][1]);
        }
#pragma unroll
        for (int e = 1; e >= 0; --e)
#pragma unroll
          for (int d = 0; d < 2; ++d) {
            kv[e][d] = mfma(frag2(pk[0][d], pk[1][d]), frag2(vf[0][e], vf[1][e]), z4);
            kv[e][d] = mfma(frag2(pk[2][d], pk[3][d]), frag2(vf[2][e], vf[3][e]), kv[e][d]);
          }
      }
      lds_st128(L0 + B_EX + w * 2048 + lane * 16, __builtin_bit_cast(u32x4, kv[1][0]));
      lds_st128(L0 + B_EX + w * 2048 + 1024 + lane * 16, __builtin_bit_cast(u32x4, kv[1][1]));
      sig(L0 + SYNC + SY_EX + w * 4);
      wait1(L0 + SYNC + SY_EX + (w ^ 1) * 4);
      const f32x4 s0 = kv[0][0] + __builtin_bit_cast(f32x4, lds_ld128(L0 + B_EX + (w ^ 1) * 2048 + lane * 16));
      const f32x4 s1 = kv[0][1] + __builtin_bit_cast(f32x4, lds_ld128(L0 + B_EX + (w ^ 1) * 2048 + 1024 + lane * 16));
      {
        const u32x2 a = pk4(s0), b = pk4(s1);
        publish((u32x4){a[0], a[1], b[0], b[1]}, wsr, lane * 16 + kvs + rt * 1024);
      }
      STAMP(6);
      if (!__all(clr == tagO)) poll_flags(clrp(), tagO);
      wo_run(0, QWO);
      STAMP(7);
      f32x4 sum0, sum1;
      {
        const unsigned e0 = q == g ? ((p & 1) ? 0x3f800000u : 0x3f80u) : 0u;
        const unsigned ea = p < 2 ? e0 : 0u, eb = p < 2 ? 0u : e0;
        const bf16x8 sel0 = as_frag((u32x4){ea, eb, 0u, 0u}), sel1 = as_frag((u32x4){0u, 0u, ea, eb});
        unsigned vo = kvs + lane * 16;
#pragma nounroll
        for (unsigned spins = 0; spins < SPIN_CAP; ++spins) {
          asm volatile("" : "+v"(vo)::"memory");
          u32x4 pr[16];
#pragma unroll
          for (int r = 0; r < 16; ++r) pr[r] = __builtin_amdgcn_raw_buffer_load_b128(wsr, vo, ((r + 2 * rt) & 15) * 1024, 16);
          SB();
          sum0 = sum1 = z4;
#pragma unroll
          for (int r = 0; r < 16; ++r) {
            sum0 = mfma(sel0, as_frag(pr[r]), sum0);
            sum1 = mfma(sel1, as_frag(pr[r]), sum1);
          }
          if (!__any(sum0[0] != sum0[0])) break;
          unsigned m = 0;
#pragma unroll
          for (int r = 0; r < 16; ++r) m = max(max(max(max(m, pr[r][0]), pr[r][1]), pr[r][2]), pr[r][3]);
          if (!__any(m == SENT)) break;
        }
      }
      asm volatile("" ::"v"(pw));
      STAMP(8);
      lds_st128(L0 + B_KVL + w * 1024 + lane * 16, __builtin_bit_cast(u32x4, frag2(pk4(sum0), pk4(sum1))));
      sig(L0 + SYNC + SY_KV + w * 4);
      STAMP(10); STAMPC(19);
      __builtin_amdgcn_s_setprio(0);
      if (cg >= 7) { STAMP_FLUSH(); return; }
      sig(L0 + SYNC + SY_WOA + w * 4);
      wait4(L0 + SYNC + SY_D6);
      wait4(L0 + SYNC + SY_QD);
      wo_run(QWO, 28);
  } else {
      asm volatile("; role A");
      setup(A_G);
      f32x4 aq[4][2], ak[5][2], av[5][2];
#pragma unroll
      for (int t = 0; t < 5; ++t)
#pragma unroll
        for (int f = 0; f < 2; ++f) { ak[t][f] = av[t][f] = z4; if (t < 4) aq[t][f] = z4; }
      const unsigned gl = L0 + A_G + (8 * g + q) * 8;
      const bool p3 = p == 3;
      const unsigned d1 = p3 ? 32u : 112u;
      bf16x8 xf[2][5], fq[2][2], fk[2][2], fv[2][2];
      unsigned xb, wb;
      auto rd = [&](int c, int ks, int part) {
        const unsigned xa = xb ^ (ks * 64), wk = wb + ks * 3584;
        if (part == 0) {
          fk[ks][0] = lds_tr2(wk + WM_B, wk + WM_B + 112);
          xf[ks][0] = as_frag(lds_ld128(xa));
          xf[ks][1] = as_frag(lds_ld128(xa + 2048));
        } else if (part == 1) {
          fv[ks][0] = lds_tr2(wk + 2 * WM_B, wk + 2 * WM_B + 112);
          xf[ks][2] = as_frag(lds_ld128(xa + 4096));
        } else if (part == 2) {
          fq[ks][0] = lds_tr2(wk, wk + 112);
          xf[ks][3] = as_frag(lds_ld128(xa + 6144));
        } else if (part == 3) {
          fk[ks][1] = lds_tr2(wk + WM_B + 32, wk + WM_B + 32 + 112);
          xf[ks][4] = as_frag(lds_ld128(xa + 8192));
        } else if (part == 4) {
          fv[ks][1] = lds_tr2(wk + 2 * WM_B + 32, wk + 2 * WM_B + 32 + 112);
        } else {
          const unsigned a = p3 ? gl + c * 512 + ks * 256 : wk + 32;
          fq[ks][1] = lds_tr2(a, a + d1);
        }
      };
      auto mm = [&](int ks, int part) {
        const int f = part / 3, m = part % 3;
#pragma unroll
        for (int t = 0; t < 5; ++t) {
          if (m == 0) ak[t][f] = mfma(fk[ks][f], xf[ks][t], ak[t][f]);
          if (m == 1) av[t][f] = mfma(xf[ks][t], fv[ks][f], av[t][f]);
          if (m == 2 && t > 0) aq[t - 1][f] = mfma(fq[ks][f], xf[ks][t], aq[t - 1][f]);
        }
      };
#pragma unroll
      for (int c = 0; c < 7; ++c) {
        xb = opaque(L0 + a_slot(c) + rh * 8192 + xsw);
        wb = opaque(L0 + a_slot(c) + A_W + wln);
        hook(c, true, false);
        LDS_BAR();
        hook(c, false, false);
        if (c == 0) { STAMP(2); STAMPC(16); }
        if (c == 3) STAMP(3);
        if (c == 6) { STAMP(4); STAMPC(17); }
        SB();
#pragma unroll
        for (int ks = 0; ks < 2; ++ks) {
#pragma unroll
          for (int part = 0; part < 6; ++part) rd(c, ks, part);
          if (ks == 1 || c > 0) {
#pragma unroll
            for (int part = 0; part < 6; ++part) mm(ks ^ 1, part);
            interleave<28, 17>();
          }
          SB();
        }
      }
      sig(L0 + SYNC + SY_D6 + w * 4);
      mm(1, 0); mm(1, 3); mm(1, 2); mm(1, 5);
      SB();
      mm(1, 1); mm(1, 4);
      STAMP(5);
      const bool odd = li & 1;
      const int kk0 = 4 * g + (li & 1);
      const bool own0 = kk0 <= li, own1 = kk0 + 2 <= li;
      const float sc = 0.18898223650461362f * kLog2e;
      const float ninf = (rt == 0 && rh == 0) ? -__builtin_inff() : 0.f;
      const unsigned l16 = lane_id() * 16;
      bf16x8 kf[5];
#pragma unroll
      for (int t = 0; t < 5; ++t) kf[t] = frag2(pk4(ak[t][0]), pk4(ak[t][1]));
      f32x4 sp[4], so[4];
#pragma unroll
      for (int t = 0; t < 4; ++t) {
        const u32x2 b = pk4(aq[t][1]);
        const bf16x8 qf = frag2(pk4(aq[t][0]), (u32x2){g3 ? 0u : b[0], g3 ? 0u : b[1]});
        sp[t] = mfma(kf[t], qf, t == 0 ? (f32x4){ninf, ninf, ninf, ninf} : z4);
        so[t] = mfma(kf[t + 1], qf, z4);
      }
      float ug;
      {
        float z0 = aq[0][1][0], z1 = aq[1][1][0], z2 = aq[2][1][0], z3 = aq[3][1][0];
        swap32(z0, z1);
        swap32(z2, z3);
        swap16(z1, z3);
        ug = 1.f + __builtin_amdgcn_exp2f(-kLog2e * z3);
      }
      u32x2 vf[5][2];
#pragma unroll
      for (int t = 0; t < 5; ++t) {
        vf[t][0] = pk4(av[t][0]);
        vf[t][1] = pk4(av[t][1]);
      }
      float l0[4], l1[4], bc[4];
#pragma unroll
      for (int t = 0; t < 4; ++t) {
        float a0 = so[t][0], a2 = so[t][2], b0 = sp[t][0], b2 = sp[t][2];
        const float c0 = own0 ? a0 : b0, c1 = own0 ? so[t][1] : sp[t][1], c2 = own1 ? a2 : b2, c3 = own1 ? so[t][3] : sp[t][3];
        l0[t] = odd ? c1 : c0;
        l1[t] = odd ? c3 : c2;
      }
      rows_bcast4(rows_reduce4<true>(fmax1(l0[0], l1[0]), fmax1(l0[1], l1[1]), fmax1(l0[2], l1[2]), fmax1(l0[3], l1[3])) * -sc, bc);
#pragma unroll
      for (int t = 0; t < 4; ++t) {
        l0[t] = __builtin_amdgcn_exp2f(__builtin_fmaf(l0[t], sc, bc[t]));
        l1[t] = __builtin_amdgcn_exp2f(__builtin_fmaf(l1[t], sc, bc[t]));
      }
      rows_bcast4(__builtin_amdgcn_rcpf(rows_reduce4<false>(l0[0] + l1[0], l0[1] + l1[1], l0[2] + l1[2], l0[3] + l1[3]) * ug), bc);
      const unsigned sh = odd ? 16u : 0u;
      const unsigned mk0 = own0 ? 0u : 0xffffu, mk1 = own1 ? 0u : 0xffffu;
#pragma unroll
      for (int t = 0; t < 4; ++t) {
        const unsigned pe = pk2(l0[t] * bc[t], l1[t] * bc[t]), ph = pe >> 16;
        const bf16x8 pf = as_frag((u32x4){(pe & mk0) << sh, (ph & mk1) << sh, (pe & (mk0 ^ 0xffffu)) << sh, (ph & (mk1 ^ 0xffffu)) << sh});
        const f32x4 r0 = mfma(frag2(vf[t][0], vf[t + 1][0]), pf, z4);
        const f32x4 r1 = mfma(frag2(vf[t][1], vf[t + 1][1]), pf, z4);
        const u32x2 c0 = pk4(r0), c1 = pk4(r1);
        __builtin_amdgcn_raw_buffer_store_b128((u32x4){c0[0], c0[1], g3 ? 0u : c1[0], g3 ? 0u : c1[1]}, wsr, l16 + obs + t * 32768, 0, 16);
      }
      STAMP(9);
      VM_WAIT(0);
      asm volatile("" ::"v"(pw));
      if (l16 == 0) __hip_atomic_store((gu64*)(ws + WS_FA) + ((rt * 2 + rh) * 16 + hp * 2 + hh) * FS, tagO, RLX_AGENT);
      STAMP(10); STAMPC(19);
      if (cg >= 7) {
        if (bid == 56 && w == 0) {
#pragma nounroll
          for (int k = 0; k < 4; ++k) {
            const int i = (k & 1) * 64 + lane;
            poll_flags(k < 2 ? (gu64*)(ws + WS_CLR) + i : (gu64*)(ws + WS_FA) + ((i >> 3) * 32 + (i & 7) * 2) * FS, tagO);
          }
          if (lane == 0) __hip_atomic_store((gu64*)(ws + WS_E), tagO, RLX_AGENT);
        }
        STAMP_FLUSH();
        return;
      }
  }

  const int pb = w >> 2;
  wait4(L0 + SYNC + (pb ? SY_WOB : SY_WOA));
  const unsigned sw = (2 * g + (q >> 1)) & 3;
  const unsigned rb = L0 + (pb * 448 + 4 * g + q) * 128 + 8 * p;
  unsigned wa[4], wa2[4];
#pragma unroll
  for (int v = 0; v < 4; ++v) {
    wa[v] = opaque(rb + ((v ^ sw) << 5));
    wa2[v] = opaque(wa[v] + (g3 ? 0u : 2048u));
  }
  auto wfrag = [&](int h, int ct) {
    const int v = ct ^ (2 * (h & 1));
    return lds_tr2(wa[v] + h * 3584, wa2[v] + h * 3584);
  };
  constexpr int NPRE = 8;
  bf16x8 wf[16][4];
#pragma unroll
  for (int h = 0; h < NPRE; ++h)
#pragma unroll
    for (int ct = 0; ct < 4; ++ct) wf[h][ct] = wfrag(h, ct);
  STAMP(11);
  const unsigned ub = WS_O + ((rg * 4 + wl) * 32 + pb * 16) * 1024;
  tagO = uniform64(*(LAS u64*)(uintptr_t)(L0 + SYNC + SY_TAG));
  {
    const gu64* fp = pb ? (gu64*)(ws + WS_FB) + ((rg * 2 + (wl >> 1)) * 8 + (li & 7)) * FS : (gu64*)(ws + WS_FA) + (rg * 16 + li) * FS;
    const u64 f0 = __hip_atomic_load(fp, RLX_AGENT);
    if (br && !pb) { VM_WAIT(0); sig(L0 + SYNC + SY_WOB + wl * 4); }
    if (!__all(f0 == tagO)) poll_flags(fp, tagO);
  }
  STAMP(12);
  u32x4 of[16];
  {
    unsigned vo = ub + lane * 16;
    asm volatile("" : "+v"(vo)::"memory");
#pragma unroll
    for (int h = 0; h < 16; ++h) of[h] = __builtin_amdgcn_raw_buffer_load_b128(wsr, vo, ((h + 2 * cg) & 15) * 1024, 16);
    SB();
  }
  f32x4 acc[4] = {z4, z4, z4, z4};
#pragma unroll
  for (int h = 0; h < 16; ++h) {
#pragma unroll
    for (int ct = 0; ct < 4; ++ct) acc[ct] = mfma(wf[h][ct], as_frag(of[h]), acc[ct]);
    SB();
    if (h + NPRE < 16) {
#pragma unroll
      for (int ct = 0; ct < 4; ++ct) wf[h + NPRE][ct] = wfrag(h + NPRE, ct);
    }
    SB();
  }
  STAMP(13);
  const unsigned xa = L0 + P2X + wl * 4096 + lane * 16;
  if (!pb) {
    wait4(L0 + SYNC + SY_D6);
#pragma unroll
    for (int ct = 0; ct < 4; ++ct) lds_st128(xa + ct * 1024, __builtin_bit_cast(u32x4, acc[ct]));
    sig(L0 + SYNC + SY_P2 + wl * 4);
  } else {
    f32x4 pa[4];
    for (;;) {
      const unsigned f = *(volatile LAS unsigned*)(uintptr_t)(L0 + SYNC + SY_P2 + wl * 4);
#pragma unroll
      for (int ct = 0; ct < 4; ++ct) pa[ct] = *(volatile LAS f32x4*)(uintptr_t)(xa + ct * 1024);
      if (__builtin_amdgcn_readfirstlane(f)) break;
      __builtin_amdgcn_s_sleep(1);
    }
    char* orow = out + (rg * 64 + wl * 16 + li) * XROW + cg * 128 + (g & 1) * 32 + (g >> 1) * 16;
#pragma unroll
    for (int k = 0; k < 2; ++k) {
      const u32x2 a = pk4(acc[2 * k] + pa[2 * k]), b = pk4(acc[2 * k + 1] + pa[2 * k + 1]);
      const auto r0 = __builtin_amdgcn_permlane16_swap(a[0], b[0], false, false);
      const auto r1 = __builtin_amdgcn_permlane16_swap(a[1], b[1], false, false);
      *(u32x4*)(orow + k * 64) = (u32x4){r0[0], r1[0], r0[1], r1[1]};
    }
  }
  STAMP(14);
  STAMP(15);
  STAMP_FLUSH();
}

}

extern "C" void kernel_launch(void* const* d_in, const int* in_sizes, int n_in, void* d_out, int out_size, void* d_ws,
                              size_t ws_size, hipStream_t stream) {
  (void)in_sizes; (void)n_in; (void)out_size; (void)ws_size;
  gated_mix_kernel<<<256, 512, 0, stream>>>((const char*)d_in[0], (const char*)d_in[1], (const char*)d_in[2],
                                            (const char*)d_in[4], (const char*)d_in[5],
                                            (const unsigned short*)d_in[6], (char*)d_out, (char*)d_ws);
}
```

```cpp
#define CODE_LINES 228
#include <hip/hip_runtime.h>
#include <stdint.h>

typedef __bf16 bf16x2 __attribute__((ext_vector_type(2)));
typedef __bf16 bf16x4 __attribute__((ext_vector_type(4)));
typedef __bf16 bf16x8 __attribute__((ext_vector_type(8)));
typedef float f32x2 __attribute__((ext_vector_type(2)));
typedef float f32x4 __attribute__((ext_vector_type(4)));
typedef unsigned u32x2 __attribute__((ext_vector_type(2)));
typedef unsigned u32x4 __attribute__((ext_vector_type(4)));
typedef unsigned long long u64;
#define LAS __attribute__((address_space(3)))
#define GAS __attribute__((address_space(1)))
typedef GAS u64 gu64;
#define RLX_AGENT __ATOMIC_RELAXED, __HIP_MEMORY_SCOPE_AGENT

namespace {

constexpr int kS = 2048, kD = 448;
constexpr int XROW = kD * 2;
constexpr int WROW = 3 * kD * 2;
constexpr int WM_B = 64 * 112;

constexpr int A_FRONT = 34816, A_SLOT = 144 * 128 + 3 * WM_B, A_W = 144 * 128;
constexpr int A_G = A_FRONT + 3 * A_SLOT;
constexpr int B_SLOT = 128 * 128 + 2 * WM_B, B_W = 128 * 128;
constexpr int B_WQ = 97536, B_G = B_WQ + 7 * WM_B, B_EX = B_G + 3584, B_KVL = B_EX + 8192;
constexpr int P2X = 114688;
constexpr int SYNC = 163584;
constexpr int LDS_B = 163840;
static_assert(A_G + 3584 <= SYNC && B_KVL + 4096 <= SYNC && A_FRONT + 2 * A_SLOT == 114688, "lds map");
__host__ __device__ constexpr int a_slot(int c) { return A_FRONT + ((c + 2) % 3) * A_SLOT; }
__host__ __device__ constexpr int b_slot(int c) { return ((c + 2) % 3) * B_SLOT; }
enum { SY_WOA = 0, SY_WOB = 16, SY_D6 = 32, SY_D6Q = 48, SY_WQ = 64, SY_QD = 80, SY_EX = 96, SY_KV = 112, SY_P2 = 128, SY_ZERO = 192, SY_TAG = 240 };

constexpr int WS_E = 0;
constexpr int WS_CLR = 4096;
constexpr int FS = 16;
constexpr int WS_FA = 8192;
constexpr int WS_FB = WS_FA + 16 * 2 * 16 * 8 * FS;
constexpr int WS_KVP = WS_FB + 16 * 4 * 8 * 8 * FS;
constexpr int WS_O = WS_KVP + 8 * 4 * 16 * 1024;
constexpr int WS_END = WS_O + 16 * 8 * 32 * 1024;

constexpr int PT_WS = (WS_END / 8) & ~127;
constexpr unsigned SPIN_CAP = 1u << 14;
constexpr unsigned SENT = 0xffffffffu;
#define QWO 14

#define STAMP(i) do {} while (0)
#define STAMPC(i) do {} while (0)
#define STAMP_FLUSH() do {} while (0)

__device__ __forceinline__ unsigned pk2(float a, float b) {
  f32x2 v = {a, b};
  bf16x2 r = __builtin_convertvector(v, bf16x2);
  return __builtin_bit_cast(unsigned, r);
}
__device__ __forceinline__ u32x2 pk4(f32x4 v) { return (u32x2){pk2(v[0], v[1]), pk2(v[2], v[3])}; }
__device__ __forceinline__ bf16x8 as_frag(u32x4 v) { return __builtin_bit_cast(bf16x8, v); }
__device__ __forceinline__ bf16x8 frag2(u32x2 a, u32x2 b) { return as_frag((u32x4){a[0], a[1], b[0], b[1]}); }
__device__ __forceinline__ f32x4 mfma(bf16x8 a, bf16x8 b, f32x4 c) {
  return __builtin_amdgcn_mfma_f32_16x16x32_bf16(a, b, c, 0, 0, 0);
}
__device__ __forceinline__ bf16x8 lds_tr2(unsigned a0, unsigned a1) {
  bf16x4 lo = __builtin_amdgcn_ds_read_tr16_b64_v4bf16((LAS bf16x4*)(uintptr_t)a0);
  bf16x4 hi = __builtin_amdgcn_ds_read_tr16_b64_v4bf16((LAS bf16x4*)(uintptr_t)a1);
  return __builtin_shufflevector(lo, hi, 0, 1, 2, 3, 4, 5, 6, 7);
}
__device__ __forceinline__ u32x4 lds_ld128(unsigned a) { return *(LAS u32x4*)(uintptr_t)a; }
__device__ __forceinline__ void lds_st128(unsigned a, u32x4 v) { *(LAS u32x4*)(uintptr_t)a = v; }
__device__ __forceinline__ void lds_st64(unsigned a, u32x2 v) { *(LAS u32x2*)(uintptr_t)a = v; }
__device__ __forceinline__ unsigned opaque(unsigned v) { asm volatile("" : "+v"(v)); return v; }

__device__ __forceinline__ void sig(unsigned a) {
  asm volatile("" ::: "memory");
  *(volatile LAS unsigned*)(uintptr_t)a = 1u;
  asm volatile("" ::: "memory");
  __builtin_amdgcn_sched_barrier(0);
}
__device__ __forceinline__ void wait1(unsigned a) {
  while (__builtin_amdgcn_readfirstlane(*(volatile LAS unsigned*)(uintptr_t)a) == 0u) __builtin_amdgcn_s_sleep(1);
  asm volatile("" ::: "memory");
}
__device__ __forceinline__ void wait4(unsigned a) {
  for (;;) {
    const u32x4 v = *(volatile LAS u32x4*)(uintptr_t)a;
    if (__builtin_amdgcn_readfirstlane(v[0] & v[1] & v[2] & v[3])) break;
    __builtin_amdgcn_s_sleep(1);
  }
  asm volatile("" ::: "memory");
}

__device__ __forceinline__ u32x4 make_srd(const void* p, unsigned bytes) {
  uint64_t a = (uint64_t)p;
  u32x4 r;
  r[0] = __builtin_amdgcn_readfirstlane((unsigned)a);
  r[1] = __builtin_amdgcn_readfirstlane((unsigned)(a >> 32) & 0xffffu);
  r[2] = bytes;
  r[3] = 0x00020000u;
  return r;
}
__device__ __forceinline__ void dma16(u32x4 srd, unsigned voff, unsigned soff, unsigned ldsaddr) {
  asm volatile(
      "s_mov_b32 m0, %3\n\t"
      "s_nop 3\n\t"
      "buffer_load_dwordx4 %0, %1, %2 offen lds"
      :
      : "v"(voff), "s"(srd), "s"(soff), "s"(ldsaddr));
}
#define VM_BAR(N) asm volatile("s_waitcnt vmcnt(" #N ") lgkmcnt(0)\n\ts_barrier" ::: "memory")
#define LDS_BAR() asm volatile("s_waitcnt lgkmcnt(0)\n\ts_barrier" ::: "memory")
#define VM_WAIT(N) asm volatile("s_waitcnt vmcnt(" #N ")" ::: "memory")
template <int N> __device__ __forceinline__ void vm_bar() { asm volatile("s_waitcnt vmcnt(%0) lgkmcnt(0)\n\ts_barrier" ::"n"(N) : "memory"); }
#define SB() __builtin_amdgcn_sched_barrier(0)

__device__ __forceinline__ float fmax1(float a, float b) { return __builtin_amdgcn_fmed3f(a, b, 3.0e38f); }
__device__ __forceinline__ void swap16(float& a, float& b) {
  auto r = __builtin_amdgcn_permlane16_swap(__float_as_uint(a), __float_as_uint(b), false, false);
  const unsigned r0 = r[0], r1 = r[1];
  a = __uint_as_float(r0);
  b = __uint_as_float(r1);
}
__device__ __forceinline__ void swap32(float& a, float& b) {
  auto r = __builtin_amdgcn_permlane32_swap(__float_as_uint(a), __float_as_uint(b), false, false);
  const unsigned r0 = r[0], r1 = r[1];
  a = __uint_as_float(r0);
  b = __uint_as_float(r1);
}
template <bool MAX>
__device__ __forceinline__ float rows_reduce4(float v0, float v1, float v2, float v3) {
  swap32(v0, v1);
  swap32(v2, v3);
  float a = MAX ? fmax1(v0, v1) : v0 + v1, b = MAX ? fmax1(v2, v3) : v2 + v3;
  swap16(a, b);
  return MAX ? fmax1(a, b) : a + b;
}
__device__ __forceinline__ void rows_bcast4(float x, float (&o)[4]) {
  float y = x;
  swap16(x, y);
  o[0] = x; o[1] = x; o[2] = y; o[3] = y;
  swap32(o[0], o[1]);
  swap32(o[2], o[3]);
}
__device__ __forceinline__ float row3_bcast(float v) {
  float a = v;
  swap32(a, v);
  a = v;
  swap16(a, v);
  return v;
}
template <int NM, int ND>
__device__ __forceinline__ void interleave() {
#pragma unroll
  for (int i = 0; i < NM; ++i) {
    __builtin_amdgcn_sched_group_barrier(0x8, 1, 0);
    if (i < ND) {
      __builtin_amdgcn_sched_group_barrier(0x2, 1, 0);
      __builtin_amdgcn_sched_group_barrier(0x100, 1, 0);
    }
  }
}
constexpr float kEps = 1e-6f, kLog2e = 1.44269504088896f;
__device__ __forceinline__ float phi(float u) {
  const float e = __builtin_amdgcn_fmed3f(__builtin_amdgcn_exp2f(__builtin_fmaf(u, kLog2e, -kEps * kLog2e)), 0.f, 1.f);
  return fmax1(u, kEps) + e;
}
__device__ __forceinline__ u32x2 phi4(f32x4 v) { return (u32x2){pk2(phi(v[0]), phi(v[1])), pk2(phi(v[2]), phi(v[3]))}; }
__device__ __forceinline__ float sigmoidf(float z) {
  return __builtin_amdgcn_rcpf(1.f + __builtin_amdgcn_exp2f(-1.44269504088896f * z));
}
__device__ __forceinline__ u64 next_tag(u64 e) {
  const u64 s = ((u64)(unsigned)__builtin_amdgcn_readfirstlane((unsigned)(e >> 32)) << 32) |
                (unsigned)__builtin_amdgcn_readfirstlane((unsigned)e);
  return s * 6364136223846793005ull + 1442695040888963407ull;
}
__device__ __forceinline__ u64 uniform64(u64 e) {
  return ((u64)(unsigned)__builtin_amdgcn_readfirstlane((unsigned)(e >> 32)) << 32) |
         (unsigned)__builtin_amdgcn_readfirstlane((unsigned)e);
}
__device__ __forceinline__ void poll_flags(const gu64* p, u64 tag) {
#pragma nounroll
  for (unsigned spins = 0; spins < SPIN_CAP; ++spins) {
    u64 v = __hip_atomic_load(p, RLX_AGENT);
    if (__all(v == tag)) break;
    __builtin_amdgcn_s_sleep(2);
  }
  asm volatile("" ::: "memory");
}
__device__ __forceinline__ void publish(u32x4 v, __amdgpu_buffer_rsrc_t r, unsigned voff) {
#pragma unroll
  for (int i = 0; i < 4; ++i) v[i] = v[i] < SENT - 1u ? v[i] : SENT - 1u;
  __builtin_amdgcn_raw_buffer_store_b128(v, r, voff, 0, 16);
}
__device__ __forceinline__ unsigned lane_id() {
  unsigned l = __builtin_amdgcn_mbcnt_hi(~0u, __builtin_amdgcn_mbcnt_lo(~0u, 0u));
  asm volatile("" : "+v"(l));
  return l;
}
__device__ __forceinline__ int wrap7(int c) { return c >= 7 ? c - 7 : c; }

__global__ __launch_bounds__(512, 2) void gated_mix_kernel(const char* __restrict__ x, const char* __restrict__ wqkvA,
                                                           const char* __restrict__ woA, const char* __restrict__ wqkvB,
                                                           const char* __restrict__ woB,
                                                           const unsigned short* __restrict__ gate_w, char* __restrict__ out,
                                                           char* ws) {
  __shared__ __attribute__((aligned(1024))) char lds[LDS_B];
  const unsigned L0 = (unsigned)(uintptr_t)(LAS char*)lds;
  const uint64_t pc0 = __builtin_amdgcn_s_getpc() & ~127ull;
  const int bid = blockIdx.x;
  const int xcd = bid & 7, idx = bid >> 3;
  const int xj = xcd >> 1, ci = idx >> 3;
  const int br = ci >> 1;
  const int hp = 2 * (br ? (xj ^ 1) : xj) + (ci & 1);
  const int rt = (xcd & 1) * 8 + (idx & 7);
  const int rot = wrap7(wrap7((idx & 7) + 2 * ci + (xj & 1)));
  const int cg = idx & 7, rg = 4 * xcd + ci;

  const int tid = threadIdx.x;
  const int lane = tid & 63;
  const int w = __builtin_amdgcn_readfirstlane(tid >> 6) & 7;
  const int wl = w & 3;
  const int li = lane & 15, g = lane >> 4, q = (lane >> 2) & 3, p = lane & 3;
  const bool g3 = g == 3;
  const f32x4 z4 = {0.f, 0.f, 0.f, 0.f};
  STAMP(0); STAMPC(18);
  unsigned vx0 = (lane >> 3) * XROW + (((lane & 7) ^ (lane >> 4)) << 4);
  unsigned vx1 = vx0 ^ 64u;
  unsigned vw0, vw1;
  {
    const unsigned s0 = wl * 64 + lane, s1 = s0 + 256;
    const unsigned p0 = (s0 * 9363u) >> 16, p1 = (s1 * 9363u) >> 16;
    const unsigned k0 = (p0 & 56) | ((p0 >> 1) & 3) | ((p0 & 1) << 2);
    const unsigned k1 = (p1 & 56) | ((p1 >> 1) & 3) | ((p1 & 1) << 2);
    vw0 = k0 * WROW + hp * 112 + (s0 - p0 * 7) * 16;
    vw1 = k1 * WROW + hp * 112 + (s1 - p1 * 7) * 16;
  }
  asm volatile("" : "+v"(vx0), "+v"(vx1), "+v"(vw0), "+v"(vw1));
  asm volatile("" ::"s"(x), "s"(wqkvA), "s"(woA), "s"(wqkvB), "s"(woB), "s"(gate_w), "s"(out), "s"(ws));
  const __amdgpu_buffer_rsrc_t wsr = __builtin_amdgcn_make_buffer_rsrc(ws, 0, WS_END, 0x00020000);
  u64 tagO;
  auto wo_run = [&](int j0, int j1) {
    const unsigned vwo = (lane >> 3) * XROW + cg * 128 + (((lane & 7) ^ (((lane >> 4) & 3) << 1)) << 4);
    const u32x4 sa = make_srd(woA, kD * XROW), sb = make_srd(woB, kD * XROW);
    unsigned so = (wl + 7 * cg + 4 * (j0 % 14)) * (8 * XROW);
    so = so >= kD * XROW ? so - kD * XROW : so;
#pragma unroll
    for (int j = j0; j < j1; ++j) {
      dma16(j < 14 ? sa : sb, vwo, so, L0 + (wl + 4 * j) * 1024);
      so += 32 * XROW;
      so = so >= kD * XROW ? so - kD * XROW : so;
    }
  };
  const u32x4 srdX = make_srd(x, kS * XROW);
  const u32x4 srdW = make_srd(br ? wqkvB : wqkvA, kD * WROW);
  const bool l3 = wl == 3;
  const int xr = rt * 128 + wl * 32 - (br ? 0 : 16);
  const unsigned sx01 = (xr < 0 ? 0 : xr) * XROW, sx23 = (xr + 16) * XROW;
  const bool l3a = l3 && !br;
  const u32x4 srd2 = l3a ? srdX : srdW;
  const unsigned v2 = l3 ? vw0 : vw1;
  const unsigned v2a = l3a ? vx0 : v2, v2b = l3a ? vx1 : v2;
  const unsigned st2 = l3a ? 128u : 64u * WROW, s2a = l3a ? (rt * 128 + 112) * XROW : 0u;
  const unsigned s2d = l3a ? 8u * XROW : (unsigned)XROW, s2e = l3a ? 8u * XROW : 2u * XROW;
  const unsigned sub2 = (l3 ? wl : wl + 4) * 1024;
  auto chunk = [&](unsigned sl, unsigned lq, unsigned lk, int cc) {
    const unsigned sw = cc * (64 * WROW), sx = cc * 128, s2 = cc * st2 + s2a;
#pragma unroll
    for (int j = 0; j < 4; ++j)
      dma16(srdX, (j & 1) ? vx1 : vx0, (j < 2 ? sx01 : sx23) + (j & 1) * 8 * XROW + sx, sl + (wl * 4 + j) * 1024);
    dma16(srdW, vw0, sw + XROW, lk + wl * 1024);
    dma16(srdW, vw0, sw + 2 * XROW, lk + WM_B + wl * 1024);
    dma16(srd2, v2b, s2 + s2d, l3a ? sl + 17408 : lk + sub2);
    dma16(srd2, v2b, s2 + s2e, l3a ? sl + 17408 : lk + WM_B + sub2);
    if (!br) {
      dma16(srdW, vw0, sw, lq + wl * 1024);
      dma16(srd2, v2a, s2, l3a ? sl + 16384 : lq + sub2);
    }
  };
  unsigned gv = 0, pt = 0;
  u64 epoch = 0;
  {
    int wx = w;
    asm volatile("" : "+s"(wx));
    if (wx < 4) {
      gv = ((const unsigned*)gate_w)[tid < 224 ? tid : 0];
      epoch = __hip_atomic_load((gu64*)(ws + WS_E), RLX_AGENT);
      const char* pb_ = wx == 0 ? ws : (wx == 1 ? out + rg * 64 * XROW : (wx == 2 ? woA : woB));
      const char* pa_ = pb_ + (lane & 7) * (wx == 0 ? PT_WS : (wx == 1 ? 7168 : 50176));
      const unsigned cl = (idx * 4 + wx) * 2 + (lane & 1);
      pa_ = (lane & 8) ? (const char*)(pc0 + (cl < CODE_LINES ? cl : 0) * 128) : pa_;
      pt = __hip_atomic_load((GAS unsigned*)pa_, RLX_AGENT);
    } else {
#pragma nounroll
      for (int c = 0; c < 2; ++c) {
        const unsigned sl = L0 + (br ? (c ? b_slot(1) : b_slot(0)) : (c ? a_slot(1) : a_slot(0)));
        chunk(sl, sl + A_W, sl + (br ? B_W : A_W + WM_B), wrap7(rot + c));
      }
    }
  }

    const int rh = w & 1, hh = w >> 1;
    const unsigned obs = WS_O + ((rt * 8 + rh * 4) * 32 + hp * 2 + hh) * 1024;
    const unsigned kvs = WS_KVP + (hp * 4 + w) * 16 * 1024;
    auto setup = [&](int goff) {
      if (tid < 60) *(LAS unsigned*)(uintptr_t)(L0 + SYNC + tid * 4) = 0u;
      const unsigned lo = (gv & 0xffffu) * 0x10001u, hi = (gv >> 16) * 0x10001u;
      const int pos = tid - rot * 32;
      if (tid < 224) lds_st128(L0 + goff + (pos < 0 ? pos + 224 : pos) * 16, (u32x4){lo, lo, hi, hi});
    };
    u64 clr = 0;
    unsigned pw = 0;
    auto clrp = [&]() { return (const gu64*)(ws + WS_CLR) + hp * 16 + (lane_id() & 15); };
    auto hook = [&](int c, bool before, bool isb) {
      if (c == 0 && !before && isb)
        __builtin_amdgcn_raw_buffer_store_b128((u32x4){SENT, SENT, SENT, SENT}, wsr, lane_id() * 16 + kvs + rt * 1024, 0, 16);
      if (c == 3 && before) {
        asm volatile("" : "+v"(epoch) : "v"(pt));
        tagO = next_tag(epoch);
        *(LAS u64*)(uintptr_t)(L0 + SYNC + SY_TAG) = tagO;
        VM_WAIT(0);
      }
      if (c == 3 && !before && isb)
        __builtin_amdgcn_raw_buffer_store_b64((u32x2){(unsigned)tagO, (unsigned)(tagO >> 32)}, wsr,
                                              (lane_id() | w) == 0 ? WS_CLR + (hp * 16 + rt) * 8 : 0x7ffffff0u, 0, 16);
      if (c == 6 && !before && isb) clr = __hip_atomic_load(clrp(), RLX_AGENT);
      if (c == 6 && !before) {
        const unsigned l = lane_id(), u = (l >> 3) & 3;
        const unsigned o = isb ? WS_O + ((rt * 8 + w * 2 + (u >> 1)) * 32 + 16 + hp * 2 + (u & 1)) * 1024 : obs + u * 32768;
        pw = __builtin_amdgcn_raw_buffer_load_b32(wsr, o + (l & 7) * 128, 0, 16);
      }
    };
    const unsigned xsw = li * 128 + ((g ^ (li >> 1)) << 4);
    const unsigned wln = (8 * g + 2 * q) * 112 + 8 * p + 56 * hh;

  if (__builtin_expect(w >= 4, 1)) {
    if (!br) {
      int cc = wrap7(rot + 1);
      unsigned sl = L0 + a_slot(2);
#pragma nounroll
      for (int c = 2; c < 7; ++c) {
        vm_bar<10>();
        cc = wrap7(cc + 1);
        chunk(sl, sl + A_W, sl + A_W + WM_B, cc);
        sl = sl == L0 + a_slot(0) ? L0 + a_slot(1) : sl + A_SLOT;
      }
      vm_bar<10>();
      if (cg >= 7) { vm_bar<0>(); STAMP_FLUSH(); return; }
      wo_run(0, 4);
      vm_bar<4>();
      wo_run(4, 28);
      VM_WAIT(14);
      sig(L0 + SYNC + SY_WOA + wl * 4);
      VM_WAIT(0);
      asm volatile("" ::"v"(gv), "v"(epoch), "v"(pt));
      sig(L0 + SYNC + SY_WOB + wl * 4);
    } else {
      auto instr = [&](int n) {
        if (n < 56) {
          const int c = n >> 3, j = n & 7, cc = wrap7(c + rot);
          const unsigned sl = L0 + b_slot(c), sw = cc * (64 * WROW), sx = cc * 128;
          if (j < 4) dma16(srdX, (j & 1) ? vx1 : vx0, sx01 + j * 8 * XROW + sx, sl + (wl * 4 + j) * 1024);
          else dma16(srdW, j < 6 ? vw0 : v2, sw + ((j & 1) + 1) * XROW, sl + B_W + (j & 1) * WM_B + (j < 6 ? wl * 1024 : sub2));
        } else {
          const int c = (n - 56) >> 1;
          dma16(srdW, (n & 1) ? v2 : vw0, wrap7(c + rot) * (64 * WROW), L0 + B_WQ + c * WM_B + ((n & 1) ? sub2 : wl * 1024));
        }
      };
      STAMP(1);
      bf16x8 xq[14][2];
      const unsigned xl = L0 + (wl * 32 + li) * 128 + ((g ^ (li >> 1)) << 4);
#pragma unroll
      for (int c = 0; c < 7; ++c) {
        if (c < 6) vm_bar<8>(); else vm_bar<8>();
#pragma unroll
        for (int ks = 0; ks < 2; ++ks) {
          const unsigned xa = (xl ^ (ks * 64)) + b_slot(c);
          xq[c * 2 + ks][0] = as_frag(lds_ld128(xa));
          xq[c * 2 + ks][1] = as_frag(lds_ld128(xa + 2048));
        }
#pragma unroll
        for (int n = (c < 6 ? 8 * c + 16 : 56 + 8); n < (c < 5 ? 8 * c + 24 : (c == 5 ? 56 + 8 : 70)); ++n) instr(n);
      }
      STAMP(4);
      VM_WAIT(0);
      asm volatile("" ::"v"(gv), "v"(epoch), "v"(pt));
      sig(L0 + SYNC + SY_WQ + wl * 4);
      wait4(L0 + SYNC + SY_WQ);
      wait4(L0 + SYNC + SY_EX);
      STAMP(3);
      f32x4 aq[2][2][2];
#pragma unroll
      for (int i = 0; i < 8; ++i) aq[i >> 2][(i >> 1) & 1][i & 1] = (f32x4){kEps, kEps, kEps, kEps};
      const unsigned wb = L0 + B_WQ + (8 * g + 2 * q) * 112 + 8 * p;
      const unsigned gl = L0 + B_G + (8 * g + q) * 8;
      const bool p3 = p == 3;
      const unsigned d1 = p3 ? 32u : 112u;
      const unsigned qa0 = opaque(p3 ? gl : wb + 32), qstep = opaque(p3 ? 256u : 3584u);
      bf16x8 fq[3][4];
      auto rdq = [&](int s) {
        const unsigned wk = wb + s * 3584;
        const unsigned a = qa0 + s * qstep;
        fq[s % 3][0] = lds_tr2(wk, wk + 112);
        fq[s % 3][1] = lds_tr2(a, a + d1);
        fq[s % 3][2] = lds_tr2(wk + 56, wk + 56 + 112);
        fq[s % 3][3] = lds_tr2(wk + 88, wk + 88 + 112);
      };
      rdq(0);
      rdq(1);
#pragma unroll
      for (int s = 0; s < 14; ++s) {
        SB();
        if (s + 2 < 14) rdq(s + 2);
#pragma unroll
        for (int i = 0; i < 4; ++i) {
          aq[0][i >> 1][i & 1] = mfma(fq[s % 3][i], xq[s][0], aq[0][i >> 1][i & 1]);
          aq[1][i >> 1][i & 1] = mfma(fq[s % 3][i], xq[s][1], aq[1][i >> 1][i & 1]);
        }
        interleave<8, 8>();
      }
      sig(L0 + SYNC + SY_QD + wl * 4);
      STAMP(5);
      u32x4 pqf[2][2];
      float omg[2];
#pragma unroll
      for (int t = 0; t < 2; ++t) {
        omg[t] = 1.f - sigmoidf(row3_bcast(aq[t][0][1][0]));
#pragma unroll
        for (int h = 0; h < 2; ++h) {
          const u32x2 a = phi4(aq[t][h][0]), b = phi4(aq[t][h][1]);
          pqf[t][h] = (u32x4){a[0], a[1], g3 ? 0u : b[0], g3 ? 0u : b[1]};
        }
      }
      tagO = uniform64(*(LAS u64*)(uintptr_t)(L0 + SYNC + SY_TAG));
      unsigned ob = WS_O + ((rt * 8 + wl * 2) * 32 + 16 + hp * 2) * 1024 + lane * 16;
      unsigned kb = L0 + B_KVL + lane * 16, zb = L0 + B_KVL + 1024 + (g * 16 + 12) * 16;
      asm volatile("" : "+v"(pqf[0][0]), "+v"(pqf[0][1]), "+v"(pqf[1][0]), "+v"(pqf[1][1]), "+v"(omg[0]), "+v"(omg[1]), "+v"(ob),
                   "+v"(kb), "+v"(zb), "+s"(tagO));
      __builtin_amdgcn_s_setprio(2);
      STAMP(6);
      wait4(L0 + SYNC + SY_KV);
      STAMP(8);
      {
        bf16x8 kv0[2], kv1[2], zf[2];
#pragma unroll
        for (int h = 0; h < 2; ++h) zf[h] = as_frag(lds_ld128(zb + h * 2048));
#pragma unroll
        for (int h = 0; h < 2; ++h) {
          kv0[h] = as_frag(lds_ld128(kb + h * 2048));
          kv1[h] = as_frag(lds_ld128(kb + h * 2048 + 1024));
        }
        SB();
        f32x4 dn[4], n0[4], n1[4];
#pragma unroll
        for (int i = 0; i < 4; ++i) dn[i] = mfma(zf[i >> 1], as_frag(pqf[i & 1][i >> 1]), (f32x4){kEps, kEps, kEps, kEps});
#pragma unroll
        for (int i = 0; i < 4; ++i) {
          n0[i] = mfma(kv0[i >> 1], as_frag(pqf[i & 1][i >> 1]), z4);
          n1[i] = mfma(kv1[i >> 1], as_frag(pqf[i & 1][i >> 1]), z4);
        }
        SB();
#pragma unroll
        for (int i = 0; i < 4; ++i) {
          const float s = omg[i & 1] * __builtin_amdgcn_rcpf(dn[i][0]);
          const u32x2 a = pk4(n0[i] * s), b = pk4(n1[i] * s);
          __builtin_amdgcn_raw_buffer_store_b128((u32x4){a[0], a[1], g3 ? 0u : b[0], b[1]}, wsr, ob + ((i & 1) * 32 + (i >> 1)) * 1024, 0, 16);
        }
      }
      STAMP(9);
      VM_WAIT(0);
      if (lane == 0) __hip_atomic_store((gu64*)(ws + WS_FB) + ((rt * 4 + wl) * 8 + hp) * FS, tagO, RLX_AGENT);
      STAMP(10); STAMPC(19);
      if (cg >= 7) { STAMP_FLUSH(); return; }
    }
  } else if (__builtin_expect(br, 1)) {
      __builtin_amdgcn_s_setprio(2);
      setup(B_G);
      f32x4 ak[4][2], av[4][2];
      {
        const float one = li == 12 ? 1.f : 0.f, i0 = rh ? one : 0.f, i1 = rh ? 0.f : one;
#pragma unroll
        for (int t = 0; t < 4; ++t) {
          ak[t][0] = ak[t][1] = (f32x4){kEps, kEps, kEps, kEps};
          av[t][0] = (f32x4){i0, i0, i0, i0};
          av[t][1] = (f32x4){i1, i1, i1, i1};
        }
      }
      bf16x8 xf[2][4], fw[2][4];
      unsigned xb, wb;
      const bool z0 = p == 3 && rh, z1 = p == 3 && !rh;
      const unsigned zaddr = L0 + SYNC + SY_ZERO;
      auto rd = [&](int ks, int part) {
        const unsigned xa = xb ^ (ks * 64), wk = wb + ks * 3584;
        if (part & 1) {
          const unsigned a = wk + WM_B + (((part >> 1) ^ rh) << 5);
          const bool z = (part >> 1) ? z1 : z0;
          fw[ks][part] = lds_tr2(z ? zaddr : a, z ? zaddr : a + 112);
        } else {
          fw[ks][part] = lds_tr2(wk + (part >> 1) * 32, wk + (part >> 1) * 32 + 112);
        }
        if (part < 2) {
          xf[ks][part * 2] = as_frag(lds_ld128(xa + part * 4096));
          xf[ks][part * 2 + 1] = as_frag(lds_ld128(xa + part * 4096 + 2048));
        }
      };
      auto mm = [&](int ks, int part) {
#pragma unroll
        for (int t = 0; t < 4; ++t) {
          if (part & 1) av[t][part >> 1] = mfma(xf[ks][t], fw[ks][part], av[t][part >> 1]);
          else ak[t][part >> 1] = mfma(xf[ks][t], fw[ks][part], ak[t][part >> 1]);
        }
      };
#pragma unroll
      for (int c = 0; c < 7; ++c) {
        xb = opaque(L0 + b_slot(c) + rh * 8192 + xsw);
        wb = opaque(L0 + b_slot(c) + B_W + wln);
        hook(c, true, true);
        LDS_BAR();
        hook(c, false, true);
        if (c == 0) { STAMP(2); STAMPC(16); }
        if (c == 3) STAMP(3);
        if (c == 6) { STAMP(4); STAMPC(17); }
        SB();
#pragma unroll
        for (int ks = 0; ks < 2; ++ks) {
#pragma unroll
          for (int part = 0; part < 4; ++part) rd(ks, part);
          if (ks == 1 || c > 0) {
#pragma unroll
            for (int part = 0; part < 4; ++part) mm(ks ^ 1, part);
            interleave<16, 12>();
          }
          SB();
        }
      }
      sig(L0 + SYNC + SY_D6 + w * 4);
      mm(1, 0); mm(1, 2);
      SB();
      mm(1, 3); mm(1, 1);
      STAMP(5);
      f32x4 kv[2][2];
      {
        u32x2 pk[4][2], vf[4][2];
#pragma unroll
        for (int t = 0; t < 4; ++t) {
          pk[t][0] = phi4(ak[t][0]);
          pk[t][1] = phi4(ak[t][1]);
          vf[t][0] = pk4(av[t][0]);
          vf[t][1] = pk4(av[t][1]);
        }
#pragma unroll
        for (int e = 1; e >= 0; --e)
#pragma unroll
          for (int d = 0; d < 2; ++d) {
            kv[e][d] = mfma(frag2(pk[0][d], pk[1][d]), frag2(vf[0][e], vf[1][e]), z4);
            kv[e][d] = mfma(frag2(pk[2][d], pk[3][d]), frag2(vf[2][e], vf[3][e]), kv[e][d]);
          }
      }
      lds_st128(L0 + B_EX + w * 2048 + lane * 16, __builtin_bit_cast(u32x4, kv[1][0]));
      lds_st128(L0 + B_EX + w * 2048 + 1024 + lane * 16, __builtin_bit_cast(u32x4, kv[1][1]));
      sig(L0 + SYNC + SY_EX + w * 4);
      wait1(L0 + SYNC + SY_EX + (w ^ 1) * 4);
      const f32x4 s0 = kv[0][0] + __builtin_bit_cast(f32x4, lds_ld128(L0 + B_EX + (w ^ 1) * 2048 + lane * 16));
      const f32x4 s1 = kv[0][1] + __builtin_bit_cast(f32x4, lds_ld128(L0 + B_EX + (w ^ 1) * 2048 + 1024 + lane * 16));
      {
        const u32x2 a = pk4(s0), b = pk4(s1);
        publish((u32x4){a[0], a[1], b[0], b[1]}, wsr, lane * 16 + kvs + rt * 1024);
      }
      STAMP(6);
      if (!__all(clr == tagO)) poll_flags(clrp(), tagO);
      wo_run(0, QWO);
      STAMP(7);
      f32x4 sum0, sum1;
      {
        const unsigned e0 = q == g ? ((p & 1) ? 0x3f800000u : 0x3f80u) : 0u;
        const unsigned ea = p < 2 ? e0 : 0u, eb = p < 2 ? 0u : e0;
        const bf16x8 sel0 = as_frag((u32x4){ea, eb, 0u, 0u}), sel1 = as_frag((u32x4){0u, 0u, ea, eb});
        unsigned vo = kvs + lane * 16;
#pragma nounroll
        for (unsigned spins = 0; spins < SPIN_CAP; ++spins) {
          asm volatile("" : "+v"(vo)::"memory");
          u32x4 pr[16];
#pragma unroll
          for (int r = 0; r < 16; ++r) pr[r] = __builtin_amdgcn_raw_buffer_load_b128(wsr, vo, ((r + 2 * rt) & 15) * 1024, 16);
          SB();
          sum0 = sum1 = z4;
#pragma unroll
          for (int r = 0; r < 16; ++r) {
            sum0 = mfma(sel0, as_frag(pr[r]), sum0);
            sum1 = mfma(sel1, as_frag(pr[r]), sum1);
          }
          if (!__any(sum0[0] != sum0[0])) break;
          unsigned m = 0;
#pragma unroll
          for (int r = 0; r < 16; ++r) m = max(max(max(max(m, pr[r][0]), pr[r][1]), pr[r][2]), pr[r][3]);
          if (!__any(m == SENT)) break;
        }
      }
      asm volatile("" ::"v"(pw));
      STAMP(8);
      lds_st128(L0 + B_KVL + w * 1024 + lane * 16, __builtin_bit_cast(u32x4, frag2(pk4(sum0), pk4(sum1))));
      sig(L0 + SYNC + SY_KV + w * 4);
      STAMP(10); STAMPC(19);
      __builtin_amdgcn_s_setprio(0);
      if (cg >= 7) { STAMP_FLUSH(); return; }
      sig(L0 + SYNC + SY_WOA + w * 4);
      wait4(L0 + SYNC + SY_D6);
      wait4(L0 + SYNC + SY_QD);
      wo_run(QWO, 28);
  } else {
      asm volatile("; role A");
      setup(A_G);
      f32x4 aq[4][2], ak[5][2], av[5][2];
#pragma unroll
      for (int t = 0; t < 5; ++t)
#pragma unroll
        for (int f = 0; f < 2; ++f) { ak[t][f] = av[t][f] = z4; if (t < 4) aq[t][f] = z4; }
      const unsigned gl = L0 + A_G + (8 * g + q) * 8;
      const bool p3 = p == 3;
      const unsigned d1 = p3 ? 32u : 112u;
      bf16x8 xf[2][5], fq[2][2], fk[2][2], fv[2][2];
      unsigned xb, wb;
      auto rd = [&](int c, int ks, int part) {
        const unsigned xa = xb ^ (ks * 64), wk = wb + ks * 3584;
        if (part == 0) {
          fk[ks][0] = lds_tr2(wk + WM_B, wk + WM_B + 112);
          xf[ks][0] = as_frag(lds_ld128(xa));
          xf[ks][1] = as_frag(lds_ld128(xa + 2048));
        } else if (part == 1) {
          fv[ks][0] = lds_tr2(wk + 2 * WM_B, wk + 2 * WM_B + 112);
          xf[ks][2] = as_frag(lds_ld128(xa + 4096));
        } else if (part == 2) {
          fq[ks][0] = lds_tr2(wk, wk + 112);
          xf[ks][3] = as_frag(lds_ld128(xa + 6144));
        } else if (part == 3) {
          fk[ks][1] = lds_tr2(wk + WM_B + 32, wk + WM_B + 32 + 112);
          xf[ks][4] = as_frag(lds_ld128(xa + 8192));
        } else if (part == 4) {
          fv[ks][1] = lds_tr2(wk + 2 * WM_B + 32, wk + 2 * WM_B + 32 + 112);
        } else {
          const unsigned a = p3 ? gl + c * 512 + ks * 256 : wk + 32;
          fq[ks][1] = lds_tr2(a, a + d1);
        }
      };
      auto mm = [&](int ks, int part) {
        const int f = part / 3, m = part % 3;
#pragma unroll
        for (int t = 0; t < 5; ++t) {
          if (m == 0) ak[t][f] = mfma(fk[ks][f], xf[ks][t], ak[t][f]);
          if (m == 1) av[t][f] = mfma(xf[ks][t], fv[ks][f], av[t][f]);
          if (m == 2 && t > 0) aq[t - 1][f] = mfma(fq[ks][f], xf[ks][t], aq[t - 1][f]);
        }
      };
#pragma unroll
      for (int c = 0; c < 7; ++c) {
        xb = opaque(L0 + a_slot(c) + rh * 8192 + xsw);
        wb = opaque(L0 + a_slot(c) + A_W + wln);
        hook(c, true, false);
        LDS_BAR();
        hook(c, false, false);
        if (c == 0) { STAMP(2); STAMPC(16); }
        if (c == 3) STAMP(3);
        if (c == 6) { STAMP(4); STAMPC(17); }
        SB();
#pragma unroll
        for (int ks = 0; ks < 2; ++ks) {
#pragma unroll
          for (int part = 0; part < 6; ++part) rd(c, ks, part);
          if (ks == 1 || c > 0) {
#pragma unroll
            for (int part = 0; part < 6; ++part) mm(ks ^ 1, part);
            interleave<28, 17>();
          }
          SB();
        }
      }
      sig(L0 + SYNC + SY_D6 + w * 4);
      mm(1, 0); mm(1, 3); mm(1, 2); mm(1, 5);
      SB();
      mm(1, 1); mm(1, 4);
      STAMP(5);
      const bool odd = li & 1;
      const int kk0 = 4 * g + (li & 1);
      const bool own0 = kk0 <= li, own1 = kk0 + 2 <= li;
      const float sc = 0.18898223650461362f * kLog2e;
      const float ninf = (rt == 0 && rh == 0) ? -__builtin_inff() : 0.f;
      const unsigned l16 = lane_id() * 16;
      bf16x8 kf[5];
#pragma unroll
      for (int t = 0; t < 5; ++t) kf[t] = frag2(pk4(ak[t][0]), pk4(ak[t][1]));
      f32x4 sp[4], so[4];
#pragma unroll
      for (int t = 0; t < 4; ++t) {
        const u32x2 b = pk4(aq[t][1]);
        const bf16x8 qf = frag2(pk4(aq[t][0]), (u32x2){g3 ? 0u : b[0], g3 ? 0u : b[1]});
        sp[t] = mfma(kf[t], qf, t == 0 ? (f32x4){ninf, ninf, ninf, ninf} : z4);
        so[t] = mfma(kf[t + 1], qf, z4);
      }
      float ug;
      {
        float z0 = aq[0][1][0], z1 = aq[1][1][0], z2 = aq[2][1][0], z3 = aq[3][1][0];
        swap32(z0, z1);
        swap32(z2, z3);
        swap16(z1, z3);
        ug = 1.f + __builtin_amdgcn_exp2f(-kLog2e * z3);
      }
      u32x2 vf[5][2];
#pragma unroll
      for (int t = 0; t < 5; ++t) {
        vf[t][0] = pk4(av[t][0]);
        vf[t][1] = pk4(av[t][1]);
      }
      float l0[4], l1[4], bc[4];
#pragma unroll
      for (int t = 0; t < 4; ++t) {
        float a0 = so[t][0], a2 = so[t][2], b0 = sp[t][0], b2 = sp[t][2];
        const float c0 = own0 ? a0 : b0, c1 = own0 ? so[t][1] : sp[t][1], c2 = own1 ? a2 : b2, c3 = own1 ? so[t][3] : sp[t][3];
        l0[t] = odd ? c1 : c0;
        l1[t] = odd ? c3 : c2;
      }
      rows_bcast4(rows_reduce4<true>(fmax1(l0[0], l1[0]), fmax1(l0[1], l1[1]), fmax1(l0[2], l1[2]), fmax1(l0[3], l1[3])) * -sc, bc);
#pragma unroll
      for (int t = 0; t < 4; ++t) {
        l0[t] = __builtin_amdgcn_exp2f(__builtin_fmaf(l0[t], sc, bc[t]));
        l1[t] = __builtin_amdgcn_exp2f(__builtin_fmaf(l1[t], sc, bc[t]));
      }
      rows_bcast4(__builtin_amdgcn_rcpf(rows_reduce4<false>(l0[0] + l1[0], l0[1] + l1[1], l0[2] + l1[2], l0[3] + l1[3]) * ug), bc);
      const unsigned sh = odd ? 16u : 0u;
      const unsigned mk0 = own0 ? 0u : 0xffffu, mk1 = own1 ? 0u : 0xffffu;
#pragma unroll
      for (int t = 0; t < 4; ++t) {
        const unsigned pe = pk2(l0[t] * bc[t], l1[t] * bc[t]), ph = pe >> 16;
        const bf16x8 pf = as_frag((u32x4){(pe & mk0) << sh, (ph & mk1) << sh, (pe & (mk0 ^ 0xffffu)) << sh, (ph & (mk1 ^ 0xffffu)) << sh});
        const f32x4 r0 = mfma(frag2(vf[t][0], vf[t + 1][0]), pf, z4);
        const f32x4 r1 = mfma(frag2(vf[t][1], vf[t + 1][1]), pf, z4);
        const u32x2 c0 = pk4(r0), c1 = pk4(r1);
        __builtin_amdgcn_raw_buffer_store_b128((u32x4){c0[0], c0[1], g3 ? 0u : c1[0], g3 ? 0u : c1[1]}, wsr, l16 + obs + t * 32768, 0, 16);
      }
      STAMP(9);
      VM_WAIT(0);
      asm volatile("" ::"v"(pw));
      if (l16 == 0) __hip_atomic_store((gu64*)(ws + WS_FA) + ((rt * 2 + rh) * 16 + hp * 2 + hh) * FS, tagO, RLX_AGENT);
      STAMP(10); STAMPC(19);
      if (cg >= 7) {
        if (bid == 56 && w == 0) {
#pragma nounroll
          for (int k = 0; k < 4; ++k) {
            const int i = (k & 1) * 64 + lane;
            poll_flags(k < 2 ? (gu64*)(ws + WS_CLR) + i : (gu64*)(ws + WS_FA) + ((i >> 3) * 32 + (i & 7) * 2) * FS, tagO);
          }
          if (lane == 0) __hip_atomic_store((gu64*)(ws + WS_E), tagO, RLX_AGENT);
        }
        STAMP_FLUSH();
        return;
      }
  }

  const int pb = w >> 2;
  wait4(L0 + SYNC + (pb ? SY_WOB : SY_WOA));
  const unsigned sw = (2 * g + (q >> 1)) & 3;
  const unsigned rb = L0 + (pb * 448 + 4 * g + q) * 128 + 8 * p;
  unsigned wa[4], wa2[4];
#pragma unroll
  for (int v = 0; v < 4; ++v) {
    wa[v] = opaque(rb + ((v ^ sw) << 5));
    wa2[v] = opaque(wa[v] + (g3 ? 0u : 2048u));
  }
  auto wfrag = [&](int h, int ct) {
    const int v = ct ^ (2 * (h & 1));
    return lds_tr2(wa[v] + h * 3584, wa2[v] + h * 3584);
  };
  constexpr int NPRE = 8;
  bf16x8 wf[16][4];
#pragma unroll
  for (int h = 0; h < NPRE; ++h)
#pragma unroll
    for (int ct = 0; ct < 4; ++ct) wf[h][ct] = wfrag(h, ct);
  STAMP(11);
  const unsigned ub = WS_O + ((rg * 4 + wl) * 32 + pb * 16) * 1024;
  tagO = uniform64(*(LAS u64*)(uintptr_t)(L0 + SYNC + SY_TAG));
  {
    const gu64* fp = pb ? (gu64*)(ws + WS_FB) + ((rg * 2 + (wl >> 1)) * 8 + (li & 7)) * FS : (gu64*)(ws + WS_FA) + (rg * 16 + li) * FS;
    const u64 f0 = __hip_atomic_load(fp, RLX_AGENT);
    if (br && !pb) { VM_WAIT(0); sig(L0 + SYNC + SY_WOB + wl * 4); }
    if (!__all(f0 == tagO)) poll_flags(fp, tagO);
  }
  STAMP(12);
  u32x4 of[16];
  {
    unsigned vo = ub + lane * 16;
    asm volatile("" : "+v"(vo)::"memory");
#pragma unroll
    for (int h = 0; h < 16; ++h) of[h] = __builtin_amdgcn_raw_buffer_load_b128(wsr, vo, ((h + 2 * cg) & 15) * 1024, 16);
    SB();
  }
  f32x4 acc[4] = {z4, z4, z4, z4};
#pragma unroll
  for (int h = 0; h < 16; ++h) {
#pragma unroll
    for (int ct = 0; ct < 4; ++ct) acc[ct] = mfma(wf[h][ct], as_frag(of[h]), acc[ct]);
    SB();
    if (h + NPRE < 16) {
#pragma unroll
      for (int ct = 0; ct < 4; ++ct) wf[h + NPRE][ct] = wfrag(h + NPRE, ct);
    }
    SB();
  }
  STAMP(13);
  const unsigned xa = L0 + P2X + wl * 4096 + lane * 16;
  if (!pb) {
    wait4(L0 + SYNC + SY_D6);
#pragma unroll
    for (int ct = 0; ct < 4; ++ct) lds_st128(xa + ct * 1024, __builtin_bit_cast(u32x4, acc[ct]));
    sig(L0 + SYNC + SY_P2 + wl * 4);
  } else {
    f32x4 pa[4];
    for (;;) {
      const unsigned f = *(volatile LAS unsigned*)(uintptr_t)(L0 + SYNC + SY_P2 + wl * 4);
#pragma unroll
      for (int ct = 0; ct < 4; ++ct) pa[ct] = *(volatile LAS f32x4*)(uintptr_t)(xa + ct * 1024);
      if (__builtin_amdgcn_readfirstlane(f)) break;
      __builtin_amdgcn_s_sleep(1);
    }
    char* orow = out + (rg * 64 + wl * 16 + li) * XROW + cg * 128 + (g & 1) * 32 + (g >> 1) * 16;
#pragma unroll
    for (int k = 0; k < 2; ++k) {
      const u32x2 a = pk4(acc[2 * k] + pa[2 * k]), b = pk4(acc[2 * k + 1] + pa[2 * k + 1]);
      const auto r0 = __builtin_amdgcn_permlane16_swap(a[0], b[0], false, false);
      const auto r1 = __builtin_amdgcn_permlane16_swap(a[1], b[1], false, false);
      *(u32x4*)(orow + k * 64) = (u32x4){r0[0], r1[0], r0[1], r1[1]};
    }
  }
  STAMP(14);
  STAMP(15);
  STAMP_FLUSH();
}

}

extern "C" void kernel_launch(void* const* d_in, const int* in_sizes, int n_in, void* d_out, int out_size, void* d_ws,
                              size_t ws_size, hipStream_t stream) {
  (void)in_sizes; (void)n_in; (void)out_size; (void)ws_size;
  gated_mix_kernel<<<256, 512, 0, stream>>>((const char*)d_in[0], (const char*)d_in[1], (const char*)d_in[2],
                                            (const char*)d_in[4], (const char*)d_in[5],
                                            (const unsigned short*)d_in[6], (char*)d_out, (char*)d_ws);
}
```

```cpp
#include <hip/hip_runtime.h>
#include <stdint.h>

typedef __bf16 bf16x2 __attribute__((ext_vector_type(2)));
typedef __bf16 bf16x4 __attribute__((ext_vector_type(4)));
typedef __bf16 bf16x8 __attribute__((ext_vector_type(8)));
typedef float f32x2 __attribute__((ext_vector_type(2)));
typedef float f32x4 __attribute__((ext_vector_type(4)));
typedef unsigned u32x2 __attribute__((ext_vector_type(2)));
typedef unsigned u32x4 __attribute__((ext_vector_type(4)));
typedef unsigned long long u64;
#define LAS __attribute__((address_space(3)))
#define GAS __attribute__((address_space(1)))
typedef GAS u64 gu64;
#define RLX_AGENT __ATOMIC_RELAXED, __HIP_MEMORY_SCOPE_AGENT

namespace {

constexpr int kS = 2048, kD = 448;
constexpr int XROW = kD * 2;
constexpr int WROW = 3 * kD * 2;
constexpr int WM_B = 64 * 112;

constexpr int A_FRONT = 34816, A_SLOT = 144 * 128 + 3 * WM_B, A_W = 144 * 128;
constexpr int A_G = A_FRONT + 3 * A_SLOT;
constexpr int B_SLOT = 128 * 128 + 2 * WM_B, B_W = 128 * 128;
constexpr int B_WQ = 97536, B_G = B_WQ + 7 * WM_B, B_EX = B_G + 3584, B_KVL = B_EX + 8192;
constexpr int P2X = 114688;
constexpr int SYNC = 163584;
constexpr int LDS_B = 163840;
static_assert(A_G + 3584 <= SYNC && B_KVL + 4096 <= SYNC && A_FRONT + 2 * A_SLOT == 114688, "lds map");
__host__ __device__ constexpr int a_slot(int c) { return A_FRONT + ((c + 2) % 3) * A_SLOT; }
__host__ __device__ constexpr int b_slot(int c) { return ((c + 2) % 3) * B_SLOT; }
enum { SY_WOA = 0, SY_WOB = 16, SY_D6 = 32, SY_D6Q = 48, SY_WQ = 64, SY_QD = 80, SY_EX = 96, SY_KV = 112, SY_P2 = 128, SY_ZERO = 192, SY_TAG = 240 };

constexpr int WS_E = 0;
constexpr int WS_CLR = 4096;
constexpr int FS = 16;
constexpr int WS_FA = 8192;
constexpr int WS_FB = WS_FA + 16 * 2 * 16 * 8 * FS;
constexpr int WS_KVP = WS_FB + 16 * 4 * 8 * 8 * FS;
constexpr int WS_O = WS_KVP + 8 * 4 * 16 * 1024;
constexpr int WS_END = WS_O + 16 * 8 * 32 * 1024;

constexpr int PT_WS = (WS_END / 8) & ~127;
constexpr unsigned SPIN_CAP = 1u << 14;
constexpr unsigned SENT = 0xffffffffu;
#define QWO 14

#define STAMP(i) do {} while (0)
#define STAMPC(i) do {} while (0)
#define STAMP_FLUSH() do {} while (0)

__device__ __forceinline__ unsigned pk2(float a, float b) {
  f32x2 v = {a, b};
  bf16x2 r = __builtin_convertvector(v, bf16x2);
  return __builtin_bit_cast(unsigned, r);
}
__device__ __forceinline__ u32x2 pk4(f32x4 v) { return (u32x2){pk2(v[0], v[1]), pk2(v[2], v[3])}; }
__device__ __forceinline__ bf16x8 as_frag(u32x4 v) { return __builtin_bit_cast(bf16x8, v); }
__device__ __forceinline__ bf16x8 frag2(u32x2 a, u32x2 b) { return as_frag((u32x4){a[0], a[1], b[0], b[1]}); }
__device__ __forceinline__ f32x4 mfma(bf16x8 a, bf16x8 b, f32x4 c) {
  return __builtin_amdgcn_mfma_f32_16x16x32_bf16(a, b, c, 0, 0, 0);
}
__device__ __forceinline__ bf16x8 lds_tr2(unsigned a0, unsigned a1) {
  bf16x4 lo = __builtin_amdgcn_ds_read_tr16_b64_v4bf16((LAS bf16x4*)(uintptr_t)a0);
  bf16x4 hi = __builtin_amdgcn_ds_read_tr16_b64_v4bf16((LAS bf16x4*)(uintptr_t)a1);
  return __builtin_shufflevector(lo, hi, 0, 1, 2, 3, 4, 5, 6, 7);
}
__device__ __forceinline__ u32x4 lds_ld128(unsigned a) { return *(LAS u32x4*)(uintptr_t)a; }
__device__ __forceinline__ void lds_st128(unsigned a, u32x4 v) { *(LAS u32x4*)(uintptr_t)a = v; }
__device__ __forceinline__ void lds_st64(unsigned a, u32x2 v) { *(LAS u32x2*)(uintptr_t)a = v; }
__device__ __forceinline__ unsigned opaque(unsigned v) { asm volatile("" : "+v"(v)); return v; }

__device__ __forceinline__ void sig(unsigned a) {
  asm volatile("" ::: "memory");
  *(volatile LAS unsigned*)(uintptr_t)a = 1u;
  asm volatile("" ::: "memory");
  __builtin_amdgcn_sched_barrier(0);
}
__device__ __forceinline__ void wait1(unsigned a) {
  while (__builtin_amdgcn_readfirstlane(*(volatile LAS unsigned*)(uintptr_t)a) == 0u) __builtin_amdgcn_s_sleep(1);
  asm volatile("" ::: "memory");
}
__device__ __forceinline__ void wait4(unsigned a) {
  for (;;) {
    const u32x4 v = *(volatile LAS u32x4*)(uintptr_t)a;
    if (__builtin_amdgcn_readfirstlane(v[0] & v[1] & v[2] & v[3])) break;
    __builtin_amdgcn_s_sleep(1);
  }
  asm volatile("" ::: "memory");
}

__device__ __forceinline__ u32x4 make_srd(const void* p, unsigned bytes) {
  uint64_t a = (uint64_t)p;
  u32x4 r;
  r[0] = __builtin_amdgcn_readfirstlane((unsigned)a);
  r[1] = __builtin_amdgcn_readfirstlane((unsigned)(a >> 32) & 0xffffu);
  r[2] = bytes;
  r[3] = 0x00020000u;
  return r;
}
__device__ __forceinline__ void dma16(u32x4 srd, unsigned voff, unsigned soff, unsigned ldsaddr) {
  asm volatile(
      "s_mov_b32 m0, %3\n\t"
      "s_nop 3\n\t"
      "buffer_load_dwordx4 %0, %1, %2 offen lds"
      :
      : "v"(voff), "s"(srd), "s"(soff), "s"(ldsaddr));
}
#define VM_BAR(N) asm volatile("s_waitcnt vmcnt(" #N ") lgkmcnt(0)\n\ts_barrier" ::: "memory")
#define LDS_BAR() asm volatile("s_waitcnt lgkmcnt(0)\n\ts_barrier" ::: "memory")
#define VM_WAIT(N) asm volatile("s_waitcnt vmcnt(" #N ")" ::: "memory")
template <int N> __device__ __forceinline__ void vm_bar() { asm volatile("s_waitcnt vmcnt(%0) lgkmcnt(0)\n\ts_barrier" ::"n"(N) : "memory"); }
#define SB() __builtin_amdgcn_sched_barrier(0)

__device__ __forceinline__ float fmax1(float a, float b) { return __builtin_amdgcn_fmed3f(a, b, 3.0e38f); }
__device__ __forceinline__ void swap16(float& a, float& b) {
  auto r = __builtin_amdgcn_permlane16_swap(__float_as_uint(a), __float_as_uint(b), false, false);
  const unsigned r0 = r[0], r1 = r[1];
  a = __uint_as_float(r0);
  b = __uint_as_float(r1);
}
__device__ __forceinline__ void swap32(float& a, float& b) {
  auto r = __builtin_amdgcn_permlane32_swap(__float_as_uint(a), __float_as_uint(b), false, false);
  const unsigned r0 = r[0], r1 = r[1];
  a = __uint_as_float(r0);
  b = __uint_as_float(r1);
}
template <bool MAX>
__device__ __forceinline__ float rows_reduce4(float v0, float v1, float v2, float v3) {
  swap32(v0, v1);
  swap32(v2, v3);
  float a = MAX ? fmax1(v0, v1) : v0 + v1, b = MAX ? fmax1(v2, v3) : v2 + v3;
  swap16(a, b);
  return MAX ? fmax1(a, b) : a + b;
}
__device__ __forceinline__ void rows_bcast4(float x, float (&o)[4]) {
  float y = x;
  swap16(x, y);
  o[0] = x; o[1] = x; o[2] = y; o[3] = y;
  swap32(o[0], o[1]);
  swap32(o[2], o[3]);
}
__device__ __forceinline__ float row3_bcast(float v) {
  float a = v;
  swap32(a, v);
  a = v;
  swap16(a, v);
  return v;
}
template <int NM, int ND>
__device__ __forceinline__ void interleave() {
#pragma unroll
  for (int i = 0; i < NM; ++i) {
    __builtin_amdgcn_sched_group_barrier(0x8, 1, 0);
    if (i < ND) {
      __builtin_amdgcn_sched_group_barrier(0x2, 1, 0);
      __builtin_amdgcn_sched_group_barrier(0x100, 1, 0);
    }
  }
}
constexpr float kEps = 1e-6f, kLog2e = 1.44269504088896f;
__device__ __forceinline__ float phi(float u) {
  const float e = __builtin_amdgcn_fmed3f(__builtin_amdgcn_exp2f(__builtin_fmaf(u, kLog2e, -kEps * kLog2e)), 0.f, 1.f);
  return fmax1(u, kEps) + e;
}
__device__ __forceinline__ u32x2 phi4(f32x4 v) { return (u32x2){pk2(phi(v[0]), phi(v[1])), pk2(phi(v[2]), phi(v[3]))}; }
__device__ __forceinline__ float sigmoidf(float z) {
  return __builtin_amdgcn_rcpf(1.f + __builtin_amdgcn_exp2f(-1.44269504088896f * z));
}
__device__ __forceinline__ u64 next_tag(u64 e) {
  const u64 s = ((u64)(unsigned)__builtin_amdgcn_readfirstlane((unsigned)(e >> 32)) << 32) |
                (unsigned)__builtin_amdgcn_readfirstlane((unsigned)e);
  return s * 6364136223846793005ull + 1442695040888963407ull;
}
__device__ __forceinline__ u64 uniform64(u64 e) {
  return ((u64)(unsigned)__builtin_amdgcn_readfirstlane((unsigned)(e >> 32)) << 32) |
         (unsigned)__builtin_amdgcn_readfirstlane((unsigned)e);
}
__device__ __forceinline__ void poll_flags(const gu64* p, u64 tag) {
#pragma nounroll
  for (unsigned spins = 0; spins < SPIN_CAP; ++spins) {
    u64 v = __hip_atomic_load(p, RLX_AGENT);
    if (__all(v == tag)) break;
    __builtin_amdgcn_s_sleep(2);
  }
  asm volatile("" ::: "memory");
}
__device__ __forceinline__ void publish(u32x4 v, __amdgpu_buffer_rsrc_t r, unsigned voff) {
#pragma unroll
  for (int i = 0; i < 4; ++i) v[i] = v[i] < SENT - 1u ? v[i] : SENT - 1u;
  __builtin_amdgcn_raw_buffer_store_b128(v, r, voff, 0, 16);
}
__device__ __forceinline__ unsigned lane_id() {
  unsigned l = __builtin_amdgcn_mbcnt_hi(~0u, __builtin_amdgcn_mbcnt_lo(~0u, 0u));
  asm volatile("" : "+v"(l));
  return l;
}
__device__ __forceinline__ int wrap7(int c) { return c >= 7 ? c - 7 : c; }

__global__ __launch_bounds__(512, 2) void gated_mix_kernel(const char* __restrict__ x, const char* __restrict__ wqkvA,
                                                           const char* __restrict__ woA, const char* __restrict__ wqkvB,
                                                           const char* __restrict__ woB,
                                                           const unsigned short* __restrict__ gate_w, char* __restrict__ out,
                                                           char* ws) {
  __shared__ __attribute__((aligned(1024))) char lds[LDS_B];
  const unsigned L0 = (unsigned)(uintptr_t)(LAS char*)lds;
  const int bid = blockIdx.x;
  const int xcd = bid & 7, idx = bid >> 3;
  const int xj = xcd >> 1, ci = idx >> 3;
  const int br = ci >> 1;
  const int hp = 2 * (br ? (xj ^ 1) : xj) + (ci & 1);
  const int rt = (xcd & 1) * 8 + (idx & 7);
  const int rot = wrap7(wrap7((idx & 7) + 2 * ci + (xj & 1)));
  const int cg = idx & 7, rg = 4 * xcd + ci;

  const int tid = threadIdx.x;
  const int lane = tid & 63;
  const int w = __builtin_amdgcn_readfirstlane(tid >> 6) & 7;
  const int wl = w & 3;
  const int li = lane & 15, g = lane >> 4, q = (lane >> 2) & 3, p = lane & 3;
  const bool g3 = g == 3;
  const f32x4 z4 = {0.f, 0.f, 0.f, 0.f};
  STAMP(0); STAMPC(18);
  unsigned vx0 = (lane >> 3) * XROW + (((lane & 7) ^ (lane >> 4)) << 4);
  unsigned vx1 = vx0 ^ 64u;
  unsigned vw0, vw1;
  {
    const unsigned s0 = wl * 64 + lane, s1 = s0 + 256;
    const unsigned p0 = (s0 * 9363u) >> 16, p1 = (s1 * 9363u) >> 16;
    const unsigned k0 = (p0 & 56) | ((p0 >> 1) & 3) | ((p0 & 1) << 2);
    const unsigned k1 = (p1 & 56) | ((p1 >> 1) & 3) | ((p1 & 1) << 2);
    vw0 = k0 * WROW + hp * 112 + (s0 - p0 * 7) * 16;
    vw1 = k1 * WROW + hp * 112 + (s1 - p1 * 7) * 16;
  }
  asm volatile("" : "+v"(vx0), "+v"(vx1), "+v"(vw0), "+v"(vw1));
  asm volatile("" ::"s"(x), "s"(wqkvA), "s"(woA), "s"(wqkvB), "s"(woB), "s"(gate_w), "s"(out), "s"(ws));
  const __amdgpu_buffer_rsrc_t wsr = __builtin_amdgcn_make_buffer_rsrc(ws, 0, WS_END, 0x00020000);
  u64 tagO;
  auto wo_run = [&](int j0, int j1) {
    const unsigned vwo = (lane >> 3) * XROW + cg * 128 + (((lane & 7) ^ (((lane >> 4) & 3) << 1)) << 4);
    const u32x4 sa = make_srd(woA, kD * XROW), sb = make_srd(woB, kD * XROW);
    unsigned so = (wl + 7 * cg + 4 * (j0 % 14)) * (8 * XROW);
    so = so >= kD * XROW ? so - kD * XROW : so;
#pragma unroll
    for (int j = j0; j < j1; ++j) {
      dma16(j < 14 ? sa : sb, vwo, so, L0 + (wl + 4 * j) * 1024);
      so += 32 * XROW;
      so = so >= kD * XROW ? so - kD * XROW : so;
    }
  };
  const u32x4 srdX = make_srd(x, kS * XROW);
  const u32x4 srdW = make_srd(br ? wqkvB : wqkvA, kD * WROW);
  const bool l3 = wl == 3;
  const int xr = rt * 128 + wl * 32 - (br ? 0 : 16);
  const unsigned sx01 = (xr < 0 ? 0 : xr) * XROW, sx23 = (xr + 16) * XROW;
  const bool l3a = l3 && !br;
  const u32x4 srd2 = l3a ? srdX : srdW;
  const unsigned v2 = l3 ? vw0 : vw1;
  const unsigned v2a = l3a ? vx0 : v2, v2b = l3a ? vx1 : v2;
  const unsigned st2 = l3a ? 128u : 64u * WROW, s2a = l3a ? (rt * 128 + 112) * XROW : 0u;
  const unsigned s2d = l3a ? 8u * XROW : (unsigned)XROW, s2e = l3a ? 8u * XROW : 2u * XROW;
  const unsigned sub2 = (l3 ? wl : wl + 4) * 1024;
  auto chunk = [&](unsigned sl, unsigned lq, unsigned lk, int cc) {
    const unsigned sw = cc * (64 * WROW), sx = cc * 128, s2 = cc * st2 + s2a;
#pragma unroll
    for (int j = 0; j < 4; ++j)
      dma16(srdX, (j & 1) ? vx1 : vx0, (j < 2 ? sx01 : sx23) + (j & 1) * 8 * XROW + sx, sl + (wl * 4 + j) * 1024);
    dma16(srdW, vw0, sw + XROW, lk + wl * 1024);
    dma16(srdW, vw0, sw + 2 * XROW, lk + WM_B + wl * 1024);
    dma16(srd2, v2b, s2 + s2d, l3a ? sl + 17408 : lk + sub2);
    dma16(srd2, v2b, s2 + s2e, l3a ? sl + 17408 : lk + WM_B + sub2);
    if (!br) {
      dma16(srdW, vw0, sw, lq + wl * 1024);
      dma16(srd2, v2a, s2, l3a ? sl + 16384 : lq + sub2);
    }
  };
  unsigned gv = 0, pt = 0;
  u64 epoch = 0;
  {
    int wx = w;
    asm volatile("" : "+s"(wx));
    if (wx < 4) {
      gv = ((const unsigned*)gate_w)[tid < 224 ? tid : 0];
      epoch = __hip_atomic_load((gu64*)(ws + WS_E), RLX_AGENT);
      const char* pb_ = wx == 0 ? ws : (wx == 1 ? out + rg * 64 * XROW : (wx == 2 ? woA : woB));
      pt = __hip_atomic_load((GAS unsigned*)(pb_ + (lane & 7) * (wx == 0 ? PT_WS : (wx == 1 ? 7168 : 50176))), RLX_AGENT);
    } else {
#pragma nounroll
      for (int c = 0; c < 2; ++c) {
        const unsigned sl = L0 + (br ? (c ? b_slot(1) : b_slot(0)) : (c ? a_slot(1) : a_slot(0)));
        chunk(sl, sl + A_W, sl + (br ? B_W : A_W + WM_B), wrap7(rot + c));
      }
    }
  }

    const int rh = w & 1, hh = w >> 1;
    const unsigned obs = WS_O + ((rt * 8 + rh * 4) * 32 + hp * 2 + hh) * 1024;
    const unsigned kvs = WS_KVP + (hp * 4 + w) * 16 * 1024;
    auto setup = [&](int goff) {
      if (tid < 60) *(LAS unsigned*)(uintptr_t)(L0 + SYNC + tid * 4) = 0u;
      const unsigned lo = (gv & 0xffffu) * 0x10001u, hi = (gv >> 16) * 0x10001u;
      const int pos = tid - rot * 32;
      if (tid < 224) lds_st128(L0 + goff + (pos < 0 ? pos + 224 : pos) * 16, (u32x4){lo, lo, hi, hi});
    };
    u64 clr = 0;
    unsigned pw = 0;
    auto clrp = [&]() { return (const gu64*)(ws + WS_CLR) + hp * 16 + (lane_id() & 15); };
    auto hook = [&](int c, bool before, bool isb) {
      if (c == 0 && !before && isb)
        __builtin_amdgcn_raw_buffer_store_b128((u32x4){SENT, SENT, SENT, SENT}, wsr, lane_id() * 16 + kvs + rt * 1024, 0, 16);
      if (c == 3 && before) {
        asm volatile("" : "+v"(epoch) : "v"(pt));
        tagO = next_tag(epoch);
        *(LAS u64*)(uintptr_t)(L0 + SYNC + SY_TAG) = tagO;
        VM_WAIT(0);
      }
      if (c == 3 && !before && isb)
        __builtin_amdgcn_raw_buffer_store_b64((u32x2){(unsigned)tagO, (unsigned)(tagO >> 32)}, wsr,
                                              (lane_id() | w) == 0 ? WS_CLR + (hp * 16 + rt) * 8 : 0x7ffffff0u, 0, 16);
      if (c == 6 && !before && isb) clr = __hip_atomic_load(clrp(), RLX_AGENT);
      if (c == 6 && !before) {
        const unsigned l = lane_id(), u = (l >> 3) & 3;
        const unsigned o = isb ? WS_O + ((rt * 8 + w * 2 + (u >> 1)) * 32 + 16 + hp * 2 + (u & 1)) * 1024 : obs + u * 32768;
        pw = __builtin_amdgcn_raw_buffer_load_b32(wsr, o + (l & 7) * 128, 0, 16);
      }
    };
    const unsigned xsw = li * 128 + ((g ^ (li >> 1)) << 4);
    const unsigned wln = (8 * g + 2 * q) * 112 + 8 * p + 56 * hh;

  if (__builtin_expect(w >= 4, 1)) {
    if (!br) {
      int cc = wrap7(rot + 1);
      unsigned sl = L0 + a_slot(2);
#pragma nounroll
      for (int c = 2; c < 7; ++c) {
        vm_bar<10>();
        cc = wrap7(cc + 1);
        chunk(sl, sl + A_W, sl + A_W + WM_B, cc);
        sl = sl == L0 + a_slot(0) ? L0 + a_slot(1) : sl + A_SLOT;
      }
      vm_bar<10>();
      if (cg >= 7) { vm_bar<0>(); STAMP_FLUSH(); return; }
      wo_run(0, 4);
      vm_bar<4>();
      wo_run(4, 28);
      VM_WAIT(14);
      sig(L0 + SYNC + SY_WOA + wl * 4);
      VM_WAIT(0);
      asm volatile("" ::"v"(gv), "v"(epoch), "v"(pt));
      sig(L0 + SYNC + SY_WOB + wl * 4);
    } else {
      auto instr = [&](int n) {
        if (n < 56) {
          const int c = n >> 3, j = n & 7, cc = wrap7(c + rot);
          const unsigned sl = L0 + b_slot(c), sw = cc * (64 * WROW), sx = cc * 128;
          if (j < 4) dma16(srdX, (j & 1) ? vx1 : vx0, sx01 + j * 8 * XROW + sx, sl + (wl * 4 + j) * 1024);
          else dma16(srdW, j < 6 ? vw0 : v2, sw + ((j & 1) + 1) * XROW, sl + B_W + (j & 1) * WM_B + (j < 6 ? wl * 1024 : sub2));
        } else {
          const int c = (n - 56) >> 1;
          dma16(srdW, (n & 1) ? v2 : vw0, wrap7(c + rot) * (64 * WROW), L0 + B_WQ + c * WM_B + ((n & 1) ? sub2 : wl * 1024));
        }
      };
      STAMP(1);
      bf16x8 xq[14][2];
      const unsigned xl = L0 + (wl * 32 + li) * 128 + ((g ^ (li >> 1)) << 4);
#pragma unroll
      for (int c = 0; c < 7; ++c) {
        if (c < 6) vm_bar<8>(); else vm_bar<8>();
#pragma unroll
        for (int ks = 0; ks < 2; ++ks) {
          const unsigned xa = (xl ^ (ks * 64)) + b_slot(c);
          xq[c * 2 + ks][0] = as_frag(lds_ld128(xa));
          xq[c * 2 + ks][1] = as_frag(lds_ld128(xa + 2048));
        }
#pragma unroll
        for (int n = (c < 6 ? 8 * c + 16 : 56 + 8); n < (c < 5 ? 8 * c + 24 : (c == 5 ? 56 + 8 : 70)); ++n) instr(n);
      }
      STAMP(4);
      VM_WAIT(0);
      asm volatile("" ::"v"(gv), "v"(epoch), "v"(pt));
      sig(L0 + SYNC + SY_WQ + wl * 4);
      wait4(L0 + SYNC + SY_WQ);
      wait4(L0 + SYNC + SY_EX);
      STAMP(3);
      f32x4 aq[2][2][2];
#pragma unroll
      for (int i = 0; i < 8; ++i) aq[i >> 2][(i >> 1) & 1][i & 1] = (f32x4){kEps, kEps, kEps, kEps};
      const unsigned wb = L0 + B_WQ + (8 * g + 2 * q) * 112 + 8 * p;
      const unsigned gl = L0 + B_G + (8 * g + q) * 8;
      const bool p3 = p == 3;
      const unsigned d1 = p3 ? 32u : 112u;
      const unsigned qa0 = opaque(p3 ? gl : wb + 32), qstep = opaque(p3 ? 256u : 3584u);
      bf16x8 fq[3][4];
      auto rdq = [&](int s) {
        const unsigned wk = wb + s * 3584;
        const unsigned a = qa0 + s * qstep;
        fq[s % 3][0] = lds_tr2(wk, wk + 112);
        fq[s % 3][1] = lds_tr2(a, a + d1);
        fq[s % 3][2] = lds_tr2(wk + 56, wk + 56 + 112);
        fq[s % 3][3] = lds_tr2(wk + 88, wk + 88 + 112);
      };
      rdq(0);
      rdq(1);
#pragma unroll
      for (int s = 0; s < 14; ++s) {
        SB();
        if (s + 2 < 14) rdq(s + 2);
#pragma unroll
        for (int i = 0; i < 4; ++i) {
          aq[0][i >> 1][i & 1] = mfma(fq[s % 3][i], xq[s][0], aq[0][i >> 1][i & 1]);
          aq[1][i >> 1][i & 1] = mfma(fq[s % 3][i], xq[s][1], aq[1][i >> 1][i & 1]);
        }
        interleave<8, 8>();
      }
      sig(L0 + SYNC + SY_QD + wl * 4);
      STAMP(5);
      u32x4 pqf[2][2];
      float omg[2];
#pragma unroll
      for (int t = 0; t < 2; ++t) {
        omg[t] = 1.f - sigmoidf(row3_bcast(aq[t][0][1][0]));
#pragma unroll
        for (int h = 0; h < 2; ++h) {
          const u32x2 a = phi4(aq[t][h][0]), b = phi4(aq[t][h][1]);
          pqf[t][h] = (u32x4){a[0], a[1], g3 ? 0u : b[0], g3 ? 0u : b[1]};
        }
      }
      tagO = uniform64(*(LAS u64*)(uintptr_t)(L0 + SYNC + SY_TAG));
      unsigned ob = WS_O + ((rt * 8 + wl * 2) * 32 + 16 + hp * 2) * 1024 + lane * 16;
      unsigned kb = L0 + B_KVL + lane * 16, zb = L0 + B_KVL + 1024 + (g * 16 + 12) * 16;
      asm volatile("" : "+v"(pqf[0][0]), "+v"(pqf[0][1]), "+v"(pqf[1][0]), "+v"(pqf[1][1]), "+v"(omg[0]), "+v"(omg[1]), "+v"(ob),
                   "+v"(kb), "+v"(zb), "+s"(tagO));
      __builtin_amdgcn_s_setprio(2);
      STAMP(6);
      wait4(L0 + SYNC + SY_KV);
      STAMP(8);
      {
        bf16x8 kv0[2], kv1[2], zf[2];
#pragma unroll
        for (int h = 0; h < 2; ++h) zf[h] = as_frag(lds_ld128(zb + h * 2048));
#pragma unroll
        for (int h = 0; h < 2; ++h) {
          kv0[h] = as_frag(lds_ld128(kb + h * 2048));
          kv1[h] = as_frag(lds_ld128(kb + h * 2048 + 1024));
        }
        SB();
        f32x4 dn[4], n0[4], n1[4];
#pragma unroll
        for (int i = 0; i < 4; ++i) dn[i] = mfma(zf[i >> 1], as_frag(pqf[i & 1][i >> 1]), (f32x4){kEps, kEps, kEps, kEps});
#pragma unroll
        for (int i = 0; i < 4; ++i) {
          n0[i] = mfma(kv0[i >> 1], as_frag(pqf[i & 1][i >> 1]), z4);
          n1[i] = mfma(kv1[i >> 1], as_frag(pqf[i & 1][i >> 1]), z4);
        }
        SB();
#pragma unroll
        for (int i = 0; i < 4; ++i) {
          const float s = omg[i & 1] * __builtin_amdgcn_rcpf(dn[i][0]);
          const u32x2 a = pk4(n0[i] * s), b = pk4(n1[i] * s);
          __builtin_amdgcn_raw_buffer_store_b128((u32x4){a[0], a[1], g3 ? 0u : b[0], b[1]}, wsr, ob + ((i & 1) * 32 + (i >> 1)) * 1024, 0, 16);
        }
      }
      STAMP(9);
      VM_WAIT(0);
      if (lane == 0) __hip_atomic_store((gu64*)(ws + WS_FB) + ((rt * 4 + wl) * 8 + hp) * FS, tagO, RLX_AGENT);
      STAMP(10); STAMPC(19);
      if (cg >= 7) { STAMP_FLUSH(); return; }
    }
  } else if (__builtin_expect(br, 1)) {
      __builtin_amdgcn_s_setprio(2);
      setup(B_G);
      f32x4 ak[4][2], av[4][2];
      {
        const float one = li == 12 ? 1.f : 0.f, i0 = rh ? one : 0.f, i1 = rh ? 0.f : one;
#pragma unroll
        for (int t = 0; t < 4; ++t) {
          ak[t][0] = ak[t][1] = (f32x4){kEps, kEps, kEps, kEps};
          av[t][0] = (f32x4){i0, i0, i0, i0};
          av[t][1] = (f32x4){i1, i1, i1, i1};
        }
      }
      bf16x8 xf[2][4], fw[2][4];
      unsigned xb, wb;
      const bool z0 = p == 3 && rh, z1 = p == 3 && !rh;
      const unsigned zaddr = L0 + SYNC + SY_ZERO;
      auto rd = [&](int ks, int part) {
        const unsigned xa = xb ^ (ks * 64), wk = wb + ks * 3584;
        if (part & 1) {
          const unsigned a = wk + WM_B + (((part >> 1) ^ rh) << 5);
          const bool z = (part >> 1) ? z1 : z0;
          fw[ks][part] = lds_tr2(z ? zaddr : a, z ? zaddr : a + 112);
        } else {
          fw[ks][part] = lds_tr2(wk + (part >> 1) * 32, wk + (part >> 1) * 32 + 112);
        }
        if (part < 2) {
          xf[ks][part * 2] = as_frag(lds_ld128(xa + part * 4096));
          xf[ks][part * 2 + 1] = as_frag(lds_ld128(xa + part * 4096 + 2048));
        }
      };
      auto mm = [&](int ks, int part) {
#pragma unroll
        for (int t = 0; t < 4; ++t) {
          if (part & 1) av[t][part >> 1] = mfma(xf[ks][t], fw[ks][part], av[t][part >> 1]);
          else ak[t][part >> 1] = mfma(xf[ks][t], fw[ks][part], ak[t][part >> 1]);
        }
      };
#pragma unroll
      for (int c = 0; c < 7; ++c) {
        xb = opaque(L0 + b_slot(c) + rh * 8192 + xsw);
        wb = opaque(L0 + b_slot(c) + B_W + wln);
        hook(c, true, true);
        LDS_BAR();
        hook(c, false, true);
        if (c == 0) { STAMP(2); STAMPC(16); }
        if (c == 3) STAMP(3);
        if (c == 6) { STAMP(4); STAMPC(17); }
        SB();
#pragma unroll
        for (int ks = 0; ks < 2; ++ks) {
#pragma unroll
          for (int part = 0; part < 4; ++part) rd(ks, part);
          if (ks == 1 || c > 0) {
#pragma unroll
            for (int part = 0; part < 4; ++part) mm(ks ^ 1, part);
            interleave<16, 12>();
          }
          SB();
        }
      }
      sig(L0 + SYNC + SY_D6 + w * 4);
      mm(1, 0); mm(1, 2);
      SB();
      mm(1, 3); mm(1, 1);
      STAMP(5);
      f32x4 kv[2][2];
      {
        u32x2 pk[4][2], vf[4][2];
#pragma unroll
        for (int t = 0; t < 4; ++t) {
          pk[t][0] = phi4(ak[t][0]);
          pk[t][1] = phi4(ak[t][1]);
          vf[t][0] = pk4(av[t][0]);
          vf[t][1] = pk4(av[t][1]);
        }
#pragma unroll
        for (int e = 1; e >= 0; --e)
#pragma unroll
          for (int d = 0; d < 2; ++d) {
            kv[e][d] = mfma(frag2(pk[0][d], pk[1][d]), frag2(vf[0][e], vf[1][e]), z4);
            kv[e][d] = mfma(frag2(pk[2][d], pk[3][d]), frag2(vf[2][e], vf[3][e]), kv[e][d]);
          }
      }
      lds_st128(L0 + B_EX + w * 2048 + lane * 16, __builtin_bit_cast(u32x4, kv[1][0]));
      lds_st128(L0 + B_EX + w * 2048 + 1024 + lane * 16, __builtin_bit_cast(u32x4, kv[1][1]));
      sig(L0 + SYNC + SY_EX + w * 4);
      wait1(L0 + SYNC + SY_EX + (w ^ 1) * 4);
      const f32x4 s0 = kv[0][0] + __builtin_bit_cast(f32x4, lds_ld128(L0 + B_EX + (w ^ 1) * 2048 + lane * 16));
      const f32x4 s1 = kv[0][1] + __builtin_bit_cast(f32x4, lds_ld128(L0 + B_EX + (w ^ 1) * 2048 + 1024 + lane * 16));
      {
        const u32x2 a = pk4(s0), b = pk4(s1);
        publish((u32x4){a[0], a[1], b[0], b[1]}, wsr, lane * 16 + kvs + rt * 1024);
      }
      STAMP(6);
      if (!__all(clr == tagO)) poll_flags(clrp(), tagO);
      wo_run(0, QWO);
      STAMP(7);
      f32x4 sum0, sum1;
      {
        const unsigned e0 = q == g ? ((p & 1) ? 0x3f800000u : 0x3f80u) : 0u;
        const unsigned ea = p < 2 ? e0 : 0u, eb = p < 2 ? 0u : e0;
        const bf16x8 sel0 = as_frag((u32x4){ea, eb, 0u, 0u}), sel1 = as_frag((u32x4){0u, 0u, ea, eb});
        unsigned vo = kvs + lane * 16;
#pragma nounroll
        for (unsigned spins = 0; spins < SPIN_CAP; ++spins) {
          asm volatile("" : "+v"(vo)::"memory");
          u32x4 pr[16];
#pragma unroll
          for (int r = 0; r < 16; ++r) pr[r] = __builtin_amdgcn_raw_buffer_load_b128(wsr, vo, ((r + 2 * rt) & 15) * 1024, 16);
          SB();
          sum0 = sum1 = z4;
#pragma unroll
          for (int r = 0; r < 16; ++r) {
            sum0 = mfma(sel0, as_frag(pr[r]), sum0);
            sum1 = mfma(sel1, as_frag(pr[r]), sum1);
          }
          if (!__any(sum0[0] != sum0[0])) break;
          unsigned m = 0;
#pragma unroll
          for (int r = 0; r < 16; ++r) m = max(max(max(max(m, pr[r][0]), pr[r][1]), pr[r][2]), pr[r][3]);
          if (!__any(m == SENT)) break;
        }
      }
      asm volatile("" ::"v"(pw));
      STAMP(8);
      lds_st128(L0 + B_KVL + w * 1024 + lane * 16, __builtin_bit_cast(u32x4, frag2(pk4(sum0), pk4(sum1))));
      sig(L0 + SYNC + SY_KV + w * 4);
      STAMP(10); STAMPC(19);
      __builtin_amdgcn_s_setprio(0);
      if (cg >= 7) { STAMP_FLUSH(); return; }
      sig(L0 + SYNC + SY_WOA + w * 4);
      wait4(L0 + SYNC + SY_D6);
      wait4(L0 + SYNC + SY_QD);
      wo_run(QWO, 28);
  } else {
      asm volatile("; role A");
      setup(A_G);
      f32x4 aq[4][2], ak[5][2], av[5][2];
#pragma unroll
      for (int t = 0; t < 5; ++t)
#pragma unroll
        for (int f = 0; f < 2; ++f) { ak[t][f] = av[t][f] = z4; if (t < 4) aq[t][f] = z4; }
      const unsigned gl = L0 + A_G + (8 * g + q) * 8;
      const bool p3 = p == 3;
      const unsigned d1 = p3 ? 32u : 112u;
      bf16x8 xf[2][5], fq[2][2], fk[2][2], fv[2][2];
      unsigned xb, wb;
      auto rd = [&](int c, int ks, int part) {
        const unsigned xa = xb ^ (ks * 64), wk = wb + ks * 3584;
        if (part == 0) {
          fk[ks][0] = lds_tr2(wk + WM_B, wk + WM_B + 112);
          xf[ks][0] = as_frag(lds_ld128(xa));
          xf[ks][1] = as_frag(lds_ld128(xa + 2048));
        } else if (part == 1) {
          fv[ks][0] = lds_tr2(wk + 2 * WM_B, wk + 2 * WM_B + 112);
          xf[ks][2] = as_frag(lds_ld128(xa + 4096));
        } else if (part == 2) {
          fq[ks][0] = lds_tr2(wk, wk + 112);
          xf[ks][3] = as_frag(lds_ld128(xa + 6144));
        } else if (part == 3) {
          fk[ks][1] = lds_tr2(wk + WM_B + 32, wk + WM_B + 32 + 112);
          xf[ks][4] = as_frag(lds_ld128(xa + 8192));
        } else if (part == 4) {
          fv[ks][1] = lds_tr2(wk + 2 * WM_B + 32, wk + 2 * WM_B + 32 + 112);
        } else {
          const unsigned a = p3 ? gl + c * 512 + ks * 256 : wk + 32;
          fq[ks][1] = lds_tr2(a, a + d1);
        }
      };
      auto mm = [&](int ks, int part) {
        const int f = part / 3, m = part % 3;
#pragma unroll
        for (int t = 0; t < 5; ++t) {
          if (m == 0) ak[t][f] = mfma(fk[ks][f], xf[ks][t], ak[t][f]);
          if (m == 1) av[t][f] = mfma(xf[ks][t], fv[ks][f], av[t][f]);
          if (m == 2 && t > 0) aq[t - 1][f] = mfma(fq[ks][f], xf[ks][t], aq[t - 1][f]);
        }
      };
#pragma unroll
      for (int c = 0; c < 7; ++c) {
        xb = opaque(L0 + a_slot(c) + rh * 8192 + xsw);
        wb = opaque(L0 + a_slot(c) + A_W + wln);
        hook(c, true, false);
        LDS_BAR();
        hook(c, false, false);
        if (c == 0) { STAMP(2); STAMPC(16); }
        if (c == 3) STAMP(3);
        if (c == 6) { STAMP(4); STAMPC(17); }
        SB();
#pragma unroll
        for (int ks = 0; ks < 2; ++ks) {
#pragma unroll
          for (int part = 0; part < 6; ++part) rd(c, ks, part);
          if (ks == 1 || c > 0) {
#pragma unroll
            for (int part = 0; part < 6; ++part) mm(ks ^ 1, part);
            interleave<28, 17>();
          }
          SB();
        }
      }
      sig(L0 + SYNC + SY_D6 + w * 4);
      mm(1, 0); mm(1, 3); mm(1, 2); mm(1, 5);
      SB();
      mm(1, 1); mm(1, 4);
      STAMP(5);
      const bool odd = li & 1;
      const int kk0 = 4 * g + (li & 1);
      const bool own0 = kk0 <= li, own1 = kk0 + 2 <= li;
      const float sc = 0.18898223650461362f * kLog2e;
      const float ninf = (rt == 0 && rh == 0) ? -__builtin_inff() : 0.f;
      const unsigned l16 = lane_id() * 16;
      bf16x8 kf[5];
#pragma unroll
      for (int t = 0; t < 5; ++t) kf[t] = frag2(pk4(ak[t][0]), pk4(ak[t][1]));
      f32x4 sp[4], so[4];
#pragma unroll
      for (int t = 0; t < 4; ++t) {
        const u32x2 b = pk4(aq[t][1]);
        const bf16x8 qf = frag2(pk4(aq[t][0]), (u32x2){g3 ? 0u : b[0], g3 ? 0u : b[1]});
        sp[t] = mfma(kf[t], qf, t == 0 ? (f32x4){ninf, ninf, ninf, ninf} : z4);
        so[t] = mfma(kf[t + 1], qf, z4);
      }
      float ug;
      {
        float z0 = aq[0][1][0], z1 = aq[1][1][0], z2 = aq[2][1][0], z3 = aq[3][1][0];
        swap32(z0, z1);
        swap32(z2, z3);
        swap16(z1, z3);
        ug = 1.f + __builtin_amdgcn_exp2f(-kLog2e * z3);
      }
      u32x2 vf[5][2];
#pragma unroll
      for (int t = 0; t < 5; ++t) {
        vf[t][0] = pk4(av[t][0]);
        vf[t][1] = pk4(av[t][1]);
      }
      float l0[4], l1[4], bc[4];
#pragma unroll
      for (int t = 0; t < 4; ++t) {
        float a0 = so[t][0], a2 = so[t][2], b0 = sp[t][0], b2 = sp[t][2];
        const float c0 = own0 ? a0 : b0, c1 = own0 ? so[t][1] : sp[t][1], c2 = own1 ? a2 : b2, c3 = own1 ? so[t][3] : sp[t][3];
        l0[t] = odd ? c1 : c0;
        l1[t] = odd ? c3 : c2;
      }
      rows_bcast4(rows_reduce4<true>(fmax1(l0[0], l1[0]), fmax1(l0[1], l1[1]), fmax1(l0[2], l1[2]), fmax1(l0[3], l1[3])) * -sc, bc);
#pragma unroll
      for (int t = 0; t < 4; ++t) {
        l0[t] = __builtin_amdgcn_exp2f(__builtin_fmaf(l0[t], sc, bc[t]));
        l1[t] = __builtin_amdgcn_exp2f(__builtin_fmaf(l1[t], sc, bc[t]));
      }
      rows_bcast4(__builtin_amdgcn_rcpf(rows_reduce4<false>(l0[0] + l1[0], l0[1] + l1[1], l0[2] + l1[2], l0[3] + l1[3]) * ug), bc);
      const unsigned sh = odd ? 16u : 0u;
      const unsigned mk0 = own0 ? 0u : 0xffffu, mk1 = own1 ? 0u : 0xffffu;
#pragma unroll
      for (int t = 0; t < 4; ++t) {
        const unsigned pe = pk2(l0[t] * bc[t], l1[t] * bc[t]), ph = pe >> 16;
        const bf16x8 pf = as_frag((u32x4){(pe & mk0) << sh, (ph & mk1) << sh, (pe & (mk0 ^ 0xffffu)) << sh, (ph & (mk1 ^ 0xffffu)) << sh});
        const f32x4 r0 = mfma(frag2(vf[t][0], vf[t + 1][0]), pf, z4);
        const f32x4 r1 = mfma(frag2(vf[t][1], vf[t + 1][1]), pf, z4);
        const u32x2 c0 = pk4(r0), c1 = pk4(r1);
        __builtin_amdgcn_raw_buffer_store_b128((u32x4){c0[0], c0[1], g3 ? 0u : c1[0], g3 ? 0u : c1[1]}, wsr, l16 + obs + t * 32768, 0, 16);
      }
      STAMP(9);
      VM_WAIT(0);
      asm volatile("" ::"v"(pw));
      if (l16 == 0) __hip_atomic_store((gu64*)(ws + WS_FA) + ((rt * 2 + rh) * 16 + hp * 2 + hh) * FS, tagO, RLX_AGENT);
      STAMP(10); STAMPC(19);
      if (cg >= 7) {
        if (bid == 56 && w == 0) {
#pragma nounroll
          for (int k = 0; k < 4; ++k) {
            const int i = (k & 1) * 64 + lane;
            poll_flags(k < 2 ? (gu64*)(ws + WS_CLR) + i : (gu64*)(ws + WS_FA) + ((i >> 3) * 32 + (i & 7) * 2) * FS, tagO);
          }
          if (lane == 0) __hip_atomic_store((gu64*)(ws + WS_E), tagO, RLX_AGENT);
        }
        STAMP_FLUSH();
        return;
      }
  }

  const int pb = w >> 2;
  wait4(L0 + SYNC + (pb ? SY_WOB : SY_WOA));
  const unsigned sw = (2 * g + (q >> 1)) & 3;
  const unsigned rb = L0 + (pb * 448 + 4 * g + q) * 128 + 8 * p;
  unsigned wa[4], wa2[4];
#pragma unroll
  for (int v = 0; v < 4; ++v) {
    wa[v] = opaque(rb + ((v ^ sw) << 5));
    wa2[v] = opaque(wa[v] + (g3 ? 0u : 2048u));
  }
  auto wfrag = [&](int h, int ct) {
    const int v = ct ^ (2 * (h & 1));
    return lds_tr2(wa[v] + h * 3584, wa2[v] + h * 3584);
  };
  constexpr int NPRE = 8;
  bf16x8 wf[16][4];
#pragma unroll
  for (int h = 0; h < NPRE; ++h)
#pragma unroll
    for (int ct = 0; ct < 4; ++ct) wf[h][ct] = wfrag(h, ct);
  STAMP(11);
  const unsigned ub = WS_O + ((rg * 4 + wl) * 32 + pb * 16) * 1024;
  tagO = uniform64(*(LAS u64*)(uintptr_t)(L0 + SYNC + SY_TAG));
  {
    const gu64* fp = pb ? (gu64*)(ws + WS_FB) + ((rg * 2 + (wl >> 1)) * 8 + (li & 7)) * FS : (gu64*)(ws + WS_FA) + (rg * 16 + li) * FS;
    const u64 f0 = __hip_atomic_load(fp, RLX_AGENT);
    if (br && !pb) { VM_WAIT(0); sig(L0 + SYNC + SY_WOB + wl * 4); }
    if (!__all(f0 == tagO)) poll_flags(fp, tagO);
  }
  STAMP(12);
  u32x4 of[16];
  {
    unsigned vo = ub + lane * 16;
    asm volatile("" : "+v"(vo)::"memory");
#pragma unroll
    for (int h = 0; h < 16; ++h) of[h] = __builtin_amdgcn_raw_buffer_load_b128(wsr, vo, ((h + 2 * cg) & 15) * 1024, 16);
    SB();
  }
  f32x4 acc[4] = {z4, z4, z4, z4};
#pragma unroll
  for (int h = 0; h < 16; ++h) {
#pragma unroll
    for (int ct = 0; ct < 4; ++ct) acc[ct] = mfma(wf[h][ct], as_frag(of[h]), acc[ct]);
    SB();
    if (h + NPRE < 16) {
#pragma unroll
      for (int ct = 0; ct < 4; ++ct) wf[h + NPRE][ct] = wfrag(h + NPRE, ct);
    }
    SB();
  }
  STAMP(13);
  const unsigned xa = L0 + P2X + wl * 4096 + lane * 16;
  if (!pb) {
    wait4(L0 + SYNC + SY_D6);
#pragma unroll
    for (int ct = 0; ct < 4; ++ct) lds_st128(xa + ct * 1024, __builtin_bit_cast(u32x4, acc[ct]));
    sig(L0 + SYNC + SY_P2 + wl * 4);
  } else {
    f32x4 pa[4];
    for (;;) {
      const unsigned f = *(volatile LAS unsigned*)(uintptr_t)(L0 + SYNC + SY_P2 + wl * 4);
#pragma unroll
      for (int ct = 0; ct < 4; ++ct) pa[ct] = *(volatile LAS f32x4*)(uintptr_t)(xa + ct * 1024);
      if (__builtin_amdgcn_readfirstlane(f)) break;
      __builtin_amdgcn_s_sleep(1);
    }
    char* orow = out + (rg * 64 + wl * 16 + li) * XROW + cg * 128 + (g & 1) * 32 + (g >> 1) * 16;
#pragma unroll
    for (int k = 0; k < 2; ++k) {
      const u32x2 a = pk4(acc[2 * k] + pa[2 * k]), b = pk4(acc[2 * k + 1] + pa[2 * k + 1]);
      const auto r0 = __builtin_amdgcn_permlane16_swap(a[0], b[0], false, false);
      const auto r1 = __builtin_amdgcn_permlane16_swap(a[1], b[1], false, false);
      *(u32x4*)(orow + k * 64) = (u32x4){r0[0], r1[0], r0[1], r1[1]};
    }
  }
  STAMP(14);
  STAMP(15);
  STAMP_FLUSH();
}

}

extern "C" void kernel_launch(void* const* d_in, const int* in_sizes, int n_in, void* d_out, int out_size, void* d_ws,
                              size_t ws_size, hipStream_t stream) {
  (void)in_sizes; (void)n_in; (void)out_size; (void)ws_size;
  gated_mix_kernel<<<256, 512, 0, stream>>>((const char*)d_in[0], (const char*)d_in[1], (const char*)d_in[2],
                                            (const char*)d_in[4], (const char*)d_in[5],
                                            (const unsigned short*)d_in[6], (char*)d_out, (char*)d_ws);
}
```

```cpp
#include <hip/hip_runtime.h>
#include <stdint.h>

typedef __bf16 bf16x2 __attribute__((ext_vector_type(2)));
typedef __bf16 bf16x4 __attribute__((ext_vector_type(4)));
typedef __bf16 bf16x8 __attribute__((ext_vector_type(8)));
typedef float f32x2 __attribute__((ext_vector_type(2)));
typedef float f32x4 __attribute__((ext_vector_type(4)));
typedef unsigned u32x2 __attribute__((ext_vector_type(2)));
typedef unsigned u32x4 __attribute__((ext_vector_type(4)));
typedef unsigned long long u64;
#define LAS __attribute__((address_space(3)))
#define GAS __attribute__((address_space(1)))
typedef GAS u64 gu64;
#define RLX_AGENT __ATOMIC_RELAXED, __HIP_MEMORY_SCOPE_AGENT

namespace {

constexpr int kS = 2048, kD = 448;
constexpr int XROW = kD * 2;
constexpr int WROW = 3 * kD * 2;
constexpr int WM_B = 64 * 112;

constexpr int A_FRONT = 34816, A_SLOT = 144 * 128 + 3 * WM_B, A_W = 144 * 128;
constexpr int A_G = A_FRONT + 3 * A_SLOT;
constexpr int B_SLOT = 128 * 128 + 2 * WM_B, B_W = 128 * 128;
constexpr int B_WQ = 97536, B_G = B_WQ + 7 * WM_B, B_EX = B_G + 3584, B_KVL = B_EX + 8192;
constexpr int P2X = 114688;
constexpr int SYNC = 163584;
constexpr int LDS_B = 163840;
static_assert(A_G + 3584 <= SYNC && B_KVL + 4096 <= SYNC && A_FRONT + 2 * A_SLOT == 114688, "lds map");
__host__ __device__ constexpr int a_slot(int c) { return A_FRONT + ((c + 2) % 3) * A_SLOT; }
__host__ __device__ constexpr int b_slot(int c) { return ((c + 2) % 3) * B_SLOT; }
enum { SY_WOA = 0, SY_WOB = 16, SY_D6 = 32, SY_WQ = 64, SY_QD = 80, SY_EX = 96, SY_KV = 112, SY_P2 = 128, SY_ZERO = 192, SY_TAG = 240 };

constexpr int WS_E = 0;
constexpr int WS_CLR = 4096;
constexpr int FS = 16;
constexpr int WS_FA = 8192;
constexpr int WS_FB = WS_FA + 16 * 2 * 16 * 8 * FS;
constexpr int WS_KVP = WS_FB + 16 * 4 * 8 * 8 * FS;
constexpr int WS_O = WS_KVP + 8 * 4 * 16 * 1024;
constexpr int WS_END = WS_O + 16 * 8 * 32 * 1024;

constexpr int PT_WS = (WS_END / 8) & ~127;
constexpr unsigned SPIN_CAP = 1u << 19;
constexpr unsigned SENT = 0xffffffffu;
constexpr int QWO = 14;

__device__ __forceinline__ unsigned pk2(float a, float b) {
  f32x2 v = {a, b};
  bf16x2 r = __builtin_convertvector(v, bf16x2);
  return __builtin_bit_cast(unsigned, r);
}
__device__ __forceinline__ u32x2 pk4(f32x4 v) { return (u32x2){pk2(v[0], v[1]), pk2(v[2], v[3])}; }
__device__ __forceinline__ bf16x8 as_frag(u32x4 v) { return __builtin_bit_cast(bf16x8, v); }
__device__ __forceinline__ bf16x8 frag2(u32x2 a, u32x2 b) { return as_frag((u32x4){a[0], a[1], b[0], b[1]}); }
__device__ __forceinline__ f32x4 mfma(bf16x8 a, bf16x8 b, f32x4 c) {
  return __builtin_amdgcn_mfma_f32_16x16x32_bf16(a, b, c, 0, 0, 0);
}
__device__ __forceinline__ bf16x8 lds_tr2(unsigned a0, unsigned a1) {
  bf16x4 lo = __builtin_amdgcn_ds_read_tr16_b64_v4bf16((LAS bf16x4*)(uintptr_t)a0);
  bf16x4 hi = __builtin_amdgcn_ds_read_tr16_b64_v4bf16((LAS bf16x4*)(uintptr_t)a1);
  return __builtin_shufflevector(lo, hi, 0, 1, 2, 3, 4, 5, 6, 7);
}
__device__ __forceinline__ u32x4 lds_ld128(unsigned a) { return *(LAS u32x4*)(uintptr_t)a; }
__device__ __forceinline__ void lds_st128(unsigned a, u32x4 v) { *(LAS u32x4*)(uintptr_t)a = v; }
__device__ __forceinline__ unsigned opaque(unsigned v) { asm volatile("" : "+v"(v)); return v; }

__device__ __forceinline__ void sig(unsigned a) {
  asm volatile("" ::: "memory");
  *(volatile LAS unsigned*)(uintptr_t)a = 1u;
  asm volatile("" ::: "memory");
  __builtin_amdgcn_sched_barrier(0);
}
__device__ __forceinline__ void wait1(unsigned a) {
  while (__builtin_amdgcn_readfirstlane(*(volatile LAS unsigned*)(uintptr_t)a) == 0u) __builtin_amdgcn_s_sleep(1);
  asm volatile("" ::: "memory");
}
__device__ __forceinline__ void wait4(unsigned a) {
  for (;;) {
    const u32x4 v = *(volatile LAS u32x4*)(uintptr_t)a;
    if (__builtin_amdgcn_readfirstlane(v[0] & v[1] & v[2] & v[3])) break;
    __builtin_amdgcn_s_sleep(1);
  }
  asm volatile("" ::: "memory");
}

__device__ __forceinline__ u32x4 make_srd(const void* p, unsigned bytes) {
  uint64_t a = (uint64_t)p;
  u32x4 r;
  r[0] = __builtin_amdgcn_readfirstlane((unsigned)a);
  r[1] = __builtin_amdgcn_readfirstlane((unsigned)(a >> 32) & 0xffffu);
  r[2] = bytes;
  r[3] = 0x00020000u;
  return r;
}
__device__ __forceinline__ void dma16(u32x4 srd, unsigned voff, unsigned soff, unsigned ldsaddr) {
  asm volatile(
      "s_mov_b32 m0, %3\n\t"
      "s_nop 3\n\t"
      "buffer_load_dwordx4 %0, %1, %2 offen lds"
      :
      : "v"(voff), "s"(srd), "s"(soff), "s"(ldsaddr));
}
#define LDS_BAR() asm volatile("s_waitcnt lgkmcnt(0)\n\ts_barrier" ::: "memory")
#define VM_WAIT(N) asm volatile("s_waitcnt vmcnt(" #N ")" ::: "memory")
template <int N> __device__ __forceinline__ void vm_bar() { asm volatile("s_waitcnt vmcnt(%0) lgkmcnt(0)\n\ts_barrier" ::"n"(N) : "memory"); }
#define SB() __builtin_amdgcn_sched_barrier(0)

__device__ __forceinline__ float fmax1(float a, float b) { return __builtin_amdgcn_fmed3f(a, b, 3.0e38f); }
__device__ __forceinline__ void swap16(float& a, float& b) {
  auto r = __builtin_amdgcn_permlane16_swap(__float_as_uint(a), __float_as_uint(b), false, false);
  const unsigned r0 = r[0], r1 = r[1];
  a = __uint_as_float(r0);
  b = __uint_as_float(r1);
}
__device__ __forceinline__ void swap32(float& a, float& b) {
  auto r = __builtin_amdgcn_permlane32_swap(__float_as_uint(a), __float_as_uint(b), false, false);
  const unsigned r0 = r[0], r1 = r[1];
  a = __uint_as_float(r0);
  b = __uint_as_float(r1);
}
template <bool MAX>
__device__ __forceinline__ float rows_reduce4(float v0, float v1, float v2, float v3) {
  swap32(v0, v1);
  swap32(v2, v3);
  float a = MAX ? fmax1(v0, v1) : v0 + v1, b = MAX ? fmax1(v2, v3) : v2 + v3;
  swap16(a, b);
  return MAX ? fmax1(a, b) : a + b;
}
__device__ __forceinline__ void rows_bcast4(float x, float (&o)[4]) {
  float y = x;
  swap16(x, y);
  o[0] = x; o[1] = x; o[2] = y; o[3] = y;
  swap32(o[0], o[1]);
  swap32(o[2], o[3]);
}
__device__ __forceinline__ float row3_bcast(float v) {
  float a = v;
  swap32(a, v);
  a = v;
  swap16(a, v);
  return v;
}
template <int NM, int ND>
__device__ __forceinline__ void interleave() {
#pragma unroll
  for (int i = 0; i < NM; ++i) {
    __builtin_amdgcn_sched_group_barrier(0x8, 1, 0);
    if (i < ND) {
      __builtin_amdgcn_sched_group_barrier(0x2, 1, 0);
      __builtin_amdgcn_sched_group_barrier(0x100, 1, 0);
    }
  }
}
constexpr float kEps = 1e-6f, kLog2e = 1.44269504088896f;
__device__ __forceinline__ float phi(float u) {
  const float e = __builtin_amdgcn_fmed3f(__builtin_amdgcn_exp2f(__builtin_fmaf(u, kLog2e, -kEps * kLog2e)), 0.f, 1.f);
  return fmax1(u, kEps) + e;
}
__device__ __forceinline__ u32x2 phi4(f32x4 v) { return (u32x2){pk2(phi(v[0]), phi(v[1])), pk2(phi(v[2]), phi(v[3]))}; }
__device__ __forceinline__ float sigmoidf(float z) {
  return __builtin_amdgcn_rcpf(1.f + __builtin_amdgcn_exp2f(-1.44269504088896f * z));
}
__device__ __forceinline__ u64 uniform64(u64 e) {
  return ((u64)(unsigned)__builtin_amdgcn_readfirstlane((unsigned)(e >> 32)) << 32) |
         (unsigned)__builtin_amdgcn_readfirstlane((unsigned)e);
}
__device__ __forceinline__ u64 next_tag(u64 e) { return uniform64(e) * 6364136223846793005ull + 1442695040888963407ull; }
__device__ __forceinline__ void poll_flags(const gu64* p, u64 tag) {
#pragma nounroll
  for (unsigned spins = 0; spins < SPIN_CAP; ++spins) {
    u64 v = __hip_atomic_load(p, RLX_AGENT);
    if (__all(v == tag)) break;
    __builtin_amdgcn_s_sleep(2);
  }
  asm volatile("" ::: "memory");
}
__device__ __forceinline__ void publish(u32x4 v, __amdgpu_buffer_rsrc_t r, unsigned voff) {
#pragma unroll
  for (int i = 0; i < 4; ++i) v[i] = v[i] < SENT - 1u ? v[i] : SENT - 1u;
  __builtin_amdgcn_raw_buffer_store_b128(v, r, voff, 0, 16);
}
__device__ __forceinline__ unsigned lane_id() {
  unsigned l = __builtin_amdgcn_mbcnt_hi(~0u, __builtin_amdgcn_mbcnt_lo(~0u, 0u));
  asm volatile("" : "+v"(l));
  return l;
}
__device__ __forceinline__ int wrap7(int c) { return c >= 7 ? c - 7 : c; }

__global__ __launch_bounds__(512, 2) void gated_mix_kernel(const char* __restrict__ x, const char* __restrict__ wqkvA,
                                                           const char* __restrict__ woA, const char* __restrict__ wqkvB,
                                                           const char* __restrict__ woB,
                                                           const unsigned short* __restrict__ gate_w, char* __restrict__ out,
                                                           char* ws) {
  __shared__ __attribute__((aligned(1024))) char lds[LDS_B];
  const unsigned L0 = (unsigned)(uintptr_t)(LAS char*)lds;
  const int bid = blockIdx.x;
  const int xcd = bid & 7, idx = bid >> 3;
  const int xj = xcd >> 1, ci = idx >> 3;
  const int br = ci >> 1;
  const int hp = 2 * (br ? (xj ^ 1) : xj) + (ci & 1);
  const int rt = (xcd & 1) * 8 + (idx & 7);
  const int rot = wrap7(wrap7((idx & 7) + 2 * ci + (xj & 1)));
  const int cg = idx & 7, rg = 4 * xcd + ci;

  const int tid = threadIdx.x;
  const int lane = tid & 63;
  const int w = __builtin_amdgcn_readfirstlane(tid >> 6) & 7;
  const int wl = w & 3;
  const int li = lane & 15, g = lane >> 4, q = (lane >> 2) & 3, p = lane & 3;
  const bool g3 = g == 3;
  const f32x4 z4 = {0.f, 0.f, 0.f, 0.f};
  unsigned vx0 = (lane >> 3) * XROW + (((lane & 7) ^ (lane >> 4)) << 4);
  unsigned vx1 = vx0 ^ 64u;
  unsigned vw0, vw1;
  {
    const unsigned s0 = wl * 64 + lane, s1 = s0 + 256;
    const unsigned p0 = (s0 * 9363u) >> 16, p1 = (s1 * 9363u) >> 16;
    const unsigned k0 = (p0 & 56) | ((p0 >> 1) & 3) | ((p0 & 1) << 2);
    const unsigned k1 = (p1 & 56) | ((p1 >> 1) & 3) | ((p1 & 1) << 2);
    vw0 = k0 * WROW + hp * 112 + (s0 - p0 * 7) * 16;
    vw1 = k1 * WROW + hp * 112 + (s1 - p1 * 7) * 16;
  }
  asm volatile("" : "+v"(vx0), "+v"(vx1), "+v"(vw0), "+v"(vw1));
  asm volatile("" ::"s"(x), "s"(wqkvA), "s"(woA), "s"(wqkvB), "s"(woB), "s"(gate_w), "s"(out), "s"(ws));
  const __amdgpu_buffer_rsrc_t wsr = __builtin_amdgcn_make_buffer_rsrc(ws, 0, WS_END, 0x00020000);
  u64 tagO;
  auto wo_run = [&](int j0, int j1) {
    const unsigned vwo = (lane >> 3) * XROW + cg * 128 + (((lane & 7) ^ (((lane >> 4) & 3) << 1)) << 4);
    const u32x4 sa = make_srd(woA, kD * XROW), sb = make_srd(woB, kD * XROW);
    unsigned so = (wl + 7 * cg + 4 * (j0 % 14)) * (8 * XROW);
    so = so >= kD * XROW ? so - kD * XROW : so;
#pragma unroll
    for (int j = j0; j < j1; ++j) {
      dma16(j < 14 ? sa : sb, vwo, so, L0 + (wl + 4 * j) * 1024);
      so += 32 * XROW;
      so = so >= kD * XROW ? so - kD * XROW : so;
    }
  };
  const u32x4 srdX = make_srd(x, kS * XROW);
  const u32x4 srdW = make_srd(br ? wqkvB : wqkvA, kD * WROW);
  const bool l3 = wl == 3;
  const int xr = rt * 128 + wl * 32 - (br ? 0 : 16);
  const unsigned sx01 = (xr < 0 ? 0 : xr) * XROW, sx23 = (xr + 16) * XROW;
  const bool l3a = l3 && !br;
  const u32x4 srd2 = l3a ? srdX : srdW;
  const unsigned v2 = l3 ? vw0 : vw1;
  const unsigned v2a = l3a ? vx0 : v2, v2b = l3a ? vx1 : v2;
  const unsigned st2 = l3a ? 128u : 64u * WROW, s2a = l3a ? (rt * 128 + 112) * XROW : 0u;
  const unsigned s2d = l3a ? 8u * XROW : (unsigned)XROW, s2e = l3a ? 8u * XROW : 2u * XROW;
  const unsigned sub2 = (l3 ? wl : wl + 4) * 1024;
  auto chunk = [&](unsigned sl, unsigned lq, unsigned lk, int cc) {
    const unsigned sw = cc * (64 * WROW), sx = cc * 128, s2 = cc * st2 + s2a;
#pragma unroll
    for (int j = 0; j < 4; ++j)
      dma16(srdX, (j & 1) ? vx1 : vx0, (j < 2 ? sx01 : sx23) + (j & 1) * 8 * XROW + sx, sl + (wl * 4 + j) * 1024);
    dma16(srdW, vw0, sw + XROW, lk + wl * 1024);
    dma16(srdW, vw0, sw + 2 * XROW, lk + WM_B + wl * 1024);
    dma16(srd2, v2b, s2 + s2d, l3a ? sl + 17408 : lk + sub2);
    dma16(srd2, v2b, s2 + s2e, l3a ? sl + 17408 : lk + WM_B + sub2);
    if (!br) {
      dma16(srdW, vw0, sw, lq + wl * 1024);
      dma16(srd2, v2a, s2, l3a ? sl + 16384 : lq + sub2);
    }
  };
  unsigned gv = 0, pt = 0;
  u64 epoch = 0;
  {
    int wx = w;
    asm volatile("" : "+s"(wx));
    if (wx < 4) {
      gv = ((const unsigned*)gate_w)[tid < 224 ? tid : 0];
      epoch = __hip_atomic_load((gu64*)(ws + WS_E), RLX_AGENT);
      const char* pb_ = wx == 0 ? ws : (wx == 1 ? out + rg * 64 * XROW : (wx == 2 ? woA : woB));
      pt = __hip_atomic_load((GAS unsigned*)(pb_ + (lane & 7) * (wx == 0 ? PT_WS : (wx == 1 ? 7168 : 50176))), RLX_AGENT);
    } else {
#pragma nounroll
      for (int c = 0; c < 2; ++c) {
        const unsigned sl = L0 + (br ? (c ? b_slot(1) : b_slot(0)) : (c ? a_slot(1) : a_slot(0)));
        chunk(sl, sl + A_W, sl + (br ? B_W : A_W + WM_B), wrap7(rot + c));
      }
    }
  }

  const int rh = w & 1, hh = w >> 1;
  const unsigned obs = WS_O + ((rt * 8 + rh * 4) * 32 + hp * 2 + hh) * 1024;
  const unsigned kvs = WS_KVP + (hp * 4 + w) * 16 * 1024;
  auto setup = [&](int goff) {
    if (tid < 60) *(LAS unsigned*)(uintptr_t)(L0 + SYNC + tid * 4) = 0u;
    const unsigned lo = (gv & 0xffffu) * 0x10001u, hi = (gv >> 16) * 0x10001u;
    const int pos = tid - rot * 32;
    if (tid < 224) lds_st128(L0 + goff + (pos < 0 ? pos + 224 : pos) * 16, (u32x4){lo, lo, hi, hi});
  };
  u64 clr = 0;
  unsigned pw = 0;
  auto clrp = [&]() { return (const gu64*)(ws + WS_CLR) + hp * 16 + (lane_id() & 15); };
  auto hook = [&](int c, bool before, bool isb) {
    if (c == 0 && !before && isb)
      __builtin_amdgcn_raw_buffer_store_b128((u32x4){SENT, SENT, SENT, SENT}, wsr, lane_id() * 16 + kvs + rt * 1024, 0, 16);
    if (c == 3 && before) {
      asm volatile("" : "+v"(epoch) : "v"(pt));
      tagO = next_tag(epoch);
      *(LAS u64*)(uintptr_t)(L0 + SYNC + SY_TAG) = tagO;
      VM_WAIT(0);
    }
    if (c == 3 && !before && isb)
      __builtin_amdgcn_raw_buffer_store_b64((u32x2){(unsigned)tagO, (unsigned)(tagO >> 32)}, wsr,
                                            (lane_id() | w) == 0 ? WS_CLR + (hp * 16 + rt) * 8 : 0x7ffffff0u, 0, 16);
    if (c == 6 && !before && isb) clr = __hip_atomic_load(clrp(), RLX_AGENT);
    if (c == 6 && !before) {
      const unsigned l = lane_id(), u = (l >> 3) & 3;
      const unsigned o = isb ? WS_O + ((rt * 8 + w * 2 + (u >> 1)) * 32 + 16 + hp * 2 + (u & 1)) * 1024 : obs + u * 32768;
      pw = __builtin_amdgcn_raw_buffer_load_b32(wsr, o + (l & 7) * 128, 0, 16);
    }
  };
  const unsigned xsw = li * 128 + ((g ^ (li >> 1)) << 4);
  const unsigned wln = (8 * g + 2 * q) * 112 + 8 * p + 56 * hh;

  if (__builtin_expect(w >= 4, 1)) {
    if (!br) {
      int cc = wrap7(rot + 1);
      unsigned sl = L0 + a_slot(2);
#pragma nounroll
      for (int c = 2; c < 7; ++c) {
        vm_bar<10>();
        cc = wrap7(cc + 1);
        chunk(sl, sl + A_W, sl + A_W + WM_B, cc);
        sl = sl == L0 + a_slot(0) ? L0 + a_slot(1) : sl + A_SLOT;
      }
      vm_bar<10>();
      if (cg >= 7) { vm_bar<0>(); return; }
      wo_run(0, 4);
      vm_bar<4>();
      wo_run(4, 28);
      VM_WAIT(14);
      sig(L0 + SYNC + SY_WOA + wl * 4);
      VM_WAIT(0);
      asm volatile("" ::"v"(gv), "v"(epoch), "v"(pt));
      sig(L0 + SYNC + SY_WOB + wl * 4);
    } else {
      auto instr = [&](int n) {
        if (n < 56) {
          const int c = n >> 3, j = n & 7, cc = wrap7(c + rot);
          const unsigned sl = L0 + b_slot(c), sw = cc * (64 * WROW), sx = cc * 128;
          if (j < 4) dma16(srdX, (j & 1) ? vx1 : vx0, sx01 + j * 8 * XROW + sx, sl + (wl * 4 + j) * 1024);
          else dma16(srdW, j < 6 ? vw0 : v2, sw + ((j & 1) + 1) * XROW, sl + B_W + (j & 1) * WM_B + (j < 6 ? wl * 1024 : sub2));
        } else {
          const int c = (n - 56) >> 1;
          dma16(srdW, (n & 1) ? v2 : vw0, wrap7(c + rot) * (64 * WROW), L0 + B_WQ + c * WM_B + ((n & 1) ? sub2 : wl * 1024));
        }
      };
      bf16x8 xq[14][2];
      const unsigned xl = L0 + (wl * 32 + li) * 128 + ((g ^ (li >> 1)) << 4);
#pragma unroll
      for (int c = 0; c < 7; ++c) {
        vm_bar<8>();
#pragma unroll
        for (int ks = 0; ks < 2; ++ks) {
          const unsigned xa = (xl ^ (ks * 64)) + b_slot(c);
          xq[c * 2 + ks][0] = as_frag(lds_ld128(xa));
          xq[c * 2 + ks][1] = as_frag(lds_ld128(xa + 2048));
        }
#pragma unroll
        for (int n = 8 * c + 16; n < (c < 6 ? 8 * c + 24 : 70); ++n) instr(n);
      }
      VM_WAIT(0);
      asm volatile("" ::"v"(gv), "v"(epoch), "v"(pt));
      sig(L0 + SYNC + SY_WQ + wl * 4);
      wait4(L0 + SYNC + SY_WQ);
      wait4(L0 + SYNC + SY_EX);
      f32x4 aq[2][2][2];
#pragma unroll
      for (int i = 0; i < 8; ++i) aq[i >> 2][(i >> 1) & 1][i & 1] = (f32x4){kEps, kEps, kEps, kEps};
      const unsigned wb = L0 + B_WQ + (8 * g + 2 * q) * 112 + 8 * p;
      const unsigned gl = L0 + B_G + (8 * g + q) * 8;
      const bool p3 = p == 3;
      const unsigned d1 = p3 ? 32u : 112u;
      const unsigned qa0 = opaque(p3 ? gl : wb + 32), qstep = opaque(p3 ? 256u : 3584u);
      bf16x8 fq[3][4];
      auto rdq = [&](int s) {
        const unsigned wk = wb + s * 3584;
        const unsigned a = qa0 + s * qstep;
        fq[s % 3][0] = lds_tr2(wk, wk + 112);
        fq[s % 3][1] = lds_tr2(a, a + d1);
        fq[s % 3][2] = lds_tr2(wk + 56, wk + 56 + 112);
        fq[s % 3][3] = lds_tr2(wk + 88, wk + 88 + 112);
      };
      rdq(0);
      rdq(1);
#pragma unroll
      for (int s = 0; s < 14; ++s) {
        SB();
        if (s + 2 < 14) rdq(s + 2);
#pragma unroll
        for (int i = 0; i < 4; ++i) {
          aq[0][i >> 1][i & 1] = mfma(fq[s % 3][i], xq[s][0], aq[0][i >> 1][i & 1]);
          aq[1][i >> 1][i & 1] = mfma(fq[s % 3][i], xq[s][1], aq[1][i >> 1][i & 1]);
        }
        interleave<8, 8>();
      }
      sig(L0 + SYNC + SY_QD + wl * 4);
      u32x4 pqf[2][2];
      float omg[2];
#pragma unroll
      for (int t = 0; t < 2; ++t) {
        omg[t] = 1.f - sigmoidf(row3_bcast(aq[t][0][1][0]));
#pragma unroll
        for (int h = 0; h < 2; ++h) {
          const u32x2 a = phi4(aq[t][h][0]), b = phi4(aq[t][h][1]);
          pqf[t][h] = (u32x4){a[0], a[1], g3 ? 0u : b[0], g3 ? 0u : b[1]};
        }
      }
      tagO = uniform64(*(LAS u64*)(uintptr_t)(L0 + SYNC + SY_TAG));
      unsigned ob = WS_O + ((rt * 8 + wl * 2) * 32 + 16 + hp * 2) * 1024 + lane * 16;
      unsigned kb = L0 + B_KVL + lane * 16, zb = L0 + B_KVL + 1024 + (g * 16 + 12) * 16;
      asm volatile("" : "+v"(pqf[0][0]), "+v"(pqf[0][1]), "+v"(pqf[1][0]), "+v"(pqf[1][1]), "+v"(omg[0]), "+v"(omg[1]), "+v"(ob),
                   "+v"(kb), "+v"(zb), "+s"(tagO));
      __builtin_amdgcn_s_setprio(2);
      wait4(L0 + SYNC + SY_KV);
      {
        bf16x8 kv0[2], kv1[2], zf[2];
#pragma unroll
        for (int h = 0; h < 2; ++h) zf[h] = as_frag(lds_ld128(zb + h * 2048));
#pragma unroll
        for (int h = 0; h < 2; ++h) {
          kv0[h] = as_frag(lds_ld128(kb + h * 2048));
          kv1[h] = as_frag(lds_ld128(kb + h * 2048 + 1024));
        }
        SB();
        f32x4 dn[4], n0[4], n1[4];
#pragma unroll
        for (int i = 0; i < 4; ++i) dn[i] = mfma(zf[i >> 1], as_frag(pqf[i & 1][i >> 1]), (f32x4){kEps, kEps, kEps, kEps});
#pragma unroll
        for (int i = 0; i < 4; ++i) {
          n0[i] = mfma(kv0[i >> 1], as_frag(pqf[i & 1][i >> 1]), z4);
          n1[i] = mfma(kv1[i >> 1], as_frag(pqf[i & 1][i >> 1]), z4);
        }
        SB();
#pragma unroll
        for (int i = 0; i < 4; ++i) {
          const float s = omg[i & 1] * __builtin_amdgcn_rcpf(dn[i][0]);
          const u32x2 a = pk4(n0[i] * s), b = pk4(n1[i] * s);
          __builtin_amdgcn_raw_buffer_store_b128((u32x4){a[0], a[1], g3 ? 0u : b[0], b[1]}, wsr, ob + ((i & 1) * 32 + (i >> 1)) * 1024, 0, 16);
        }
      }
      VM_WAIT(0);
      if (lane == 0) __hip_atomic_store((gu64*)(ws + WS_FB) + ((rt * 4 + wl) * 8 + hp) * FS, tagO, RLX_AGENT);
      if (cg >= 7) return;
    }
  } else if (__builtin_expect(br, 1)) {
    __builtin_amdgcn_s_setprio(2);
    setup(B_G);
    f32x4 ak[4][2], av[4][2];
    {
      const float one = li == 12 ? 1.f : 0.f, i0 = rh ? one : 0.f, i1 = rh ? 0.f : one;
#pragma unroll
      for (int t = 0; t < 4; ++t) {
        ak[t][0] = ak[t][1] = (f32x4){kEps, kEps, kEps, kEps};
        av[t][0] = (f32x4){i0, i0, i0, i0};
        av[t][1] = (f32x4){i1, i1, i1, i1};
      }
    }
    bf16x8 xf[2][4], fw[2][4];
    unsigned xb, wb;
    const bool z0 = p == 3 && rh, z1 = p == 3 && !rh;
    const unsigned zaddr = L0 + SYNC + SY_ZERO;
    auto rd = [&](int ks, int part) {
      const unsigned xa = xb ^ (ks * 64), wk = wb + ks * 3584;
      if (part & 1) {
        const unsigned a = wk + WM_B + (((part >> 1) ^ rh) << 5);
        const bool z = (part >> 1) ? z1 : z0;
        fw[ks][part] = lds_tr2(z ? zaddr : a, z ? zaddr : a + 112);
      } else {
        fw[ks][part] = lds_tr2(wk + (part >> 1) * 32, wk + (part >> 1) * 32 + 112);
      }
      if (part < 2) {
        xf[ks][part * 2] = as_frag(lds_ld128(xa + part * 4096));
        xf[ks][part * 2 + 1] = as_frag(lds_ld128(xa + part * 4096 + 2048));
      }
    };
    auto mm = [&](int ks, int part) {
#pragma unroll
      for (int t = 0; t < 4; ++t) {
        if (part & 1) av[t][part >> 1] = mfma(xf[ks][t], fw[ks][part], av[t][part >> 1]);
        else ak[t][part >> 1] = mfma(xf[ks][t], fw[ks][part], ak[t][part >> 1]);
      }
    };
#pragma unroll
    for (int c = 0; c < 7; ++c) {
      xb = opaque(L0 + b_slot(c) + rh * 8192 + xsw);
      wb = opaque(L0 + b_slot(c) + B_W + wln);
      hook(c, true, true);
      LDS_BAR();
      hook(c, false, true);
      SB();
#pragma unroll
      for (int ks = 0; ks < 2; ++ks) {
#pragma unroll
        for (int part = 0; part < 4; ++part) rd(ks, part);
        if (ks == 1 || c > 0) {
#pragma unroll
          for (int part = 0; part < 4; ++part) mm(ks ^ 1, part);
          interleave<16, 12>();
        }
        SB();
      }
    }
    sig(L0 + SYNC + SY_D6 + w * 4);
    mm(1, 0); mm(1, 2);
    SB();
    mm(1, 3); mm(1, 1);
    f32x4 kv[2][2];
    {
      u32x2 pk[4][2], vf[4][2];
#pragma unroll
      for (int t = 0; t < 4; ++t) {
        pk[t][0] = phi4(ak[t][0]);
        pk[t][1] = phi4(ak[t][1]);
        vf[t][0] = pk4(av[t][0]);
        vf[t][1] = pk4(av[t][1]);
      }
#pragma unroll
      for (int e = 1; e >= 0; --e)
#pragma unroll
        for (int d = 0; d < 2; ++d) {
          kv[e][d] = mfma(frag2(pk[0][d], pk[1][d]), frag2(vf[0][e], vf[1][e]), z4);
          kv[e][d] = mfma(frag2(pk[2][d], pk[3][d]), frag2(vf[2][e], vf[3][e]), kv[e][d]);
        }
    }
    lds_st128(L0 + B_EX + w * 2048 + lane * 16, __builtin_bit_cast(u32x4, kv[1][0]));
    lds_st128(L0 + B_EX + w * 2048 + 1024 + lane * 16, __builtin_bit_cast(u32x4, kv[1][1]));
    sig(L0 + SYNC + SY_EX + w * 4);
    wait1(L0 + SYNC + SY_EX + (w ^ 1) * 4);
    const f32x4 s0 = kv[0][0] + __builtin_bit_cast(f32x4, lds_ld128(L0 + B_EX + (w ^ 1) * 2048 + lane * 16));
    const f32x4 s1 = kv[0][1] + __builtin_bit_cast(f32x4, lds_ld128(L0 + B_EX + (w ^ 1) * 2048 + 1024 + lane * 16));
    {
      const u32x2 a = pk4(s0), b = pk4(s1);
      publish((u32x4){a[0], a[1], b[0], b[1]}, wsr, lane * 16 + kvs + rt * 1024);
    }
    if (!__all(clr == tagO)) poll_flags(clrp(), tagO);
    wo_run(0, QWO);
    f32x4 sum0, sum1;
    {
      const unsigned e0 = q == g ? ((p & 1) ? 0x3f800000u : 0x3f80u) : 0u;
      const unsigned ea = p < 2 ? e0 : 0u, eb = p < 2 ? 0u : e0;
      const bf16x8 sel0 = as_frag((u32x4){ea, eb, 0u, 0u}), sel1 = as_frag((u32x4){0u, 0u, ea, eb});
      unsigned vo = kvs + lane * 16;
#pragma nounroll
      for (unsigned spins = 0; spins < SPIN_CAP; ++spins) {
        asm volatile("" : "+v"(vo)::"memory");
        u32x4 pr[16];
#pragma unroll
        for (int r = 0; r < 16; ++r) pr[r] = __builtin_amdgcn_raw_buffer_load_b128(wsr, vo, ((r + 2 * rt) & 15) * 1024, 16);
        SB();
        sum0 = sum1 = z4;
#pragma unroll
        for (int r = 0; r < 16; ++r) {
          sum0 = mfma(sel0, as_frag(pr[r]), sum0);
          sum1 = mfma(sel1, as_frag(pr[r]), sum1);
        }
        if (!__any(sum0[0] != sum0[0])) break;
        unsigned m = 0;
#pragma unroll
        for (int r = 0; r < 16; ++r) m = max(max(max(max(m, pr[r][0]), pr[r][1]), pr[r][2]), pr[r][3]);
        if (!__any(m == SENT)) break;
      }
    }
    asm volatile("" ::"v"(pw));
    lds_st128(L0 + B_KVL + w * 1024 + lane * 16, __builtin_bit_cast(u32x4, frag2(pk4(sum0), pk4(sum1))));
    sig(L0 + SYNC + SY_KV + w * 4);
    __builtin_amdgcn_s_setprio(0);
    if (cg >= 7) return;
    sig(L0 + SYNC + SY_WOA + w * 4);
    wait4(L0 + SYNC + SY_D6);
    wait4(L0 + SYNC + SY_QD);
    wo_run(QWO, 28);
  } else {
    asm volatile("; role A");
    setup(A_G);
    f32x4 aq[4][2], ak[5][2], av[5][2];
#pragma unroll
    for (int t = 0; t < 5; ++t)
#pragma unroll
      for (int f = 0; f < 2; ++f) { ak[t][f] = av[t][f] = z4; if (t < 4) aq[t][f] = z4; }
    const unsigned gl = L0 + A_G + (8 * g + q) * 8;
    const bool p3 = p == 3;
    const unsigned d1 = p3 ? 32u : 112u;
    bf16x8 xf[2][5], fq[2][2], fk[2][2], fv[2][2];
    unsigned xb, wb;
    auto rd = [&](int c, int ks, int part) {
      const unsigned xa = xb ^ (ks * 64), wk = wb + ks * 3584;
      if (part == 0) {
        fk[ks][0] = lds_tr2(wk + WM_B, wk + WM_B + 112);
        xf[ks][0] = as_frag(lds_ld128(xa));
        xf[ks][1] = as_frag(lds_ld128(xa + 2048));
      } else if (part == 1) {
        fv[ks][0] = lds_tr2(wk + 2 * WM_B, wk + 2 * WM_B + 112);
        xf[ks][2] = as_frag(lds_ld128(xa + 4096));
      } else if (part == 2) {
        fq[ks][0] = lds_tr2(wk, wk + 112);
        xf[ks][3] = as_frag(lds_ld128(xa + 6144));
      } else if (part == 3) {
        fk[ks][1] = lds_tr2(wk + WM_B + 32, wk + WM_B + 32 + 112);
        xf[ks][4] = as_frag(lds_ld128(xa + 8192));
      } else if (part == 4) {
        fv[ks][1] = lds_tr2(wk + 2 * WM_B + 32, wk + 2 * WM_B + 32 + 112);
      } else {
        const unsigned a = p3 ? gl + c * 512 + ks * 256 : wk + 32;
        fq[ks][1] = lds_tr2(a, a + d1);
      }
    };
    auto mm = [&](int ks, int part) {
      const int f = part / 3, m = part % 3;
#pragma unroll
      for (int t = 0; t < 5; ++t) {
        if (m == 0) ak[t][f] = mfma(fk[ks][f], xf[ks][t], ak[t][f]);
        if (m == 1) av[t][f] = mfma(xf[ks][t], fv[ks][f], av[t][f]);
        if (m == 2 && t > 0) aq[t - 1][f] = mfma(fq[ks][f], xf[ks][t], aq[t - 1][f]);
      }
    };
#pragma unroll
    for (int c = 0; c < 7; ++c) {
      xb = opaque(L0 + a_slot(c) + rh * 8192 + xsw);
      wb = opaque(L0 + a_slot(c) + A_W + wln);
      hook(c, true, false);
      LDS_BAR();
      hook(c, false, false);
      SB();
#pragma unroll
      for (int ks = 0; ks < 2; ++ks) {
#pragma unroll
        for (int part = 0; part < 6; ++part) rd(c, ks, part);
        if (ks == 1 || c > 0) {
#pragma unroll
          for (int part = 0; part < 6; ++part) mm(ks ^ 1, part);
          interleave<28, 17>();
        }
        SB();
      }
    }
    sig(L0 + SYNC + SY_D6 + w * 4);
    mm(1, 0); mm(1, 3); mm(1, 2); mm(1, 5);
    SB();
    mm(1, 1); mm(1, 4);
    const bool odd = li & 1;
    const int kk0 = 4 * g + (li & 1);
    const bool own0 = kk0 <= li, own1 = kk0 + 2 <= li;
    const float sc = 0.18898223650461362f * kLog2e;
    const float ninf = (rt == 0 && rh == 0) ? -__builtin_inff() : 0.f;
    const unsigned l16 = lane_id() * 16;
    bf16x8 kf[5];
#pragma unroll
    for (int t = 0; t < 5; ++t) kf[t] = frag2(pk4(ak[t][0]), pk4(ak[t][1]));
    f32x4 sp[4], so[4];
#pragma unroll
    for (int t = 0; t < 4; ++t) {
      const u32x2 b = pk4(aq[t][1]);
      const bf16x8 qf = frag2(pk4(aq[t][0]), (u32x2){g3 ? 0u : b[0], g3 ? 0u : b[1]});
      sp[t] = mfma(kf[t], qf, t == 0 ? (f32x4){ninf, ninf, ninf, ninf} : z4);
      so[t] = mfma(kf[t + 1], qf, z4);
    }
    float ug;
    {
      float z0 = aq[0][1][0], z1 = aq[1][1][0], z2 = aq[2][1][0], z3 = aq[3][1][0];
      swap32(z0, z1);
      swap32(z2, z3);
      swap16(z1, z3);
      ug = 1.f + __builtin_amdgcn_exp2f(-kLog2e * z3);
    }
    u32x2 vf[5][2];
#pragma unroll
    for (int t = 0; t < 5; ++t) {
      vf[t][0] = pk4(av[t][0]);
      vf[t][1] = pk4(av[t][1]);
    }
    float l0[4], l1[4], bc[4];
#pragma unroll
    for (int t = 0; t < 4; ++t) {
      float a0 = so[t][0], a2 = so[t][2], b0 = sp[t][0], b2 = sp[t][2];
      const float c0 = own0 ? a0 : b0, c1 = own0 ? so[t][1] : sp[t][1], c2 = own1 ? a2 : b2, c3 = own1 ? so[t][3] : sp[t][3];
      l0[t] = odd ? c1 : c0;
      l1[t] = odd ? c3 : c2;
    }
    rows_bcast4(rows_reduce4<true>(fmax1(l0[0], l1[0]), fmax1(l0[1], l1[1]), fmax1(l0[2], l1[2]), fmax1(l0[3], l1[3])) * -sc, bc);
#pragma unroll
    for (int t = 0; t < 4; ++t) {
      l0[t] = __builtin_amdgcn_exp2f(__builtin_fmaf(l0[t], sc, bc[t]));
      l1[t] = __builtin_amdgcn_exp2f(__builtin_fmaf(l1[t], sc, bc[t]));
    }
    rows_bcast4(__builtin_amdgcn_rcpf(rows_reduce4<false>(l0[0] + l1[0], l0[1] + l1[1], l0[2] + l1[2], l0[3] + l1[3]) * ug), bc);
    const unsigned sh = odd ? 16u : 0u;
    const unsigned mk0 = own0 ? 0u : 0xffffu, mk1 = own1 ? 0u : 0xffffu;
#pragma unroll
    for (int t = 0; t < 4; ++t) {
      const unsigned pe = pk2(l0[t] * bc[t], l1[t] * bc[t]), ph = pe >> 16;
      const bf16x8 pf = as_frag((u32x4){(pe & mk0) << sh, (ph & mk1) << sh, (pe & (mk0 ^ 0xffffu)) << sh, (ph & (mk1 ^ 0xffffu)) << sh});
      const f32x4 r0 = mfma(frag2(vf[t][0], vf[t + 1][0]), pf, z4);
      const f32x4 r1 = mfma(frag2(vf[t][1], vf[t + 1][1]), pf, z4);
      const u32x2 c0 = pk4(r0), c1 = pk4(r1);
      __builtin_amdgcn_raw_buffer_store_b128((u32x4){c0[0], c0[1], g3 ? 0u : c1[0], g3 ? 0u : c1[1]}, wsr, l16 + obs + t * 32768, 0, 16);
    }
    VM_WAIT(0);
    asm volatile("" ::"v"(pw));
    if (l16 == 0) __hip_atomic_store((gu64*)(ws + WS_FA) + ((rt * 2 + rh) * 16 + hp * 2 + hh) * FS, tagO, RLX_AGENT);
    if (cg >= 7) {
      if (bid == 56 && w == 0) {
#pragma nounroll
        for (int k = 0; k < 4; ++k) {
          const int i = (k & 1) * 64 + lane;
          poll_flags(k < 2 ? (gu64*)(ws + WS_CLR) + i : (gu64*)(ws + WS_FA) + ((i >> 3) * 32 + (i & 7) * 2) * FS, tagO);
        }
        if (lane == 0) __hip_atomic_store((gu64*)(ws + WS_E), tagO, RLX_AGENT);
      }
      return;
    }
  }

  const int pb = w >> 2;
  wait4(L0 + SYNC + (pb ? SY_WOB : SY_WOA));
  const unsigned sw = (2 * g + (q >> 1)) & 3;
  const unsigned rb = L0 + (pb * 448 + 4 * g + q) * 128 + 8 * p;
  unsigned wa[4], wa2[4];
#pragma unroll
  for (int v = 0; v < 4; ++v) {
    wa[v] = opaque(rb + ((v ^ sw) << 5));
    wa2[v] = opaque(wa[v] + (g3 ? 0u : 2048u));
  }
  auto wfrag = [&](int h, int ct) {
    const int v = ct ^ (2 * (h & 1));
    return lds_tr2(wa[v] + h * 3584, wa2[v] + h * 3584);
  };
  constexpr int NPRE = 8;
  bf16x8 wf[16][4];
#pragma unroll
  for (int h = 0; h < NPRE; ++h)
#pragma unroll
    for (int ct = 0; ct < 4; ++ct) wf[h][ct] = wfrag(h, ct);
  const unsigned ub = WS_O + ((rg * 4 + wl) * 32 + pb * 16) * 1024;
  tagO = uniform64(*(LAS u64*)(uintptr_t)(L0 + SYNC + SY_TAG));
  {
    const gu64* fp = pb ? (gu64*)(ws + WS_FB) + ((rg * 2 + (wl >> 1)) * 8 + (li & 7)) * FS : (gu64*)(ws + WS_FA) + (rg * 16 + li) * FS;
    const u64 f0 = __hip_atomic_load(fp, RLX_AGENT);
    if (br && !pb) { VM_WAIT(0); sig(L0 + SYNC + SY_WOB + wl * 4); }
    if (!__all(f0 == tagO)) poll_flags(fp, tagO);
  }
  u32x4 of[16];
  {
    unsigned vo = ub + lane * 16;
    asm volatile("" : "+v"(vo)::"memory");
#pragma unroll
    for (int h = 0; h < 16; ++h) of[h] = __builtin_amdgcn_raw_buffer_load_b128(wsr, vo, ((h + 2 * cg) & 15) * 1024, 16);
    SB();
  }
  f32x4 acc[4] = {z4, z4, z4, z4};
#pragma unroll
  for (int h = 0; h < 16; ++h) {
#pragma unroll
    for (int ct = 0; ct < 4; ++ct) acc[ct] = mfma(wf[h][ct], as_frag(of[h]), acc[ct]);
    SB();
    if (h + NPRE < 16) {
#pragma unroll
      for (int ct = 0; ct < 4; ++ct) wf[h + NPRE][ct] = wfrag(h + NPRE, ct);
    }
    SB();
  }
  const unsigned xa = L0 + P2X + wl * 4096 + lane * 16;
  if (!pb) {
    wait4(L0 + SYNC + SY_D6);
#pragma unroll
    for (int ct = 0; ct < 4; ++ct) lds_st128(xa + ct * 1024, __builtin_bit_cast(u32x4, acc[ct]));
    sig(L0 + SYNC + SY_P2 + wl * 4);
  } else {
    f32x4 pa[4];
    for (;;) {
      const unsigned f = *(volatile LAS unsigned*)(uintptr_t)(L0 + SYNC + SY_P2 + wl * 4);
#pragma unroll
      for (int ct = 0; ct < 4; ++ct) pa[ct] = *(volatile LAS f32x4*)(uintptr_t)(xa + ct * 1024);
      if (__builtin_amdgcn_readfirstlane(f)) break;
      __builtin_amdgcn_s_sleep(1);
    }
    char* orow = out + (rg * 64 + wl * 16 + li) * XROW + cg * 128 + (g & 1) * 32 + (g >> 1) * 16;
#pragma unroll
    for (int k = 0; k < 2; ++k) {
      const u32x2 a = pk4(acc[2 * k] + pa[2 * k]), b = pk4(acc[2 * k + 1] + pa[2 * k + 1]);
      const auto r0 = __builtin_amdgcn_permlane16_swap(a[0], b[0], false, false);
      const auto r1 = __builtin_amdgcn_permlane16_swap(a[1], b[1], false, false);
      *(u32x4*)(orow + k * 64) = (u32x4){r0[0], r1[0], r0[1], r1[1]};
    }
  }
}

}

extern "C" void kernel_launch(void* const* d_in, const int* in_sizes, int n_in, void* d_out, int out_size, void* d_ws,
                              size_t ws_size, hipStream_t stream) {
  (void)in_sizes; (void)n_in; (void)out_size; (void)ws_size;
  gated_mix_kernel<<<256, 512, 0, stream>>>((const char*)d_in[0], (const char*)d_in[1], (const char*)d_in[2],
                                            (const char*)d_in[4], (const char*)d_in[5],
                                            (const unsigned short*)d_in[6], (char*)d_out, (char*)d_ws);
}
```

```cpp
#define DIAG_A 0
#define DIAG_B 0
#define DIAG_ROLE 0
#include <hip/hip_runtime.h>
#include <stdint.h>

typedef __bf16 bf16x2 __attribute__((ext_vector_type(2)));
typedef __bf16 bf16x4 __attribute__((ext_vector_type(4)));
typedef __bf16 bf16x8 __attribute__((ext_vector_type(8)));
typedef float f32x2 __attribute__((ext_vector_type(2)));
typedef float f32x4 __attribute__((ext_vector_type(4)));
typedef unsigned u32x2 __attribute__((ext_vector_type(2)));
typedef unsigned u32x4 __attribute__((ext_vector_type(4)));
typedef unsigned long long u64;
#define LAS __attribute__((address_space(3)))
#define GAS __attribute__((address_space(1)))
typedef GAS u64 gu64;
#define RLX_AGENT __ATOMIC_RELAXED, __HIP_MEMORY_SCOPE_AGENT

namespace {

constexpr int kS = 2048, kD = 448;
constexpr int XROW = kD * 2;
constexpr int WROW = 3 * kD * 2;
constexpr int WM_B = 64 * 112;

constexpr int A_FRONT = 34816, A_SLOT = 144 * 128 + 3 * WM_B, A_W = 144 * 128;
constexpr int A_G = A_FRONT + 3 * A_SLOT;
constexpr int B_SLOT = 128 * 128 + 2 * WM_B, B_W = 128 * 128;
constexpr int B_WQ = 97536, B_G = B_WQ + 7 * WM_B, B_EX = B_G + 3584, B_KVL = B_EX + 8192;
constexpr int P2X = 114688;
constexpr int SYNC = 163584;
constexpr int LDS_B = 163840;
static_assert(A_G + 3584 <= SYNC && B_KVL + 4096 <= SYNC && A_FRONT + 2 * A_SLOT == 114688, "lds map");
__host__ __device__ constexpr int a_slot(int c) { return A_FRONT + ((c + 2) % 3) * A_SLOT; }
__host__ __device__ constexpr int b_slot(int c) { return ((c + 2) % 3) * B_SLOT; }
enum { SY_WOA = 0, SY_WOB = 16, SY_D6 = 32, SY_D6Q = 48, SY_WQ = 64, SY_QD = 80, SY_EX = 96, SY_KV = 112, SY_P2 = 128, SY_ZERO = 192, SY_TAG = 240 };

constexpr int WS_E = 0;
constexpr int WS_CLR = 4096;
constexpr int FS = 16;
constexpr int WS_FA = 8192;
constexpr int WS_FB = WS_FA + 16 * 2 * 16 * 8 * FS;
constexpr int WS_KVP = WS_FB + 16 * 4 * 8 * 8 * FS;
constexpr int WS_O = WS_KVP + 8 * 4 * 16 * 1024;
constexpr int WS_END = WS_O + 16 * 8 * 32 * 1024;

constexpr int PT_WS = (WS_END / 8) & ~127;
constexpr unsigned SPIN_CAP = 1u << 14;
constexpr unsigned SENT = 0xffffffffu;
#define QWO 14

#define STAMP(i) do { if (i == DIAG_A) dta = (unsigned)__builtin_amdgcn_s_memrealtime(); if (i == DIAG_B) dtb = (unsigned)__builtin_amdgcn_s_memrealtime(); } while (0)
#define STAMPC(i) do {} while (0)
#define STAMP_FLUSH() do { if (br * 2 + (w >> 2) == DIAG_ROLE) { unsigned d = (dtb && dta) ? (dtb - dta) * 8u : 0u; d = d > 100000u ? 100000u : d; const unsigned t0 = dt0; while ((unsigned)__builtin_amdgcn_s_memrealtime() - t0 < 4000u + d) __builtin_amdgcn_s_sleep(2); } } while (0)

__device__ __forceinline__ unsigned pk2(float a, float b) {
  f32x2 v = {a, b};
  bf16x2 r = __builtin_convertvector(v, bf16x2);
  return __builtin_bit_cast(unsigned, r);
}
__device__ __forceinline__ u32x2 pk4(f32x4 v) { return (u32x2){pk2(v[0], v[1]), pk2(v[2], v[3])}; }
__device__ __forceinline__ bf16x8 as_frag(u32x4 v) { return __builtin_bit_cast(bf16x8, v); }
__device__ __forceinline__ bf16x8 frag2(u32x2 a, u32x2 b) { return as_frag((u32x4){a[0], a[1], b[0], b[1]}); }
__device__ __forceinline__ f32x4 mfma(bf16x8 a, bf16x8 b, f32x4 c) {
  return __builtin_amdgcn_mfma_f32_16x16x32_bf16(a, b, c, 0, 0, 0);
}
__device__ __forceinline__ bf16x8 lds_tr2(unsigned a0, unsigned a1) {
  bf16x4 lo = __builtin_amdgcn_ds_read_tr16_b64_v4bf16((LAS bf16x4*)(uintptr_t)a0);
  bf16x4 hi = __builtin_amdgcn_ds_read_tr16_b64_v4bf16((LAS bf16x4*)(uintptr_t)a1);
  return __builtin_shufflevector(lo, hi, 0, 1, 2, 3, 4, 5, 6, 7);
}
__device__ __forceinline__ u32x4 lds_ld128(unsigned a) { return *(LAS u32x4*)(uintptr_t)a; }
__device__ __forceinline__ void lds_st128(unsigned a, u32x4 v) { *(LAS u32x4*)(uintptr_t)a = v; }
__device__ __forceinline__ void lds_st64(unsigned a, u32x2 v) { *(LAS u32x2*)(uintptr_t)a = v; }
__device__ __forceinline__ unsigned opaque(unsigned v) { asm volatile("" : "+v"(v)); return v; }

__device__ __forceinline__ void sig(unsigned a) {
  asm volatile("" ::: "memory");
  *(volatile LAS unsigned*)(uintptr_t)a = 1u;
  asm volatile("" ::: "memory");
  __builtin_amdgcn_sched_barrier(0);
}
__device__ __forceinline__ void wait1(unsigned a) {
  while (__builtin_amdgcn_readfirstlane(*(volatile LAS unsigned*)(uintptr_t)a) == 0u) __builtin_amdgcn_s_sleep(1);
  asm volatile("" ::: "memory");
}
__device__ __forceinline__ void wait4(unsigned a) {
  for (;;) {
    const u32x4 v = *(volatile LAS u32x4*)(uintptr_t)a;
    if (__builtin_amdgcn_readfirstlane(v[0] & v[1] & v[2] & v[3])) break;
    __builtin_amdgcn_s_sleep(1);
  }
  asm volatile("" ::: "memory");
}

__device__ __forceinline__ u32x4 make_srd(const void* p, unsigned bytes) {
  uint64_t a = (uint64_t)p;
  u32x4 r;
  r[0] = __builtin_amdgcn_readfirstlane((unsigned)a);
  r[1] = __builtin_amdgcn_readfirstlane((unsigned)(a >> 32) & 0xffffu);
  r[2] = bytes;
  r[3] = 0x00020000u;
  return r;
}
__device__ __forceinline__ void dma16(u32x4 srd, unsigned voff, unsigned soff, unsigned ldsaddr) {
  asm volatile(
      "s_mov_b32 m0, %3\n\t"
      "s_nop 3\n\t"
      "buffer_load_dwordx4 %0, %1, %2 offen lds"
      :
      : "v"(voff), "s"(srd), "s"(soff), "s"(ldsaddr));
}
#define VM_BAR(N) asm volatile("s_waitcnt vmcnt(" #N ") lgkmcnt(0)\n\ts_barrier" ::: "memory")
#define LDS_BAR() asm volatile("s_waitcnt lgkmcnt(0)\n\ts_barrier" ::: "memory")
#define VM_WAIT(N) asm volatile("s_waitcnt vmcnt(" #N ")" ::: "memory")
template <int N> __device__ __forceinline__ void vm_bar() { asm volatile("s_waitcnt vmcnt(%0) lgkmcnt(0)\n\ts_barrier" ::"n"(N) : "memory"); }
#define SB() __builtin_amdgcn_sched_barrier(0)

__device__ __forceinline__ float fmax1(float a, float b) { return __builtin_amdgcn_fmed3f(a, b, 3.0e38f); }
__device__ __forceinline__ void swap16(float& a, float& b) {
  auto r = __builtin_amdgcn_permlane16_swap(__float_as_uint(a), __float_as_uint(b), false, false);
  const unsigned r0 = r[0], r1 = r[1];
  a = __uint_as_float(r0);
  b = __uint_as_float(r1);
}
__device__ __forceinline__ void swap32(float& a, float& b) {
  auto r = __builtin_amdgcn_permlane32_swap(__float_as_uint(a), __float_as_uint(b), false, false);
  const unsigned r0 = r[0], r1 = r[1];
  a = __uint_as_float(r0);
  b = __uint_as_float(r1);
}
template <bool MAX>
__device__ __forceinline__ float rows_reduce4(float v0, float v1, float v2, float v3) {
  swap32(v0, v1);
  swap32(v2, v3);
  float a = MAX ? fmax1(v0, v1) : v0 + v1, b = MAX ? fmax1(v2, v3) : v2 + v3;
  swap16(a, b);
  return MAX ? fmax1(a, b) : a + b;
}
__device__ __forceinline__ void rows_bcast4(float x, float (&o)[4]) {
  float y = x;
  swap16(x, y);
  o[0] = x; o[1] = x; o[2] = y; o[3] = y;
  swap32(o[0], o[1]);
  swap32(o[2], o[3]);
}
__device__ __forceinline__ float row3_bcast(float v) {
  float a = v;
  swap32(a, v);
  a = v;
  swap16(a, v);
  return v;
}
template <int NM, int ND>
__device__ __forceinline__ void interleave() {
#pragma unroll
  for (int i = 0; i < NM; ++i) {
    __builtin_amdgcn_sched_group_barrier(0x8, 1, 0);
    if (i < ND) {
      __builtin_amdgcn_sched_group_barrier(0x2, 1, 0);
      __builtin_amdgcn_sched_group_barrier(0x100, 1, 0);
    }
  }
}
constexpr float kEps = 1e-6f, kLog2e = 1.44269504088896f;
__device__ __forceinline__ float phi(float u) {
  const float e = __builtin_amdgcn_fmed3f(__builtin_amdgcn_exp2f(__builtin_fmaf(u, kLog2e, -kEps * kLog2e)), 0.f, 1.f);
  return fmax1(u, kEps) + e;
}
__device__ __forceinline__ u32x2 phi4(f32x4 v) { return (u32x2){pk2(phi(v[0]), phi(v[1])), pk2(phi(v[2]), phi(v[3]))}; }
__device__ __forceinline__ float sigmoidf(float z) {
  return __builtin_amdgcn_rcpf(1.f + __builtin_amdgcn_exp2f(-1.44269504088896f * z));
}
__device__ __forceinline__ u64 next_tag(u64 e) {
  const u64 s = ((u64)(unsigned)__builtin_amdgcn_readfirstlane((unsigned)(e >> 32)) << 32) |
                (unsigned)__builtin_amdgcn_readfirstlane((unsigned)e);
  return s * 6364136223846793005ull + 1442695040888963407ull;
}
__device__ __forceinline__ u64 uniform64(u64 e) {
  return ((u64)(unsigned)__builtin_amdgcn_readfirstlane((unsigned)(e >> 32)) << 32) |
         (unsigned)__builtin_amdgcn_readfirstlane((unsigned)e);
}
__device__ __forceinline__ void poll_flags(const gu64* p, u64 tag) {
#pragma nounroll
  for (unsigned spins = 0; spins < SPIN_CAP; ++spins) {
    u64 v = __hip_atomic_load(p, RLX_AGENT);
    if (__all(v == tag)) break;
    __builtin_amdgcn_s_sleep(2);
  }
  asm volatile("" ::: "memory");
}
__device__ __forceinline__ void publish(u32x4 v, __amdgpu_buffer_rsrc_t r, unsigned voff) {
#pragma unroll
  for (int i = 0; i < 4; ++i) v[i] = v[i] < SENT - 1u ? v[i] : SENT - 1u;
  __builtin_amdgcn_raw_buffer_store_b128(v, r, voff, 0, 16);
}
__device__ __forceinline__ unsigned lane_id() {
  unsigned l = __builtin_amdgcn_mbcnt_hi(~0u, __builtin_amdgcn_mbcnt_lo(~0u, 0u));
  asm volatile("" : "+v"(l));
  return l;
}
__device__ __forceinline__ int wrap7(int c) { return c >= 7 ? c - 7 : c; }

__global__ __launch_bounds__(512, 2) void gated_mix_kernel(const char* __restrict__ x, const char* __restrict__ wqkvA,
                                                           const char* __restrict__ woA, const char* __restrict__ wqkvB,
                                                           const char* __restrict__ woB,
                                                           const unsigned short* __restrict__ gate_w, char* __restrict__ out,
                                                           char* ws) {
  __shared__ __attribute__((aligned(1024))) char lds[LDS_B];
  const unsigned L0 = (unsigned)(uintptr_t)(LAS char*)lds;
  const int bid = blockIdx.x;
  const int xcd = bid & 7, idx = bid >> 3;
  const int xj = xcd >> 1, ci = idx >> 3;
  const int br = ci >> 1;
  const int hp = 2 * (br ? (xj ^ 1) : xj) + (ci & 1);
  const int rt = (xcd & 1) * 8 + (idx & 7);
  const int rot = wrap7(wrap7((idx & 7) + 2 * ci + (xj & 1)));
  const int cg = idx & 7, rg = 4 * xcd + ci;

  const int tid = threadIdx.x;
  const int lane = tid & 63;
  const int w = __builtin_amdgcn_readfirstlane(tid >> 6) & 7;
  const int wl = w & 3;
  const int li = lane & 15, g = lane >> 4, q = (lane >> 2) & 3, p = lane & 3;
  const bool g3 = g == 3;
  const f32x4 z4 = {0.f, 0.f, 0.f, 0.f};
  unsigned dta = 0, dtb = 0;
  const unsigned dt0 = (unsigned)__builtin_amdgcn_s_memrealtime();
  STAMP(0); STAMPC(18);
  unsigned vx0 = (lane >> 3) * XROW + (((lane & 7) ^ (lane >> 4)) << 4);
  unsigned vx1 = vx0 ^ 64u;
  unsigned vw0, vw1;
  {
    const unsigned s0 = wl * 64 + lane, s1 = s0 + 256;
    const unsigned p0 = (s0 * 9363u) >> 16, p1 = (s1 * 9363u) >> 16;
    const unsigned k0 = (p0 & 56) | ((p0 >> 1) & 3) | ((p0 & 1) << 2);
    const unsigned k1 = (p1 & 56) | ((p1 >> 1) & 3) | ((p1 & 1) << 2);
    vw0 = k0 * WROW + hp * 112 + (s0 - p0 * 7) * 16;
    vw1 = k1 * WROW + hp * 112 + (s1 - p1 * 7) * 16;
  }
  asm volatile("" : "+v"(vx0), "+v"(vx1), "+v"(vw0), "+v"(vw1));
  asm volatile("" ::"s"(x), "s"(wqkvA), "s"(woA), "s"(wqkvB), "s"(woB), "s"(gate_w), "s"(out), "s"(ws));
  const __amdgpu_buffer_rsrc_t wsr = __builtin_amdgcn_make_buffer_rsrc(ws, 0, WS_END, 0x00020000);
  u64 tagO;
  auto wo_run = [&](int j0, int j1) {
    const unsigned vwo = (lane >> 3) * XROW + cg * 128 + (((lane & 7) ^ (((lane >> 4) & 3) << 1)) << 4);
    const u32x4 sa = make_srd(woA, kD * XROW), sb = make_srd(woB, kD * XROW);
    unsigned so = (wl + 7 * cg + 4 * (j0 % 14)) * (8 * XROW);
    so = so >= kD * XROW ? so - kD * XROW : so;
#pragma unroll
    for (int j = j0; j < j1; ++j) {
      dma16(j < 14 ? sa : sb, vwo, so, L0 + (wl + 4 * j) * 1024);
      so += 32 * XROW;
      so = so >= kD * XROW ? so - kD * XROW : so;
    }
  };
  const u32x4 srdX = make_srd(x, kS * XROW);
  const u32x4 srdW = make_srd(br ? wqkvB : wqkvA, kD * WROW);
  const bool l3 = wl == 3;
  const int xr = rt * 128 + wl * 32 - (br ? 0 : 16);
  const unsigned sx01 = (xr < 0 ? 0 : xr) * XROW, sx23 = (xr + 16) * XROW;
  const bool l3a = l3 && !br;
  const u32x4 srd2 = l3a ? srdX : srdW;
  const unsigned v2 = l3 ? vw0 : vw1;
  const unsigned v2a = l3a ? vx0 : v2, v2b = l3a ? vx1 : v2;
  const unsigned st2 = l3a ? 128u : 64u * WROW, s2a = l3a ? (rt * 128 + 112) * XROW : 0u;
  const unsigned s2d = l3a ? 8u * XROW : (unsigned)XROW, s2e = l3a ? 8u * XROW : 2u * XROW;
  const unsigned sub2 = (l3 ? wl : wl + 4) * 1024;
  auto chunk = [&](unsigned sl, unsigned lq, unsigned lk, int cc) {
    const unsigned sw = cc * (64 * WROW), sx = cc * 128, s2 = cc * st2 + s2a;
#pragma unroll
    for (int j = 0; j < 4; ++j)
      dma16(srdX, (j & 1) ? vx1 : vx0, (j < 2 ? sx01 : sx23) + (j & 1) * 8 * XROW + sx, sl + (wl * 4 + j) * 1024);
    dma16(srdW, vw0, sw + XROW, lk + wl * 1024);
    dma16(srdW, vw0, sw + 2 * XROW, lk + WM_B + wl * 1024);
    dma16(srd2, v2b, s2 + s2d, l3a ? sl + 17408 : lk + sub2);
    dma16(srd2, v2b, s2 + s2e, l3a ? sl + 17408 : lk + WM_B + sub2);
    if (!br) {
      dma16(srdW, vw0, sw, lq + wl * 1024);
      dma16(srd2, v2a, s2, l3a ? sl + 16384 : lq + sub2);
    }
  };
  unsigned gv = 0, pt = 0;
  u64 epoch = 0;
  {
    int wx = w;
    asm volatile("" : "+s"(wx));
    if (wx < 4) {
      gv = ((const unsigned*)gate_w)[tid < 224 ? tid : 0];
      epoch = __hip_atomic_load((gu64*)(ws + WS_E), RLX_AGENT);
      const char* pb_ = wx == 0 ? ws : (wx == 1 ? out + rg * 64 * XROW : (wx == 2 ? woA : woB));
      pt = __hip_atomic_load((GAS unsigned*)(pb_ + (lane & 7) * (wx == 0 ? PT_WS : (wx == 1 ? 7168 : 50176))), RLX_AGENT);
    } else {
#pragma nounroll
      for (int c = 0; c < 2; ++c) {
        const unsigned sl = L0 + (br ? (c ? b_slot(1) : b_slot(0)) : (c ? a_slot(1) : a_slot(0)));
        chunk(sl, sl + A_W, sl + (br ? B_W : A_W + WM_B), wrap7(rot + c));
      }
    }
  }

    const int rh = w & 1, hh = w >> 1;
    const unsigned obs = WS_O + ((rt * 8 + rh * 4) * 32 + hp * 2 + hh) * 1024;
    const unsigned kvs = WS_KVP + (hp * 4 + w) * 16 * 1024;
    auto setup = [&](int goff) {
      if (tid < 60) *(LAS unsigned*)(uintptr_t)(L0 + SYNC + tid * 4) = 0u;
      const unsigned lo = (gv & 0xffffu) * 0x10001u, hi = (gv >> 16) * 0x10001u;
      const int pos = tid - rot * 32;
      if (tid < 224) lds_st128(L0 + goff + (pos < 0 ? pos + 224 : pos) * 16, (u32x4){lo, lo, hi, hi});
    };
    u64 clr = 0;
    unsigned pw = 0;
    auto clrp = [&]() { return (const gu64*)(ws + WS_CLR) + hp * 16 + (lane_id() & 15); };
    auto hook = [&](int c, bool before, bool isb) {
      if (c == 0 && !before && isb)
        __builtin_amdgcn_raw_buffer_store_b128((u32x4){SENT, SENT, SENT, SENT}, wsr, lane_id() * 16 + kvs + rt * 1024, 0, 16);
      if (c == 3 && before) {
        asm volatile("" : "+v"(epoch) : "v"(pt));
        tagO = next_tag(epoch);
        *(LAS u64*)(uintptr_t)(L0 + SYNC + SY_TAG) = tagO;
        VM_WAIT(0);
      }
      if (c == 3 && !before && isb)
        __builtin_amdgcn_raw_buffer_store_b64((u32x2){(unsigned)tagO, (unsigned)(tagO >> 32)}, wsr,
                                              (lane_id() | w) == 0 ? WS_CLR + (hp * 16 + rt) * 8 : 0x7ffffff0u, 0, 16);
      if (c == 6 && !before && isb) clr = __hip_atomic_load(clrp(), RLX_AGENT);
      if (c == 6 && !before) {
        const unsigned l = lane_id(), u = (l >> 3) & 3;
        const unsigned o = isb ? WS_O + ((rt * 8 + w * 2 + (u >> 1)) * 32 + 16 + hp * 2 + (u & 1)) * 1024 : obs + u * 32768;
        pw = __builtin_amdgcn_raw_buffer_load_b32(wsr, o + (l & 7) * 128, 0, 16);
      }
    };
    const unsigned xsw = li * 128 + ((g ^ (li >> 1)) << 4);
    const unsigned wln = (8 * g + 2 * q) * 112 + 8 * p + 56 * hh;

  if (__builtin_expect(w >= 4, 1)) {
    if (!br) {
      int cc = wrap7(rot + 1);
      unsigned sl = L0 + a_slot(2);
#pragma nounroll
      for (int c = 2; c < 7; ++c) {
        vm_bar<10>();
        cc = wrap7(cc + 1);
        chunk(sl, sl + A_W, sl + A_W + WM_B, cc);
        sl = sl == L0 + a_slot(0) ? L0 + a_slot(1) : sl + A_SLOT;
      }
      vm_bar<10>();
      if (cg >= 7) { vm_bar<0>(); STAMP_FLUSH(); return; }
      wo_run(0, 4);
      vm_bar<4>();
      wo_run(4, 28);
      VM_WAIT(14);
      sig(L0 + SYNC + SY_WOA + wl * 4);
      VM_WAIT(0);
      asm volatile("" ::"v"(gv), "v"(epoch), "v"(pt));
      sig(L0 + SYNC + SY_WOB + wl * 4);
    } else {
      auto instr = [&](int n) {
        if (n < 56) {
          const int c = n >> 3, j = n & 7, cc = wrap7(c + rot);
          const unsigned sl = L0 + b_slot(c), sw = cc * (64 * WROW), sx = cc * 128;
          if (j < 4) dma16(srdX, (j & 1) ? vx1 : vx0, sx01 + j * 8 * XROW + sx, sl + (wl * 4 + j) * 1024);
          else dma16(srdW, j < 6 ? vw0 : v2, sw + ((j & 1) + 1) * XROW, sl + B_W + (j & 1) * WM_B + (j < 6 ? wl * 1024 : sub2));
        } else {
          const int c = (n - 56) >> 1;
          dma16(srdW, (n & 1) ? v2 : vw0, wrap7(c + rot) * (64 * WROW), L0 + B_WQ + c * WM_B + ((n & 1) ? sub2 : wl * 1024));
        }
      };
      STAMP(1);
      bf16x8 xq[14][2];
      const unsigned xl = L0 + (wl * 32 + li) * 128 + ((g ^ (li >> 1)) << 4);
#pragma unroll
      for (int c = 0; c < 7; ++c) {
        if (c < 6) vm_bar<8>(); else vm_bar<8>();
#pragma unroll
        for (int ks = 0; ks < 2; ++ks) {
          const unsigned xa = (xl ^ (ks * 64)) + b_slot(c);
          xq[c * 2 + ks][0] = as_frag(lds_ld128(xa));
          xq[c * 2 + ks][1] = as_frag(lds_ld128(xa + 2048));
        }
#pragma unroll
        for (int n = (c < 6 ? 8 * c + 16 : 56 + 8); n < (c < 5 ? 8 * c + 24 : (c == 5 ? 56 + 8 : 70)); ++n) instr(n);
      }
      STAMP(4);
      VM_WAIT(0);
      asm volatile("" ::"v"(gv), "v"(epoch), "v"(pt));
      sig(L0 + SYNC + SY_WQ + wl * 4);
      wait4(L0 + SYNC + SY_WQ);
      wait4(L0 + SYNC + SY_EX);
      STAMP(3);
      f32x4 aq[2][2][2];
#pragma unroll
      for (int i = 0; i < 8; ++i) aq[i >> 2][(i >> 1) & 1][i & 1] = (f32x4){kEps, kEps, kEps, kEps};
      const unsigned wb = L0 + B_WQ + (8 * g + 2 * q) * 112 + 8 * p;
      const unsigned gl = L0 + B_G + (8 * g + q) * 8;
      const bool p3 = p == 3;
      const unsigned d1 = p3 ? 32u : 112u;
      const unsigned qa0 = opaque(p3 ? gl : wb + 32), qstep = opaque(p3 ? 256u : 3584u);
      bf16x8 fq[3][4];
      auto rdq = [&](int s) {
        const unsigned wk = wb + s * 3584;
        const unsigned a = qa0 + s * qstep;
        fq[s % 3][0] = lds_tr2(wk, wk + 112);
        fq[s % 3][1] = lds_tr2(a, a + d1);
        fq[s % 3][2] = lds_tr2(wk + 56, wk + 56 + 112);
        fq[s % 3][3] = lds_tr2(wk + 88, wk + 88 + 112);
      };
      rdq(0);
      rdq(1);
#pragma unroll
      for (int s = 0; s < 14; ++s) {
        SB();
        if (s + 2 < 14) rdq(s + 2);
#pragma unroll
        for (int i = 0; i < 4; ++i) {
          aq[0][i >> 1][i & 1] = mfma(fq[s % 3][i], xq[s][0], aq[0][i >> 1][i & 1]);
          aq[1][i >> 1][i & 1] = mfma(fq[s % 3][i], xq[s][1], aq[1][i >> 1][i & 1]);
        }
        interleave<8, 8>();
      }
      sig(L0 + SYNC + SY_QD + wl * 4);
      STAMP(5);
      u32x4 pqf[2][2];
      float omg[2];
#pragma unroll
      for (int t = 0; t < 2; ++t) {
        omg[t] = 1.f - sigmoidf(row3_bcast(aq[t][0][1][0]));
#pragma unroll
        for (int h = 0; h < 2; ++h) {
          const u32x2 a = phi4(aq[t][h][0]), b = phi4(aq[t][h][1]);
          pqf[t][h] = (u32x4){a[0], a[1], g3 ? 0u : b[0], g3 ? 0u : b[1]};
        }
      }
      tagO = uniform64(*(LAS u64*)(uintptr_t)(L0 + SYNC + SY_TAG));
      unsigned ob = WS_O + ((rt * 8 + wl * 2) * 32 + 16 + hp * 2) * 1024 + lane * 16;
      unsigned kb = L0 + B_KVL + lane * 16, zb = L0 + B_KVL + 1024 + (g * 16 + 12) * 16;
      asm volatile("" : "+v"(pqf[0][0]), "+v"(pqf[0][1]), "+v"(pqf[1][0]), "+v"(pqf[1][1]), "+v"(omg[0]), "+v"(omg[1]), "+v"(ob),
                   "+v"(kb), "+v"(zb), "+s"(tagO));
      __builtin_amdgcn_s_setprio(2);
      STAMP(6);
      wait4(L0 + SYNC + SY_KV);
      STAMP(8);
      {
        bf16x8 kv0[2], kv1[2], zf[2];
#pragma unroll
        for (int h = 0; h < 2; ++h) zf[h] = as_frag(lds_ld128(zb + h * 2048));
#pragma unroll
        for (int h = 0; h < 2; ++h) {
          kv0[h] = as_frag(lds_ld128(kb + h * 2048));
          kv1[h] = as_frag(lds_ld128(kb + h * 2048 + 1024));
        }
        SB();
        f32x4 dn[4], n0[4], n1[4];
#pragma unroll
        for (int i = 0; i < 4; ++i) dn[i] = mfma(zf[i >> 1], as_frag(pqf[i & 1][i >> 1]), (f32x4){kEps, kEps, kEps, kEps});
#pragma unroll
        for (int i = 0; i < 4; ++i) {
          n0[i] = mfma(kv0[i >> 1], as_frag(pqf[i & 1][i >> 1]), z4);
          n1[i] = mfma(kv1[i >> 1], as_frag(pqf[i & 1][i >> 1]), z4);
        }
        SB();
#pragma unroll
        for (int i = 0; i < 4; ++i) {
          const float s = omg[i & 1] * __builtin_amdgcn_rcpf(dn[i][0]);
          const u32x2 a = pk4(n0[i] * s), b = pk4(n1[i] * s);
          __builtin_amdgcn_raw_buffer_store_b128((u32x4){a[0], a[1], g3 ? 0u : b[0], b[1]}, wsr, ob + ((i & 1) * 32 + (i >> 1)) * 1024, 0, 16);
        }
      }
      STAMP(9);
      VM_WAIT(0);
      if (lane == 0) __hip_atomic_store((gu64*)(ws + WS_FB) + ((rt * 4 + wl) * 8 + hp) * FS, tagO, RLX_AGENT);
      STAMP(10); STAMPC(19);
      if (cg >= 7) { STAMP_FLUSH(); return; }
    }
  } else if (__builtin_expect(br, 1)) {
      __builtin_amdgcn_s_setprio(2);
      setup(B_G);
      f32x4 ak[4][2], av[4][2];
      {
        const float one = li == 12 ? 1.f : 0.f, i0 = rh ? one : 0.f, i1 = rh ? 0.f : one;
#pragma unroll
        for (int t = 0; t < 4; ++t) {
          ak[t][0] = ak[t][1] = (f32x4){kEps, kEps, kEps, kEps};
          av[t][0] = (f32x4){i0, i0, i0, i0};
          av[t][1] = (f32x4){i1, i1, i1, i1};
        }
      }
      bf16x8 xf[2][4], fw[2][4];
      unsigned xb, wb;
      const bool z0 = p == 3 && rh, z1 = p == 3 && !rh;
      const unsigned zaddr = L0 + SYNC + SY_ZERO;
      auto rd = [&](int ks, int part) {
        const unsigned xa = xb ^ (ks * 64), wk = wb + ks * 3584;
        if (part & 1) {
          const unsigned a = wk + WM_B + (((part >> 1) ^ rh) << 5);
          const bool z = (part >> 1) ? z1 : z0;
          fw[ks][part] = lds_tr2(z ? zaddr : a, z ? zaddr : a + 112);
        } else {
          fw[ks][part] = lds_tr2(wk + (part >> 1) * 32, wk + (part >> 1) * 32 + 112);
        }
        if (part < 2) {
          xf[ks][part * 2] = as_frag(lds_ld128(xa + part * 4096));
          xf[ks][part * 2 + 1] = as_frag(lds_ld128(xa + part * 4096 + 2048));
        }
      };
      auto mm = [&](int ks, int part) {
#pragma unroll
        for (int t = 0; t < 4; ++t) {
          if (part & 1) av[t][part >> 1] = mfma(xf[ks][t], fw[ks][part], av[t][part >> 1]);
          else ak[t][part >> 1] = mfma(xf[ks][t], fw[ks][part], ak[t][part >> 1]);
        }
      };
#pragma unroll
      for (int c = 0; c < 7; ++c) {
        xb = opaque(L0 + b_slot(c) + rh * 8192 + xsw);
        wb = opaque(L0 + b_slot(c) + B_W + wln);
        hook(c, true, true);
        LDS_BAR();
        hook(c, false, true);
        if (c == 0) { STAMP(2); STAMPC(16); }
        if (c == 3) STAMP(3);
        if (c == 6) { STAMP(4); STAMPC(17); }
        SB();
#pragma unroll
        for (int ks = 0; ks < 2; ++ks) {
#pragma unroll
          for (int part = 0; part < 4; ++part) rd(ks, part);
          if (ks == 1 || c > 0) {
#pragma unroll
            for (int part = 0; part < 4; ++part) mm(ks ^ 1, part);
            interleave<16, 12>();
          }
          SB();
        }
      }
      sig(L0 + SYNC + SY_D6 + w * 4);
      mm(1, 0); mm(1, 2);
      SB();
      mm(1, 3); mm(1, 1);
      STAMP(5);
      f32x4 kv[2][2];
      {
        u32x2 pk[4][2], vf[4][2];
#pragma unroll
        for (int t = 0; t < 4; ++t) {
          pk[t][0] = phi4(ak[t][0]);
          pk[t][1] = phi4(ak[t][1]);
          vf[t][0] = pk4(av[t][0]);
          vf[t][1] = pk4(av[t][1]);
        }
#pragma unroll
        for (int e = 1; e >= 0; --e)
#pragma unroll
          for (int d = 0; d < 2; ++d) {
            kv[e][d] = mfma(frag2(pk[0][d], pk[1][d]), frag2(vf[0][e], vf[1][e]), z4);
            kv[e][d] = mfma(frag2(pk[2][d], pk[3][d]), frag2(vf[2][e], vf[3][e]), kv[e][d]);
          }
      }
      lds_st128(L0 + B_EX + w * 2048 + lane * 16, __builtin_bit_cast(u32x4, kv[1][0]));
      lds_st128(L0 + B_EX + w * 2048 + 1024 + lane * 16, __builtin_bit_cast(u32x4, kv[1][1]));
      sig(L0 + SYNC + SY_EX + w * 4);
      wait1(L0 + SYNC + SY_EX + (w ^ 1) * 4);
      const f32x4 s0 = kv[0][0] + __builtin_bit_cast(f32x4, lds_ld128(L0 + B_EX + (w ^ 1) * 2048 + lane * 16));
      const f32x4 s1 = kv[0][1] + __builtin_bit_cast(f32x4, lds_ld128(L0 + B_EX + (w ^ 1) * 2048 + 1024 + lane * 16));
      {
        const u32x2 a = pk4(s0), b = pk4(s1);
        publish((u32x4){a[0], a[1], b[0], b[1]}, wsr, lane * 16 + kvs + rt * 1024);
      }
      STAMP(6);
      if (!__all(clr == tagO)) poll_flags(clrp(), tagO);
      wo_run(0, QWO);
      STAMP(7);
      f32x4 sum0, sum1;
      {
        const unsigned e0 = q == g ? ((p & 1) ? 0x3f800000u : 0x3f80u) : 0u;
        const unsigned ea = p < 2 ? e0 : 0u, eb = p < 2 ? 0u : e0;
        const bf16x8 sel0 = as_frag((u32x4){ea, eb, 0u, 0u}), sel1 = as_frag((u32x4){0u, 0u, ea, eb});
        unsigned vo = kvs + lane * 16;
#pragma nounroll
        for (unsigned spins = 0; spins < SPIN_CAP; ++spins) {
          asm volatile("" : "+v"(vo)::"memory");
          u32x4 pr[16];
#pragma unroll
          for (int r = 0; r < 16; ++r) pr[r] = __builtin_amdgcn_raw_buffer_load_b128(wsr, vo, ((r + 2 * rt) & 15) * 1024, 16);
          SB();
          sum0 = sum1 = z4;
#pragma unroll
          for (int r = 0; r < 16; ++r) {
            sum0 = mfma(sel0, as_frag(pr[r]), sum0);
            sum1 = mfma(sel1, as_frag(pr[r]), sum1);
          }
          if (!__any(sum0[0] != sum0[0])) break;
          unsigned m = 0;
#pragma unroll
          for (int r = 0; r < 16; ++r) m = max(max(max(max(m, pr[r][0]), pr[r][1]), pr[r][2]), pr[r][3]);
          if (!__any(m == SENT)) break;
        }
      }
      asm volatile("" ::"v"(pw));
      STAMP(8);
      lds_st128(L0 + B_KVL + w * 1024 + lane * 16, __builtin_bit_cast(u32x4, frag2(pk4(sum0), pk4(sum1))));
      sig(L0 + SYNC + SY_KV + w * 4);
      STAMP(10); STAMPC(19);
      __builtin_amdgcn_s_setprio(0);
      if (cg >= 7) { STAMP_FLUSH(); return; }
      sig(L0 + SYNC + SY_WOA + w * 4);
      wait4(L0 + SYNC + SY_D6);
      wait4(L0 + SYNC + SY_QD);
      wo_run(QWO, 28);
  } else {
      asm volatile("; role A");
      setup(A_G);
      f32x4 aq[4][2], ak[5][2], av[5][2];
#pragma unroll
      for (int t = 0; t < 5; ++t)
#pragma unroll
        for (int f = 0; f < 2; ++f) { ak[t][f] = av[t][f] = z4; if (t < 4) aq[t][f] = z4; }
      const unsigned gl = L0 + A_G + (8 * g + q) * 8;
      const bool p3 = p == 3;
      const unsigned d1 = p3 ? 32u : 112u;
      bf16x8 xf[2][5], fq[2][2], fk[2][2], fv[2][2];
      unsigned xb, wb;
      auto rd = [&](int c, int ks, int part) {
        const unsigned xa = xb ^ (ks * 64), wk = wb + ks * 3584;
        if (part == 0) {
          fk[ks][0] = lds_tr2(wk + WM_B, wk + WM_B + 112);
          xf[ks][0] = as_frag(lds_ld128(xa));
          xf[ks][1] = as_frag(lds_ld128(xa + 2048));
        } else if (part == 1) {
          fv[ks][0] = lds_tr2(wk + 2 * WM_B, wk + 2 * WM_B + 112);
          xf[ks][2] = as_frag(lds_ld128(xa + 4096));
        } else if (part == 2) {
          fq[ks][0] = lds_tr2(wk, wk + 112);
          xf[ks][3] = as_frag(lds_ld128(xa + 6144));
        } else if (part == 3) {
          fk[ks][1] = lds_tr2(wk + WM_B + 32, wk + WM_B + 32 + 112);
          xf[ks][4] = as_frag(lds_ld128(xa + 8192));
        } else if (part == 4) {
          fv[ks][1] = lds_tr2(wk + 2 * WM_B + 32, wk + 2 * WM_B + 32 + 112);
        } else {
          const unsigned a = p3 ? gl + c * 512 + ks * 256 : wk + 32;
          fq[ks][1] = lds_tr2(a, a + d1);
        }
      };
      auto mm = [&](int ks, int part) {
        const int f = part / 3, m = part % 3;
#pragma unroll
        for (int t = 0; t < 5; ++t) {
          if (m == 0) ak[t][f] = mfma(fk[ks][f], xf[ks][t], ak[t][f]);
          if (m == 1) av[t][f] = mfma(xf[ks][t], fv[ks][f], av[t][f]);
          if (m == 2 && t > 0) aq[t - 1][f] = mfma(fq[ks][f], xf[ks][t], aq[t - 1][f]);
        }
      };
#pragma unroll
      for (int c = 0; c < 7; ++c) {
        xb = opaque(L0 + a_slot(c) + rh * 8192 + xsw);
        wb = opaque(L0 + a_slot(c) + A_W + wln);
        hook(c, true, false);
        LDS_BAR();
        hook(c, false, false);
        if (c == 0) { STAMP(2); STAMPC(16); }
        if (c == 3) STAMP(3);
        if (c == 6) { STAMP(4); STAMPC(17); }
        SB();
#pragma unroll
        for (int ks = 0; ks < 2; ++ks) {
#pragma unroll
          for (int part = 0; part < 6; ++part) rd(c, ks, part);
          if (ks == 1 || c > 0) {
#pragma unroll
            for (int part = 0; part < 6; ++part) mm(ks ^ 1, part);
            interleave<28, 17>();
          }
          SB();
        }
      }
      sig(L0 + SYNC + SY_D6 + w * 4);
      mm(1, 0); mm(1, 3); mm(1, 2); mm(1, 5);
      SB();
      mm(1, 1); mm(1, 4);
      STAMP(5);
      const bool odd = li & 1;
      const int kk0 = 4 * g + (li & 1);
      const bool own0 = kk0 <= li, own1 = kk0 + 2 <= li;
      const float sc = 0.18898223650461362f * kLog2e;
      const float ninf = (rt == 0 && rh == 0) ? -__builtin_inff() : 0.f;
      const unsigned l16 = lane_id() * 16;
      bf16x8 kf[5];
#pragma unroll
      for (int t = 0; t < 5; ++t) kf[t] = frag2(pk4(ak[t][0]), pk4(ak[t][1]));
      f32x4 sp[4], so[4];
#pragma unroll
      for (int t = 0; t < 4; ++t) {
        const u32x2 b = pk4(aq[t][1]);
        const bf16x8 qf = frag2(pk4(aq[t][0]), (u32x2){g3 ? 0u : b[0], g3 ? 0u : b[1]});
        sp[t] = mfma(kf[t], qf, t == 0 ? (f32x4){ninf, ninf, ninf, ninf} : z4);
        so[t] = mfma(kf[t + 1], qf, z4);
      }
      float ug;
      {
        float z0 = aq[0][1][0], z1 = aq[1][1][0], z2 = aq[2][1][0], z3 = aq[3][1][0];
        swap32(z0, z1);
        swap32(z2, z3);
        swap16(z1, z3);
        ug = 1.f + __builtin_amdgcn_exp2f(-kLog2e * z3);
      }
      u32x2 vf[5][2];
#pragma unroll
      for (int t = 0; t < 5; ++t) {
        vf[t][0] = pk4(av[t][0]);
        vf[t][1] = pk4(av[t][1]);
      }
      float l0[4], l1[4], bc[4];
#pragma unroll
      for (int t = 0; t < 4; ++t) {
        float a0 = so[t][0], a2 = so[t][2], b0 = sp[t][0], b2 = sp[t][2];
        const float c0 = own0 ? a0 : b0, c1 = own0 ? so[t][1] : sp[t][1], c2 = own1 ? a2 : b2, c3 = own1 ? so[t][3] : sp[t][3];
        l0[t] = odd ? c1 : c0;
        l1[t] = odd ? c3 : c2;
      }
      rows_bcast4(rows_reduce4<true>(fmax1(l0[0], l1[0]), fmax1(l0[1], l1[1]), fmax1(l0[2], l1[2]), fmax1(l0[3], l1[3])) * -sc, bc);
#pragma unroll
      for (int t = 0; t < 4; ++t) {
        l0[t] = __builtin_amdgcn_exp2f(__builtin_fmaf(l0[t], sc, bc[t]));
        l1[t] = __builtin_amdgcn_exp2f(__builtin_fmaf(l1[t], sc, bc[t]));
      }
      rows_bcast4(__builtin_amdgcn_rcpf(rows_reduce4<false>(l0[0] + l1[0], l0[1] + l1[1], l0[2] + l1[2], l0[3] + l1[3]) * ug), bc);
      const unsigned sh = odd ? 16u : 0u;
      const unsigned mk0 = own0 ? 0u : 0xffffu, mk1 = own1 ? 0u : 0xffffu;
#pragma unroll
      for (int t = 0; t < 4; ++t) {
        const unsigned pe = pk2(l0[t] * bc[t], l1[t] * bc[t]), ph = pe >> 16;
        const bf16x8 pf = as_frag((u32x4){(pe & mk0) << sh, (ph & mk1) << sh, (pe & (mk0 ^ 0xffffu)) << sh, (ph & (mk1 ^ 0xffffu)) << sh});
        const f32x4 r0 = mfma(frag2(vf[t][0], vf[t + 1][0]), pf, z4);
        const f32x4 r1 = mfma(frag2(vf[t][1], vf[t + 1][1]), pf, z4);
        const u32x2 c0 = pk4(r0), c1 = pk4(r1);
        __builtin_amdgcn_raw_buffer_store_b128((u32x4){c0[0], c0[1], g3 ? 0u : c1[0], g3 ? 0u : c1[1]}, wsr, l16 + obs + t * 32768, 0, 16);
      }
      STAMP(9);
      VM_WAIT(0);
      asm volatile("" ::"v"(pw));
      if (l16 == 0) __hip_atomic_store((gu64*)(ws + WS_FA) + ((rt * 2 + rh) * 16 + hp * 2 + hh) * FS, tagO, RLX_AGENT);
      STAMP(10); STAMPC(19);
      if (cg >= 7) {
        if (bid == 56 && w == 0) {
#pragma nounroll
          for (int k = 0; k < 4; ++k) {
            const int i = (k & 1) * 64 + lane;
            poll_flags(k < 2 ? (gu64*)(ws + WS_CLR) + i : (gu64*)(ws + WS_FA) + ((i >> 3) * 32 + (i & 7) * 2) * FS, tagO);
          }
          if (lane == 0) __hip_atomic_store((gu64*)(ws + WS_E), tagO, RLX_AGENT);
        }
        STAMP_FLUSH();
        return;
      }
  }

  const int pb = w >> 2;
  wait4(L0 + SYNC + (pb ? SY_WOB : SY_WOA));
  const unsigned sw = (2 * g + (q >> 1)) & 3;
  const unsigned rb = L0 + (pb * 448 + 4 * g + q) * 128 + 8 * p;
  unsigned wa[4], wa2[4];
#pragma unroll
  for (int v = 0; v < 4; ++v) {
    wa[v] = opaque(rb + ((v ^ sw) << 5));
    wa2[v] = opaque(wa[v] + (g3 ? 0u : 2048u));
  }
  auto wfrag = [&](int h, int ct) {
    const int v = ct ^ (2 * (h & 1));
    return lds_tr2(wa[v] + h * 3584, wa2[v] + h * 3584);
  };
  constexpr int NPRE = 8;
  bf16x8 wf[16][4];
#pragma unroll
  for (int h = 0; h < NPRE; ++h)
#pragma unroll
    for (int ct = 0; ct < 4; ++ct) wf[h][ct] = wfrag(h, ct);
  STAMP(11);
  const unsigned ub = WS_O + ((rg * 4 + wl) * 32 + pb * 16) * 1024;
  tagO = uniform64(*(LAS u64*)(uintptr_t)(L0 + SYNC + SY_TAG));
  {
    const gu64* fp = pb ? (gu64*)(ws + WS_FB) + ((rg * 2 + (wl >> 1)) * 8 + (li & 7)) * FS : (gu64*)(ws + WS_FA) + (rg * 16 + li) * FS;
    const u64 f0 = __hip_atomic_load(fp, RLX_AGENT);
    if (br && !pb) { VM_WAIT(0); sig(L0 + SYNC + SY_WOB + wl * 4); }
    if (!__all(f0 == tagO)) poll_flags(fp, tagO);
  }
  STAMP(12);
  u32x4 of[16];
  {
    unsigned vo = ub + lane * 16;
    asm volatile("" : "+v"(vo)::"memory");
#pragma unroll
    for (int h = 0; h < 16; ++h) of[h] = __builtin_amdgcn_raw_buffer_load_b128(wsr, vo, ((h + 2 * cg) & 15) * 1024, 16);
    SB();
  }
  f32x4 acc[4] = {z4, z4, z4, z4};
#pragma unroll
  for (int h = 0; h < 16; ++h) {
#pragma unroll
    for (int ct = 0; ct < 4; ++ct) acc[ct] = mfma(wf[h][ct], as_frag(of[h]), acc[ct]);
    SB();
    if (h + NPRE < 16) {
#pragma unroll
      for (int ct = 0; ct < 4; ++ct) wf[h + NPRE][ct] = wfrag(h + NPRE, ct);
    }
    SB();
  }
  STAMP(13);
  const unsigned xa = L0 + P2X + wl * 4096 + lane * 16;
  if (!pb) {
    wait4(L0 + SYNC + SY_D6);
#pragma unroll
    for (int ct = 0; ct < 4; ++ct) lds_st128(xa + ct * 1024, __builtin_bit_cast(u32x4, acc[ct]));
    sig(L0 + SYNC + SY_P2 + wl * 4);
  } else {
    f32x4 pa[4];
    for (;;) {
      const unsigned f = *(volatile LAS unsigned*)(uintptr_t)(L0 + SYNC + SY_P2 + wl * 4);
#pragma unroll
      for (int ct = 0; ct < 4; ++ct) pa[ct] = *(volatile LAS f32x4*)(uintptr_t)(xa + ct * 1024);
      if (__builtin_amdgcn_readfirstlane(f)) break;
      __builtin_amdgcn_s_sleep(1);
    }
    char* orow = out + (rg * 64 + wl * 16 + li) * XROW + cg * 128 + (g & 1) * 32 + (g >> 1) * 16;
#pragma unroll
    for (int k = 0; k < 2; ++k) {
      const u32x2 a = pk4(acc[2 * k] + pa[2 * k]), b = pk4(acc[2 * k + 1] + pa[2 * k + 1]);
      const auto r0 = __builtin_amdgcn_permlane16_swap(a[0], b[0], false, false);
      const auto r1 = __builtin_amdgcn_permlane16_swap(a[1], b[1], false, false);
      __builtin_amdgcn_raw_buffer_store_b128((u32x4){r0[0], r1[0], r0[1], r1[1]}, __builtin_amdgcn_make_buffer_rsrc(out, 0, kS * XROW, 0x00020000), (unsigned)(orow - out) + k * 64, 0, 17);
    }
  }
  STAMP(14);
  STAMP(15);
  STAMP_FLUSH();
}

}

extern "C" void kernel_launch(void* const* d_in, const int* in_sizes, int n_in, void* d_out, int out_size, void* d_ws,
                              size_t ws_size, hipStream_t stream) {
  (void)in_sizes; (void)n_in; (void)out_size; (void)ws_size;
  gated_mix_kernel<<<256, 512, 0, stream>>>((const char*)d_in[0], (const char*)d_in[1], (const char*)d_in[2],
                                            (const char*)d_in[4], (const char*)d_in[5],
                                            (const unsigned short*)d_in[6], (char*)d_out, (char*)d_ws);
}
```

```cpp
#include <hip/hip_runtime.h>
#include <stdint.h>

typedef __bf16 bf16x2 __attribute__((ext_vector_type(2)));
typedef __bf16 bf16x4 __attribute__((ext_vector_type(4)));
typedef __bf16 bf16x8 __attribute__((ext_vector_type(8)));
typedef float f32x2 __attribute__((ext_vector_type(2)));
typedef float f32x4 __attribute__((ext_vector_type(4)));
typedef unsigned u32x2 __attribute__((ext_vector_type(2)));
typedef unsigned u32x4 __attribute__((ext_vector_type(4)));
typedef unsigned long long u64;
#define LAS __attribute__((address_space(3)))
#define GAS __attribute__((address_space(1)))
typedef GAS u64 gu64;
#define RLX_AGENT __ATOMIC_RELAXED, __HIP_MEMORY_SCOPE_AGENT

namespace {

constexpr int kS = 2048, kD = 448;
constexpr int XROW = kD * 2;
constexpr int WROW = 3 * kD * 2;
constexpr int WM_B = 64 * 112;

constexpr int A_FRONT = 34816, A_SLOT = 144 * 128 + 3 * WM_B, A_W = 144 * 128;
constexpr int A_G = A_FRONT + 3 * A_SLOT;
constexpr int B_SLOT = 128 * 128 + 2 * WM_B, B_W = 128 * 128;
constexpr int B_WQ = 97536, B_G = B_WQ + 7 * WM_B, B_EX = B_G + 3584, B_KVL = B_EX + 8192;
constexpr int P2X = 114688;
constexpr int SYNC = 163584;
constexpr int LDS_B = 163840;
static_assert(A_G + 3584 <= SYNC && B_KVL + 4096 <= SYNC && A_FRONT + 2 * A_SLOT == 114688, "lds map");
__host__ __device__ constexpr int a_slot(int c) { return A_FRONT + ((c + 2) % 3) * A_SLOT; }
__host__ __device__ constexpr int b_slot(int c) { return ((c + 2) % 3) * B_SLOT; }
enum { SY_WOA = 0, SY_WOB = 16, SY_D6 = 32, SY_D6Q = 48, SY_WQ = 64, SY_QD = 80, SY_EX = 96, SY_KV = 112, SY_P2 = 128, SY_ZERO = 192, SY_TAG = 240 };

constexpr int WS_E = 0;
constexpr int WS_CLR = 4096;
constexpr int FS = 16;
constexpr int WS_FA = 8192;
constexpr int WS_FB = WS_FA + 16 * 2 * 16 * 8 * FS;
constexpr int WS_KVP = WS_FB + 16 * 4 * 8 * 8 * FS;
constexpr int WS_O = WS_KVP + 8 * 4 * 16 * 1024;
constexpr int WS_END = WS_O + 16 * 8 * 32 * 1024;

constexpr int PT_WS = (WS_END / 8) & ~127;
constexpr unsigned SPIN_CAP = 1u << 14;
constexpr unsigned SENT = 0xffffffffu;
#define QWO 14

#define STAMP(i) do {} while (0)
#define STAMPC(i) do {} while (0)
#define STAMP_FLUSH() do {} while (0)

__device__ __forceinline__ unsigned pk2(float a, float b) {
  f32x2 v = {a, b};
  bf16x2 r = __builtin_convertvector(v, bf16x2);
  return __builtin_bit_cast(unsigned, r);
}
__device__ __forceinline__ u32x2 pk4(f32x4 v) { return (u32x2){pk2(v[0], v[1]), pk2(v[2], v[3])}; }
__device__ __forceinline__ bf16x8 as_frag(u32x4 v) { return __builtin_bit_cast(bf16x8, v); }
__device__ __forceinline__ bf16x8 frag2(u32x2 a, u32x2 b) { return as_frag((u32x4){a[0], a[1], b[0], b[1]}); }
__device__ __forceinline__ f32x4 mfma(bf16x8 a, bf16x8 b, f32x4 c) {
  return __builtin_amdgcn_mfma_f32_16x16x32_bf16(a, b, c, 0, 0, 0);
}
__device__ __forceinline__ bf16x8 lds_tr2(unsigned a0, unsigned a1) {
  bf16x4 lo = __builtin_amdgcn_ds_read_tr16_b64_v4bf16((LAS bf16x4*)(uintptr_t)a0);
  bf16x4 hi = __builtin_amdgcn_ds_read_tr16_b64_v4bf16((LAS bf16x4*)(uintptr_t)a1);
  return __builtin_shufflevector(lo, hi, 0, 1, 2, 3, 4, 5, 6, 7);
}
__device__ __forceinline__ u32x4 lds_ld128(unsigned a) { return *(LAS u32x4*)(uintptr_t)a; }
__device__ __forceinline__ void lds_st128(unsigned a, u32x4 v) { *(LAS u32x4*)(uintptr_t)a = v; }
__device__ __forceinline__ void lds_st64(unsigned a, u32x2 v) { *(LAS u32x2*)(uintptr_t)a = v; }
__device__ __forceinline__ unsigned opaque(unsigned v) { asm volatile("" : "+v"(v)); return v; }

__device__ __forceinline__ void sig(unsigned a) {
  asm volatile("" ::: "memory");
  *(volatile LAS unsigned*)(uintptr_t)a = 1u;
  asm volatile("" ::: "memory");
  __builtin_amdgcn_sched_barrier(0);
}
__device__ __forceinline__ void wait1(unsigned a) {
  while (__builtin_amdgcn_readfirstlane(*(volatile LAS unsigned*)(uintptr_t)a) == 0u) __builtin_amdgcn_s_sleep(1);
  asm volatile("" ::: "memory");
}
__device__ __forceinline__ void wait4(unsigned a) {
  for (;;) {
    const u32x4 v = *(volatile LAS u32x4*)(uintptr_t)a;
    if (__builtin_amdgcn_readfirstlane(v[0] & v[1] & v[2] & v[3])) break;
    __builtin_amdgcn_s_sleep(1);
  }
  asm volatile("" ::: "memory");
}

__device__ __forceinline__ u32x4 make_srd(const void* p, unsigned bytes) {
  uint64_t a = (uint64_t)p;
  u32x4 r;
  r[0] = __builtin_amdgcn_readfirstlane((unsigned)a);
  r[1] = __builtin_amdgcn_readfirstlane((unsigned)(a >> 32) & 0xffffu);
  r[2] = bytes;
  r[3] = 0x00020000u;
  return r;
}
__device__ __forceinline__ void dma16(u32x4 srd, unsigned voff, unsigned soff, unsigned ldsaddr) {
  asm volatile(
      "s_mov_b32 m0, %3\n\t"
      "s_nop 3\n\t"
      "buffer_load_dwordx4 %0, %1, %2 offen lds"
      :
      : "v"(voff), "s"(srd), "s"(soff), "s"(ldsaddr));
}
#define VM_BAR(N) asm volatile("s_waitcnt vmcnt(" #N ") lgkmcnt(0)\n\ts_barrier" ::: "memory")
#define LDS_BAR() asm volatile("s_waitcnt lgkmcnt(0)\n\ts_barrier" ::: "memory")
#define VM_WAIT(N) asm volatile("s_waitcnt vmcnt(" #N ")" ::: "memory")
template <int N> __device__ __forceinline__ void vm_bar() { asm volatile("s_waitcnt vmcnt(%0) lgkmcnt(0)\n\ts_barrier" ::"n"(N) : "memory"); }
#define SB() __builtin_amdgcn_sched_barrier(0)

__device__ __forceinline__ float fmax1(float a, float b) { return __builtin_amdgcn_fmed3f(a, b, 3.0e38f); }
__device__ __forceinline__ void swap16(float& a, float& b) {
  auto r = __builtin_amdgcn_permlane16_swap(__float_as_uint(a), __float_as_uint(b), false, false);
  const unsigned r0 = r[0], r1 = r[1];
  a = __uint_as_float(r0);
  b = __uint_as_float(r1);
}
__device__ __forceinline__ void swap32(float& a, float& b) {
  auto r = __builtin_amdgcn_permlane32_swap(__float_as_uint(a), __float_as_uint(b), false, false);
  const unsigned r0 = r[0], r1 = r[1];
  a = __uint_as_float(r0);
  b = __uint_as_float(r1);
}
template <bool MAX>
__device__ __forceinline__ float rows_reduce4(float v0, float v1, float v2, float v3) {
  swap32(v0, v1);
  swap32(v2, v3);
  float a = MAX ? fmax1(v0, v1) : v0 + v1, b = MAX ? fmax1(v2, v3) : v2 + v3;
  swap16(a, b);
  return MAX ? fmax1(a, b) : a + b;
}
__device__ __forceinline__ void rows_bcast4(float x, float (&o)[4]) {
  float y = x;
  swap16(x, y);
  o[0] = x; o[1] = x; o[2] = y; o[3] = y;
  swap32(o[0], o[1]);
  swap32(o[2], o[3]);
}
__device__ __forceinline__ float row3_bcast(float v) {
  float a = v;
  swap32(a, v);
  a = v;
  swap16(a, v);
  return v;
}
template <int NM, int ND>
__device__ __forceinline__ void interleave() {
#pragma unroll
  for (int i = 0; i < NM; ++i) {
    __builtin_amdgcn_sched_group_barrier(0x8, 1, 0);
    if (i < ND) {
      __builtin_amdgcn_sched_group_barrier(0x2, 1, 0);
      __builtin_amdgcn_sched_group_barrier(0x100, 1, 0);
    }
  }
}
constexpr float kEps = 1e-6f, kLog2e = 1.44269504088896f;
__device__ __forceinline__ float phi(float u) {
  const float e = __builtin_amdgcn_fmed3f(__builtin_amdgcn_exp2f(__builtin_fmaf(u, kLog2e, -kEps * kLog2e)), 0.f, 1.f);
  return fmax1(u, kEps) + e;
}
__device__ __forceinline__ u32x2 phi4(f32x4 v) { return (u32x2){pk2(phi(v[0]), phi(v[1])), pk2(phi(v[2]), phi(v[3]))}; }
__device__ __forceinline__ float sigmoidf(float z) {
  return __builtin_amdgcn_rcpf(1.f + __builtin_amdgcn_exp2f(-1.44269504088896f * z));
}
__device__ __forceinline__ u64 next_tag(u64 e) {
  const u64 s = ((u64)(unsigned)__builtin_amdgcn_readfirstlane((unsigned)(e >> 32)) << 32) |
                (unsigned)__builtin_amdgcn_readfirstlane((unsigned)e);
  return s * 6364136223846793005ull + 1442695040888963407ull;
}
__device__ __forceinline__ u64 uniform64(u64 e) {
  return ((u64)(unsigned)__builtin_amdgcn_readfirstlane((unsigned)(e >> 32)) << 32) |
         (unsigned)__builtin_amdgcn_readfirstlane((unsigned)e);
}
__device__ __forceinline__ void poll_flags(const gu64* p, u64 tag) {
#pragma nounroll
  for (unsigned spins = 0; spins < SPIN_CAP; ++spins) {
    u64 v = __hip_atomic_load(p, RLX_AGENT);
    if (__all(v == tag)) break;
    __builtin_amdgcn_s_sleep(2);
  }
  asm volatile("" ::: "memory");
}
__device__ __forceinline__ void publish(u32x4 v, __amdgpu_buffer_rsrc_t r, unsigned voff) {
#pragma unroll
  for (int i = 0; i < 4; ++i) v[i] = v[i] < SENT - 1u ? v[i] : SENT - 1u;
  __builtin_amdgcn_raw_buffer_store_b128(v, r, voff, 0, 16);
}
__device__ __forceinline__ unsigned lane_id() {
  unsigned l = __builtin_amdgcn_mbcnt_hi(~0u, __builtin_amdgcn_mbcnt_lo(~0u, 0u));
  asm volatile("" : "+v"(l));
  return l;
}
__device__ __forceinline__ int wrap7(int c) { return c >= 7 ? c - 7 : c; }

__global__ __launch_bounds__(512, 2) void gated_mix_kernel(const char* __restrict__ x, const char* __restrict__ wqkvA,
                                                           const char* __restrict__ woA, const char* __restrict__ wqkvB,
                                                           const char* __restrict__ woB,
                                                           const unsigned short* __restrict__ gate_w, char* __restrict__ out,
                                                           char* ws) {
  __shared__ __attribute__((aligned(1024))) char lds[LDS_B];
  const unsigned L0 = (unsigned)(uintptr_t)(LAS char*)lds;
  const int bid = blockIdx.x;
  const int xcd = bid & 7, idx = bid >> 3;
  const int xj = xcd >> 1, ci = idx >> 3;
  const int br = ci >> 1;
  const int hp = 2 * (br ? (xj ^ 1) : xj) + (ci & 1);
  const int rt = (xcd & 1) * 8 + (idx & 7);
  const int rot = wrap7(wrap7((idx & 7) + 2 * ci + (xj & 1)));
  const int cg = idx & 7, rg = 4 * xcd + ci;

  const int tid = threadIdx.x;
  const int lane = tid & 63;
  const int w = __builtin_amdgcn_readfirstlane(tid >> 6) & 7;
  const int wl = w & 3;
  const int li = lane & 15, g = lane >> 4, q = (lane >> 2) & 3, p = lane & 3;
  const bool g3 = g == 3;
  const f32x4 z4 = {0.f, 0.f, 0.f, 0.f};
  STAMP(0); STAMPC(18);
  unsigned vx0 = (lane >> 3) * XROW + (((lane & 7) ^ (lane >> 4)) << 4);
  unsigned vx1 = vx0 ^ 64u;
  unsigned vw0, vw1;
  {
    const unsigned s0 = wl * 64 + lane, s1 = s0 + 256;
    const unsigned p0 = (s0 * 9363u) >> 16, p1 = (s1 * 9363u) >> 16;
    const unsigned k0 = (p0 & 56) | ((p0 >> 1) & 3) | ((p0 & 1) << 2);
    const unsigned k1 = (p1 & 56) | ((p1 >> 1) & 3) | ((p1 & 1) << 2);
    vw0 = k0 * WROW + hp * 112 + (s0 - p0 * 7) * 16;
    vw1 = k1 * WROW + hp * 112 + (s1 - p1 * 7) * 16;
  }
  asm volatile("" : "+v"(vx0), "+v"(vx1), "+v"(vw0), "+v"(vw1));
  asm volatile("" ::"s"(x), "s"(wqkvA), "s"(woA), "s"(wqkvB), "s"(woB), "s"(gate_w), "s"(out), "s"(ws));
  const __amdgpu_buffer_rsrc_t wsr = __builtin_amdgcn_make_buffer_rsrc(ws, 0, WS_END, 0x00020000);
  u64 tagO;
  auto wo_run = [&](int j0, int j1) {
    const unsigned vwo = (lane >> 3) * XROW + cg * 128 + (((lane & 7) ^ (((lane >> 4) & 3) << 1)) << 4);
    const u32x4 sa = make_srd(woA, kD * XROW), sb = make_srd(woB, kD * XROW);
    unsigned so = (wl + 7 * cg + 4 * (j0 % 14)) * (8 * XROW);
    so = so >= kD * XROW ? so - kD * XROW : so;
#pragma unroll
    for (int j = j0; j < j1; ++j) {
      dma16(j < 14 ? sa : sb, vwo, so, L0 + (wl + 4 * j) * 1024);
      so += 32 * XROW;
      so = so >= kD * XROW ? so - kD * XROW : so;
    }
  };
  const u32x4 srdX = make_srd(x, kS * XROW);
  const u32x4 srdW = make_srd(br ? wqkvB : wqkvA, kD * WROW);
  const bool l3 = wl == 3;
  const int xr = rt * 128 + wl * 32 - (br ? 0 : 16);
  const unsigned sx01 = (xr < 0 ? 0 : xr) * XROW, sx23 = (xr + 16) * XROW;
  const bool l3a = l3 && !br;
  const u32x4 srd2 = l3a ? srdX : srdW;
  const unsigned v2 = l3 ? vw0 : vw1;
  const unsigned v2a = l3a ? vx0 : v2, v2b = l3a ? vx1 : v2;
  const unsigned st2 = l3a ? 128u : 64u * WROW, s2a = l3a ? (rt * 128 + 112) * XROW : 0u;
  const unsigned s2d = l3a ? 8u * XROW : (unsigned)XROW, s2e = l3a ? 8u * XROW : 2u * XROW;
  const unsigned sub2 = (l3 ? wl : wl + 4) * 1024;
  auto chunk = [&](unsigned sl, unsigned lq, unsigned lk, int cc) {
    const unsigned sw = cc * (64 * WROW), sx = cc * 128, s2 = cc * st2 + s2a;
#pragma unroll
    for (int j = 0; j < 4; ++j)
      dma16(srdX, (j & 1) ? vx1 : vx0, (j < 2 ? sx01 : sx23) + (j & 1) * 8 * XROW + sx, sl + (wl * 4 + j) * 1024);
    dma16(srdW, vw0, sw + XROW, lk + wl * 1024);
    dma16(srdW, vw0, sw + 2 * XROW, lk + WM_B + wl * 1024);
    dma16(srd2, v2b, s2 + s2d, l3a ? sl + 17408 : lk + sub2);
    dma16(srd2, v2b, s2 + s2e, l3a ? sl + 17408 : lk + WM_B + sub2);
    if (!br) {
      dma16(srdW, vw0, sw, lq + wl * 1024);
      dma16(srd2, v2a, s2, l3a ? sl + 16384 : lq + sub2);
    }
  };
  unsigned gv = 0, pt = 0;
  u64 epoch = 0;
  {
    int wx = w;
    asm volatile("" : "+s"(wx));
    if (wx < 4) {
      gv = ((const unsigned*)gate_w)[tid < 224 ? tid : 0];
      epoch = __hip_atomic_load((gu64*)(ws + WS_E), RLX_AGENT);
      const char* pb_ = wx == 0 ? ws : (wx == 1 ? out + rg * 64 * XROW : (wx == 2 ? woA : woB));
      pt = __hip_atomic_load((GAS unsigned*)(pb_ + (lane & 7) * (wx == 0 ? PT_WS : (wx == 1 ? 7168 : 50176))), RLX_AGENT);
    } else {
#pragma nounroll
      for (int c = 0; c < 2; ++c) {
        const unsigned sl = L0 + (br ? (c ? b_slot(1) : b_slot(0)) : (c ? a_slot(1) : a_slot(0)));
        chunk(sl, sl + A_W, sl + (br ? B_W : A_W + WM_B), wrap7(rot + c));
      }
    }
  }

    const int rh = w & 1, hh = w >> 1;
    const unsigned obs = WS_O + ((rt * 8 + rh * 4) * 32 + hp * 2 + hh) * 1024;
    const unsigned kvs = WS_KVP + (hp * 4 + w) * 16 * 1024;
    auto setup = [&](int goff) {
      if (tid < 60) *(LAS unsigned*)(uintptr_t)(L0 + SYNC + tid * 4) = 0u;
      const unsigned lo = (gv & 0xffffu) * 0x10001u, hi = (gv >> 16) * 0x10001u;
      const int pos = tid - rot * 32;
      if (tid < 224) lds_st128(L0 + goff + (pos < 0 ? pos + 224 : pos) * 16, (u32x4){lo, lo, hi, hi});
    };
    u64 clr = 0;
    unsigned pw = 0;
    auto clrp = [&]() { return (const gu64*)(ws + WS_CLR) + hp * 16 + (lane_id() & 15); };
    auto hook = [&](int c, bool before, bool isb) {
      if (c == 0 && !before && isb)
        __builtin_amdgcn_raw_buffer_store_b128((u32x4){SENT, SENT, SENT, SENT}, wsr, lane_id() * 16 + kvs + rt * 1024, 0, 16);
      if (c == 3 && before) {
        asm volatile("" : "+v"(epoch) : "v"(pt));
        tagO = next_tag(epoch);
        *(LAS u64*)(uintptr_t)(L0 + SYNC + SY_TAG) = tagO;
        VM_WAIT(0);
      }
      if (c == 3 && !before && isb)
        __builtin_amdgcn_raw_buffer_store_b64((u32x2){(unsigned)tagO, (unsigned)(tagO >> 32)}, wsr,
                                              (lane_id() | w) == 0 ? WS_CLR + (hp * 16 + rt) * 8 : 0x7ffffff0u, 0, 16);
      if (c == 6 && !before && isb) clr = __hip_atomic_load(clrp(), RLX_AGENT);
      if (c == 6 && !before) {
        const unsigned l = lane_id(), u = (l >> 3) & 3;
        const unsigned o = isb ? WS_O + ((rt * 8 + w * 2 + (u >> 1)) * 32 + 16 + hp * 2 + (u & 1)) * 1024 : obs + u * 32768;
        pw = __builtin_amdgcn_raw_buffer_load_b32(wsr, o + (l & 7) * 128, 0, 16);
      }
    };
    const unsigned xsw = li * 128 + ((g ^ (li >> 1)) << 4);
    const unsigned wln = (8 * g + 2 * q) * 112 + 8 * p + 56 * hh;

  if (__builtin_expect(w >= 4, 1)) {
    if (!br) {
      int cc = wrap7(rot + 1);
      unsigned sl = L0 + a_slot(2);
#pragma nounroll
      for (int c = 2; c < 7; ++c) {
        vm_bar<10>();
        cc = wrap7(cc + 1);
        chunk(sl, sl + A_W, sl + A_W + WM_B, cc);
        sl = sl == L0 + a_slot(0) ? L0 + a_slot(1) : sl + A_SLOT;
      }
      vm_bar<10>();
      if (cg >= 7) { vm_bar<0>(); STAMP_FLUSH(); return; }
      wo_run(0, 4);
      vm_bar<4>();
      wo_run(4, 28);
      VM_WAIT(14);
      sig(L0 + SYNC + SY_WOA + wl * 4);
      VM_WAIT(0);
      asm volatile("" ::"v"(gv), "v"(epoch), "v"(pt));
      sig(L0 + SYNC + SY_WOB + wl * 4);
    } else {
      auto instr = [&](int n) {
        if (n < 56) {
          const int c = n >> 3, j = n & 7, cc = wrap7(c + rot);
          const unsigned sl = L0 + b_slot(c), sw = cc * (64 * WROW), sx = cc * 128;
          if (j < 4) dma16(srdX, (j & 1) ? vx1 : vx0, sx01 + j * 8 * XROW + sx, sl + (wl * 4 + j) * 1024);
          else dma16(srdW, j < 6 ? vw0 : v2, sw + ((j & 1) + 1) * XROW, sl + B_W + (j & 1) * WM_B + (j < 6 ? wl * 1024 : sub2));
        } else {
          const int c = (n - 56) >> 1;
          dma16(srdW, (n & 1) ? v2 : vw0, wrap7(c + rot) * (64 * WROW), L0 + B_WQ + c * WM_B + ((n & 1) ? sub2 : wl * 1024));
        }
      };
      STAMP(1);
      bf16x8 xq[14][2];
      const unsigned xl = L0 + (wl * 32 + li) * 128 + ((g ^ (li >> 1)) << 4);
#pragma unroll
      for (int c = 0; c < 7; ++c) {
        if (c < 6) vm_bar<8>(); else vm_bar<8>();
#pragma unroll
        for (int ks = 0; ks < 2; ++ks) {
          const unsigned xa = (xl ^ (ks * 64)) + b_slot(c);
          xq[c * 2 + ks][0] = as_frag(lds_ld128(xa));
          xq[c * 2 + ks][1] = as_frag(lds_ld128(xa + 2048));
        }
#pragma unroll
        for (int n = (c < 6 ? 8 * c + 16 : 56 + 8); n < (c < 5 ? 8 * c + 24 : (c == 5 ? 56 + 8 : 70)); ++n) instr(n);
      }
      STAMP(4);
      VM_WAIT(0);
      asm volatile("" ::"v"(gv), "v"(epoch), "v"(pt));
      sig(L0 + SYNC + SY_WQ + wl * 4);
      wait4(L0 + SYNC + SY_WQ);
      wait4(L0 + SYNC + SY_EX);
      STAMP(3);
      f32x4 aq[2][2][2];
#pragma unroll
      for (int i = 0; i < 8; ++i) aq[i >> 2][(i >> 1) & 1][i & 1] = (f32x4){kEps, kEps, kEps, kEps};
      const unsigned wb = L0 + B_WQ + (8 * g + 2 * q) * 112 + 8 * p;
      const unsigned gl = L0 + B_G + (8 * g + q) * 8;
      const bool p3 = p == 3;
      const unsigned d1 = p3 ? 32u : 112u;
      const unsigned qa0 = opaque(p3 ? gl : wb + 32), qstep = opaque(p3 ? 256u : 3584u);
      bf16x8 fq[3][4];
      auto rdq = [&](int s) {
        const unsigned wk = wb + s * 3584;
        const unsigned a = qa0 + s * qstep;
        fq[s % 3][0] = lds_tr2(wk, wk + 112);
        fq[s % 3][1] = lds_tr2(a, a + d1);
        fq[s % 3][2] = lds_tr2(wk + 56, wk + 56 + 112);
        fq[s % 3][3] = lds_tr2(wk + 88, wk + 88 + 112);
      };
      rdq(0);
      rdq(1);
#pragma unroll
      for (int s = 0; s < 14; ++s) {
        SB();
        if (s + 2 < 14) rdq(s + 2);
#pragma unroll
        for (int i = 0; i < 4; ++i) {
          aq[0][i >> 1][i & 1] = mfma(fq[s % 3][i], xq[s][0], aq[0][i >> 1][i & 1]);
          aq[1][i >> 1][i & 1] = mfma(fq[s % 3][i], xq[s][1], aq[1][i >> 1][i & 1]);
        }
        interleave<8, 8>();
      }
      sig(L0 + SYNC + SY_QD + wl * 4);
      STAMP(5);
      u32x4 pqf[2][2];
      float omg[2];
#pragma unroll
      for (int t = 0; t < 2; ++t) {
        omg[t] = 1.f - sigmoidf(row3_bcast(aq[t][0][1][0]));
#pragma unroll
        for (int h = 0; h < 2; ++h) {
          const u32x2 a = phi4(aq[t][h][0]), b = phi4(aq[t][h][1]);
          pqf[t][h] = (u32x4){a[0], a[1], g3 ? 0u : b[0], g3 ? 0u : b[1]};
        }
      }
      tagO = uniform64(*(LAS u64*)(uintptr_t)(L0 + SYNC + SY_TAG));
      unsigned ob = WS_O + ((rt * 8 + wl * 2) * 32 + 16 + hp * 2) * 1024 + lane * 16;
      unsigned kb = L0 + B_KVL + lane * 16, zb = L0 + B_KVL + 1024 + (g * 16 + 12) * 16;
      asm volatile("" : "+v"(pqf[0][0]), "+v"(pqf[0][1]), "+v"(pqf[1][0]), "+v"(pqf[1][1]), "+v"(omg[0]), "+v"(omg[1]), "+v"(ob),
                   "+v"(kb), "+v"(zb), "+s"(tagO));
      __builtin_amdgcn_s_setprio(2);
      STAMP(6);
      wait4(L0 + SYNC + SY_KV);
      STAMP(8);
      {
        bf16x8 kv0[2], kv1[2], zf[2];
#pragma unroll
        for (int h = 0; h < 2; ++h) zf[h] = as_frag(lds_ld128(zb + h * 2048));
#pragma unroll
        for (int h = 0; h < 2; ++h) {
          kv0[h] = as_frag(lds_ld128(kb + h * 2048));
          kv1[h] = as_frag(lds_ld128(kb + h * 2048 + 1024));
        }
        SB();
        f32x4 dn[4], n0[4], n1[4];
#pragma unroll
        for (int i = 0; i < 4; ++i) dn[i] = mfma(zf[i >> 1], as_frag(pqf[i & 1][i >> 1]), (f32x4){kEps, kEps, kEps, kEps});
#pragma unroll
        for (int i = 0; i < 4; ++i) {
          n0[i] = mfma(kv0[i >> 1], as_frag(pqf[i & 1][i >> 1]), z4);
          n1[i] = mfma(kv1[i >> 1], as_frag(pqf[i & 1][i >> 1]), z4);
        }
        SB();
#pragma unroll
        for (int i = 0; i < 4; ++i) {
          const float s = omg[i & 1] * __builtin_amdgcn_rcpf(dn[i][0]);
          const u32x2 a = pk4(n0[i] * s), b = pk4(n1[i] * s);
          __builtin_amdgcn_raw_buffer_store_b128((u32x4){a[0], a[1], g3 ? 0u : b[0], b[1]}, wsr, ob + ((i & 1) * 32 + (i >> 1)) * 1024, 0, 16);
        }
      }
      STAMP(9);
      VM_WAIT(0);
      if (lane == 0) __hip_atomic_store((gu64*)(ws + WS_FB) + ((rt * 4 + wl) * 8 + hp) * FS, tagO, RLX_AGENT);
      STAMP(10); STAMPC(19);
      if (cg >= 7) { STAMP_FLUSH(); return; }
    }
  } else if (__builtin_expect(br, 1)) {
      __builtin_amdgcn_s_setprio(2);
      setup(B_G);
      f32x4 ak[4][2], av[4][2];
      {
        const float one = li == 12 ? 1.f : 0.f, i0 = rh ? one : 0.f, i1 = rh ? 0.f : one;
#pragma unroll
        for (int t = 0; t < 4; ++t) {
          ak[t][0] = ak[t][1] = (f32x4){kEps, kEps, kEps, kEps};
          av[t][0] = (f32x4){i0, i0, i0, i0};
          av[t][1] = (f32x4){i1, i1, i1, i1};
        }
      }
      bf16x8 xf[2][4], fw[2][4];
      unsigned xb, wb;
      const bool z0 = p == 3 && rh, z1 = p == 3 && !rh;
      const unsigned zaddr = L0 + SYNC + SY_ZERO;
      auto rd = [&](int ks, int part) {
        const unsigned xa = xb ^ (ks * 64), wk = wb + ks * 3584;
        if (part & 1) {
          const unsigned a = wk + WM_B + (((part >> 1) ^ rh) << 5);
          const bool z = (part >> 1) ? z1 : z0;
          fw[ks][part] = lds_tr2(z ? zaddr : a, z ? zaddr : a + 112);
        } else {
          fw[ks][part] = lds_tr2(wk + (part >> 1) * 32, wk + (part >> 1) * 32 + 112);
        }
        if (part < 2) {
          xf[ks][part * 2] = as_frag(lds_ld128(xa + part * 4096));
          xf[ks][part * 2 + 1] = as_frag(lds_ld128(xa + part * 4096 + 2048));
        }
      };
      auto mm = [&](int ks, int part) {
#pragma unroll
        for (int t = 0; t < 4; ++t) {
          if (part & 1) av[t][part >> 1] = mfma(xf[ks][t], fw[ks][part], av[t][part >> 1]);
          else ak[t][part >> 1] = mfma(xf[ks][t], fw[ks][part], ak[t][part >> 1]);
        }
      };
#pragma unroll
      for (int c = 0; c < 7; ++c) {
        xb = opaque(L0 + b_slot(c) + rh * 8192 + xsw);
        wb = opaque(L0 + b_slot(c) + B_W + wln);
        hook(c, true, true);
        LDS_BAR();
        hook(c, false, true);
        if (c == 0) { STAMP(2); STAMPC(16); }
        if (c == 3) STAMP(3);
        if (c == 6) { STAMP(4); STAMPC(17); }
        SB();
#pragma unroll
        for (int ks = 0; ks < 2; ++ks) {
#pragma unroll
          for (int part = 0; part < 4; ++part) rd(ks, part);
          if (ks == 1 || c > 0) {
#pragma unroll
            for (int part = 0; part < 4; ++part) mm(ks ^ 1, part);
            interleave<16, 12>();
          }
          SB();
        }
      }
      sig(L0 + SYNC + SY_D6 + w * 4);
      mm(1, 0); mm(1, 2);
      SB();
      mm(1, 3); mm(1, 1);
      STAMP(5);
      f32x4 kv[2][2];
      {
        u32x2 pk[4][2], vf[4][2];
#pragma unroll
        for (int t = 0; t < 4; ++t) {
          pk[t][0] = phi4(ak[t][0]);
          pk[t][1] = phi4(ak[t][1]);
          vf[t][0] = pk4(av[t][0]);
          vf[t][1] = pk4(av[t][1]);
        }
#pragma unroll
        for (int e = 1; e >= 0; --e)
#pragma unroll
          for (int d = 0; d < 2; ++d) {
            kv[e][d] = mfma(frag2(pk[0][d], pk[1][d]), frag2(vf[0][e], vf[1][e]), z4);
            kv[e][d] = mfma(frag2(pk[2][d], pk[3][d]), frag2(vf[2][e], vf[3][e]), kv[e][d]);
          }
      }
      lds_st128(L0 + B_EX + w * 2048 + lane * 16, __builtin_bit_cast(u32x4, kv[1][0]));
      lds_st128(L0 + B_EX + w * 2048 + 1024 + lane * 16, __builtin_bit_cast(u32x4, kv[1][1]));
      sig(L0 + SYNC + SY_EX + w * 4);
      wait1(L0 + SYNC + SY_EX + (w ^ 1) * 4);
      const f32x4 s0 = kv[0][0] + __builtin_bit_cast(f32x4, lds_ld128(L0 + B_EX + (w ^ 1) * 2048 + lane * 16));
      const f32x4 s1 = kv[0][1] + __builtin_bit_cast(f32x4, lds_ld128(L0 + B_EX + (w ^ 1) * 2048 + 1024 + lane * 16));
      {
        const u32x2 a = pk4(s0), b = pk4(s1);
        publish((u32x4){a[0], a[1], b[0], b[1]}, wsr, lane * 16 + kvs + rt * 1024);
      }
      STAMP(6);
      if (!__all(clr == tagO)) poll_flags(clrp(), tagO);
      wo_run(0, QWO);
      STAMP(7);
      f32x4 sum0, sum1;
      {
        const unsigned e0 = q == g ? ((p & 1) ? 0x3f800000u : 0x3f80u) : 0u;
        const unsigned ea = p < 2 ? e0 : 0u, eb = p < 2 ? 0u : e0;
        const bf16x8 sel0 = as_frag((u32x4){ea, eb, 0u, 0u}), sel1 = as_frag((u32x4){0u, 0u, ea, eb});
        unsigned vo = kvs + lane * 16;
#pragma nounroll
        for (unsigned spins = 0; spins < SPIN_CAP; ++spins) {
          asm volatile("" : "+v"(vo)::"memory");
          u32x4 pr[16];
#pragma unroll
          for (int r = 0; r < 16; ++r) pr[r] = __builtin_amdgcn_raw_buffer_load_b128(wsr, vo, ((r + 2 * rt) & 15) * 1024, 16);
          SB();
          sum0 = sum1 = z4;
#pragma unroll
          for (int r = 0; r < 16; ++r) {
            sum0 = mfma(sel0, as_frag(pr[r]), sum0);
            sum1 = mfma(sel1, as_frag(pr[r]), sum1);
          }
          if (!__any(sum0[0] != sum0[0])) break;
          unsigned m = 0;
#pragma unroll
          for (int r = 0; r < 16; ++r) m = max(max(max(max(m, pr[r][0]), pr[r][1]), pr[r][2]), pr[r][3]);
          if (!__any(m == SENT)) break;
        }
      }
      asm volatile("" ::"v"(pw));
      STAMP(8);
      lds_st128(L0 + B_KVL + w * 1024 + lane * 16, __builtin_bit_cast(u32x4, frag2(pk4(sum0), pk4(sum1))));
      sig(L0 + SYNC + SY_KV + w * 4);
      STAMP(10); STAMPC(19);
      __builtin_amdgcn_s_setprio(0);
      if (cg >= 7) { STAMP_FLUSH(); return; }
      sig(L0 + SYNC + SY_WOA + w * 4);
      wait4(L0 + SYNC + SY_D6);
      wait4(L0 + SYNC + SY_QD);
      wo_run(QWO, 28);
  } else {
      asm volatile("; role A");
      setup(A_G);
      f32x4 aq[4][2], ak[5][2], av[5][2];
#pragma unroll
      for (int t = 0; t < 5; ++t)
#pragma unroll
        for (int f = 0; f < 2; ++f) { ak[t][f] = av[t][f] = z4; if (t < 4) aq[t][f] = z4; }
      const unsigned gl = L0 + A_G + (8 * g + q) * 8;
      const bool p3 = p == 3;
      const unsigned d1 = p3 ? 32u : 112u;
      bf16x8 xf[2][5], fq[2][2], fk[2][2], fv[2][2];
      unsigned xb, wb;
      auto rd = [&](int c, int ks, int part) {
        const unsigned xa = xb ^ (ks * 64), wk = wb + ks * 3584;
        if (part == 0) {
          fk[ks][0] = lds_tr2(wk + WM_B, wk + WM_B + 112);
          xf[ks][0] = as_frag(lds_ld128(xa));
          xf[ks][1] = as_frag(lds_ld128(xa + 2048));
        } else if (part == 1) {
          fv[ks][0] = lds_tr2(wk + 2 * WM_B, wk + 2 * WM_B + 112);
          xf[ks][2] = as_frag(lds_ld128(xa + 4096));
        } else if (part == 2) {
          fq[ks][0] = lds_tr2(wk, wk + 112);
          xf[ks][3] = as_frag(lds_ld128(xa + 6144));
        } else if (part == 3) {
          fk[ks][1] = lds_tr2(wk + WM_B + 32, wk + WM_B + 32 + 112);
          xf[ks][4] = as_frag(lds_ld128(xa + 8192));
        } else if (part == 4) {
          fv[ks][1] = lds_tr2(wk + 2 * WM_B + 32, wk + 2 * WM_B + 32 + 112);
        } else {
          const unsigned a = p3 ? gl + c * 512 + ks * 256 : wk + 32;
          fq[ks][1] = lds_tr2(a, a + d1);
        }
      };
      auto mm = [&](int ks, int part) {
        const int f = part / 3, m = part % 3;
#pragma unroll
        for (int t = 0; t < 5; ++t) {
          if (m == 0) ak[t][f] = mfma(fk[ks][f], xf[ks][t], ak[t][f]);
          if (m == 1) av[t][f] = mfma(xf[ks][t], fv[ks][f], av[t][f]);
          if (m == 2 && t > 0) aq[t - 1][f] = mfma(fq[ks][f], xf[ks][t], aq[t - 1][f]);
        }
      };
#pragma unroll
      for (int c = 0; c < 7; ++c) {
        xb = opaque(L0 + a_slot(c) + rh * 8192 + xsw);
        wb = opaque(L0 + a_slot(c) + A_W + wln);
        hook(c, true, false);
        LDS_BAR();
        hook(c, false, false);
        if (c == 0) { STAMP(2); STAMPC(16); }
        if (c == 3) STAMP(3);
        if (c == 6) { STAMP(4); STAMPC(17); }
        SB();
#pragma unroll
        for (int ks = 0; ks < 2; ++ks) {
#pragma unroll
          for (int part = 0; part < 6; ++part) rd(c, ks, part);
          if (ks == 1 || c > 0) {
#pragma unroll
            for (int part = 0; part < 6; ++part) mm(ks ^ 1, part);
            interleave<28, 17>();
          }
          SB();
        }
      }
      sig(L0 + SYNC + SY_D6 + w * 4);
      mm(1, 0); mm(1, 3); mm(1, 2); mm(1, 5);
      SB();
      mm(1, 1); mm(1, 4);
      STAMP(5);
      const bool odd = li & 1;
      const int kk0 = 4 * g + (li & 1);
      const bool own0 = kk0 <= li, own1 = kk0 + 2 <= li;
      const float sc = 0.18898223650461362f * kLog2e;
      const float ninf = (rt == 0 && rh == 0) ? -__builtin_inff() : 0.f;
      const unsigned l16 = lane_id() * 16;
      bf16x8 kf[5];
#pragma unroll
      for (int t = 0; t < 5; ++t) kf[t] = frag2(pk4(ak[t][0]), pk4(ak[t][1]));
      f32x4 sp[4], so[4];
#pragma unroll
      for (int t = 0; t < 4; ++t) {
        const u32x2 b = pk4(aq[t][1]);
        const bf16x8 qf = frag2(pk4(aq[t][0]), (u32x2){g3 ? 0u : b[0], g3 ? 0u : b[1]});
        sp[t] = mfma(kf[t], qf, t == 0 ? (f32x4){ninf, ninf, ninf, ninf} : z4);
        so[t] = mfma(kf[t + 1], qf, z4);
      }
      float ug;
      {
        float z0 = aq[0][1][0], z1 = aq[1][1][0], z2 = aq[2][1][0], z3 = aq[3][1][0];
        swap32(z0, z1);
        swap32(z2, z3);
        swap16(z1, z3);
        ug = 1.f + __builtin_amdgcn_exp2f(-kLog2e * z3);
      }
      u32x2 vf[5][2];
#pragma unroll
      for (int t = 0; t < 5; ++t) {
        vf[t][0] = pk4(av[t][0]);
        vf[t][1] = pk4(av[t][1]);
      }
      float l0[4], l1[4], bc[4];
#pragma unroll
      for (int t = 0; t < 4; ++t) {
        float a0 = so[t][0], a2 = so[t][2], b0 = sp[t][0], b2 = sp[t][2];
        const float c0 = own0 ? a0 : b0, c1 = own0 ? so[t][1] : sp[t][1], c2 = own1 ? a2 : b2, c3 = own1 ? so[t][3] : sp[t][3];
        l0[t] = odd ? c1 : c0;
        l1[t] = odd ? c3 : c2;
      }
      rows_bcast4(rows_reduce4<true>(fmax1(l0[0], l1[0]), fmax1(l0[1], l1[1]), fmax1(l0[2], l1[2]), fmax1(l0[3], l1[3])) * -sc, bc);
#pragma unroll
      for (int t = 0; t < 4; ++t) {
        l0[t] = __builtin_amdgcn_exp2f(__builtin_fmaf(l0[t], sc, bc[t]));
        l1[t] = __builtin_amdgcn_exp2f(__builtin_fmaf(l1[t], sc, bc[t]));
      }
      rows_bcast4(__builtin_amdgcn_rcpf(rows_reduce4<false>(l0[0] + l1[0], l0[1] + l1[1], l0[2] + l1[2], l0[3] + l1[3]) * ug), bc);
      const unsigned sh = odd ? 16u : 0u;
      const unsigned mk0 = own0 ? 0u : 0xffffu, mk1 = own1 ? 0u : 0xffffu;
#pragma unroll
      for (int t = 0; t < 4; ++t) {
        const unsigned pe = pk2(l0[t] * bc[t], l1[t] * bc[t]), ph = pe >> 16;
        const bf16x8 pf = as_frag((u32x4){(pe & mk0) << sh, (ph & mk1) << sh, (pe & (mk0 ^ 0xffffu)) << sh, (ph & (mk1 ^ 0xffffu)) << sh});
        const f32x4 r0 = mfma(frag2(vf[t][0], vf[t + 1][0]), pf, z4);
        const f32x4 r1 = mfma(frag2(vf[t][1], vf[t + 1][1]), pf, z4);
        const u32x2 c0 = pk4(r0), c1 = pk4(r1);
        __builtin_amdgcn_raw_buffer_store_b128((u32x4){c0[0], c0[1], g3 ? 0u : c1[0], g3 ? 0u : c1[1]}, wsr, l16 + obs + t * 32768, 0, 16);
      }
      STAMP(9);
      VM_WAIT(0);
      asm volatile("" ::"v"(pw));
      if (l16 == 0) __hip_atomic_store((gu64*)(ws + WS_FA) + ((rt * 2 + rh) * 16 + hp * 2 + hh) * FS, tagO, RLX_AGENT);
      STAMP(10); STAMPC(19);
      if (cg >= 7) {
        if (bid == 56 && w == 0) {
#pragma nounroll
          for (int k = 0; k < 4; ++k) {
            const int i = (k & 1) * 64 + lane;
            poll_flags(k < 2 ? (gu64*)(ws + WS_CLR) + i : (gu64*)(ws + WS_FA) + ((i >> 3) * 32 + (i & 7) * 2) * FS, tagO);
          }
          if (lane == 0) __hip_atomic_store((gu64*)(ws + WS_E), tagO, RLX_AGENT);
        }
        STAMP_FLUSH();
        return;
      }
  }

  const int pb = w >> 2;
  wait4(L0 + SYNC + (pb ? SY_WOB : SY_WOA));
  const unsigned sw = (2 * g + (q >> 1)) & 3;
  const unsigned rb = L0 + (pb * 448 + 4 * g + q) * 128 + 8 * p;
  unsigned wa[4], wa2[4];
#pragma unroll
  for (int v = 0; v < 4; ++v) {
    wa[v] = opaque(rb + ((v ^ sw) << 5));
    wa2[v] = opaque(wa[v] + (g3 ? 0u : 2048u));
  }
  auto wfrag = [&](int h, int ct) {
    const int v = ct ^ (2 * (h & 1));
    return lds_tr2(wa[v] + h * 3584, wa2[v] + h * 3584);
  };
  constexpr int NPRE = 8;
  bf16x8 wf[16][4];
#pragma unroll
  for (int h = 0; h < NPRE; ++h)
#pragma unroll
    for (int ct = 0; ct < 4; ++ct) wf[h][ct] = wfrag(h, ct);
  STAMP(11);
  const unsigned ub = WS_O + ((rg * 4 + wl) * 32 + pb * 16) * 1024;
  tagO = uniform64(*(LAS u64*)(uintptr_t)(L0 + SYNC + SY_TAG));
  {
    const gu64* fp = pb ? (gu64*)(ws + WS_FB) + ((rg * 2 + (wl >> 1)) * 8 + (li & 7)) * FS : (gu64*)(ws + WS_FA) + (rg * 16 + li) * FS;
    const u64 f0 = __hip_atomic_load(fp, RLX_AGENT);
    if (br && !pb) { VM_WAIT(0); sig(L0 + SYNC + SY_WOB + wl * 4); }
    if (!__all(f0 == tagO)) poll_flags(fp, tagO);
  }
  STAMP(12);
  u32x4 of[16];
  {
    unsigned vo = ub + lane * 16;
    asm volatile("" : "+v"(vo)::"memory");
#pragma unroll
    for (int h = 0; h < 16; ++h) of[h] = __builtin_amdgcn_raw_buffer_load_b128(wsr, vo, ((h + 2 * cg) & 15) * 1024, 16);
    SB();
  }
  f32x4 acc[4] = {z4, z4, z4, z4};
#pragma unroll
  for (int h = 0; h < 16; ++h) {
#pragma unroll
    for (int ct = 0; ct < 4; ++ct) acc[ct] = mfma(wf[h][ct], as_frag(of[h]), acc[ct]);
    SB();
    if (h + NPRE < 16) {
#pragma unroll
      for (int ct = 0; ct < 4; ++ct) wf[h + NPRE][ct] = wfrag(h + NPRE, ct);
    }
    SB();
  }
  STAMP(13);
  const unsigned xa = L0 + P2X + wl * 4096 + lane * 16;
  if (!pb) {
    wait4(L0 + SYNC + SY_D6);
#pragma unroll
    for (int ct = 0; ct < 4; ++ct) lds_st128(xa + ct * 1024, __builtin_bit_cast(u32x4, acc[ct]));
    sig(L0 + SYNC + SY_P2 + wl * 4);
  } else {
    f32x4 pa[4];
    for (;;) {
      const unsigned f = *(volatile LAS unsigned*)(uintptr_t)(L0 + SYNC + SY_P2 + wl * 4);
#pragma unroll
      for (int ct = 0; ct < 4; ++ct) pa[ct] = *(volatile LAS f32x4*)(uintptr_t)(xa + ct * 1024);
      if (__builtin_amdgcn_readfirstlane(f)) break;
      __builtin_amdgcn_s_sleep(1);
    }
    const unsigned tb = L0 + P2X + wl * 4096;
#pragma unroll
    for (int ct = 0; ct < 4; ++ct) {
      const u32x2 a = pk4(acc[ct] + pa[ct]);
      *(LAS u32x2*)(uintptr_t)(tb + li * 128 + (((ct * 2 + (g >> 1)) ^ (li & 7)) << 4) + (g & 1) * 8) = a;
    }
    const __amdgpu_buffer_rsrc_t orr = __builtin_amdgcn_make_buffer_rsrc(out, 0, kS * XROW, 0x00020000);
    const unsigned ro = (rg * 64 + wl * 16 + (lane >> 3)) * XROW + cg * 128 + (lane & 7) * 16;
#pragma unroll
    for (int k = 0; k < 2; ++k) {
      const u32x4 v = lds_ld128(tb + k * 1024 + (lane >> 3) * 128 + (((lane & 7) ^ (lane >> 3)) << 4));
      __builtin_amdgcn_raw_buffer_store_b128(v, orr, ro + k * 8 * XROW, 0, 16);
    }
  }
  STAMP(14);
  STAMP(15);
  STAMP_FLUSH();
}

}

extern "C" void kernel_launch(void* const* d_in, const int* in_sizes, int n_in, void* d_out, int out_size, void* d_ws,
                              size_t ws_size, hipStream_t stream) {
  (void)in_sizes; (void)n_in; (void)out_size; (void)ws_size;
  gated_mix_kernel<<<256, 512, 0, stream>>>((const char*)d_in[0], (const char*)d_in[1], (const char*)d_in[2],
                                            (const char*)d_in[4], (const char*)d_in[5],
                                            (const unsigned short*)d_in[6], (char*)d_out, (char*)d_ws);
}
```

```cpp
#include <hip/hip_runtime.h>
#include <stdint.h>

typedef __bf16 bf16x2 __attribute__((ext_vector_type(2)));
typedef __bf16 bf16x4 __attribute__((ext_vector_type(4)));
typedef __bf16 bf16x8 __attribute__((ext_vector_type(8)));
typedef float f32x2 __attribute__((ext_vector_type(2)));
typedef float f32x4 __attribute__((ext_vector_type(4)));
typedef unsigned u32x2 __attribute__((ext_vector_type(2)));
typedef unsigned u32x4 __attribute__((ext_vector_type(4)));
typedef unsigned long long u64;
#define LAS __attribute__((address_space(3)))
#define GAS __attribute__((address_space(1)))
typedef GAS u64 gu64;
#define RLX_AGENT __ATOMIC_RELAXED, __HIP_MEMORY_SCOPE_AGENT

namespace {

constexpr int kS = 2048, kD = 448;
constexpr int XROW = kD * 2;
constexpr int WROW = 3 * kD * 2;
constexpr int WM_B = 64 * 112;

constexpr int A_FRONT = 34816, A_SLOT = 144 * 128 + 3 * WM_B, A_W = 144 * 128;
constexpr int A_G = A_FRONT + 3 * A_SLOT;
constexpr int B_SLOT = 128 * 128 + 2 * WM_B, B_W = 128 * 128;
constexpr int B_WQ = 97536, B_G = B_WQ + 7 * WM_B, B_EX = B_G + 3584, B_KVL = B_EX + 8192;
constexpr int P2X = 114688;
constexpr int SYNC = 163584;
constexpr int LDS_B = 163840;
static_assert(A_G + 3584 <= SYNC && B_KVL + 4096 <= SYNC && A_FRONT + 2 * A_SLOT == 114688, "lds map");
__host__ __device__ constexpr int a_slot(int c) { return A_FRONT + ((c + 2) % 3) * A_SLOT; }
__host__ __device__ constexpr int b_slot(int c) { return ((c + 2) % 3) * B_SLOT; }
enum { SY_WOA = 0, SY_WOB = 16, SY_D6 = 32, SY_WQ = 64, SY_QD = 80, SY_EX = 96, SY_KV = 112, SY_P2 = 128, SY_ZERO = 192, SY_TAG = 240 };

constexpr int WS_E = 0;
constexpr int WS_CLR = 4096;
constexpr int FS = 16;
constexpr int WS_FA = 8192;
constexpr int WS_FB = WS_FA + 16 * 2 * 16 * 8 * FS;
constexpr int WS_KVP = WS_FB + 16 * 4 * 8 * 8 * FS;
constexpr int WS_O = WS_KVP + 8 * 4 * 16 * 1024;
constexpr int WS_END = WS_O + 16 * 8 * 32 * 1024;

constexpr int PT_WS = (WS_END / 8) & ~127;
constexpr unsigned SPIN_CAP = 1u << 19;
constexpr unsigned SENT = 0xffffffffu;
constexpr int QWO = 14;

__device__ __forceinline__ unsigned pk2(float a, float b) {
  f32x2 v = {a, b};
  bf16x2 r = __builtin_convertvector(v, bf16x2);
  return __builtin_bit_cast(unsigned, r);
}
__device__ __forceinline__ u32x2 pk4(f32x4 v) { return (u32x2){pk2(v[0], v[1]), pk2(v[2], v[3])}; }
__device__ __forceinline__ bf16x8 as_frag(u32x4 v) { return __builtin_bit_cast(bf16x8, v); }
__device__ __forceinline__ bf16x8 frag2(u32x2 a, u32x2 b) { return as_frag((u32x4){a[0], a[1], b[0], b[1]}); }
__device__ __forceinline__ f32x4 mfma(bf16x8 a, bf16x8 b, f32x4 c) {
  return __builtin_amdgcn_mfma_f32_16x16x32_bf16(a, b, c, 0, 0, 0);
}
__device__ __forceinline__ bf16x8 lds_tr2(unsigned a0, unsigned a1) {
  bf16x4 lo = __builtin_amdgcn_ds_read_tr16_b64_v4bf16((LAS bf16x4*)(uintptr_t)a0);
  bf16x4 hi = __builtin_amdgcn_ds_read_tr16_b64_v4bf16((LAS bf16x4*)(uintptr_t)a1);
  return __builtin_shufflevector(lo, hi, 0, 1, 2, 3, 4, 5, 6, 7);
}
__device__ __forceinline__ u32x4 lds_ld128(unsigned a) { return *(LAS u32x4*)(uintptr_t)a; }
__device__ __forceinline__ void lds_st128(unsigned a, u32x4 v) { *(LAS u32x4*)(uintptr_t)a = v; }
__device__ __forceinline__ unsigned opaque(unsigned v) { asm volatile("" : "+v"(v)); return v; }

__device__ __forceinline__ void sig(unsigned a) {
  asm volatile("" ::: "memory");
  *(volatile LAS unsigned*)(uintptr_t)a = 1u;
  asm volatile("" ::: "memory");
  __builtin_amdgcn_sched_barrier(0);
}
__device__ __forceinline__ void wait1(unsigned a) {
  while (__builtin_amdgcn_readfirstlane(*(volatile LAS unsigned*)(uintptr_t)a) == 0u) __builtin_amdgcn_s_sleep(1);
  asm volatile("" ::: "memory");
}
__device__ __forceinline__ void wait4(unsigned a) {
  for (;;) {
    const u32x4 v = *(volatile LAS u32x4*)(uintptr_t)a;
    if (__builtin_amdgcn_readfirstlane(v[0] & v[1] & v[2] & v[3])) break;
    __builtin_amdgcn_s_sleep(1);
  }
  asm volatile("" ::: "memory");
}

__device__ __forceinline__ u32x4 make_srd(const void* p, unsigned bytes) {
  uint64_t a = (uint64_t)p;
  u32x4 r;
  r[0] = __builtin_amdgcn_readfirstlane((unsigned)a);
  r[1] = __builtin_amdgcn_readfirstlane((unsigned)(a >> 32) & 0xffffu);
  r[2] = bytes;
  r[3] = 0x00020000u;
  return r;
}
__device__ __forceinline__ void dma16(u32x4 srd, unsigned voff, unsigned soff, unsigned ldsaddr) {
  asm volatile(
      "s_mov_b32 m0, %3\n\t"
      "s_nop 3\n\t"
      "buffer_load_dwordx4 %0, %1, %2 offen lds"
      :
      : "v"(voff), "s"(srd), "s"(soff), "s"(ldsaddr));
}
#define LDS_BAR() asm volatile("s_waitcnt lgkmcnt(0)\n\ts_barrier" ::: "memory")
#define VM_WAIT(N) asm volatile("s_waitcnt vmcnt(" #N ")" ::: "memory")
template <int N> __device__ __forceinline__ void vm_bar() { asm volatile("s_waitcnt vmcnt(%0) lgkmcnt(0)\n\ts_barrier" ::"n"(N) : "memory"); }
#define SB() __builtin_amdgcn_sched_barrier(0)

__device__ __forceinline__ float fmax1(float a, float b) { return __builtin_amdgcn_fmed3f(a, b, 3.0e38f); }
__device__ __forceinline__ void swap16(float& a, float& b) {
  auto r = __builtin_amdgcn_permlane16_swap(__float_as_uint(a), __float_as_uint(b), false, false);
  const unsigned r0 = r[0], r1 = r[1];
  a = __uint_as_float(r0);
  b = __uint_as_float(r1);
}
__device__ __forceinline__ void swap32(float& a, float& b) {
  auto r = __builtin_amdgcn_permlane32_swap(__float_as_uint(a), __float_as_uint(b), false, false);
  const unsigned r0 = r[0], r1 = r[1];
  a = __uint_as_float(r0);
  b = __uint_as_float(r1);
}
template <bool MAX>
__device__ __forceinline__ float rows_reduce4(float v0, float v1, float v2, float v3) {
  swap32(v0, v1);
  swap32(v2, v3);
  float a = MAX ? fmax1(v0, v1) : v0 + v1, b = MAX ? fmax1(v2, v3) : v2 + v3;
  swap16(a, b);
  return MAX ? fmax1(a, b) : a + b;
}
__device__ __forceinline__ void rows_bcast4(float x, float (&o)[4]) {
  float y = x;
  swap16(x, y);
  o[0] = x; o[1] = x; o[2] = y; o[3] = y;
  swap32(o[0], o[1]);
  swap32(o[2], o[3]);
}
__device__ __forceinline__ float row3_bcast(float v) {
  float a = v;
  swap32(a, v);
  a = v;
  swap16(a, v);
  return v;
}
template <int NM, int ND>
__device__ __forceinline__ void interleave() {
#pragma unroll
  for (int i = 0; i < NM; ++i) {
    __builtin_amdgcn_sched_group_barrier(0x8, 1, 0);
    if (i < ND) {
      __builtin_amdgcn_sched_group_barrier(0x2, 1, 0);
      __builtin_amdgcn_sched_group_barrier(0x100, 1, 0);
    }
  }
}
constexpr float kEps = 1e-6f, kLog2e = 1.44269504088896f;
__device__ __forceinline__ float phi(float u) {
  const float e = __builtin_amdgcn_fmed3f(__builtin_amdgcn_exp2f(__builtin_fmaf(u, kLog2e, -kEps * kLog2e)), 0.f, 1.f);
  return fmax1(u, kEps) + e;
}
__device__ __forceinline__ u32x2 phi4(f32x4 v) { return (u32x2){pk2(phi(v[0]), phi(v[1])), pk2(phi(v[2]), phi(v[3]))}; }
__device__ __forceinline__ float sigmoidf(float z) {
  return __builtin_amdgcn_rcpf(1.f + __builtin_amdgcn_exp2f(-1.44269504088896f * z));
}
__device__ __forceinline__ u64 uniform64(u64 e) {
  return ((u64)(unsigned)__builtin_amdgcn_readfirstlane((unsigned)(e >> 32)) << 32) |
         (unsigned)__builtin_amdgcn_readfirstlane((unsigned)e);
}
__device__ __forceinline__ u64 next_tag(u64 e) { return uniform64(e) * 6364136223846793005ull + 1442695040888963407ull; }
__device__ __forceinline__ void poll_flags(const gu64* p, u64 tag) {
#pragma nounroll
  for (unsigned spins = 0; spins < SPIN_CAP; ++spins) {
    u64 v = __hip_atomic_load(p, RLX_AGENT);
    if (__all(v == tag)) break;
    __builtin_amdgcn_s_sleep(2);
  }
  asm volatile("" ::: "memory");
}
__device__ __forceinline__ void publish(u32x4 v, __amdgpu_buffer_rsrc_t r, unsigned voff) {
#pragma unroll
  for (int i = 0; i < 4; ++i) v[i] = v[i] < SENT - 1u ? v[i] : SENT - 1u;
  __builtin_amdgcn_raw_buffer_store_b128(v, r, voff, 0, 16);
}
__device__ __forceinline__ unsigned lane_id() {
  unsigned l = __builtin_amdgcn_mbcnt_hi(~0u, __builtin_amdgcn_mbcnt_lo(~0u, 0u));
  asm volatile("" : "+v"(l));
  return l;
}
__device__ __forceinline__ int wrap7(int c) { return c >= 7 ? c - 7 : c; }

__global__ __launch_bounds__(512, 2) void gated_mix_kernel(const char* __restrict__ x, const char* __restrict__ wqkvA,
                                                           const char* __restrict__ woA, const char* __restrict__ wqkvB,
                                                           const char* __restrict__ woB,
                                                           const unsigned short* __restrict__ gate_w, char* __restrict__ out,
                                                           char* ws) {
  __shared__ __attribute__((aligned(1024))) char lds[LDS_B];
  const unsigned L0 = (unsigned)(uintptr_t)(LAS char*)lds;
  const int bid = blockIdx.x;
  const int xcd = bid & 7, idx = bid >> 3;
  const int xj = xcd >> 1, ci = idx >> 3;
  const int br = ci >> 1;
  const int hp = 2 * (br ? (xj ^ 1) : xj) + (ci & 1);
  const int rt = (xcd & 1) * 8 + (idx & 7);
  const int rot = wrap7(wrap7((idx & 7) + 2 * ci + (xj & 1)));
  const int cg = idx & 7, rg = 4 * xcd + ci;

  const int tid = threadIdx.x;
  const int lane = tid & 63;
  const int w = __builtin_amdgcn_readfirstlane(tid >> 6) & 7;
  const int wl = w & 3;
  const int li = lane & 15, g = lane >> 4, q = (lane >> 2) & 3, p = lane & 3;
  const bool g3 = g == 3;
  const f32x4 z4 = {0.f, 0.f, 0.f, 0.f};
  unsigned vx0 = (lane >> 3) * XROW + (((lane & 7) ^ (lane >> 4)) << 4);
  unsigned vx1 = vx0 ^ 64u;
  unsigned vw0, vw1;
  {
    const unsigned s0 = wl * 64 + lane, s1 = s0 + 256;
    const unsigned p0 = (s0 * 9363u) >> 16, p1 = (s1 * 9363u) >> 16;
    const unsigned k0 = (p0 & 56) | ((p0 >> 1) & 3) | ((p0 & 1) << 2);
    const unsigned k1 = (p1 & 56) | ((p1 >> 1) & 3) | ((p1 & 1) << 2);
    vw0 = k0 * WROW + hp * 112 + (s0 - p0 * 7) * 16;
    vw1 = k1 * WROW + hp * 112 + (s1 - p1 * 7) * 16;
  }
  asm volatile("" : "+v"(vx0), "+v"(vx1), "+v"(vw0), "+v"(vw1));
  asm volatile("" ::"s"(x), "s"(wqkvA), "s"(woA), "s"(wqkvB), "s"(woB), "s"(gate_w), "s"(out), "s"(ws));
  const __amdgpu_buffer_rsrc_t wsr = __builtin_amdgcn_make_buffer_rsrc(ws, 0, WS_END, 0x00020000);
  u64 tagO;
  auto wo_run = [&](int j0, int j1) {
    const unsigned vwo = (lane >> 3) * XROW + cg * 128 + (((lane & 7) ^ (((lane >> 4) & 3) << 1)) << 4);
    const u32x4 sa = make_srd(woA, kD * XROW), sb = make_srd(woB, kD * XROW);
    unsigned so = (wl + 7 * cg + 4 * (j0 % 14)) * (8 * XROW);
    so = so >= kD * XROW ? so - kD * XROW : so;
#pragma unroll
    for (int j = j0; j < j1; ++j) {
      dma16(j < 14 ? sa : sb, vwo, so, L0 + (wl + 4 * j) * 1024);
      so += 32 * XROW;
      so = so >= kD * XROW ? so - kD * XROW : so;
    }
  };
  const u32x4 srdX = make_srd(x, kS * XROW);
  const u32x4 srdW = make_srd(br ? wqkvB : wqkvA, kD * WROW);
  const bool l3 = wl == 3;
  const int xr = rt * 128 + wl * 32 - (br ? 0 : 16);
  const unsigned sx01 = (xr < 0 ? 0 : xr) * XROW, sx23 = (xr + 16) * XROW;
  const bool l3a = l3 && !br;
  const u32x4 srd2 = l3a ? srdX : srdW;
  const unsigned v2 = l3 ? vw0 : vw1;
  const unsigned v2a = l3a ? vx0 : v2, v2b = l3a ? vx1 : v2;
  const unsigned st2 = l3a ? 128u : 64u * WROW, s2a = l3a ? (rt * 128 + 112) * XROW : 0u;
  const unsigned s2d = l3a ? 8u * XROW : (unsigned)XROW, s2e = l3a ? 8u * XROW : 2u * XROW;
  const unsigned sub2 = (l3 ? wl : wl + 4) * 1024;
  auto chunk = [&](unsigned sl, unsigned lq, unsigned lk, int cc) {
    const unsigned sw = cc * (64 * WROW), sx = cc * 128, s2 = cc * st2 + s2a;
#pragma unroll
    for (int j = 0; j < 4; ++j)
      dma16(srdX, (j & 1) ? vx1 : vx0, (j < 2 ? sx01 : sx23) + (j & 1) * 8 * XROW + sx, sl + (wl * 4 + j) * 1024);
    dma16(srdW, vw0, sw + XROW, lk + wl * 1024);
    dma16(srdW, vw0, sw + 2 * XROW, lk + WM_B + wl * 1024);
    dma16(srd2, v2b, s2 + s2d, l3a ? sl + 17408 : lk + sub2);
    dma16(srd2, v2b, s2 + s2e, l3a ? sl + 17408 : lk + WM_B + sub2);
    if (!br) {
      dma16(srdW, vw0, sw, lq + wl * 1024);
      dma16(srd2, v2a, s2, l3a ? sl + 16384 : lq + sub2);
    }
  };
  unsigned gv = 0, pt = 0;
  u64 epoch = 0;
  {
    int wx = w;
    asm volatile("" : "+s"(wx));
    if (wx < 4) {
      gv = ((const unsigned*)gate_w)[tid < 224 ? tid : 0];
      epoch = __hip_atomic_load((gu64*)(ws + WS_E), RLX_AGENT);
      const char* pb_ = wx == 0 ? ws : (wx == 1 ? out + rg * 64 * XROW : (wx == 2 ? woA : woB));
      pt = __hip_atomic_load((GAS unsigned*)(pb_ + (lane & 7) * (wx == 0 ? PT_WS : (wx == 1 ? 7168 : 50176))), RLX_AGENT);
    } else {
#pragma nounroll
      for (int c = 0; c < 2; ++c) {
        const unsigned sl = L0 + (br ? (c ? b_slot(1) : b_slot(0)) : (c ? a_slot(1) : a_slot(0)));
        chunk(sl, sl + A_W, sl + (br ? B_W : A_W + WM_B), wrap7(rot + c));
      }
    }
  }

  const int rh = w & 1, hh = w >> 1;
  const unsigned obs = WS_O + ((rt * 8 + rh * 4) * 32 + hp * 2 + hh) * 1024;
  const unsigned kvs = WS_KVP + (hp * 4 + w) * 16 * 1024;
  auto setup = [&](int goff) {
    if (tid < 60) *(LAS unsigned*)(uintptr_t)(L0 + SYNC + tid * 4) = 0u;
    const unsigned lo = (gv & 0xffffu) * 0x10001u, hi = (gv >> 16) * 0x10001u;
    const int pos = tid - rot * 32;
    if (tid < 224) lds_st128(L0 + goff + (pos < 0 ? pos + 224 : pos) * 16, (u32x4){lo, lo, hi, hi});
  };
  u64 clr = 0;
  auto clrp = [&]() { return (const gu64*)(ws + WS_CLR) + hp * 16 + (lane_id() & 15); };
  auto hook = [&](int c, bool before, bool isb) {
    if (c == 0 && !before && isb)
      __builtin_amdgcn_raw_buffer_store_b128((u32x4){SENT, SENT, SENT, SENT}, wsr, lane_id() * 16 + kvs + rt * 1024, 0, 16);
    if (c == 3 && before) {
      asm volatile("" : "+v"(epoch) : "v"(pt));
      tagO = next_tag(epoch);
      *(LAS u64*)(uintptr_t)(L0 + SYNC + SY_TAG) = tagO;
      VM_WAIT(0);
    }
    if (c == 3 && !before && isb)
      __builtin_amdgcn_raw_buffer_store_b64((u32x2){(unsigned)tagO, (unsigned)(tagO >> 32)}, wsr,
                                            (lane_id() | w) == 0 ? WS_CLR + (hp * 16 + rt) * 8 : 0x7ffffff0u, 0, 16);
    if (c == 6 && !before && isb) clr = __hip_atomic_load(clrp(), RLX_AGENT);
  };
  const unsigned xsw = li * 128 + ((g ^ (li >> 1)) << 4);
  const unsigned wln = (8 * g + 2 * q) * 112 + 8 * p + 56 * hh;

  if (__builtin_expect(w >= 4, 1)) {
    if (!br) {
      int cc = wrap7(rot + 1);
      unsigned sl = L0 + a_slot(2);
#pragma nounroll
      for (int c = 2; c < 7; ++c) {
        vm_bar<10>();
        cc = wrap7(cc + 1);
        chunk(sl, sl + A_W, sl + A_W + WM_B, cc);
        sl = sl == L0 + a_slot(0) ? L0 + a_slot(1) : sl + A_SLOT;
      }
      vm_bar<10>();
      if (cg >= 7) { vm_bar<0>(); return; }
      wo_run(0, 4);
      vm_bar<4>();
      wo_run(4, 28);
      VM_WAIT(14);
      sig(L0 + SYNC + SY_WOA + wl * 4);
      VM_WAIT(0);
      asm volatile("" ::"v"(gv), "v"(epoch), "v"(pt));
      sig(L0 + SYNC + SY_WOB + wl * 4);
    } else {
      auto instr = [&](int n) {
        if (n < 56) {
          const int c = n >> 3, j = n & 7, cc = wrap7(c + rot);
          const unsigned sl = L0 + b_slot(c), sw = cc * (64 * WROW), sx = cc * 128;
          if (j < 4) dma16(srdX, (j & 1) ? vx1 : vx0, sx01 + j * 8 * XROW + sx, sl + (wl * 4 + j) * 1024);
          else dma16(srdW, j < 6 ? vw0 : v2, sw + ((j & 1) + 1) * XROW, sl + B_W + (j & 1) * WM_B + (j < 6 ? wl * 1024 : sub2));
        } else {
          const int c = (n - 56) >> 1;
          dma16(srdW, (n & 1) ? v2 : vw0, wrap7(c + rot) * (64 * WROW), L0 + B_WQ + c * WM_B + ((n & 1) ? sub2 : wl * 1024));
        }
      };
      bf16x8 xq[14][2];
      const unsigned xl = L0 + (wl * 32 + li) * 128 + ((g ^ (li >> 1)) << 4);
#pragma unroll
      for (int c = 0; c < 7; ++c) {
        vm_bar<8>();
#pragma unroll
        for (int ks = 0; ks < 2; ++ks) {
          const unsigned xa = (xl ^ (ks * 64)) + b_slot(c);
          xq[c * 2 + ks][0] = as_frag(lds_ld128(xa));
          xq[c * 2 + ks][1] = as_frag(lds_ld128(xa + 2048));
        }
#pragma unroll
        for (int n = 8 * c + 16; n < (c < 6 ? 8 * c + 24 : 70); ++n) instr(n);
      }
      VM_WAIT(0);
      asm volatile("" ::"v"(gv), "v"(epoch), "v"(pt));
      sig(L0 + SYNC + SY_WQ + wl * 4);
      wait4(L0 + SYNC + SY_WQ);
      wait4(L0 + SYNC + SY_EX);
      f32x4 aq[2][2][2];
#pragma unroll
      for (int i = 0; i < 8; ++i) aq[i >> 2][(i >> 1) & 1][i & 1] = (f32x4){kEps, kEps, kEps, kEps};
      const unsigned wb = L0 + B_WQ + (8 * g + 2 * q) * 112 + 8 * p;
      const unsigned gl = L0 + B_G + (8 * g + q) * 8;
      const bool p3 = p == 3;
      const unsigned d1 = p3 ? 32u : 112u;
      const unsigned qa0 = opaque(p3 ? gl : wb + 32), qstep = opaque(p3 ? 256u : 3584u);
      bf16x8 fq[3][4];
      auto rdq = [&](int s) {
        const unsigned wk = wb + s * 3584;
        const unsigned a = qa0 + s * qstep;
        fq[s % 3][0] = lds_tr2(wk, wk + 112);
        fq[s % 3][1] = lds_tr2(a, a + d1);
        fq[s % 3][2] = lds_tr2(wk + 56, wk + 56 + 112);
        fq[s % 3][3] = lds_tr2(wk + 88, wk + 88 + 112);
      };
      rdq(0);
      rdq(1);
#pragma unroll
      for (int s = 0; s < 14; ++s) {
        SB();
        if (s + 2 < 14) rdq(s + 2);
#pragma unroll
        for (int i = 0; i < 4; ++i) {
          aq[0][i >> 1][i & 1] = mfma(fq[s % 3][i], xq[s][0], aq[0][i >> 1][i & 1]);
          aq[1][i >> 1][i & 1] = mfma(fq[s % 3][i], xq[s][1], aq[1][i >> 1][i & 1]);
        }
        interleave<8, 8>();
      }
      sig(L0 + SYNC + SY_QD + wl * 4);
      u32x4 pqf[2][2];
      float omg[2];
#pragma unroll
      for (int t = 0; t < 2; ++t) {
        omg[t] = 1.f - sigmoidf(row3_bcast(aq[t][0][1][0]));
#pragma unroll
        for (int h = 0; h < 2; ++h) {
          const u32x2 a = phi4(aq[t][h][0]), b = phi4(aq[t][h][1]);
          pqf[t][h] = (u32x4){a[0], a[1], g3 ? 0u : b[0], g3 ? 0u : b[1]};
        }
      }
      tagO = uniform64(*(LAS u64*)(uintptr_t)(L0 + SYNC + SY_TAG));
      unsigned ob = WS_O + ((rt * 8 + wl * 2) * 32 + 16 + hp * 2) * 1024 + lane * 16;
      unsigned kb = L0 + B_KVL + lane * 16, zb = L0 + B_KVL + 1024 + (g * 16 + 12) * 16;
      asm volatile("" : "+v"(pqf[0][0]), "+v"(pqf[0][1]), "+v"(pqf[1][0]), "+v"(pqf[1][1]), "+v"(omg[0]), "+v"(omg[1]), "+v"(ob),
                   "+v"(kb), "+v"(zb), "+s"(tagO));
      __builtin_amdgcn_s_setprio(2);
      wait4(L0 + SYNC + SY_KV);
      {
        bf16x8 kv0[2], kv1[2], zf[2];
#pragma unroll
        for (int h = 0; h < 2; ++h) zf[h] = as_frag(lds_ld128(zb + h * 2048));
#pragma unroll
        for (int h = 0; h < 2; ++h) {
          kv0[h] = as_frag(lds_ld128(kb + h * 2048));
          kv1[h] = as_frag(lds_ld128(kb + h * 2048 + 1024));
        }
        SB();
        f32x4 dn[4], n0[4], n1[4];
#pragma unroll
        for (int i = 0; i < 4; ++i) dn[i] = mfma(zf[i >> 1], as_frag(pqf[i & 1][i >> 1]), (f32x4){kEps, kEps, kEps, kEps});
#pragma unroll
        for (int i = 0; i < 4; ++i) {
          n0[i] = mfma(kv0[i >> 1], as_frag(pqf[i & 1][i >> 1]), z4);
          n1[i] = mfma(kv1[i >> 1], as_frag(pqf[i & 1][i >> 1]), z4);
        }
        SB();
#pragma unroll
        for (int i = 0; i < 4; ++i) {
          const float s = omg[i & 1] * __builtin_amdgcn_rcpf(dn[i][0]);
          const u32x2 a = pk4(n0[i] * s), b = pk4(n1[i] * s);
          __builtin_amdgcn_raw_buffer_store_b128((u32x4){a[0], a[1], g3 ? 0u : b[0], b[1]}, wsr, ob + ((i & 1) * 32 + (i >> 1)) * 1024, 0, 16);
        }
      }
      VM_WAIT(0);
      if (lane == 0) __hip_atomic_store((gu64*)(ws + WS_FB) + ((rt * 4 + wl) * 8 + hp) * FS, tagO, RLX_AGENT);
      if (cg >= 7) return;
    }
  } else if (__builtin_expect(br, 1)) {
    __builtin_amdgcn_s_setprio(2);
    setup(B_G);
    f32x4 ak[4][2], av[4][2];
    {
      const float one = li == 12 ? 1.f : 0.f, i0 = rh ? one : 0.f, i1 = rh ? 0.f : one;
#pragma unroll
      for (int t = 0; t < 4; ++t) {
        ak[t][0] = ak[t][1] = (f32x4){kEps, kEps, kEps, kEps};
        av[t][0] = (f32x4){i0, i0, i0, i0};
        av[t][1] = (f32x4){i1, i1, i1, i1};
      }
    }
    bf16x8 xf[2][4], fw[2][4];
    unsigned xb, wb;
    const bool z0 = p == 3 && rh, z1 = p == 3 && !rh;
    const unsigned zaddr = L0 + SYNC + SY_ZERO;
    auto rd = [&](int ks, int part) {
      const unsigned xa = xb ^ (ks * 64), wk = wb + ks * 3584;
      if (part & 1) {
        const unsigned a = wk + WM_B + (((part >> 1) ^ rh) << 5);
        const bool z = (part >> 1) ? z1 : z0;
        fw[ks][part] = lds_tr2(z ? zaddr : a, z ? zaddr : a + 112);
      } else {
        fw[ks][part] = lds_tr2(wk + (part >> 1) * 32, wk + (part >> 1) * 32 + 112);
      }
      if (part < 2) {
        xf[ks][part * 2] = as_frag(lds_ld128(xa + part * 4096));
        xf[ks][part * 2 + 1] = as_frag(lds_ld128(xa + part * 4096 + 2048));
      }
    };
    auto mm = [&](int ks, int part) {
#pragma unroll
      for (int t = 0; t < 4; ++t) {
        if (part & 1) av[t][part >> 1] = mfma(xf[ks][t], fw[ks][part], av[t][part >> 1]);
        else ak[t][part >> 1] = mfma(xf[ks][t], fw[ks][part], ak[t][part >> 1]);
      }
    };
#pragma unroll
    for (int c = 0; c < 7; ++c) {
      xb = opaque(L0 + b_slot(c) + rh * 8192 + xsw);
      wb = opaque(L0 + b_slot(c) + B_W + wln);
      hook(c, true, true);
      LDS_BAR();
      hook(c, false, true);
      SB();
#pragma unroll
      for (int ks = 0; ks < 2; ++ks) {
#pragma unroll
        for (int part = 0; part < 4; ++part) rd(ks, part);
        if (ks == 1 || c > 0) {
#pragma unroll
          for (int part = 0; part < 4; ++part) mm(ks ^ 1, part);
          interleave<16, 12>();
        }
        SB();
      }
    }
    sig(L0 + SYNC + SY_D6 + w * 4);
    mm(1, 0); mm(1, 2);
    SB();
    mm(1, 3); mm(1, 1);
    f32x4 kv[2][2];
    {
      u32x2 pk[4][2], vf[4][2];
#pragma unroll
      for (int t = 0; t < 4; ++t) {
        pk[t][0] = phi4(ak[t][0]);
        pk[t][1] = phi4(ak[t][1]);
        vf[t][0] = pk4(av[t][0]);
        vf[t][1] = pk4(av[t][1]);
      }
#pragma unroll
      for (int e = 1; e >= 0; --e)
#pragma unroll
        for (int d = 0; d < 2; ++d) {
          kv[e][d] = mfma(frag2(pk[0][d], pk[1][d]), frag2(vf[0][e], vf[1][e]), z4);
          kv[e][d] = mfma(frag2(pk[2][d], pk[3][d]), frag2(vf[2][e], vf[3][e]), kv[e][d]);
        }
    }
    lds_st128(L0 + B_EX + w * 2048 + lane * 16, __builtin_bit_cast(u32x4, kv[1][0]));
    lds_st128(L0 + B_EX + w * 2048 + 1024 + lane * 16, __builtin_bit_cast(u32x4, kv[1][1]));
    sig(L0 + SYNC + SY_EX + w * 4);
    wait1(L0 + SYNC + SY_EX + (w ^ 1) * 4);
    const f32x4 s0 = kv[0][0] + __builtin_bit_cast(f32x4, lds_ld128(L0 + B_EX + (w ^ 1) * 2048 + lane * 16));
    const f32x4 s1 = kv[0][1] + __builtin_bit_cast(f32x4, lds_ld128(L0 + B_EX + (w ^ 1) * 2048 + 1024 + lane * 16));
    {
      const u32x2 a = pk4(s0), b = pk4(s1);
      publish((u32x4){a[0], a[1], b[0], b[1]}, wsr, lane * 16 + kvs + rt * 1024);
    }
    if (!__all(clr == tagO)) poll_flags(clrp(), tagO);
    wo_run(0, QWO);
    f32x4 sum0, sum1;
    {
      const unsigned e0 = q == g ? ((p & 1) ? 0x3f800000u : 0x3f80u) : 0u;
      const unsigned ea = p < 2 ? e0 : 0u, eb = p < 2 ? 0u : e0;
      const bf16x8 sel0 = as_frag((u32x4){ea, eb, 0u, 0u}), sel1 = as_frag((u32x4){0u, 0u, ea, eb});
      unsigned vo = kvs + lane * 16;
#pragma nounroll
      for (unsigned spins = 0; spins < SPIN_CAP; ++spins) {
        asm volatile("" : "+v"(vo)::"memory");
        u32x4 pr[16];
#pragma unroll
        for (int r = 0; r < 16; ++r) pr[r] = __builtin_amdgcn_raw_buffer_load_b128(wsr, vo, ((r + 2 * rt) & 15) * 1024, 16);
        SB();
        sum0 = sum1 = z4;
#pragma unroll
        for (int r = 0; r < 16; ++r) {
          sum0 = mfma(sel0, as_frag(pr[r]), sum0);
          sum1 = mfma(sel1, as_frag(pr[r]), sum1);
        }
        if (!__any(sum0[0] != sum0[0])) break;
        unsigned m = 0;
#pragma unroll
        for (int r = 0; r < 16; ++r) m = max(max(max(max(m, pr[r][0]), pr[r][1]), pr[r][2]), pr[r][3]);
        if (!__any(m == SENT)) break;
      }
    }
    lds_st128(L0 + B_KVL + w * 1024 + lane * 16, __builtin_bit_cast(u32x4, frag2(pk4(sum0), pk4(sum1))));
    sig(L0 + SYNC + SY_KV + w * 4);
    __builtin_amdgcn_s_setprio(0);
    if (cg >= 7) return;
    sig(L0 + SYNC + SY_WOA + w * 4);
    wait4(L0 + SYNC + SY_D6);
    wait4(L0 + SYNC + SY_QD);
    wo_run(QWO, 28);
  } else {
    asm volatile("; role A");
    setup(A_G);
    f32x4 aq[4][2], ak[5][2], av[5][2];
#pragma unroll
    for (int t = 0; t < 5; ++t)
#pragma unroll
      for (int f = 0; f < 2; ++f) { ak[t][f] = av[t][f] = z4; if (t < 4) aq[t][f] = z4; }
    const unsigned gl = L0 + A_G + (8 * g + q) * 8;
    const bool p3 = p == 3;
    const unsigned d1 = p3 ? 32u : 112u;
    bf16x8 xf[2][5], fq[2][2], fk[2][2], fv[2][2];
    unsigned xb, wb;
    auto rd = [&](int c, int ks, int part) {
      const unsigned xa = xb ^ (ks * 64), wk = wb + ks * 3584;
      if (part == 0) {
        fk[ks][0] = lds_tr2(wk + WM_B, wk + WM_B + 112);
        xf[ks][0] = as_frag(lds_ld128(xa));
        xf[ks][1] = as_frag(lds_ld128(xa + 2048));
      } else if (part == 1) {
        fv[ks][0] = lds_tr2(wk + 2 * WM_B, wk + 2 * WM_B + 112);
        xf[ks][2] = as_frag(lds_ld128(xa + 4096));
      } else if (part == 2) {
        fq[ks][0] = lds_tr2(wk, wk + 112);
        xf[ks][3] = as_frag(lds_ld128(xa + 6144));
      } else if (part == 3) {
        fk[ks][1] = lds_tr2(wk + WM_B + 32, wk + WM_B + 32 + 112);
        xf[ks][4] = as_frag(lds_ld128(xa + 8192));
      } else if (part == 4) {
        fv[ks][1] = lds_tr2(wk + 2 * WM_B + 32, wk + 2 * WM_B + 32 + 112);
      } else {
        const unsigned a = p3 ? gl + c * 512 + ks * 256 : wk + 32;
        fq[ks][1] = lds_tr2(a, a + d1);
      }
    };
    auto mm = [&](int ks, int part) {
      const int f = part / 3, m = part % 3;
#pragma unroll
      for (int t = 0; t < 5; ++t) {
        if (m == 0) ak[t][f] = mfma(fk[ks][f], xf[ks][t], ak[t][f]);
        if (m == 1) av[t][f] = mfma(xf[ks][t], fv[ks][f], av[t][f]);
        if (m == 2 && t > 0) aq[t - 1][f] = mfma(fq[ks][f], xf[ks][t], aq[t - 1][f]);
      }
    };
#pragma unroll
    for (int c = 0; c < 7; ++c) {
      xb = opaque(L0 + a_slot(c) + rh * 8192 + xsw);
      wb = opaque(L0 + a_slot(c) + A_W + wln);
      hook(c, true, false);
      LDS_BAR();
      hook(c, false, false);
      SB();
#pragma unroll
      for (int ks = 0; ks < 2; ++ks) {
#pragma unroll
        for (int part = 0; part < 6; ++part) rd(c, ks, part);
        if (ks == 1 || c > 0) {
#pragma unroll
          for (int part = 0; part < 6; ++part) mm(ks ^ 1, part);
          interleave<28, 17>();
        }
        SB();
      }
    }
    sig(L0 + SYNC + SY_D6 + w * 4);
    mm(1, 0); mm(1, 3); mm(1, 2); mm(1, 5);
    SB();
    mm(1, 1); mm(1, 4);
    const bool odd = li & 1;
    const int kk0 = 4 * g + (li & 1);
    const bool own0 = kk0 <= li, own1 = kk0 + 2 <= li;
    const float sc = 0.18898223650461362f * kLog2e;
    const float ninf = (rt == 0 && rh == 0) ? -__builtin_inff() : 0.f;
    const unsigned l16 = lane_id() * 16;
    bf16x8 kf[5];
#pragma unroll
    for (int t = 0; t < 5; ++t) kf[t] = frag2(pk4(ak[t][0]), pk4(ak[t][1]));
    f32x4 sp[4], so[4];
#pragma unroll
    for (int t = 0; t < 4; ++t) {
      const u32x2 b = pk4(aq[t][1]);
      const bf16x8 qf = frag2(pk4(aq[t][0]), (u32x2){g3 ? 0u : b[0], g3 ? 0u : b[1]});
      sp[t] = mfma(kf[t], qf, t == 0 ? (f32x4){ninf, ninf, ninf, ninf} : z4);
      so[t] = mfma(kf[t + 1], qf, z4);
    }
    float ug;
    {
      float z0 = aq[0][1][0], z1 = aq[1][1][0], z2 = aq[2][1][0], z3 = aq[3][1][0];
      swap32(z0, z1);
      swap32(z2, z3);
      swap16(z1, z3);
      ug = 1.f + __builtin_amdgcn_exp2f(-kLog2e * z3);
    }
    u32x2 vf[5][2];
#pragma unroll
    for (int t = 0; t < 5; ++t) {
      vf[t][0] = pk4(av[t][0]);
      vf[t][1] = pk4(av[t][1]);
    }
    float l0[4], l1[4], bc[4];
#pragma unroll
    for (int t = 0; t < 4; ++t) {
      float a0 = so[t][0], a2 = so[t][2], b0 = sp[t][0], b2 = sp[t][2];
      const float c0 = own0 ? a0 : b0, c1 = own0 ? so[t][1] : sp[t][1], c2 = own1 ? a2 : b2, c3 = own1 ? so[t][3] : sp[t][3];
      l0[t] = odd ? c1 : c0;
      l1[t] = odd ? c3 : c2;
    }
    rows_bcast4(rows_reduce4<true>(fmax1(l0[0], l1[0]), fmax1(l0[1], l1[1]), fmax1(l0[2], l1[2]), fmax1(l0[3], l1[3])) * -sc, bc);
#pragma unroll
    for (int t = 0; t < 4; ++t) {
      l0[t] = __builtin_amdgcn_exp2f(__builtin_fmaf(l0[t], sc, bc[t]));
      l1[t] = __builtin_amdgcn_exp2f(__builtin_fmaf(l1[t], sc, bc[t]));
    }
    rows_bcast4(__builtin_amdgcn_rcpf(rows_reduce4<false>(l0[0] + l1[0], l0[1] + l1[1], l0[2] + l1[2], l0[3] + l1[3]) * ug), bc);
    const unsigned sh = odd ? 16u : 0u;
    const unsigned mk0 = own0 ? 0u : 0xffffu, mk1 = own1 ? 0u : 0xffffu;
#pragma unroll
    for (int t = 0; t < 4; ++t) {
      const unsigned pe = pk2(l0[t] * bc[t], l1[t] * bc[t]), ph = pe >> 16;
      const bf16x8 pf = as_frag((u32x4){(pe & mk0) << sh, (ph & mk1) << sh, (pe & (mk0 ^ 0xffffu)) << sh, (ph & (mk1 ^ 0xffffu)) << sh});
      const f32x4 r0 = mfma(frag2(vf[t][0], vf[t + 1][0]), pf, z4);
      const f32x4 r1 = mfma(frag2(vf[t][1], vf[t + 1][1]), pf, z4);
      const u32x2 c0 = pk4(r0), c1 = pk4(r1);
      __builtin_amdgcn_raw_buffer_store_b128((u32x4){c0[0], c0[1], g3 ? 0u : c1[0], g3 ? 0u : c1[1]}, wsr, l16 + obs + t * 32768, 0, 16);
    }
    VM_WAIT(0);
    if (l16 == 0) __hip_atomic_store((gu64*)(ws + WS_FA) + ((rt * 2 + rh) * 16 + hp * 2 + hh) * FS, tagO, RLX_AGENT);
    if (cg >= 7) {
      if (bid == 56 && w == 0) {
#pragma nounroll
        for (int k = 0; k < 4; ++k) {
          const int i = (k & 1) * 64 + lane;
          poll_flags(k < 2 ? (gu64*)(ws + WS_CLR) + i : (gu64*)(ws + WS_FA) + ((i >> 3) * 32 + (i & 7) * 2) * FS, tagO);
        }
        if (lane == 0) __hip_atomic_store((gu64*)(ws + WS_E), tagO, RLX_AGENT);
      }
      return;
    }
  }

  const int pb = w >> 2;
  wait4(L0 + SYNC + (pb ? SY_WOB : SY_WOA));
  const unsigned sw = (2 * g + (q >> 1)) & 3;
  const unsigned rb = L0 + (pb * 448 + 4 * g + q) * 128 + 8 * p;
  unsigned wa[4], wa2[4];
#pragma unroll
  for (int v = 0; v < 4; ++v) {
    wa[v] = opaque(rb + ((v ^ sw) << 5));
    wa2[v] = opaque(wa[v] + (g3 ? 0u : 2048u));
  }
  auto wfrag = [&](int h, int ct) {
    const int v = ct ^ (2 * (h & 1));
    return lds_tr2(wa[v] + h * 3584, wa2[v] + h * 3584);
  };
  constexpr int NPRE = 8;
  bf16x8 wf[16][4];
#pragma unroll
  for (int h = 0; h < NPRE; ++h)
#pragma unroll
    for (int ct = 0; ct < 4; ++ct) wf[h][ct] = wfrag(h, ct);
  const unsigned ub = WS_O + ((rg * 4 + wl) * 32 + pb * 16) * 1024;
  tagO = uniform64(*(LAS u64*)(uintptr_t)(L0 + SYNC + SY_TAG));
  {
    const gu64* fp = pb ? (gu64*)(ws + WS_FB) + ((rg * 2 + (wl >> 1)) * 8 + (li & 7)) * FS : (gu64*)(ws + WS_FA) + (rg * 16 + li) * FS;
    const u64 f0 = __hip_atomic_load(fp, RLX_AGENT);
    if (br && !pb) { VM_WAIT(0); sig(L0 + SYNC + SY_WOB + wl * 4); }
    if (!__all(f0 == tagO)) poll_flags(fp, tagO);
  }
  u32x4 of[16];
  {
    unsigned vo = ub + lane * 16;
    asm volatile("" : "+v"(vo)::"memory");
#pragma unroll
    for (int h = 0; h < 16; ++h) of[h] = __builtin_amdgcn_raw_buffer_load_b128(wsr, vo, ((h + 2 * cg) & 15) * 1024, 16);
    SB();
  }
  f32x4 acc[4] = {z4, z4, z4, z4};
#pragma unroll
  for (int h = 0; h < 16; ++h) {
#pragma unroll
    for (int ct = 0; ct < 4; ++ct) acc[ct] = mfma(wf[h][ct], as_frag(of[h]), acc[ct]);
    SB();
    if (h + NPRE < 16) {
#pragma unroll
      for (int ct = 0; ct < 4; ++ct) wf[h + NPRE][ct] = wfrag(h + NPRE, ct);
    }
    SB();
  }
  const unsigned xa = L0 + P2X + wl * 4096 + lane * 16;
  if (!pb) {
    wait4(L0 + SYNC + SY_D6);
#pragma unroll
    for (int ct = 0; ct < 4; ++ct) lds_st128(xa + ct * 1024, __builtin_bit_cast(u32x4, acc[ct]));
    sig(L0 + SYNC + SY_P2 + wl * 4);
  } else {
    f32x4 pa[4];
    for (;;) {
      const unsigned f = *(volatile LAS unsigned*)(uintptr_t)(L0 + SYNC + SY_P2 + wl * 4);
#pragma unroll
      for (int ct = 0; ct < 4; ++ct) pa[ct] = *(volatile LAS f32x4*)(uintptr_t)(xa + ct * 1024);
      if (__builtin_amdgcn_readfirstlane(f)) break;
      __builtin_amdgcn_s_sleep(1);
    }
    const unsigned tb = L0 + P2X + wl * 4096;
#pragma unroll
    for (int ct = 0; ct < 4; ++ct) {
      const u32x2 a = pk4(acc[ct] + pa[ct]);
      *(LAS u32x2*)(uintptr_t)(tb + li * 128 + (((ct * 2 + (g >> 1)) ^ (li & 7)) << 4) + (g & 1) * 8) = a;
    }
    const __amdgpu_buffer_rsrc_t orr = __builtin_amdgcn_make_buffer_rsrc(out, 0, kS * XROW, 0x00020000);
    const unsigned ro = (rg * 64 + wl * 16 + (lane >> 3)) * XROW + cg * 128 + (lane & 7) * 16;
#pragma unroll
    for (int k = 0; k < 2; ++k) {
      const u32x4 v = lds_ld128(tb + k * 1024 + (lane >> 3) * 128 + (((lane & 7) ^ (lane >> 3)) << 4));
      __builtin_amdgcn_raw_buffer_store_b128(v, orr, ro + k * 8 * XROW, 0, 16);
    }
  }
}

}

extern "C" void kernel_launch(void* const* d_in, const int* in_sizes, int n_in, void* d_out, int out_size, void* d_ws,
                              size_t ws_size, hipStream_t stream) {
  (void)in_sizes; (void)n_in; (void)out_size; (void)ws_size;
  gated_mix_kernel<<<256, 512, 0, stream>>>((const char*)d_in[0], (const char*)d_in[1], (const char*)d_in[2],
                                            (const char*)d_in[4], (const char*)d_in[5],
                                            (const unsigned short*)d_in[6], (char*)d_out, (char*)d_ws);
}
```
